```python
import torch

def _softmax(x, axis=-1):
    return torch.softmax(x, dim=axis)

def _gelu(x):
    return torch.nn.functional.gelu(x, approximate='tanh')

def _silu(x):
    return torch.nn.functional.silu(x)

def _sigmoid(x):
    return torch.sigmoid(x)

def _layernorm(x, axis=-1, eps=1e-05):
    m = x.mean(dim=axis, keepdim=True)
    v = x.var(dim=axis, keepdim=True, unbiased=False)
    return (x - m) / torch.sqrt(v + eps)

def _scatter_add(tgt, idx, src, axis=0):
    return tgt.clone().index_add_(axis, idx, src)

def _segment_sum(src, seg, n):
    out = torch.zeros((n,) + src.shape[1:], dtype=src.dtype)
    return out.index_add_(0, seg, src)

def _pad(x, pw):
    flat = tuple((v for pair in reversed(pw) for v in pair))
    return torch.nn.functional.pad(x, flat)
kernel_name = 'interleave_layers__linear_attn_block__memory_aug_recurrent_mem_tr__prompt'

def setup_inputs():

    _w = lambda a: (lambda t: t.to(torch.bfloat16) if t.is_floating_point() else t)(torch.as_tensor(a))

    def _A_setup():
        B, S, D = (1, 2048, 640)
        return {'x': _w((lambda *s: torch.randn(*s, dtype=torch.bfloat16) * 0.05)(B, S, D)), 'wqkv': _w((lambda *s: torch.randn(*s, dtype=torch.bfloat16) * 0.05)(D, 3 * D)), 'wo': _w((lambda *s: torch.randn(*s, dtype=torch.bfloat16) * 0.05)(D, D))}
    _A_in = _A_setup()

    def _B_setup():
        B, S, Dm = (1, 2048, 640)
        H, Dh = (32, 192)
        M, seg = (8, 32)
        seg = min(seg, S)
        S = S // seg * seg
        return {'x': _w((lambda *s: torch.randn(*s, dtype=torch.bfloat16) * 0.05)(B, S, Dm)), 'mem0': _w((lambda *s: torch.randn(*s, dtype=torch.bfloat16) * 0.8)(B, M, Dm)), 'Wq': _w((lambda *s: torch.randn(*s, dtype=torch.bfloat16) * 1.6)(Dm, H, Dh) * Dm ** (-0.5)), 'Wk': _w((lambda *s: torch.randn(*s, dtype=torch.bfloat16) * 1.6)(Dm, H, Dh) * Dm ** (-0.5)), 'Wv': _w((lambda *s: torch.randn(*s, dtype=torch.bfloat16) * 0.8)(Dm, H, Dh) * Dm ** (-0.5)), 'Wo': _w((lambda *s: torch.randn(*s, dtype=torch.bfloat16) * 0.8)(H, Dh, Dm) * (H * Dh) ** (-0.5)), 'seg_len': seg}
    _B_in = _B_setup()
    return {'A_x': _A_in['x'], 'A_wqkv': _A_in['wqkv'], 'A_wo': _A_in['wo'], 'B_x': _B_in['x'], 'B_mem0': _B_in['mem0'], 'B_Wq': _B_in['Wq'], 'B_Wk': _B_in['Wk'], 'B_Wv': _B_in['Wv'], 'B_Wo': _B_in['Wo'], 'B_seg_len': _B_in['seg_len']}

def reference(A_x, A_wqkv, A_wo, B_x, B_mem0, B_Wq, B_Wk, B_Wv, B_Wo, B_seg_len):

    def _A_ref(x, wqkv, wo):
        B, S, D = x.shape
        H = 32
        Dh = D // H
        qkv = (lambda x, *d: x.permute(*d))((x @ wqkv).reshape(B, S, 3, H, Dh), 2, 0, 3, 1, 4)
        q, k, v = (qkv[0], qkv[1], qkv[2])
        phi = lambda t: torch.where(t > 0, t + 1, torch.exp(t)) + 1e-06
        pq, pk = (phi(q), phi(k))
        kv = torch.einsum('bhsd,bhse->bhde', pk, v)
        z = torch.einsum('bhsd->bhd', pk)
        num = torch.einsum('bhqd,bhde->bhqe', pq, kv)
        den = torch.einsum('bhqd,bhd->bhq', pq, z)[..., None] + 1e-06
        o = num / den
        return (lambda x, *d: x.permute(*d))(o, 0, 2, 1, 3).reshape(B, S, D) @ wo

    def _B_ref(x, mem0, Wq, Wk, Wv, Wo, seg_len):
        B, S, Dm = x.shape
        H, Dh = (Wq.shape[1], Wq.shape[2])
        M = mem0.shape[1]
        scale = Dh ** (-0.5)
        n_seg = S // seg_len
        L = M + seg_len
        idx = torch.arange(L)
        mask = (idx[:, None] < M) | (idx[:, None] >= idx[None, :])
        mem = mem0
        outs = []
        for s in range(n_seg):
            xs = x[:, s * seg_len:(s + 1) * seg_len, :].clone()
            h = torch.concatenate([mem, xs], axis=1)
            q = torch.einsum('btd,dhe->bhte', h, Wq)
            k = torch.einsum('btd,dhe->bhte', h, Wk)
            v = torch.einsum('btd,dhe->bhte', h, Wv)
            scores = torch.einsum('bhqe,bhke->bhqk', q, k) * scale
            scores = torch.where(mask[None, None], scores, -1000000000.0)
            p = _softmax(scores, axis=-1)
            o = torch.einsum('bhqk,bhke->bhqe', p, v)
            o = torch.einsum('bhte,hed->btd', o, Wo)
            mem = o[:, :M, :].clone()
            outs.append(o[:, M:, :])
        return torch.concatenate(outs, axis=1)
    x = A_x
    x = x + _A_ref(_layernorm(x, axis=-1), A_wqkv, A_wo)
    x = x + _B_ref(_layernorm(x, axis=-1), B_mem0, B_Wq, B_Wk, B_Wv, B_Wo, B_seg_len)
    x = x + _A_ref(_layernorm(x, axis=-1), A_wqkv, A_wo)
    x = x + _B_ref(_layernorm(x, axis=-1), B_mem0, B_Wq, B_Wk, B_Wv, B_Wo, B_seg_len)
    return x
```

```cpp
#include <hip/hip_runtime.h>
#include <hip/hip_bf16.h>
#include <stdint.h>

typedef __attribute__((ext_vector_type(8))) __bf16 bf16x8;
typedef __attribute__((ext_vector_type(4))) __bf16 bf16x4;
typedef __attribute__((ext_vector_type(2))) __bf16 bf16x2;
typedef __attribute__((ext_vector_type(4))) short s16x4;
typedef __attribute__((ext_vector_type(16))) __bf16 bf16x16;
typedef __attribute__((ext_vector_type(4))) float f32x4;
typedef __attribute__((ext_vector_type(4))) unsigned u32x4;
typedef __attribute__((ext_vector_type(2))) unsigned u32x2;
typedef unsigned short u16;
#define GAS __attribute__((address_space(1)))
#define LO(v) __builtin_shufflevector(v, v, 0,1,2,3,4,5,6,7)
#define HI(v) __builtin_shufflevector(v, v, 8,9,10,11,12,13,14,15)
#define CAT4(a, b) __builtin_shufflevector(a, b, 0,1,2,3,4,5,6,7)
#define CAT8(a, b) __builtin_shufflevector(a, b, 0,1,2,3,4,5,6,7,8,9,10,11,12,13,14,15)

constexpr int S_ = 2048, D_ = 640, NSEG = 64;
constexpr size_t OFF_COMM = 0;
constexpr size_t SZ_COMM = ((size_t)16 << 20) + (256 << 10);
constexpr size_t OFF_PX = OFF_COMM + SZ_COMM;
constexpr size_t SZ_PXL = (size_t)64 * 81920;
constexpr size_t OFF_HF = OFF_PX + 2 * SZ_PXL;
constexpr size_t SZ_HF = (size_t)2 * 640 * 6144 * 2;
constexpr size_t OFF_CF = OFF_HF + SZ_HF;
constexpr size_t SZ_CF = (size_t)32 * 640 * 640 * 2;
constexpr size_t SZ_X = (size_t)S_ * D_ * 2;
constexpr size_t OFF_XNF = OFF_CF + SZ_CF;
constexpr size_t OFF_X1 = OFF_XNF + SZ_X;
constexpr size_t OFF_X2 = OFF_X1 + SZ_X;
constexpr size_t OFF_XN = OFF_X2 + SZ_X;
constexpr size_t OFF_QKV = OFF_XN + SZ_X;
constexpr size_t OFF_KV = OFF_QKV + 3 * SZ_X;
constexpr size_t OFF_O = OFF_KV + 65536;
constexpr size_t OFF_CTL = OFF_O + SZ_X;

__device__ __forceinline__ float bf2f(u16 v) { return __uint_as_float((unsigned)v << 16); }
__device__ __forceinline__ u16 f2bf(float f) { return __builtin_bit_cast(u16, (__bf16)f); }
__device__ __forceinline__ float rbf(float f) { return (float)(__bf16)f; }
__device__ __forceinline__ bf16x4 cvt4(f32x4 v) { bf16x4 r; r[0] = (__bf16)v[0]; r[1] = (__bf16)v[1]; r[2] = (__bf16)v[2]; r[3] = (__bf16)v[3]; return r; }
__host__ __device__ __forceinline__ int pi16(int p) { return 8 * ((p >> 1) & 1) + 2 * (p >> 2) + (p & 1); }
__host__ __device__ __forceinline__ int dcol(int t, int u, int gb, int j) { return 64 * t + 16 * (2 * u + (gb >> 1)) + pi16(8 * (gb & 1) + j); }
__host__ __device__ __forceinline__ int slot_t(int w, int i) { return i < 2 ? w + 4 * i : (i < 5 ? (w ^ (i - 1)) : (i < 8 ? 4 + (w ^ (i - 4)) : i)); }

__global__ void prep_hf(const u16* __restrict__ Wq, const u16* __restrict__ Wk, u32x4* __restrict__ hf) {
  int id = blockIdx.x * 256 + threadIdx.x;
  int lane = id & 63, u = (id >> 6) & 1, rest = id >> 7;
  int n = rest % 6; rest /= 6; int slot = rest % 10; rest /= 10; int w = rest & 3, h = rest >> 2;
  const u16* W = n < 3 ? Wq : Wk;
  int e = 48 * w + 16 * (n % 3) + (lane & 15), t = slot_t(w, slot), gb = lane >> 4;
  unsigned v[4];
  for (int j = 0; j < 4; ++j) {
    unsigned a = W[(size_t)dcol(t, u, gb, 2 * j) * 6144 + h * 192 + e], b = W[(size_t)dcol(t, u, gb, 2 * j + 1) * 6144 + h * 192 + e];
    v[j] = a | (b << 16);
  }
  hf[id] = (u32x4){v[0], v[1], v[2], v[3]};
}
__global__ void __launch_bounds__(256) prep_cf(const u16* __restrict__ Wv, const u16* __restrict__ Wo, u32x4* __restrict__ cf) {
  int h = blockIdx.x >> 3, ch = blockIdx.x & 7, wave = threadIdx.x >> 6, lane = threadIdx.x & 63, g = lane >> 4, n16 = lane & 15;
  bf16x8 bfr[5][6];
#pragma unroll
  for (int ct = 0; ct < 5; ++ct)
#pragma unroll
    for (int ks = 0; ks < 6; ++ks) {
      bf16x8 v;
#pragma unroll
      for (int j = 0; j < 8; ++j) v[j] = __builtin_bit_cast(__bf16, Wo[((size_t)h * 192 + 32 * ks + 8 * g + j) * 640 + ch * 80 + ct * 16 + n16]);
      bfr[ct][ks] = v;
    }
  for (int p = wave; p < 20; p += 4) {
    int t = p >> 1, u = p & 1;
    f32x4 acc[2][5];
#pragma unroll
    for (int a = 0; a < 2; ++a) {
      int d = dcol(t, u, n16 >> 2, 4 * a + (n16 & 3));
#pragma unroll
      for (int ct = 0; ct < 5; ++ct) acc[a][ct] = (f32x4){0, 0, 0, 0};
#pragma unroll
      for (int ks = 0; ks < 6; ++ks) {
        bf16x8 af = *(const bf16x8*)(Wv + (size_t)d * 6144 + h * 192 + 32 * ks + 8 * g);
#pragma unroll
        for (int ct = 0; ct < 5; ++ct) acc[a][ct] = __builtin_amdgcn_mfma_f32_16x16x32_bf16(af, bfr[ct][ks], acc[a][ct], 0, 0, 0);
      }
    }
#pragma unroll
    for (int ct = 0; ct < 5; ++ct) {
      int c = ch * 10 + ct * 2 + (n16 >> 3), w = h >> 3, n = (h & 7) >> 1, e = h & 1;
      bf16x8 o = CAT4(cvt4(acc[0][ct]), cvt4(acc[1][ct]));
      cf[((((size_t)c * 4 + w) * 10 + t) * 4 + n) * 128 + u * 64 + g * 16 + ((n16 & 7) >> 1) * 4 + e * 2 + (n16 & 1)] = __builtin_bit_cast(u32x4, o);
    }
  }
}
__device__ __forceinline__ unsigned sbase(int layer, int s, unsigned cal) {
  unsigned a = (unsigned)(layer * 32 + (s >> 1)) << 18;
  return a + ((((cal >> (a >> 21)) ^ s) & 1) << 13);
}
__global__ void prep_misc(const u32x4* __restrict__ mem0, unsigned char* comm, unsigned* ctl) {
  __shared__ unsigned cal;
  int lane = threadIdx.x;
  if (lane == 0) {
    unsigned m = 0;
    for (int pg = 0; pg < 8; ++pg) {
      unsigned best[2] = {1000000, 1000000};
      for (int rep = 0; rep < 3; ++rep)
        for (int cc = 0; cc < 2; ++cc) {
          unsigned* p = (unsigned*)(comm + ((size_t)pg << 21) + 3 * 16384 + cc * 8192 + 8064);
          unsigned v, z = 0;
          unsigned c0 = (unsigned)__builtin_amdgcn_s_memtime();
          asm volatile("global_atomic_or %0, %1, %2, off sc0\n s_waitcnt vmcnt(0)" : "=&v"(v) : "v"(p), "v"(z) : "memory");
          unsigned c1 = (unsigned)__builtin_amdgcn_s_memtime();
          best[cc] = min(best[cc], c1 - c0);
        }
      m |= (best[1] < best[0] ? 1u : 0u) << pg;
    }
    cal = m; ctl[1] = m; ctl[0] = 0;
  }
  __syncthreads();
  if (threadIdx.x < 8) *(unsigned*)(comm + sbase(threadIdx.x >> 2, 0, cal) + (threadIdx.x & 3) * 65536 + 3 * 16384 + 7168) = 0u;
  for (int i = threadIdx.x; i < 2 * 4 * 640; i += blockDim.x) {
    int layer = i / 2560, cp = (i / 640) & 3, q = i % 640, c = q >> 3, r = q & 7, t = c >> 3;
    u32x4 v = mem0[r * 80 + c];
    v = __builtin_elementwise_min(v, (u32x4){0xFFFFFFFEu, 0xFFFFFFFEu, 0xFFFFFFFEu, 0xFFFFFFFEu});
    *(u32x4*)(comm + sbase(layer, 0, cal) + cp * 65536 + (t & 3) * 16384 + 5120 + (t >> 2) * 1024 + (c & 7) * 128 + r * 16) = v;
  }
}

__global__ void ln_rows(const u16* __restrict__ x, u16* __restrict__ xn, int frag) {
  int row = blockIdx.x * 4 + (threadIdx.x >> 6), lane = threadIdx.x & 63;
  float v[10], s = 0;
  for (int i = 0; i < 10; ++i) { v[i] = bf2f(x[row * 640 + lane + 64 * i]); s += v[i]; }
  for (int o = 32; o; o >>= 1) s += __shfl_xor(s, o);
  float m = s / 640.f, q = 0;
  for (int i = 0; i < 10; ++i) { float d = v[i] - m; q += d * d; }
  for (int o = 32; o; o >>= 1) q += __shfl_xor(q, o);
  float rs = 1.f / sqrtf(q / 640.f + 1e-5f);
  for (int i = 0; i < 10; ++i) {
    u16 o = f2bf((v[i] - m) * rs);
    if (!frag) xn[row * 640 + lane + 64 * i] = o;
    else {
      int d = lane, t = i, grp = d >> 4, u = grp >> 1, a = d & 15;
      int p = ((a & 7) >> 1) * 4 + (a >> 3) * 2 + (a & 1), gb = (grp & 1) * 2 + (p >> 3), j = p & 7;
      int seg = row >> 5, tile = (row >> 4) & 1, tok = row & 15;
      xn[(((size_t)(seg * 4 + 2 * u + tile) * 10 + t) * 64 + gb * 16 + tok) * 8 + j] = o;
    }
  }
}
__global__ void gemm_naive(const u16* __restrict__ a, const u16* __restrict__ b, const u16* __restrict__ res, u16* __restrict__ c, int N, int mode) {
  int n = blockIdx.x * 64 + (threadIdx.x & 63), m0 = blockIdx.y * 16 + (threadIdx.x >> 6) * 4;
  float acc[4] = {0, 0, 0, 0};
  for (int k = 0; k < 640; ++k) { float w = bf2f(b[(size_t)k * N + n]); for (int i = 0; i < 4; ++i) acc[i] += bf2f(a[(m0 + i) * 640 + k]) * w; }
  for (int i = 0; i < 4; ++i) {
    float v = acc[i];
    if (mode == 0) { v = rbf(v); if (n < 1280) v = (v > 0 ? v + 1.f : __expf(v)) + 1e-6f; }
    else v += bf2f(res[(size_t)(m0 + i) * N + n]);
    c[(size_t)(m0 + i) * N + n] = f2bf(v);
  }
}
__global__ void kv_naive(const u16* __restrict__ qkv, float* __restrict__ kv) {
  int h = blockIdx.x, i = threadIdx.x; if (i >= 420) return;
  int d = i / 21, e = i % 21; float acc = 0;
  for (int s = 0; s < S_; ++s) acc += bf2f(qkv[(size_t)s * 1920 + 640 + h * 20 + d]) * (e < 20 ? bf2f(qkv[(size_t)s * 1920 + 1280 + h * 20 + e]) : 1.f);
  kv[h * 420 + i] = rbf(acc);
}
__global__ void o_naive(const u16* __restrict__ qkv, const float* __restrict__ kv, u16* __restrict__ o) {
  int s = blockIdx.x, i = threadIdx.x, h = i / 20, e = i % 20; float num = 0, den = 0;
  for (int d = 0; d < 20; ++d) { float q = bf2f(qkv[(size_t)s * 1920 + h * 20 + d]); num += q * kv[h * 420 + d * 21 + e]; den += q * kv[h * 420 + d * 21 + 20]; }
  o[s * 640 + i] = f2bf(num / (den + 1e-6f));
}

typedef __attribute__((ext_vector_type(16))) unsigned u32x16;
typedef __attribute__((ext_vector_type(8))) unsigned u32x8;
struct RecArgs { const unsigned char* hf; const unsigned char* cf; const unsigned char* xnf; const u16* xres; u16* xout; unsigned char* comm; unsigned char* px; unsigned* ctl; int layer, dp, dm, skew, delta, lat, pubw, pad; };
#define SRD(vec, i, ptr, bytes) do { unsigned long long p_ = (unsigned long long)(ptr); vec[i] = (unsigned)p_; vec[i + 1] = (unsigned)(p_ >> 32) & 0xFFFFu; vec[i + 2] = (unsigned)(bytes); vec[i + 3] = 0x00020000u; } while (0)

__global__ void __launch_bounds__(256, 1) rec_kernel(RecArgs a) {
  const unsigned b = blockIdx.x, wave = __builtin_amdgcn_readfirstlane(threadIdx.x >> 6), lane = threadIdx.x & 63;
  const unsigned par = (b & 7) >> 2, sub = b & 3, jj = b >> 3;
  if (jj >= 28) return;
  const unsigned cal = __builtin_amdgcn_readfirstlane(a.ctl[1]);
  const unsigned g = lane >> 4, n16 = lane & 15, L16 = lane * 16, OOB = 0x80000000u;
  const bool isH = jj < 8;
  const unsigned h = sub * 8 + jj, c = sub * 20 + (jj - 8);
  u32x16 sa, sb; u32x8 sc;
  SRD(sa, 0, a.comm, SZ_COMM); SRD(sa, 4, a.px, SZ_PXL); SRD(sa, 8, a.xnf, SZ_X);
  if (isH) SRD(sa, 12, a.hf + (size_t)(h * 4 + wave) * 122880, 122880); else SRD(sa, 12, a.cf + (size_t)(c * 4 + wave) * 81920, 81920);
  SRD(sb, 0, a.xres, SZ_X); SRD(sb, 4, a.xout, SZ_X); SRD(sb, 8, a.ctl, 131072);
  sb[12] = wave; sb[13] = par; sb[14] = a.layer * 32; sb[15] = cal;
  sc[0] = isH ? (wave < 2 ? OOB : 0u) : (unsigned)a.dp; sc[1] = isH ? (wave == 1 ? 20480 - 512 + h * 128 : wave * 20480 + h * 640) : (unsigned)a.dm; sc[2] = a.skew; sc[3] = a.delta; sc[4] = a.lat; sc[5] = (wave == 1 ? 8 : wave * 20480) + h * 640; sc[6] = sub * 65536;
  sc[7] = isH ? wave * 65536 + (h >> 3) * 16384 + (h & 7) * 640 : (unsigned)a.pubw;
  const unsigned dbgoff = 256 + (a.layer * 1024 + b * 4 + wave) * 32;
  const unsigned mo = sub * 65536 + wave * 16384 + 5120 + L16, mo2 = sub * 65536 + (wave & 1) * 16384 + 7168 + (wave >> 1) * 512 + lane * 8;
  const unsigned m2w = (8 + (wave & 1)) * 1024 + (wave >> 1) * 512 + lane * 8, idx = (lane & 8) ? 0xEEEEu : 0x4444u, sch = sub * 65536 + 3 * 16384 + 7168;
  u32x16 va; u32x8 vb;
  if (isH) {
    const unsigned r8 = lane >> 3, q8 = lane & 7, dump = 62464 + L16;
    va[0] = L16; va[1] = L16 + (wave ^ 1) * 1024; va[2] = L16 + (wave ^ 2) * 1024; va[3] = L16 + (wave ^ 3) * 1024; va[4] = L16 + wave * 1024;
    va[5] = 65536 + wave * 24576 + L16; va[6] = mo; va[7] = mo2; va[8] = 40960 + m2w; va[9] = idx;
    const unsigned sw = 57344 + wave * 1024 + n16 * 128 + (((2 * g) ^ n16) & 7) * 16;
    va[10] = n16 < 8 ? sw : dump; va[11] = n16 < 8 ? (sw ^ 16) : dump;
    va[12] = (n16 < 8 && g >= 2) ? 61440 + wave * 256 + n16 * 32 + (g - 2) * 16 : dump;
    va[13] = 57344 + r8 * 128 + (q8 ^ r8) * 16; va[14] = 61440 + lane * 4;
    va[15] = (q8 >> 1) * 128 + r8 * 16 + (q8 & 1) * 8;
    vb[0] = (q8 & 1) ? OOB : 512 + r8 * 16 + q8 * 2; vb[1] = sch;
    vb[2] = (q8 & 1) * 16 + (q8 >> 1) * 4 - (wave * 8 + r8); vb[3] = 0;
#pragma unroll
    for (int j = 0; j < 4; ++j) {
      const unsigned base = (j < 2 ? 0 : 8) + 4 * (g & 1) + 2 * (j & 1);
      vb[4 + j] = (n16 == base ? 0x3F80u : 0u) | (n16 == base + 1 ? 0x3F800000u : 0u);
    }
  } else {
    const unsigned e1 = (lane >> 3) & 1, r7 = lane & 7, t = c >> 3;
    va[0] = L16; va[1] = 81920 + wave * 1024 + L16; va[2] = mo; va[3] = mo2; va[4] = 81920 + m2w; va[5] = idx;
    va[6] = sub * 65536 + e1 * 640 + g * 128 + r7 * 16; va[7] = sub * 65536 + (2 * g + e1) * 640 + 512 + r7 * 16;
    va[8] = e1 * 640 + g * 128 + r7 * 16; va[9] = (2 * g + e1) * 640 + 512 + r7 * 16;
    va[10] = sch;
    va[11] = (t & 3) * 16384 + 5120 + (t >> 2) * 1024 + (c & 7) * 128 + r7 * 16 + g * 4 + e1 * 65536;
    va[12] = 0;
    va[13] = wave == 1 ? 20480 + (2 * g + e1) * 128 + r7 * 16 : OOB;
    va[14] = (e1 && wave != 1) ? OOB : (e1 * 8 + r7) * 1280 + c * 16 + g * 4; va[15] = 0;
    vb[0] = (c == 0 && wave == 1 && lane < 4) ? lane * 65536 + 3 * 16384 + 7168 : OOB;
    vb[1] = vb[2] = vb[3] = vb[4] = vb[5] = vb[6] = vb[7] = 0;
  }
  sc[6] = jj;
  asm volatile(
  "s_nop 7\n s_cmp_ge_u32 s54, 8\n s_cbranch_scc1 C_ENTRY_%=\n"
  "s_mov_b32 s76, 0x3dd53b95\n"
  "s_mov_b32 s77, 0xceabfb8f\n"
  "s_mov_b32 s78, 0xfffffffe\n"
  "s_mov_b32 s56, s45\n"
  "v_cmp_ge_i32 s[8:9], 0, v18\n"
  "v_cmp_ge_i32 s[10:11], -1, v18\n"
  "v_cmp_ge_i32 s[12:13], -2, v18\n"
  "v_cmp_ge_i32 s[14:15], -3, v18\n"
  "s_add_u32 s60, s56, 0\n"
  "s_mul_i32 s72, s60, 40960\n"
  "s_mul_i32 s73, s44, 10240\n"
  "s_add_u32 s72, s72, s73\n"
  "s_mov_b32 m0, s73\n"
  "s_nop 0\n"
  "buffer_load_dwordx4 v0, s[24:27], s72 offen offset:0 lds\n"
  "buffer_load_dwordx4 v0, s[24:27], s72 offen offset:1024 lds\n"
  "buffer_load_dwordx4 v0, s[24:27], s72 offen offset:2048 lds\n"
  "buffer_load_dwordx4 v0, s[24:27], s72 offen offset:3072 lds\n"
  "s_mul_i32 s69, s44, 24576\n"
  "s_add_u32 s69, s69, 65536\n"
  "s_mov_b32 s68, 0\n"
  "buffer_load_dwordx4 a[0:3], v0, s[28:31], s68 offen offset:0\n"
  "buffer_load_dwordx4 a[4:7], v0, s[28:31], s68 offen offset:1024\n"
  "s_mov_b32 s68, 2048\n"
  "buffer_load_dwordx4 a[8:11], v0, s[28:31], s68 offen offset:0\n"
  "buffer_load_dwordx4 a[12:15], v0, s[28:31], s68 offen offset:1024\n"
  "s_mov_b32 s68, 4096\n"
  "buffer_load_dwordx4 a[16:19], v0, s[28:31], s68 offen offset:0\n"
  "buffer_load_dwordx4 a[20:23], v0, s[28:31], s68 offen offset:1024\n"
  "s_mov_b32 s68, 6144\n"
  "buffer_load_dwordx4 a[24:27], v0, s[28:31], s68 offen offset:0\n"
  "buffer_load_dwordx4 a[28:31], v0, s[28:31], s68 offen offset:1024\n"
  "s_mov_b32 s68, 8192\n"
  "buffer_load_dwordx4 a[32:35], v0, s[28:31], s68 offen offset:0\n"
  "buffer_load_dwordx4 a[36:39], v0, s[28:31], s68 offen offset:1024\n"
  "s_mov_b32 s68, 10240\n"
  "s_add_u32 m0, s69, 0\n"
  "s_nop 0\n"
  "buffer_load_dwordx4 v0, s[28:31], s68 offen offset:0 lds\n"
  "buffer_load_dwordx4 v0, s[28:31], s68 offen offset:1024 lds\n"
  "s_mov_b32 s68, 12288\n"
  "buffer_load_dwordx4 a[40:43], v0, s[28:31], s68 offen offset:0\n"
  "buffer_load_dwordx4 a[44:47], v0, s[28:31], s68 offen offset:1024\n"
  "s_mov_b32 s68, 14336\n"
  "buffer_load_dwordx4 a[48:51], v0, s[28:31], s68 offen offset:0\n"
  "buffer_load_dwordx4 a[52:55], v0, s[28:31], s68 offen offset:1024\n"
  "s_mov_b32 s68, 16384\n"
  "buffer_load_dwordx4 a[56:59], v0, s[28:31], s68 offen offset:0\n"
  "buffer_load_dwordx4 a[60:63], v0, s[28:31], s68 offen offset:1024\n"
  "s_mov_b32 s68, 18432\n"
  "buffer_load_dwordx4 a[64:67], v0, s[28:31], s68 offen offset:0\n"
  "buffer_load_dwordx4 a[68:71], v0, s[28:31], s68 offen offset:1024\n"
  "s_mov_b32 s68, 20480\n"
  "buffer_load_dwordx4 a[72:75], v0, s[28:31], s68 offen offset:0\n"
  "buffer_load_dwordx4 a[76:79], v0, s[28:31], s68 offen offset:1024\n"
  "s_mov_b32 s68, 22528\n"
  "s_add_u32 m0, s69, 2048\n"
  "s_nop 0\n"
  "buffer_load_dwordx4 v0, s[28:31], s68 offen offset:0 lds\n"
  "buffer_load_dwordx4 v0, s[28:31], s68 offen offset:1024 lds\n"
  "s_mov_b32 s68, 24576\n"
  "buffer_load_dwordx4 a[80:83], v0, s[28:31], s68 offen offset:0\n"
  "buffer_load_dwordx4 a[84:87], v0, s[28:31], s68 offen offset:1024\n"
  "s_mov_b32 s68, 26624\n"
  "buffer_load_dwordx4 a[88:91], v0, s[28:31], s68 offen offset:0\n"
  "buffer_load_dwordx4 a[92:95], v0, s[28:31], s68 offen offset:1024\n"
  "s_mov_b32 s68, 28672\n"
  "buffer_load_dwordx4 a[96:99], v0, s[28:31], s68 offen offset:0\n"
  "buffer_load_dwordx4 a[100:103], v0, s[28:31], s68 offen offset:1024\n"
  "s_mov_b32 s68, 30720\n"
  "buffer_load_dwordx4 a[104:107], v0, s[28:31], s68 offen offset:0\n"
  "buffer_load_dwordx4 a[108:111], v0, s[28:31], s68 offen offset:1024\n"
  "s_mov_b32 s68, 32768\n"
  "s_add_u32 m0, s69, 4096\n"
  "s_nop 0\n"
  "buffer_load_dwordx4 v0, s[28:31], s68 offen offset:0 lds\n"
  "buffer_load_dwordx4 v0, s[28:31], s68 offen offset:1024 lds\n"
  "s_mov_b32 s68, 34816\n"
  "s_add_u32 m0, s69, 6144\n"
  "s_nop 0\n"
  "buffer_load_dwordx4 v0, s[28:31], s68 offen offset:0 lds\n"
  "buffer_load_dwordx4 v0, s[28:31], s68 offen offset:1024 lds\n"
  "s_mov_b32 s68, 36864\n"
  "buffer_load_dwordx4 a[112:115], v0, s[28:31], s68 offen offset:0\n"
  "buffer_load_dwordx4 a[116:119], v0, s[28:31], s68 offen offset:1024\n"
  "s_mov_b32 s68, 38912\n"
  "buffer_load_dwordx4 a[120:123], v0, s[28:31], s68 offen offset:0\n"
  "buffer_load_dwordx4 a[124:127], v0, s[28:31], s68 offen offset:1024\n"
  "s_mov_b32 s68, 40960\n"
  "buffer_load_dwordx4 a[128:131], v0, s[28:31], s68 offen offset:0\n"
  "buffer_load_dwordx4 a[132:135], v0, s[28:31], s68 offen offset:1024\n"
  "s_mov_b32 s68, 43008\n"
  "buffer_load_dwordx4 a[136:139], v0, s[28:31], s68 offen offset:0\n"
  "buffer_load_dwordx4 a[140:143], v0, s[28:31], s68 offen offset:1024\n"
  "s_mov_b32 s68, 45056\n"
  "buffer_load_dwordx4 a[144:147], v0, s[28:31], s68 offen offset:0\n"
  "buffer_load_dwordx4 a[148:151], v0, s[28:31], s68 offen offset:1024\n"
  "s_mov_b32 s68, 47104\n"
  "s_add_u32 m0, s69, 8192\n"
  "s_nop 0\n"
  "buffer_load_dwordx4 v0, s[28:31], s68 offen offset:0 lds\n"
  "buffer_load_dwordx4 v0, s[28:31], s68 offen offset:1024 lds\n"
  "s_mov_b32 s68, 49152\n"
  "buffer_load_dwordx4 a[152:155], v0, s[28:31], s68 offen offset:0\n"
  "buffer_load_dwordx4 a[156:159], v0, s[28:31], s68 offen offset:1024\n"
  "s_mov_b32 s68, 51200\n"
  "buffer_load_dwordx4 a[160:163], v0, s[28:31], s68 offen offset:0\n"
  "buffer_load_dwordx4 a[164:167], v0, s[28:31], s68 offen offset:1024\n"
  "s_mov_b32 s68, 53248\n"
  "buffer_load_dwordx4 a[168:171], v0, s[28:31], s68 offen offset:0\n"
  "buffer_load_dwordx4 a[172:175], v0, s[28:31], s68 offen offset:1024\n"
  "s_mov_b32 s68, 55296\n"
  "buffer_load_dwordx4 a[176:179], v0, s[28:31], s68 offen offset:0\n"
  "buffer_load_dwordx4 a[180:183], v0, s[28:31], s68 offen offset:1024\n"
  "s_mov_b32 s68, 57344\n"
  "s_add_u32 m0, s69, 10240\n"
  "s_waitcnt vmcnt(50)\n"
  "buffer_load_dwordx4 v0, s[28:31], s68 offen offset:0 lds\n"
  "buffer_load_dwordx4 v0, s[28:31], s68 offen offset:1024 lds\n"
  "s_mov_b32 s68, 59392\n"
  "s_add_u32 m0, s69, 12288\n"
  "s_nop 0\n"
  "buffer_load_dwordx4 v0, s[28:31], s68 offen offset:0 lds\n"
  "buffer_load_dwordx4 v0, s[28:31], s68 offen offset:1024 lds\n"
  "s_mov_b32 s68, 61440\n"
  "buffer_load_dwordx4 a[184:187], v0, s[28:31], s68 offen offset:0\n"
  "buffer_load_dwordx4 a[188:191], v0, s[28:31], s68 offen offset:1024\n"
  "s_mov_b32 s68, 63488\n"
  "buffer_load_dwordx4 a[192:195], v0, s[28:31], s68 offen offset:0\n"
  "buffer_load_dwordx4 a[196:199], v0, s[28:31], s68 offen offset:1024\n"
  "s_mov_b32 s68, 65536\n"
  "buffer_load_dwordx4 a[200:203], v0, s[28:31], s68 offen offset:0\n"
  "buffer_load_dwordx4 a[204:207], v0, s[28:31], s68 offen offset:1024\n"
  "s_mov_b32 s68, 67584\n"
  "s_add_u32 m0, s69, 14336\n"
  "s_waitcnt vmcnt(50)\n"
  "buffer_load_dwordx4 v0, s[28:31], s68 offen offset:0 lds\n"
  "buffer_load_dwordx4 v0, s[28:31], s68 offen offset:1024 lds\n"
  "s_mov_b32 s68, 69632\n"
  "buffer_load_dwordx4 a[208:211], v0, s[28:31], s68 offen offset:0\n"
  "buffer_load_dwordx4 a[212:215], v0, s[28:31], s68 offen offset:1024\n"
  "s_mov_b32 s68, 71680\n"
  "buffer_load_dwordx4 a[216:219], v0, s[28:31], s68 offen offset:0\n"
  "buffer_load_dwordx4 a[220:223], v0, s[28:31], s68 offen offset:1024\n"
  "s_mov_b32 s68, 73728\n"
  "buffer_load_dwordx4 a[224:227], v0, s[28:31], s68 offen offset:0\n"
  "buffer_load_dwordx4 a[228:231], v0, s[28:31], s68 offen offset:1024\n"
  "s_mov_b32 s68, 75776\n"
  "buffer_load_dwordx4 a[232:235], v0, s[28:31], s68 offen offset:0\n"
  "buffer_load_dwordx4 a[236:239], v0, s[28:31], s68 offen offset:1024\n"
  "s_mov_b32 s68, 77824\n"
  "s_waitcnt vmcnt(50)\n"
  "buffer_load_dwordx4 a[240:243], v0, s[28:31], s68 offen offset:0\n"
  "buffer_load_dwordx4 a[244:247], v0, s[28:31], s68 offen offset:1024\n"
  "s_mov_b32 s68, 79872\n"
  "buffer_load_dwordx4 a[248:251], v0, s[28:31], s68 offen offset:0\n"
  "buffer_load_dwordx4 a[252:255], v0, s[28:31], s68 offen offset:1024\n"
  "s_mov_b32 s68, 81920\n"
  "s_add_u32 m0, s69, 16384\n"
  "s_nop 0\n"
  "buffer_load_dwordx4 v0, s[28:31], s68 offen offset:0 lds\n"
  "buffer_load_dwordx4 v0, s[28:31], s68 offen offset:1024 lds\n"
  "s_mov_b32 s68, 83968\n"
  "buffer_load_dwordx4 v[128:131], v0, s[28:31], s68 offen offset:0\n"
  "buffer_load_dwordx4 v[132:135], v0, s[28:31], s68 offen offset:1024\n"
  "s_mov_b32 s68, 86016\n"
  "buffer_load_dwordx4 v[136:139], v0, s[28:31], s68 offen offset:0\n"
  "buffer_load_dwordx4 v[140:143], v0, s[28:31], s68 offen offset:1024\n"
  "s_mov_b32 s68, 88064\n"
  "s_waitcnt vmcnt(50)\n"
  "buffer_load_dwordx4 v[144:147], v0, s[28:31], s68 offen offset:0\n"
  "buffer_load_dwordx4 v[148:151], v0, s[28:31], s68 offen offset:1024\n"
  "s_mov_b32 s68, 90112\n"
  "buffer_load_dwordx4 v[152:155], v0, s[28:31], s68 offen offset:0\n"
  "buffer_load_dwordx4 v[156:159], v0, s[28:31], s68 offen offset:1024\n"
  "s_mov_b32 s68, 92160\n"
  "s_add_u32 m0, s69, 18432\n"
  "s_nop 0\n"
  "buffer_load_dwordx4 v0, s[28:31], s68 offen offset:0 lds\n"
  "buffer_load_dwordx4 v0, s[28:31], s68 offen offset:1024 lds\n"
  "s_mov_b32 s68, 94208\n"
  "buffer_load_dwordx4 v[160:163], v0, s[28:31], s68 offen offset:0\n"
  "buffer_load_dwordx4 v[164:167], v0, s[28:31], s68 offen offset:1024\n"
  "s_mov_b32 s68, 96256\n"
  "buffer_load_dwordx4 v[168:171], v0, s[28:31], s68 offen offset:0\n"
  "buffer_load_dwordx4 v[172:175], v0, s[28:31], s68 offen offset:1024\n"
  "s_mov_b32 s68, 98304\n"
  "s_waitcnt vmcnt(50)\n"
  "buffer_load_dwordx4 v[176:179], v0, s[28:31], s68 offen offset:0\n"
  "buffer_load_dwordx4 v[180:183], v0, s[28:31], s68 offen offset:1024\n"
  "s_mov_b32 s68, 100352\n"
  "buffer_load_dwordx4 v[184:187], v0, s[28:31], s68 offen offset:0\n"
  "buffer_load_dwordx4 v[188:191], v0, s[28:31], s68 offen offset:1024\n"
  "s_mov_b32 s68, 102400\n"
  "buffer_load_dwordx4 v[192:195], v0, s[28:31], s68 offen offset:0\n"
  "buffer_load_dwordx4 v[196:199], v0, s[28:31], s68 offen offset:1024\n"
  "s_mov_b32 s68, 104448\n"
  "buffer_load_dwordx4 v[200:203], v0, s[28:31], s68 offen offset:0\n"
  "buffer_load_dwordx4 v[204:207], v0, s[28:31], s68 offen offset:1024\n"
  "s_mov_b32 s68, 106496\n"
  "s_add_u32 m0, s69, 20480\n"
  "s_nop 0\n"
  "buffer_load_dwordx4 v0, s[28:31], s68 offen offset:0 lds\n"
  "buffer_load_dwordx4 v0, s[28:31], s68 offen offset:1024 lds\n"
  "s_mov_b32 s68, 108544\n"
  "s_waitcnt vmcnt(50)\n"
  "buffer_load_dwordx4 v[208:211], v0, s[28:31], s68 offen offset:0\n"
  "buffer_load_dwordx4 v[212:215], v0, s[28:31], s68 offen offset:1024\n"
  "s_mov_b32 s68, 110592\n"
  "buffer_load_dwordx4 v[216:219], v0, s[28:31], s68 offen offset:0\n"
  "buffer_load_dwordx4 v[220:223], v0, s[28:31], s68 offen offset:1024\n"
  "s_mov_b32 s68, 112640\n"
  "buffer_load_dwordx4 v[224:227], v0, s[28:31], s68 offen offset:0\n"
  "buffer_load_dwordx4 v[228:231], v0, s[28:31], s68 offen offset:1024\n"
  "s_mov_b32 s68, 114688\n"
  "buffer_load_dwordx4 v[232:235], v0, s[28:31], s68 offen offset:0\n"
  "buffer_load_dwordx4 v[236:239], v0, s[28:31], s68 offen offset:1024\n"
  "s_mov_b32 s68, 116736\n"
  "buffer_load_dwordx4 v[240:243], v0, s[28:31], s68 offen offset:0\n"
  "buffer_load_dwordx4 v[244:247], v0, s[28:31], s68 offen offset:1024\n"
  "s_mov_b32 s68, 118784\n"
  "s_waitcnt vmcnt(50)\n"
  "buffer_load_dwordx4 v[248:251], v0, s[28:31], s68 offen offset:0\n"
  "buffer_load_dwordx4 v[252:255], v0, s[28:31], s68 offen offset:1024\n"
  "s_mov_b32 s68, 120832\n"
  "s_add_u32 m0, s69, 22528\n"
  "s_nop 0\n"
  "buffer_load_dwordx4 v0, s[28:31], s68 offen offset:0 lds\n"
  "buffer_load_dwordx4 v0, s[28:31], s68 offen offset:1024 lds\n"
  "s_waitcnt vmcnt(0)\n"
  "s_barrier\n"
  "ds_read_b128 v[24:27], v4 offset:0\n"
  "ds_read_b128 v[28:31], v4 offset:10240\n"
  "ds_read_b128 v[32:35], v4 offset:20480\n"
  "ds_read_b128 v[36:39], v4 offset:30720\n"
  "ds_read_b128 v[80:83], v5 offset:0\n"
  "ds_read_b128 v[84:87], v5 offset:1024\n"
  "s_waitcnt lgkmcnt(0)\n"
  "H_LOOP_%=:\n"
  "s_add_u32 s60, s56, 0\n"
  "s_lshr_b32 s61, s60, 1\n"
  "s_add_u32 s61, s61, s46\n"
  "s_lshl_b32 s57, s61, 18\n"
  "s_lshr_b32 s61, s61, 3\n"
  "s_lshr_b32 s61, s47, s61\n"
  "s_xor_b32 s60, s60, s61\n"
  "s_and_b32 s60, s60, 1\n"
  "s_lshl_b32 s60, s60, 13\n"
  "s_add_u32 s57, s57, s60\n"
  "s_add_u32 s63, s57, s55\n"
  "v_mfma_f32_16x16x32_bf16 v[56:59], a[0:3], v[24:27], 0\n"
  "v_mfma_f32_16x16x32_bf16 v[104:107], a[0:3], v[28:31], 0\n"
  "ds_read_b128 v[88:91], v5 offset:4096\n"
  "ds_read_b128 v[92:95], v5 offset:5120\n"
  "v_mfma_f32_16x16x32_bf16 v[60:63], a[8:11], v[24:27], 0\n"
  "s_add_u32 s60, s56, 0\n"
  "s_mul_i32 s72, s60, 40960\n"
  "s_mul_i32 s73, s44, 10240\n"
  "s_add_u32 s73, s73, 4096\n"
  "s_add_u32 s72, s72, s73\n"
  "s_mov_b32 m0, s73\n"
  "s_nop 0\n"
  "buffer_load_dwordx4 v0, s[24:27], s72 offen offset:0 lds\n"
  "v_mfma_f32_16x16x32_bf16 v[108:111], a[8:11], v[28:31], 0\n"
  "ds_read_b128 v[96:99], v5 offset:6144\n"
  "ds_read_b128 v[100:103], v5 offset:7168\n"
  "v_mfma_f32_16x16x32_bf16 v[64:67], a[16:19], v[24:27], 0\n"
  "v_mfma_f32_16x16x32_bf16 v[112:115], a[16:19], v[28:31], 0\n"
  "ds_read_b128 v[40:43], v1 offset:0\n"
  "v_mfma_f32_16x16x32_bf16 v[68:71], a[24:27], v[24:27], 0\n"
  "v_mfma_f32_16x16x32_bf16 v[116:119], a[24:27], v[28:31], 0\n"
  "ds_read_b128 v[44:47], v1 offset:10240\n"
  "v_mfma_f32_16x16x32_bf16 v[72:75], a[32:35], v[24:27], 0\n"
  "v_mfma_f32_16x16x32_bf16 v[120:123], a[32:35], v[28:31], 0\n"
  "ds_read_b128 v[48:51], v1 offset:20480\n"
  "v_mfma_f32_16x16x32_bf16 v[76:79], v[80:83], v[24:27], 0\n"
  "buffer_load_dwordx4 v0, s[24:27], s72 offen offset:1024 lds\n"
  "v_mfma_f32_16x16x32_bf16 v[124:127], v[80:83], v[28:31], 0\n"
  "ds_read_b128 v[52:55], v1 offset:30720\n"
  "v_mfma_f32_16x16x32_bf16 v[56:59], a[4:7], v[32:35], v[56:59]\n"
  "v_mfma_f32_16x16x32_bf16 v[104:107], a[4:7], v[36:39], v[104:107]\n"
  "v_mfma_f32_16x16x32_bf16 v[60:63], a[12:15], v[32:35], v[60:63]\n"
  "v_mfma_f32_16x16x32_bf16 v[108:111], a[12:15], v[36:39], v[108:111]\n"
  "v_mfma_f32_16x16x32_bf16 v[64:67], a[20:23], v[32:35], v[64:67]\n"
  "v_mfma_f32_16x16x32_bf16 v[112:115], a[20:23], v[36:39], v[112:115]\n"
  "v_mfma_f32_16x16x32_bf16 v[68:71], a[28:31], v[32:35], v[68:71]\n"
  "buffer_load_dwordx4 v0, s[24:27], s72 offen offset:2048 lds\n"
  "v_mfma_f32_16x16x32_bf16 v[116:119], a[28:31], v[36:39], v[116:119]\n"
  "v_mfma_f32_16x16x32_bf16 v[72:75], a[36:39], v[32:35], v[72:75]\n"
  "v_mfma_f32_16x16x32_bf16 v[120:123], a[36:39], v[36:39], v[120:123]\n"
  "v_mfma_f32_16x16x32_bf16 v[76:79], v[84:87], v[32:35], v[76:79]\n"
  "v_mfma_f32_16x16x32_bf16 v[124:127], v[84:87], v[36:39], v[124:127]\n"
  "s_waitcnt lgkmcnt(3)\n"
  "v_mfma_f32_16x16x32_bf16 v[56:59], a[80:83], v[40:43], v[56:59]\n"
  "s_waitcnt lgkmcnt(2)\n"
  "v_mfma_f32_16x16x32_bf16 v[104:107], a[80:83], v[44:47], v[104:107]\n"
  "ds_read_b128 v[80:83], v5 offset:8192\n"
  "ds_read_b128 v[84:87], v5 offset:9216\n"
  "v_mfma_f32_16x16x32_bf16 v[60:63], a[88:91], v[40:43], v[60:63]\n"
  "buffer_load_dwordx4 v0, s[24:27], s72 offen offset:3072 lds\n"
  "v_mfma_f32_16x16x32_bf16 v[108:111], a[88:91], v[44:47], v[108:111]\n"
  "ds_read_b128 v[24:27], v2 offset:0\n"
  "v_mfma_f32_16x16x32_bf16 v[64:67], a[96:99], v[40:43], v[64:67]\n"
  "v_mfma_f32_16x16x32_bf16 v[112:115], a[96:99], v[44:47], v[112:115]\n"
  "ds_read_b128 v[28:31], v2 offset:10240\n"
  "v_mfma_f32_16x16x32_bf16 v[68:71], a[104:107], v[40:43], v[68:71]\n"
  "v_mfma_f32_16x16x32_bf16 v[116:119], a[104:107], v[44:47], v[116:119]\n"
  "ds_read_b128 v[32:35], v2 offset:20480\n"
  "v_mfma_f32_16x16x32_bf16 v[72:75], v[88:91], v[40:43], v[72:75]\n"
  "v_mfma_f32_16x16x32_bf16 v[120:123], v[88:91], v[44:47], v[120:123]\n"
  "ds_read_b128 v[36:39], v2 offset:30720\n"
  "v_mfma_f32_16x16x32_bf16 v[76:79], v[96:99], v[40:43], v[76:79]\n"
  "s_add_u32 s72, s72, 4096\n"
  "s_add_u32 s73, s73, 4096\n"
  "s_mov_b32 m0, s73\n"
  "s_nop 0\n"
  "buffer_load_dwordx4 v0, s[24:27], s72 offen offset:0 lds\n"
  "v_mfma_f32_16x16x32_bf16 v[124:127], v[96:99], v[44:47], v[124:127]\n"
  "s_waitcnt lgkmcnt(7)\n"
  "v_mfma_f32_16x16x32_bf16 v[56:59], a[84:87], v[48:51], v[56:59]\n"
  "s_waitcnt lgkmcnt(6)\n"
  "v_mfma_f32_16x16x32_bf16 v[104:107], a[84:87], v[52:55], v[104:107]\n"
  "v_mfma_f32_16x16x32_bf16 v[60:63], a[92:95], v[48:51], v[60:63]\n"
  "v_mfma_f32_16x16x32_bf16 v[108:111], a[92:95], v[52:55], v[108:111]\n"
  "v_mfma_f32_16x16x32_bf16 v[64:67], a[100:103], v[48:51], v[64:67]\n"
  "v_mfma_f32_16x16x32_bf16 v[112:115], a[100:103], v[52:55], v[112:115]\n"
  "v_mfma_f32_16x16x32_bf16 v[68:71], a[108:111], v[48:51], v[68:71]\n"
  "buffer_load_dwordx4 v0, s[24:27], s72 offen offset:1024 lds\n"
  "v_mfma_f32_16x16x32_bf16 v[116:119], a[108:111], v[52:55], v[116:119]\n"
  "v_mfma_f32_16x16x32_bf16 v[72:75], v[92:95], v[48:51], v[72:75]\n"
  "v_mfma_f32_16x16x32_bf16 v[120:123], v[92:95], v[52:55], v[120:123]\n"
  "v_mfma_f32_16x16x32_bf16 v[76:79], v[100:103], v[48:51], v[76:79]\n"
  "v_mfma_f32_16x16x32_bf16 v[124:127], v[100:103], v[52:55], v[124:127]\n"
  "s_waitcnt lgkmcnt(3)\n"
  "v_mfma_f32_16x16x32_bf16 v[56:59], a[112:115], v[24:27], v[56:59]\n"
  "s_waitcnt lgkmcnt(2)\n"
  "v_mfma_f32_16x16x32_bf16 v[104:107], a[112:115], v[28:31], v[104:107]\n"
  "ds_read_b128 v[88:91], v5 offset:10240\n"
  "ds_read_b128 v[92:95], v5 offset:11264\n"
  "v_mfma_f32_16x16x32_bf16 v[60:63], a[120:123], v[24:27], v[60:63]\n"
  "v_mfma_f32_16x16x32_bf16 v[108:111], a[120:123], v[28:31], v[108:111]\n"
  "ds_read_b128 v[96:99], v5 offset:12288\n"
  "ds_read_b128 v[100:103], v5 offset:13312\n"
  "v_mfma_f32_16x16x32_bf16 v[64:67], a[128:131], v[24:27], v[64:67]\n"
  "v_mfma_f32_16x16x32_bf16 v[112:115], a[128:131], v[28:31], v[112:115]\n"
  "ds_read_b128 v[40:43], v3 offset:0\n"
  "v_mfma_f32_16x16x32_bf16 v[68:71], a[136:139], v[24:27], v[68:71]\n"
  "v_mfma_f32_16x16x32_bf16 v[116:119], a[136:139], v[28:31], v[116:119]\n"
  "ds_read_b128 v[44:47], v3 offset:10240\n"
  "v_mfma_f32_16x16x32_bf16 v[72:75], a[144:147], v[24:27], v[72:75]\n"
  "v_mfma_f32_16x16x32_bf16 v[120:123], a[144:147], v[28:31], v[120:123]\n"
  "ds_read_b128 v[48:51], v3 offset:20480\n"
  "v_mfma_f32_16x16x32_bf16 v[76:79], v[80:83], v[24:27], v[76:79]\n"
  "v_mfma_f32_16x16x32_bf16 v[124:127], v[80:83], v[28:31], v[124:127]\n"
  "ds_read_b128 v[52:55], v3 offset:30720\n"
  "s_waitcnt lgkmcnt(9)\n"
  "v_mfma_f32_16x16x32_bf16 v[56:59], a[116:119], v[32:35], v[56:59]\n"
  "s_waitcnt lgkmcnt(8)\n"
  "v_mfma_f32_16x16x32_bf16 v[104:107], a[116:119], v[36:39], v[104:107]\n"
  "v_mfma_f32_16x16x32_bf16 v[60:63], a[124:127], v[32:35], v[60:63]\n"
  "v_mfma_f32_16x16x32_bf16 v[108:111], a[124:127], v[36:39], v[108:111]\n"
  "v_mfma_f32_16x16x32_bf16 v[64:67], a[132:135], v[32:35], v[64:67]\n"
  "v_mfma_f32_16x16x32_bf16 v[112:115], a[132:135], v[36:39], v[112:115]\n"
  "v_mfma_f32_16x16x32_bf16 v[68:71], a[140:143], v[32:35], v[68:71]\n"
  "v_mfma_f32_16x16x32_bf16 v[116:119], a[140:143], v[36:39], v[116:119]\n"
  "v_mfma_f32_16x16x32_bf16 v[72:75], a[148:151], v[32:35], v[72:75]\n"
  "v_mfma_f32_16x16x32_bf16 v[120:123], a[148:151], v[36:39], v[120:123]\n"
  "v_mfma_f32_16x16x32_bf16 v[76:79], v[84:87], v[32:35], v[76:79]\n"
  "v_mfma_f32_16x16x32_bf16 v[124:127], v[84:87], v[36:39], v[124:127]\n"
  "s_waitcnt lgkmcnt(3)\n"
  "v_mfma_f32_16x16x32_bf16 v[56:59], a[152:155], v[40:43], v[56:59]\n"
  "s_waitcnt lgkmcnt(2)\n"
  "v_mfma_f32_16x16x32_bf16 v[104:107], a[152:155], v[44:47], v[104:107]\n"
  "ds_read_b128 v[80:83], v5 offset:2048\n"
  "ds_read_b128 v[84:87], v5 offset:3072\n"
  "v_mfma_f32_16x16x32_bf16 v[60:63], a[160:163], v[40:43], v[60:63]\n"
  "v_mfma_f32_16x16x32_bf16 v[108:111], a[160:163], v[44:47], v[108:111]\n"
  "v_mfma_f32_16x16x32_bf16 v[64:67], a[168:171], v[40:43], v[64:67]\n"
  "v_mfma_f32_16x16x32_bf16 v[112:115], a[168:171], v[44:47], v[112:115]\n"
  "v_mfma_f32_16x16x32_bf16 v[68:71], a[176:179], v[40:43], v[68:71]\n"
  "v_mfma_f32_16x16x32_bf16 v[116:119], a[176:179], v[44:47], v[116:119]\n"
  "v_mfma_f32_16x16x32_bf16 v[72:75], v[88:91], v[40:43], v[72:75]\n"
  "v_mfma_f32_16x16x32_bf16 v[120:123], v[88:91], v[44:47], v[120:123]\n"
  "v_mfma_f32_16x16x32_bf16 v[76:79], v[96:99], v[40:43], v[76:79]\n"
  "v_mfma_f32_16x16x32_bf16 v[124:127], v[96:99], v[44:47], v[124:127]\n"
  "s_waitcnt lgkmcnt(3)\n"
  "v_mfma_f32_16x16x32_bf16 v[56:59], a[156:159], v[48:51], v[56:59]\n"
  "s_waitcnt lgkmcnt(2)\n"
  "v_mfma_f32_16x16x32_bf16 v[104:107], a[156:159], v[52:55], v[104:107]\n"
  "s_waitcnt vmcnt(0)\n"
  "s_waitcnt lgkmcnt(0)\n"
  "s_barrier\n"
  "v_mfma_f32_16x16x32_bf16 v[60:63], a[164:167], v[48:51], v[60:63]\n"
  "v_mfma_f32_16x16x32_bf16 v[108:111], a[164:167], v[52:55], v[108:111]\n"
  "ds_read_b128 v[24:27], v4 offset:4096\n"
  "v_mfma_f32_16x16x32_bf16 v[64:67], a[172:175], v[48:51], v[64:67]\n"
  "v_mfma_f32_16x16x32_bf16 v[112:115], a[172:175], v[52:55], v[112:115]\n"
  "ds_read_b128 v[28:31], v4 offset:14336\n"
  "v_mfma_f32_16x16x32_bf16 v[68:71], a[180:183], v[48:51], v[68:71]\n"
  "v_mfma_f32_16x16x32_bf16 v[116:119], a[180:183], v[52:55], v[116:119]\n"
  "ds_read_b128 v[32:35], v4 offset:24576\n"
  "v_mfma_f32_16x16x32_bf16 v[72:75], v[92:95], v[48:51], v[72:75]\n"
  "v_mfma_f32_16x16x32_bf16 v[120:123], v[92:95], v[52:55], v[120:123]\n"
  "ds_read_b128 v[36:39], v4 offset:34816\n"
  "v_mfma_f32_16x16x32_bf16 v[76:79], v[100:103], v[48:51], v[76:79]\n"
  "v_mfma_f32_16x16x32_bf16 v[124:127], v[100:103], v[52:55], v[124:127]\n"
  "s_waitcnt lgkmcnt(3)\n"
  "v_mfma_f32_16x16x32_bf16 v[56:59], a[40:43], v[24:27], v[56:59]\n"
  "s_waitcnt lgkmcnt(2)\n"
  "v_mfma_f32_16x16x32_bf16 v[104:107], a[40:43], v[28:31], v[104:107]\n"
  "ds_read_b128 v[88:91], v5 offset:14336\n"
  "ds_read_b128 v[92:95], v5 offset:15360\n"
  "v_mfma_f32_16x16x32_bf16 v[60:63], a[48:51], v[24:27], v[60:63]\n"
  "s_add_u32 s60, s56, 2\n"
  "s_mul_i32 s72, s60, 40960\n"
  "s_mul_i32 s73, s44, 10240\n"
  "s_add_u32 s72, s72, s73\n"
  "s_mov_b32 m0, s73\n"
  "s_nop 0\n"
  "buffer_load_dwordx4 v0, s[24:27], s72 offen offset:0 lds\n"
  "v_mfma_f32_16x16x32_bf16 v[108:111], a[48:51], v[28:31], v[108:111]\n"
  "ds_read_b128 v[40:43], v1 offset:4096\n"
  "v_mfma_f32_16x16x32_bf16 v[64:67], a[56:59], v[24:27], v[64:67]\n"
  "v_mfma_f32_16x16x32_bf16 v[112:115], a[56:59], v[28:31], v[112:115]\n"
  "ds_read_b128 v[44:47], v1 offset:14336\n"
  "v_mfma_f32_16x16x32_bf16 v[68:71], a[64:67], v[24:27], v[68:71]\n"
  "v_mfma_f32_16x16x32_bf16 v[116:119], a[64:67], v[28:31], v[116:119]\n"
  "ds_read_b128 v[48:51], v1 offset:24576\n"
  "v_mfma_f32_16x16x32_bf16 v[72:75], a[72:75], v[24:27], v[72:75]\n"
  "v_mfma_f32_16x16x32_bf16 v[120:123], a[72:75], v[28:31], v[120:123]\n"
  "ds_read_b128 v[52:55], v1 offset:34816\n"
  "v_mfma_f32_16x16x32_bf16 v[76:79], v[80:83], v[24:27], v[76:79]\n"
  "v_mfma_f32_16x16x32_bf16 v[124:127], v[80:83], v[28:31], v[124:127]\n"
  "s_waitcnt lgkmcnt(7)\n"
  "v_mfma_f32_16x16x32_bf16 v[56:59], a[44:47], v[32:35], v[56:59]\n"
  "s_waitcnt lgkmcnt(6)\n"
  "v_mfma_f32_16x16x32_bf16 v[104:107], a[44:47], v[36:39], v[104:107]\n"
  "v_mfma_f32_16x16x32_bf16 v[60:63], a[52:55], v[32:35], v[60:63]\n"
  "buffer_load_dwordx4 v0, s[24:27], s72 offen offset:1024 lds\n"
  "v_mfma_f32_16x16x32_bf16 v[108:111], a[52:55], v[36:39], v[108:111]\n"
  "v_mfma_f32_16x16x32_bf16 v[64:67], a[60:63], v[32:35], v[64:67]\n"
  "v_mfma_f32_16x16x32_bf16 v[112:115], a[60:63], v[36:39], v[112:115]\n"
  "v_mfma_f32_16x16x32_bf16 v[68:71], a[68:71], v[32:35], v[68:71]\n"
  "v_mfma_f32_16x16x32_bf16 v[116:119], a[68:71], v[36:39], v[116:119]\n"
  "v_mfma_f32_16x16x32_bf16 v[72:75], a[76:79], v[32:35], v[72:75]\n"
  "v_mfma_f32_16x16x32_bf16 v[120:123], a[76:79], v[36:39], v[120:123]\n"
  "v_mfma_f32_16x16x32_bf16 v[76:79], v[84:87], v[32:35], v[76:79]\n"
  "v_mfma_f32_16x16x32_bf16 v[124:127], v[84:87], v[36:39], v[124:127]\n"
  "s_waitcnt lgkmcnt(3)\n"
  "v_mfma_f32_16x16x32_bf16 v[56:59], a[184:187], v[40:43], v[56:59]\n"
  "s_waitcnt lgkmcnt(2)\n"
  "v_mfma_f32_16x16x32_bf16 v[104:107], a[184:187], v[44:47], v[104:107]\n"
  "ds_read_b128 v[96:99], v5 offset:16384\n"
  "ds_read_b128 v[100:103], v5 offset:17408\n"
  "v_mfma_f32_16x16x32_bf16 v[60:63], a[192:195], v[40:43], v[60:63]\n"
  "buffer_load_dwordx4 v0, s[24:27], s72 offen offset:2048 lds\n"
  "v_mfma_f32_16x16x32_bf16 v[108:111], a[192:195], v[44:47], v[108:111]\n"
  "ds_read_b128 v[24:27], v2 offset:4096\n"
  "v_mfma_f32_16x16x32_bf16 v[64:67], a[200:203], v[40:43], v[64:67]\n"
  "v_mfma_f32_16x16x32_bf16 v[112:115], a[200:203], v[44:47], v[112:115]\n"
  "ds_read_b128 v[28:31], v2 offset:14336\n"
  "v_mfma_f32_16x16x32_bf16 v[68:71], v[88:91], v[40:43], v[68:71]\n"
  "v_mfma_f32_16x16x32_bf16 v[116:119], v[88:91], v[44:47], v[116:119]\n"
  "ds_read_b128 v[32:35], v2 offset:24576\n"
  "v_mfma_f32_16x16x32_bf16 v[72:75], a[208:211], v[40:43], v[72:75]\n"
  "v_mfma_f32_16x16x32_bf16 v[120:123], a[208:211], v[44:47], v[120:123]\n"
  "ds_read_b128 v[36:39], v2 offset:34816\n"
  "v_mfma_f32_16x16x32_bf16 v[76:79], a[216:219], v[40:43], v[76:79]\n"
  "v_mfma_f32_16x16x32_bf16 v[124:127], a[216:219], v[44:47], v[124:127]\n"
  "s_waitcnt lgkmcnt(7)\n"
  "v_mfma_f32_16x16x32_bf16 v[56:59], a[188:191], v[48:51], v[56:59]\n"
  "s_waitcnt lgkmcnt(6)\n"
  "v_mfma_f32_16x16x32_bf16 v[104:107], a[188:191], v[52:55], v[104:107]\n"
  "v_mfma_f32_16x16x32_bf16 v[60:63], a[196:199], v[48:51], v[60:63]\n"
  "buffer_load_dwordx4 v0, s[24:27], s72 offen offset:3072 lds\n"
  "v_mfma_f32_16x16x32_bf16 v[108:111], a[196:199], v[52:55], v[108:111]\n"
  "v_mfma_f32_16x16x32_bf16 v[64:67], a[204:207], v[48:51], v[64:67]\n"
  "v_mfma_f32_16x16x32_bf16 v[112:115], a[204:207], v[52:55], v[112:115]\n"
  "v_mfma_f32_16x16x32_bf16 v[68:71], v[92:95], v[48:51], v[68:71]\n"
  "v_mfma_f32_16x16x32_bf16 v[116:119], v[92:95], v[52:55], v[116:119]\n"
  "v_mfma_f32_16x16x32_bf16 v[72:75], a[212:215], v[48:51], v[72:75]\n"
  "v_mfma_f32_16x16x32_bf16 v[120:123], a[212:215], v[52:55], v[120:123]\n"
  "v_mfma_f32_16x16x32_bf16 v[76:79], a[220:223], v[48:51], v[76:79]\n"
  "v_mfma_f32_16x16x32_bf16 v[124:127], a[220:223], v[52:55], v[124:127]\n"
  "s_waitcnt lgkmcnt(3)\n"
  "v_mfma_f32_16x16x32_bf16 v[56:59], a[224:227], v[24:27], v[56:59]\n"
  "s_waitcnt lgkmcnt(2)\n"
  "v_mfma_f32_16x16x32_bf16 v[104:107], a[224:227], v[28:31], v[104:107]\n"
  "ds_read_b128 v[80:83], v5 offset:18432\n"
  "ds_read_b128 v[84:87], v5 offset:19456\n"
  "v_mfma_f32_16x16x32_bf16 v[60:63], a[232:235], v[24:27], v[60:63]\n"
  "v_mfma_f32_16x16x32_bf16 v[108:111], a[232:235], v[28:31], v[108:111]\n"
  "ds_read_b128 v[40:43], v3 offset:4096\n"
  "v_mfma_f32_16x16x32_bf16 v[64:67], a[240:243], v[24:27], v[64:67]\n"
  "v_mfma_f32_16x16x32_bf16 v[112:115], a[240:243], v[28:31], v[112:115]\n"
  "ds_read_b128 v[44:47], v3 offset:14336\n"
  "v_mfma_f32_16x16x32_bf16 v[68:71], a[248:251], v[24:27], v[68:71]\n"
  "v_mfma_f32_16x16x32_bf16 v[116:119], a[248:251], v[28:31], v[116:119]\n"
  "ds_read_b128 v[48:51], v3 offset:24576\n"
  "v_mfma_f32_16x16x32_bf16 v[72:75], v[96:99], v[24:27], v[72:75]\n"
  "v_mfma_f32_16x16x32_bf16 v[120:123], v[96:99], v[28:31], v[120:123]\n"
  "ds_read_b128 v[52:55], v3 offset:34816\n"
  "v_mfma_f32_16x16x32_bf16 v[76:79], v[128:131], v[24:27], v[76:79]\n"
  "v_mfma_f32_16x16x32_bf16 v[124:127], v[128:131], v[28:31], v[124:127]\n"
  "s_waitcnt lgkmcnt(7)\n"
  "v_mfma_f32_16x16x32_bf16 v[56:59], a[228:231], v[32:35], v[56:59]\n"
  "s_waitcnt lgkmcnt(6)\n"
  "v_mfma_f32_16x16x32_bf16 v[104:107], a[228:231], v[36:39], v[104:107]\n"
  "v_mfma_f32_16x16x32_bf16 v[60:63], a[236:239], v[32:35], v[60:63]\n"
  "v_mfma_f32_16x16x32_bf16 v[108:111], a[236:239], v[36:39], v[108:111]\n"
  "v_mfma_f32_16x16x32_bf16 v[64:67], a[244:247], v[32:35], v[64:67]\n"
  "v_mfma_f32_16x16x32_bf16 v[112:115], a[244:247], v[36:39], v[112:115]\n"
  "v_mfma_f32_16x16x32_bf16 v[68:71], a[252:255], v[32:35], v[68:71]\n"
  "v_mfma_f32_16x16x32_bf16 v[116:119], a[252:255], v[36:39], v[116:119]\n"
  "v_mfma_f32_16x16x32_bf16 v[72:75], v[100:103], v[32:35], v[72:75]\n"
  "v_mfma_f32_16x16x32_bf16 v[120:123], v[100:103], v[36:39], v[120:123]\n"
  "v_mfma_f32_16x16x32_bf16 v[76:79], v[132:135], v[32:35], v[76:79]\n"
  "v_mfma_f32_16x16x32_bf16 v[124:127], v[132:135], v[36:39], v[124:127]\n"
  "s_waitcnt lgkmcnt(3)\n"
  "v_mfma_f32_16x16x32_bf16 v[56:59], v[136:139], v[40:43], v[56:59]\n"
  "s_waitcnt lgkmcnt(2)\n"
  "v_mfma_f32_16x16x32_bf16 v[104:107], v[136:139], v[44:47], v[104:107]\n"
  "ds_read_b128 v[88:91], v5 offset:20480\n"
  "ds_read_b128 v[92:95], v5 offset:21504\n"
  "v_mfma_f32_16x16x32_bf16 v[60:63], v[144:147], v[40:43], v[60:63]\n"
  "v_mfma_f32_16x16x32_bf16 v[108:111], v[144:147], v[44:47], v[108:111]\n"
  "ds_read_b128 v[24:27], v0 offset:8192\n"
  "v_mfma_f32_16x16x32_bf16 v[64:67], v[152:155], v[40:43], v[64:67]\n"
  "v_mfma_f32_16x16x32_bf16 v[112:115], v[152:155], v[44:47], v[112:115]\n"
  "ds_read_b128 v[28:31], v0 offset:18432\n"
  "v_mfma_f32_16x16x32_bf16 v[68:71], v[80:83], v[40:43], v[68:71]\n"
  "v_mfma_f32_16x16x32_bf16 v[116:119], v[80:83], v[44:47], v[116:119]\n"
  "ds_read_b128 v[32:35], v0 offset:28672\n"
  "v_mfma_f32_16x16x32_bf16 v[72:75], v[160:163], v[40:43], v[72:75]\n"
  "v_mfma_f32_16x16x32_bf16 v[120:123], v[160:163], v[44:47], v[120:123]\n"
  "ds_read_b128 v[36:39], v0 offset:38912\n"
  "v_mfma_f32_16x16x32_bf16 v[76:79], v[168:171], v[40:43], v[76:79]\n"
  "v_mfma_f32_16x16x32_bf16 v[124:127], v[168:171], v[44:47], v[124:127]\n"
  "s_waitcnt lgkmcnt(7)\n"
  "v_mfma_f32_16x16x32_bf16 v[56:59], v[140:143], v[48:51], v[56:59]\n"
  "s_waitcnt lgkmcnt(6)\n"
  "v_mfma_f32_16x16x32_bf16 v[104:107], v[140:143], v[52:55], v[104:107]\n"
  "v_mfma_f32_16x16x32_bf16 v[60:63], v[148:151], v[48:51], v[60:63]\n"
  "v_mfma_f32_16x16x32_bf16 v[108:111], v[148:151], v[52:55], v[108:111]\n"
  "v_mfma_f32_16x16x32_bf16 v[64:67], v[156:159], v[48:51], v[64:67]\n"
  "v_mfma_f32_16x16x32_bf16 v[112:115], v[156:159], v[52:55], v[112:115]\n"
  "v_mfma_f32_16x16x32_bf16 v[68:71], v[84:87], v[48:51], v[68:71]\n"
  "v_mfma_f32_16x16x32_bf16 v[116:119], v[84:87], v[52:55], v[116:119]\n"
  "v_mfma_f32_16x16x32_bf16 v[72:75], v[164:167], v[48:51], v[72:75]\n"
  "v_mfma_f32_16x16x32_bf16 v[120:123], v[164:167], v[52:55], v[120:123]\n"
  "v_mfma_f32_16x16x32_bf16 v[76:79], v[172:175], v[48:51], v[76:79]\n"
  "v_mfma_f32_16x16x32_bf16 v[124:127], v[172:175], v[52:55], v[124:127]\n"
  "s_waitcnt lgkmcnt(3)\n"
  "v_mfma_f32_16x16x32_bf16 v[56:59], v[176:179], v[24:27], v[56:59]\n"
  "s_waitcnt lgkmcnt(2)\n"
  "v_mfma_f32_16x16x32_bf16 v[104:107], v[176:179], v[28:31], v[104:107]\n"
  "ds_read_b128 v[96:99], v5 offset:22528\n"
  "ds_read_b128 v[100:103], v5 offset:23552\n"
  "v_mfma_f32_16x16x32_bf16 v[60:63], v[184:187], v[24:27], v[60:63]\n"
  "v_mfma_f32_16x16x32_bf16 v[108:111], v[184:187], v[28:31], v[108:111]\n"
  "ds_read_b128 v[40:43], v0 offset:9216\n"
  "v_mfma_f32_16x16x32_bf16 v[64:67], v[192:195], v[24:27], v[64:67]\n"
  "v_mfma_f32_16x16x32_bf16 v[112:115], v[192:195], v[28:31], v[112:115]\n"
  "ds_read_b128 v[44:47], v0 offset:19456\n"
  "v_mfma_f32_16x16x32_bf16 v[68:71], v[200:203], v[24:27], v[68:71]\n"
  "v_mfma_f32_16x16x32_bf16 v[116:119], v[200:203], v[28:31], v[116:119]\n"
  "ds_read_b128 v[48:51], v0 offset:29696\n"
  "v_mfma_f32_16x16x32_bf16 v[72:75], v[88:91], v[24:27], v[72:75]\n"
  "v_mfma_f32_16x16x32_bf16 v[120:123], v[88:91], v[28:31], v[120:123]\n"
  "ds_read_b128 v[52:55], v0 offset:39936\n"
  "v_mfma_f32_16x16x32_bf16 v[76:79], v[208:211], v[24:27], v[76:79]\n"
  "v_mfma_f32_16x16x32_bf16 v[124:127], v[208:211], v[28:31], v[124:127]\n"
  "buffer_load_dword v19, v17, s[16:19], s57 offen sc1\n"
  "s_waitcnt lgkmcnt(7)\n"
  "v_mfma_f32_16x16x32_bf16 v[56:59], v[180:183], v[32:35], v[56:59]\n"
  "s_waitcnt lgkmcnt(6)\n"
  "v_mfma_f32_16x16x32_bf16 v[104:107], v[180:183], v[36:39], v[104:107]\n"
  "v_mfma_f32_16x16x32_bf16 v[60:63], v[188:191], v[32:35], v[60:63]\n"
  "v_mfma_f32_16x16x32_bf16 v[108:111], v[188:191], v[36:39], v[108:111]\n"
  "v_mfma_f32_16x16x32_bf16 v[64:67], v[196:199], v[32:35], v[64:67]\n"
  "v_mfma_f32_16x16x32_bf16 v[112:115], v[196:199], v[36:39], v[112:115]\n"
  "v_mfma_f32_16x16x32_bf16 v[68:71], v[204:207], v[32:35], v[68:71]\n"
  "v_mfma_f32_16x16x32_bf16 v[116:119], v[204:207], v[36:39], v[116:119]\n"
  "v_mfma_f32_16x16x32_bf16 v[72:75], v[92:95], v[32:35], v[72:75]\n"
  "v_mfma_f32_16x16x32_bf16 v[120:123], v[92:95], v[36:39], v[120:123]\n"
  "v_mfma_f32_16x16x32_bf16 v[76:79], v[212:215], v[32:35], v[76:79]\n"
  "v_mfma_f32_16x16x32_bf16 v[124:127], v[212:215], v[36:39], v[124:127]\n"
  "s_waitcnt lgkmcnt(3)\n"
  "v_mfma_f32_16x16x32_bf16 v[56:59], v[216:219], v[40:43], v[56:59]\n"
  "s_waitcnt lgkmcnt(2)\n"
  "v_mfma_f32_16x16x32_bf16 v[104:107], v[216:219], v[44:47], v[104:107]\n"
  "v_mfma_f32_16x16x32_bf16 v[60:63], v[224:227], v[40:43], v[60:63]\n"
  "v_mfma_f32_16x16x32_bf16 v[108:111], v[224:227], v[44:47], v[108:111]\n"
  "v_mfma_f32_16x16x32_bf16 v[64:67], v[232:235], v[40:43], v[64:67]\n"
  "v_mfma_f32_16x16x32_bf16 v[112:115], v[232:235], v[44:47], v[112:115]\n"
  "v_mfma_f32_16x16x32_bf16 v[68:71], v[240:243], v[40:43], v[68:71]\n"
  "v_mfma_f32_16x16x32_bf16 v[116:119], v[240:243], v[44:47], v[116:119]\n"
  "v_mfma_f32_16x16x32_bf16 v[72:75], v[248:251], v[40:43], v[72:75]\n"
  "v_mfma_f32_16x16x32_bf16 v[120:123], v[248:251], v[44:47], v[120:123]\n"
  "v_mfma_f32_16x16x32_bf16 v[76:79], v[96:99], v[40:43], v[76:79]\n"
  "v_mfma_f32_16x16x32_bf16 v[124:127], v[96:99], v[44:47], v[124:127]\n"
  "s_waitcnt lgkmcnt(1)\n"
  "v_mfma_f32_16x16x32_bf16 v[56:59], v[220:223], v[48:51], v[56:59]\n"
  "s_waitcnt lgkmcnt(0)\n"
  "v_mfma_f32_16x16x32_bf16 v[104:107], v[220:223], v[52:55], v[104:107]\n"
  "v_mfma_f32_16x16x32_bf16 v[60:63], v[228:231], v[48:51], v[60:63]\n"
  "v_mfma_f32_16x16x32_bf16 v[108:111], v[228:231], v[52:55], v[108:111]\n"
  "v_mfma_f32_16x16x32_bf16 v[64:67], v[236:239], v[48:51], v[64:67]\n"
  "v_mfma_f32_16x16x32_bf16 v[112:115], v[236:239], v[52:55], v[112:115]\n"
  "v_mfma_f32_16x16x32_bf16 v[68:71], v[244:247], v[48:51], v[68:71]\n"
  "v_mfma_f32_16x16x32_bf16 v[116:119], v[244:247], v[52:55], v[116:119]\n"
  "v_mfma_f32_16x16x32_bf16 v[72:75], v[252:255], v[48:51], v[72:75]\n"
  "v_mfma_f32_16x16x32_bf16 v[120:123], v[252:255], v[52:55], v[120:123]\n"
  "v_mfma_f32_16x16x32_bf16 v[76:79], v[100:103], v[48:51], v[76:79]\n"
  "v_mfma_f32_16x16x32_bf16 v[124:127], v[100:103], v[52:55], v[124:127]\n"
  "s_mov_b32 s62, 0\n"
  "s_waitcnt vmcnt(0)\n"
  "v_readfirstlane_b32 s59, v19\n"
  "s_cmp_lg_u32 s59, -1\n"
  "s_cbranch_scc1 H_SCHOK_%=\n"
  "H_SCHP_%=:\n"
  "s_nop 7\n"
  "buffer_load_dword v19, v17, s[16:19], s57 offen sc1\n"
  "s_waitcnt vmcnt(0)\n"
  "v_readfirstlane_b32 s59, v19\n"
  "s_cmp_lg_u32 s59, -1\n"
  "s_cbranch_scc1 H_SCHOK_%=\n"
  "s_sleep 2\n"
  "s_add_u32 s62, s62, 1\n"
  "s_cmp_gt_u32 s62, 300000\n"
  "s_cbranch_scc1 H_FAIL_%=\n"
  "s_and_b32 s60, s62, 255\n"
  "s_cmp_lg_u32 s60, 0\n"
  "s_cbranch_scc1 H_SCHP_%=\n"
  "v_mov_b32 v19, 0\n"
  "buffer_load_dword v19, v19, s[40:43], 0 offen sc1\n"
  "s_waitcnt vmcnt(0)\n"
  "v_readfirstlane_b32 s60, v19\n"
  "s_cmp_lg_u32 s60, 0\n"
  "s_cbranch_scc1 H_EXIT_%=\n"
  "s_branch H_SCHP_%=\n"
  "H_SCHOK_%=:\n"
  "s_nop 7\n"
  "s_memrealtime s[64:65]\n"
  "s_waitcnt lgkmcnt(0)\n"
  "s_sub_i32 s60, s59, s64\n"
  "s_cmp_gt_i32 s60, 2000\n"
  "s_cselect_b32 s59, s64, s59\n"
  "s_cmp_lt_i32 s60, -100000\n"
  "s_cselect_b32 s59, s64, s59\n"
  "v_cvt_pk_bf16_f32 v40, v104, v105\n"
  "v_cvt_pk_bf16_f32 v41, v106, v107\n"
  "v_cvt_pk_bf16_f32 v42, v108, v109\n"
  "v_cvt_pk_bf16_f32 v43, v110, v111\n"
  "s_mov_b32 s62, 0\n"
  "H_WT_%=:\n"
  "s_memrealtime s[64:65]\n"
  "s_waitcnt lgkmcnt(0)\n"
  "s_sub_i32 s60, s64, s59\n"
  "s_cmp_lt_i32 s60, 0\n"
  "s_cbranch_scc1 H_WT_%=\n"
  "buffer_load_dwordx4 v[104:107], v6, s[16:19], s57 offen sc1\n"
  "v_cvt_pk_bf16_f32 v46, v112, v113\n"
  "v_cvt_pk_bf16_f32 v47, v114, v115\n"
  "v_cvt_pk_bf16_f32 v28, v116, v117\n"
  "v_cvt_pk_bf16_f32 v29, v118, v119\n"
  "v_cvt_pk_bf16_f32 v30, v120, v121\n"
  "v_cvt_pk_bf16_f32 v31, v122, v123\n"
  "v_cvt_pk_bf16_f32 v34, v124, v125\n"
  "v_cvt_pk_bf16_f32 v35, v126, v127\n"
  "v_cvt_pk_bf16_f32 v36, v56, v57\n"
  "v_cvt_pk_bf16_f32 v37, v58, v59\n"
  "buffer_load_dwordx4 v[108:111], v6, s[16:19], s57 offen offset:1024 sc1\n"
  "v_cvt_pk_bf16_f32 v38, v60, v61\n"
  "v_cvt_pk_bf16_f32 v39, v62, v63\n"
  "v_cvt_pk_bf16_f32 v44, v64, v65\n"
  "v_cvt_pk_bf16_f32 v45, v66, v67\n"
  "v_cvt_pk_bf16_f32 v24, v68, v69\n"
  "v_cvt_pk_bf16_f32 v25, v70, v71\n"
  "v_cvt_pk_bf16_f32 v26, v72, v73\n"
  "v_cvt_pk_bf16_f32 v27, v74, v75\n"
  "v_cvt_pk_bf16_f32 v32, v76, v77\n"
  "v_cvt_pk_bf16_f32 v33, v78, v79\n"
  "buffer_load_dwordx2 v[54:55], v7, s[16:19], s57 offen sc1\n"
  "ds_read_b128 v[80:83], v5 offset:0\n"
  "ds_read_b128 v[84:87], v5 offset:1024\n"
  "ds_read_b128 v[88:91], v5 offset:2048\n"
  "ds_read_b128 v[92:95], v5 offset:3072\n"
  "ds_read_b128 v[96:99], v5 offset:4096\n"
  "ds_read_b128 v[100:103], v5 offset:5120\n"
  "v_mov_b32 v56, 0\n"
  "v_mov_b32 v57, 0\n"
  "v_mov_b32 v58, 0\n"
  "v_mov_b32 v59, 0\n"
  "v_mov_b32 v60, 0\n"
  "v_mov_b32 v61, 0\n"
  "v_mov_b32 v62, 0\n"
  "v_mov_b32 v63, 0\n"
  "v_mov_b32 v64, 0\n"
  "v_mov_b32 v65, 0\n"
  "v_mov_b32 v66, 0\n"
  "v_mov_b32 v67, 0\n"
  "v_mov_b32 v68, 0\n"
  "v_mov_b32 v69, 0\n"
  "v_mov_b32 v70, 0\n"
  "v_mov_b32 v71, 0\n"
  "v_mov_b32 v72, 0\n"
  "v_mov_b32 v73, 0\n"
  "v_mov_b32 v74, 0\n"
  "v_mov_b32 v75, 0\n"
  "v_mov_b32 v76, 0\n"
  "v_mov_b32 v77, 0\n"
  "v_mov_b32 v78, 0\n"
  "v_mov_b32 v79, 0\n"
  "s_waitcnt lgkmcnt(0)\n"
  "H_D0_%=:\n"
  "s_waitcnt vmcnt(2)\n"
  "v_smfmac_f32_16x16x64_bf16 v[56:59], v[104:107], a[0:7], v9\n"
  "ds_write_b128 v4, v[104:107] offset:40960\n"
  "v_smfmac_f32_16x16x64_bf16 v[60:63], v[104:107], a[8:15], v9\n"
  "v_max3_u32 v19, v104, v105, v106\n"
  "v_max_u32 v19, v107, v19\n"
  "v_smfmac_f32_16x16x64_bf16 v[64:67], v[104:107], a[16:23], v9\n"
  "v_cmp_eq_u32 vcc, -1, v19\n"
  "v_smfmac_f32_16x16x64_bf16 v[68:71], v[104:107], a[24:31], v9\n"
  "s_cbranch_vccnz H_FIX0_%=\n"
  "v_smfmac_f32_16x16x64_bf16 v[72:75], v[104:107], a[32:39], v9\n"
  "s_waitcnt lgkmcnt(0)\n"
  "s_barrier\n"
  "ds_read_b128 v[112:115], v1 offset:40960\n"
  "s_waitcnt vmcnt(1)\n"
  "v_max3_u32 v19, v108, v109, v110\n"
  "v_max_u32 v19, v111, v19\n"
  "v_smfmac_f32_16x16x64_bf16 v[76:79], v[104:107], v[80:87], v9\n"
  "v_cmp_eq_u32 vcc, -1, v19\n"
  "s_cbranch_vccnz H_FIX1_%=\n"
  "H_D1_%=:\n"
  "v_smfmac_f32_16x16x64_bf16 v[56:59], v[108:111], a[40:47], v9\n"
  "ds_read_b128 v[116:119], v2 offset:40960\n"
  "v_smfmac_f32_16x16x64_bf16 v[60:63], v[108:111], a[48:55], v9\n"
  "ds_read_b128 v[80:83], v5 offset:6144\n"
  "ds_read_b128 v[84:87], v5 offset:7168\n"
  "v_smfmac_f32_16x16x64_bf16 v[64:67], v[108:111], a[56:63], v9\n"
  "ds_write_b128 v4, v[108:111] offset:45056\n"
  "v_smfmac_f32_16x16x64_bf16 v[68:71], v[108:111], a[64:71], v9\n"
  "ds_read_b128 v[120:123], v3 offset:40960\n"
  "v_smfmac_f32_16x16x64_bf16 v[72:75], v[108:111], a[72:79], v9\n"
  "s_waitcnt vmcnt(0)\n"
  "v_max_u32 v19, v54, v55\n"
  "v_smfmac_f32_16x16x64_bf16 v[76:79], v[108:111], v[88:95], v9\n"
  "v_cmp_eq_u32 vcc, -1, v19\n"
  "s_waitcnt lgkmcnt(5)\n"
  "v_smfmac_f32_16x16x64_bf16 v[56:59], v[112:115], a[80:87], v9\n"
  "s_cbranch_vccnz H_FIX2_%=\n"
  "H_D2_%=:\n"
  "v_smfmac_f32_16x16x64_bf16 v[60:63], v[112:115], a[88:95], v9\n"
  "ds_write_b64 v8, v[54:55]\n"
  "v_smfmac_f32_16x16x64_bf16 v[64:67], v[112:115], a[96:103], v9\n"
  "ds_read_b128 v[88:91], v5 offset:8192\n"
  "ds_read_b128 v[92:95], v5 offset:9216\n"
  "v_smfmac_f32_16x16x64_bf16 v[68:71], v[112:115], a[104:111], v9\n"
  "v_smfmac_f32_16x16x64_bf16 v[72:75], v[112:115], v[96:103], v9\n"
  "s_waitcnt lgkmcnt(5)\n"
  "v_smfmac_f32_16x16x64_bf16 v[76:79], v[112:115], v[80:87], v9\n"
  "s_waitcnt lgkmcnt(2)\n"
  "s_barrier\n"
  "v_smfmac_f32_16x16x64_bf16 v[56:59], v[116:119], a[112:119], v9\n"
  "ds_read_b128 v[96:99], v5 offset:10240\n"
  "v_smfmac_f32_16x16x64_bf16 v[60:63], v[116:119], a[120:127], v9\n"
  "ds_read_b128 v[100:103], v5 offset:11264\n"
  "v_smfmac_f32_16x16x64_bf16 v[64:67], v[116:119], a[128:135], v9\n"
  "ds_read_b128 v[80:83], v5 offset:12288\n"
  "v_smfmac_f32_16x16x64_bf16 v[68:71], v[116:119], a[136:143], v9\n"
  "ds_read_b128 v[84:87], v5 offset:13312\n"
  "v_smfmac_f32_16x16x64_bf16 v[72:75], v[116:119], a[144:151], v9\n"
  "ds_read_b128 v[124:127], v1 offset:45056\n"
  "s_waitcnt lgkmcnt(5)\n"
  "v_smfmac_f32_16x16x64_bf16 v[76:79], v[116:119], v[88:95], v9\n"
  "ds_read_b128 v[104:107], v2 offset:45056\n"
  "v_smfmac_f32_16x16x64_bf16 v[56:59], v[120:123], a[152:159], v9\n"
  "ds_read_b128 v[88:91], v5 offset:14336\n"
  "v_smfmac_f32_16x16x64_bf16 v[60:63], v[120:123], a[160:167], v9\n"
  "ds_read_b128 v[92:95], v5 offset:15360\n"
  "v_smfmac_f32_16x16x64_bf16 v[64:67], v[120:123], a[168:175], v9\n"
  "ds_read_b128 v[108:111], v3 offset:45056\n"
  "v_smfmac_f32_16x16x64_bf16 v[68:71], v[120:123], a[176:183], v9\n"
  "ds_read_b128 v[112:115], v0 offset:49152\n"
  "s_waitcnt lgkmcnt(8)\n"
  "v_smfmac_f32_16x16x64_bf16 v[72:75], v[120:123], v[96:103], v9\n"
  "ds_read_b128 v[116:119], v0 offset:50176\n"
  "s_waitcnt lgkmcnt(7)\n"
  "v_smfmac_f32_16x16x64_bf16 v[76:79], v[120:123], v[80:87], v9\n"
  "s_waitcnt lgkmcnt(6)\n"
  "v_smfmac_f32_16x16x64_bf16 v[56:59], v[124:127], a[184:191], v9\n"
  "ds_read_b128 v[96:99], v5 offset:18432\n"
  "s_waitcnt lgkmcnt(4)\n"
  "v_smfmac_f32_16x16x64_bf16 v[68:71], v[124:127], v[88:95], v9\n"
  "ds_read_b128 v[100:103], v5 offset:19456\n"
  "v_smfmac_f32_16x16x64_bf16 v[56:59], v[104:107], a[224:231], v9\n"
  "ds_read_b128 v[80:83], v5 offset:16384\n"
  "v_smfmac_f32_16x16x64_bf16 v[68:71], v[104:107], a[248:255], v9\n"
  "ds_read_b128 v[84:87], v5 offset:17408\n"
  "s_waitcnt lgkmcnt(6)\n"
  "v_smfmac_f32_16x16x64_bf16 v[56:59], v[108:111], v[136:143], v9\n"
  "ds_read_b128 v[88:91], v5 offset:20480\n"
  "s_waitcnt lgkmcnt(3)\n"
  "v_smfmac_f32_16x16x64_bf16 v[68:71], v[108:111], v[96:103], v9\n"
  "ds_read_b128 v[92:95], v5 offset:21504\n"
  "v_smfmac_f32_16x16x64_bf16 v[56:59], v[112:115], v[176:183], v9\n"
  "ds_read_b128 v[96:99], v5 offset:22528\n"
  "v_smfmac_f32_16x16x64_bf16 v[68:71], v[112:115], v[200:207], v9\n"
  "ds_read_b128 v[100:103], v5 offset:23552\n"
  "v_smfmac_f32_16x16x64_bf16 v[56:59], v[116:119], v[216:223], v9\n"
  "v_smfmac_f32_16x16x64_bf16 v[68:71], v[116:119], v[240:247], v9\n"
  "v_smfmac_f32_16x16x64_bf16 v[60:63], v[124:127], a[192:199], v9\n"
  "s_nop 1\n"
  "v_smfmac_f32_16x16x64_bf16 v[72:75], v[124:127], a[208:215], v9\n"
  "s_nop 1\n"
  "v_smfmac_f32_16x16x64_bf16 v[60:63], v[104:107], a[232:239], v9\n"
  "s_nop 1\n"
  "s_waitcnt lgkmcnt(4)\n"
  "v_smfmac_f32_16x16x64_bf16 v[72:75], v[104:107], v[80:87], v9\n"
  "v_cvt_pk_bf16_f32 v56, v56, v57\n"
  "v_cvt_pk_bf16_f32 v57, v58, v59\n"
  "v_smfmac_f32_16x16x64_bf16 v[60:63], v[108:111], v[144:151], v9\n"
  "v_cvt_pk_bf16_f32 v58, v68, v69\n"
  "v_cvt_pk_bf16_f32 v59, v70, v71\n"
  "v_smfmac_f32_16x16x64_bf16 v[72:75], v[108:111], v[160:167], v9\n"
  "s_nop 1\n"
  "v_mfma_f32_16x16x32_bf16 v[68:71], v[56:59], v[20:23], 0\n"
  "v_smfmac_f32_16x16x64_bf16 v[60:63], v[112:115], v[184:191], v9\n"
  "s_nop 1\n"
  "s_waitcnt lgkmcnt(2)\n"
  "v_smfmac_f32_16x16x64_bf16 v[72:75], v[112:115], v[88:95], v9\n"
  "s_nop 1\n"
  "v_smfmac_f32_16x16x64_bf16 v[60:63], v[116:119], v[224:231], v9\n"
  "s_nop 1\n"
  "v_smfmac_f32_16x16x64_bf16 v[72:75], v[116:119], v[248:255], v9\n"
  "v_cvt_pk_bf16_f32 v48, v68, v69\n"
  "v_cvt_pk_bf16_f32 v49, v70, v71\n"
  "v_smfmac_f32_16x16x64_bf16 v[64:67], v[124:127], a[200:207], v9\n"
  "s_nop 1\n"
  "v_smfmac_f32_16x16x64_bf16 v[76:79], v[124:127], a[216:223], v9\n"
  "s_nop 1\n"
  "v_smfmac_f32_16x16x64_bf16 v[64:67], v[104:107], a[240:247], v9\n"
  "v_cvt_pk_bf16_f32 v60, v60, v61\n"
  "v_cvt_pk_bf16_f32 v61, v62, v63\n"
  "v_smfmac_f32_16x16x64_bf16 v[76:79], v[104:107], v[128:135], v9\n"
  "v_cvt_pk_bf16_f32 v62, v72, v73\n"
  "v_cvt_pk_bf16_f32 v63, v74, v75\n"
  "v_smfmac_f32_16x16x64_bf16 v[64:67], v[108:111], v[152:159], v9\n"
  "s_nop 1\n"
  "v_mfma_f32_16x16x32_bf16 v[72:75], v[60:63], v[20:23], 0\n"
  "v_smfmac_f32_16x16x64_bf16 v[76:79], v[108:111], v[168:175], v9\n"
  "s_nop 1\n"
  "v_smfmac_f32_16x16x64_bf16 v[64:67], v[112:115], v[192:199], v9\n"
  "s_nop 1\n"
  "v_smfmac_f32_16x16x64_bf16 v[76:79], v[112:115], v[208:215], v9\n"
  "s_nop 1\n"
  "v_smfmac_f32_16x16x64_bf16 v[64:67], v[116:119], v[232:239], v9\n"
  "v_cvt_pk_bf16_f32 v50, v72, v73\n"
  "v_cvt_pk_bf16_f32 v51, v74, v75\n"
  "s_waitcnt lgkmcnt(0)\n"
  "v_smfmac_f32_16x16x64_bf16 v[76:79], v[116:119], v[96:103], v9\n"
  "v_mfma_f32_16x16x32_bf16 v[120:123], v[24:27], v[48:51], 0\n"
  "s_nop 1\n"
  "v_mfma_f32_16x16x32_bf16 v[56:59], v[28:31], v[48:51], 0\n"
  "s_nop 1\n"
  "v_cvt_pk_bf16_f32 v64, v64, v65\n"
  "v_cvt_pk_bf16_f32 v65, v66, v67\n"
  "v_cvt_pk_bf16_f32 v66, v76, v77\n"
  "v_cvt_pk_bf16_f32 v67, v78, v79\n"
  "v_mfma_f32_16x16x32_bf16 v[68:71], v[48:51], v[48:51], 0\n"
  "s_nop 0\n"
  "v_mfma_f32_16x16x32_bf16 v[76:79], v[64:67], v[20:23], 0\n"
  "s_nop 7\n"
  "v_cvt_pk_bf16_f32 v52, v76, v77\n"
  "v_cvt_pk_bf16_f32 v53, v78, v79\n"
  "s_nop 1\n"
  "v_mfma_f32_16x16x16_bf16 v[120:123], v[32:33], v[52:53], v[120:123]\n"
  "s_nop 1\n"
  "v_mfma_f32_16x16x16_bf16 v[56:59], v[34:35], v[52:53], v[56:59]\n"
  "s_nop 1\n"
  "v_mfma_f32_16x16x16_bf16 v[68:71], v[52:53], v[52:53], v[68:71]\n"
  "s_nop 1\n"
  "ds_write_b128 v10, v[120:123]\n"
  "s_nop 1\n"
  "ds_write_b128 v11, v[56:59]\n"
  "s_nop 1\n"
  "ds_write_b128 v12, v[68:71]\n"
  "s_waitcnt lgkmcnt(0)\n"
  "s_barrier\n"
  "ds_read_b128 v[60:63], v13\n"
  "ds_read_b128 v[64:67], v13 offset:1024\n"
  "ds_read2st64_b32 v[96:97], v14 offset0:0 offset1:1\n"
  "ds_read2st64_b32 v[98:99], v14 offset0:2 offset1:3\n"
  "ds_read_b128 v[72:75], v13 offset:2048\n"
  "ds_read_b128 v[76:79], v13 offset:3072\n"
  "s_waitcnt lgkmcnt(4)\n"
  "v_pk_add_f32 v[60:61], v[60:61], v[64:65]\n"
  "v_pk_add_f32 v[62:63], v[62:63], v[66:67]\n"
  "s_waitcnt lgkmcnt(2)\n"
  "v_pk_add_f32 v[96:97], v[96:97], v[98:99]\n"
  "v_add_f32 v96, v96, v97\n"
  "s_waitcnt lgkmcnt(1)\n"
  "v_pk_add_f32 v[60:61], v[60:61], v[72:73]\n"
  "v_pk_add_f32 v[62:63], v[62:63], v[74:75]\n"
  "s_waitcnt lgkmcnt(0)\n"
  "v_pk_add_f32 v[60:61], v[60:61], v[76:77]\n"
  "v_pk_add_f32 v[62:63], v[62:63], v[78:79]\n"
  "v_max3_f32 v83, v60, v61, v96\n"
  "v_max3_f32 v83, v62, v63, v83\n"
  "v_lshrrev_b32 v92, 4, v0\n"
  "s_lshl_b32 s68, s44, 12\n"
  "v_max_f32_dpp v83, v83, v83 quad_perm:[1,0,3,2] row_mask:0xf bank_mask:0xf\n"
  "v_and_b32 v93, 15, v92\n"
  "v_lshrrev_b32 v94, 4, v92\n"
  "v_max_f32_dpp v83, v83, v83 quad_perm:[2,3,0,1] row_mask:0xf bank_mask:0xf\n"
  "v_and_b32 v92, 7, v93\n"
  "v_lshlrev_b32 v95, 1, v94\n"
  "v_max_f32_dpp v83, v83, v83 row_half_mirror row_mask:0xf bank_mask:0xf\n"
  "v_mul_f32 v83, s76, v83\n"
  "v_fma_f32 v84, v60, s76, -v83\n"
  "v_fma_f32 v85, v61, s76, -v83\n"
  "v_fma_f32 v86, v62, s76, -v83\n"
  "v_fma_f32 v87, v63, s76, -v83\n"
  "v_fma_f32 v88, v96, s76, -v83\n"
  "v_exp_f32 v84, v84\n"
  "v_exp_f32 v85, v85\n"
  "v_exp_f32 v86, v86\n"
  "v_exp_f32 v87, v87\n"
  "v_exp_f32 v88, v88\n"
  "v_add_f32 v80, v84, v85\n"
  "v_add_f32 v81, v86, v87\n"
  "v_add_f32 v80, v80, v88\n"
  "v_add_f32 v83, v80, v81\n"
  "v_xor_b32 v92, v92, v95\n"
  "v_lshlrev_b32 v94, 4, v94\n"
  "v_add_f32_dpp v83, v83, v83 quad_perm:[1,0,3,2] row_mask:0xf bank_mask:0xf\n"
  "v_lshlrev_b32 v92, 4, v92\n"
  "s_lshl_b32 s69, s44, 10\n"
  "v_add_f32_dpp v83, v83, v83 quad_perm:[2,3,0,1] row_mask:0xf bank_mask:0xf\n"
  "v_lshl_add_u32 v92, v93, 7, v92\n"
  "v_lshl_add_u32 v93, v93, 5, v94\n"
  "v_add_f32_dpp v83, v83, v83 row_half_mirror row_mask:0xf bank_mask:0xf\n"
  "v_rcp_f32 v83, v83\n"
  "v_add_u32 v92, s68, v92\n"
  "v_mul_f32 v88, v88, v83\n"
  "v_mul_f32 v84, v84, v83\n"
  "v_mul_f32 v85, v85, v83\n"
  "v_mul_f32 v86, v86, v83\n"
  "v_mul_f32 v87, v87, v83\n"
  "v_mov_b32_dpp v89, v88 quad_perm:[1,0,3,2] row_mask:0xf bank_mask:0xf\n"
  "v_cvt_pk_bf16_f32 v80, v84, v85\n"
  "v_cvt_pk_bf16_f32 v81, v86, v87\n"
  "v_min_u32 v80, s78, v80\n"
  "v_min_u32 v81, s78, v81\n"
  "v_cvt_pk_bf16_f32 v82, v88, v89\n"
  "buffer_store_dwordx2 v[80:81], v15, s[16:19], s63 offen sc1\n"
  "v_min_u32 v82, s78, v82\n"
  "buffer_store_dword v82, v16, s[16:19], s63 offen sc1\n"
  "v_mfma_f32_16x16x32_bf16 v[104:107], v[24:27], v[36:39], 0\n"
  "v_xor_b32 v95, 16, v92\n"
  "v_add_u32 v93, s69, v93\n"
  "v_mfma_f32_16x16x32_bf16 v[108:111], v[24:27], v[40:43], 0\n"
  "s_lshl_b32 s70, s44, 8\n"
  "s_sub_u32 s69, s69, 16384\n"
  "v_mfma_f32_16x16x32_bf16 v[112:115], v[28:31], v[40:43], 0\n"
  "s_sub_u32 s70, s70, 4096\n"
  "v_add_u32 v100, s69, v13\n"
  "v_mfma_f32_16x16x32_bf16 v[116:119], v[48:51], v[36:39], 0\n"
  "v_add_u32 v101, s70, v14\n"
  "s_mul_i32 s71, s56, 81920\n"
  "v_mfma_f32_16x16x32_bf16 v[120:123], v[48:51], v[40:43], 0\n"
  "s_add_u32 s68, s71, s49\n"
  "s_add_u32 s71, s71, s53\n"
  "s_barrier\n"
  "v_mfma_f32_16x16x16_bf16 v[104:107], v[32:33], v[44:45], v[104:107]\n"
  "v_and_b32 v94, s48, v16\n"
  "v_or_b32 v94, v94, v15\n"
  "v_mfma_f32_16x16x16_bf16 v[108:111], v[32:33], v[46:47], v[108:111]\n"
  "s_nop 1\n"
  "v_mfma_f32_16x16x16_bf16 v[112:115], v[34:35], v[46:47], v[112:115]\n"
  "s_nop 1\n"
  "v_mfma_f32_16x16x16_bf16 v[116:119], v[52:53], v[44:45], v[116:119]\n"
  "s_nop 1\n"
  "v_mfma_f32_16x16x16_bf16 v[120:123], v[52:53], v[46:47], v[120:123]\n"
  "s_nop 1\n"
  "ds_write_b128 v92, v[104:107] offset:40960\n"
  "ds_write_b128 v92, v[108:111] offset:43008\n"
  "ds_write_b128 v95, v[112:115] offset:43008\n"
  "s_mov_b32 exec_lo, 0\n"
  "ds_write_b128 v93, v[116:119] offset:57312\n"
  "s_nop 0\n"
  "ds_write_b128 v93, v[120:123] offset:57824\n"
  "s_mov_b32 exec_lo, -1\n"
  "s_waitcnt lgkmcnt(0)\n"
  "s_barrier\n"
  "ds_read_b128 v[60:63], v100\n"
  "ds_read_b128 v[64:67], v100 offset:4096\n"
  "ds_read2st64_b32 v[96:97], v101 offset0:0 offset1:4\n"
  "ds_read2st64_b32 v[98:99], v101 offset0:8 offset1:12\n"
  "ds_read_b128 v[72:75], v100 offset:8192\n"
  "ds_read_b128 v[76:79], v100 offset:12288\n"
  "ds_read_b128 v[24:27], v4 offset:0\n"
  "ds_read_b128 v[28:31], v4 offset:10240\n"
  "ds_read_b128 v[32:35], v4 offset:20480\n"
  "ds_read_b128 v[36:39], v4 offset:30720\n"
  "ds_read_b128 v[80:83], v5 offset:0\n"
  "ds_read_b128 v[84:87], v5 offset:1024\n"
  "s_waitcnt lgkmcnt(10)\n"
  "v_pk_add_f32 v[60:61], v[60:61], v[64:65]\n"
  "v_pk_add_f32 v[62:63], v[62:63], v[66:67]\n"
  "s_waitcnt lgkmcnt(8)\n"
  "v_pk_add_f32 v[96:97], v[96:97], v[98:99]\n"
  "v_add_f32 v96, v96, v97\n"
  "s_waitcnt lgkmcnt(7)\n"
  "v_pk_add_f32 v[60:61], v[60:61], v[72:73]\n"
  "v_pk_add_f32 v[62:63], v[62:63], v[74:75]\n"
  "s_waitcnt lgkmcnt(6)\n"
  "v_pk_add_f32 v[60:61], v[60:61], v[76:77]\n"
  "v_pk_add_f32 v[62:63], v[62:63], v[78:79]\n"
  "v_mul_f32 v60, s76, v60\n"
  "v_mul_f32 v61, s76, v61\n"
  "v_mul_f32 v62, s76, v62\n"
  "v_mul_f32 v63, s76, v63\n"
  "v_mul_f32 v96, s76, v96\n"
  "v_mov_b32 v104, s77\n"
  "v_cndmask_b32 v60, v104, v60, s[8:9]\n"
  "v_cndmask_b32 v61, v104, v61, s[10:11]\n"
  "v_cndmask_b32 v62, v104, v62, s[12:13]\n"
  "v_cndmask_b32 v63, v104, v63, s[14:15]\n"
  "v_max3_f32 v107, v60, v61, v96\n"
  "v_max3_f32 v107, v62, v63, v107\n"
  "s_nop 1\n"
  "v_max_f32_dpp v107, v107, v107 quad_perm:[1,0,3,2] row_mask:0xf bank_mask:0xf\n"
  "s_nop 1\n"
  "v_max_f32_dpp v107, v107, v107 quad_perm:[2,3,0,1] row_mask:0xf bank_mask:0xf\n"
  "s_nop 1\n"
  "v_max_f32_dpp v107, v107, v107 row_half_mirror row_mask:0xf bank_mask:0xf\n"
  "v_sub_f32 v108, v60, v107\n"
  "v_sub_f32 v109, v61, v107\n"
  "v_sub_f32 v110, v62, v107\n"
  "v_sub_f32 v111, v63, v107\n"
  "v_sub_f32 v112, v96, v107\n"
  "v_exp_f32 v108, v108\n"
  "v_exp_f32 v109, v109\n"
  "v_exp_f32 v110, v110\n"
  "v_exp_f32 v111, v111\n"
  "v_exp_f32 v112, v112\n"
  "v_add_f32 v104, v108, v109\n"
  "v_add_f32 v105, v110, v111\n"
  "v_add_f32 v104, v104, v112\n"
  "v_add_f32 v107, v104, v105\n"
  "s_nop 1\n"
  "v_add_f32_dpp v107, v107, v107 quad_perm:[1,0,3,2] row_mask:0xf bank_mask:0xf\n"
  "s_nop 1\n"
  "v_add_f32_dpp v107, v107, v107 quad_perm:[2,3,0,1] row_mask:0xf bank_mask:0xf\n"
  "s_nop 1\n"
  "v_add_f32_dpp v107, v107, v107 row_half_mirror row_mask:0xf bank_mask:0xf\n"
  "v_rcp_f32 v107, v107\n"
  "s_nop 0\n"
  "v_mul_f32 v112, v112, v107\n"
  "v_mul_f32 v108, v108, v107\n"
  "v_mul_f32 v109, v109, v107\n"
  "v_mul_f32 v110, v110, v107\n"
  "v_mul_f32 v111, v111, v107\n"
  "v_mov_b32_dpp v113, v112 quad_perm:[1,0,3,2] row_mask:0xf bank_mask:0xf\n"
  "v_cvt_pk_bf16_f32 v104, v108, v109\n"
  "v_cvt_pk_bf16_f32 v105, v110, v111\n"
  "v_min_u32 v104, s78, v104\n"
  "v_min_u32 v105, s78, v105\n"
  "v_cvt_pk_bf16_f32 v106, v112, v113\n"
  "buffer_store_dwordx2 v[104:105], v94, s[20:23], s71 offen sc1\n"
  "v_min_u32 v106, s78, v106\n"
  "buffer_store_dword v106, v16, s[20:23], s68 offen sc1\n"
  "s_waitcnt lgkmcnt(0)\n"
  "s_add_u32 s56, s56, 2\n"
  "s_cmp_lt_u32 s56, 64\n"
  "s_cbranch_scc1 H_LOOP_%=\n"
  "s_branch H_EXIT_%=\n"
  "H_FIX0_%=:\n"
  "s_nop 7\n"
  "s_waitcnt vmcnt(0) lgkmcnt(0)\n"
  "v_mov_b32 v56, 0\n"
  "v_mov_b32 v57, 0\n"
  "v_mov_b32 v58, 0\n"
  "v_mov_b32 v59, 0\n"
  "v_mov_b32 v60, 0\n"
  "v_mov_b32 v61, 0\n"
  "v_mov_b32 v62, 0\n"
  "v_mov_b32 v63, 0\n"
  "v_mov_b32 v64, 0\n"
  "v_mov_b32 v65, 0\n"
  "v_mov_b32 v66, 0\n"
  "v_mov_b32 v67, 0\n"
  "v_mov_b32 v68, 0\n"
  "v_mov_b32 v69, 0\n"
  "v_mov_b32 v70, 0\n"
  "v_mov_b32 v71, 0\n"
  "H_FIX0P_%=:\n"
  "buffer_load_dwordx4 v[104:107], v6, s[16:19], s57 offen sc1\n"
  "buffer_load_dwordx4 v[108:111], v6, s[16:19], s57 offen offset:1024 sc1\n"
  "buffer_load_dwordx2 v[54:55], v7, s[16:19], s57 offen sc1\n"
  "s_waitcnt vmcnt(2)\n"
  "v_max3_u32 v19, v104, v105, v106\n"
  "s_waitcnt vmcnt(1)\n"
  "v_max3_u32 v19, v107, v108, v19\n"
  "v_max3_u32 v19, v109, v110, v19\n"
  "s_waitcnt vmcnt(0)\n"
  "v_max3_u32 v19, v111, v54, v19\n"
  "v_max_u32 v19, v55, v19\n"
  "v_cmp_eq_u32 vcc, -1, v19\n"
  "s_nop 1\n"
  "s_cbranch_vccz H_D0_%=\n"
  "s_add_u32 s62, s62, 1\n"
  "s_cmp_gt_u32 s62, 300000\n"
  "s_cbranch_scc1 H_FAIL_%=\n"
  "s_and_b32 s60, s62, 255\n"
  "s_cmp_lg_u32 s60, 0\n"
  "s_cbranch_scc1 H_FIX0P_%=\n"
  "v_mov_b32 v19, 0\n"
  "buffer_load_dword v19, v19, s[40:43], 0 offen sc1\n"
  "s_waitcnt vmcnt(0)\n"
  "v_readfirstlane_b32 s60, v19\n"
  "s_cmp_lg_u32 s60, 0\n"
  "s_cbranch_scc1 H_EXIT_%=\n"
  "s_branch H_FIX0P_%=\n"
  "H_FIX1_%=:\n"
  "s_nop 7\n"
  "s_waitcnt vmcnt(0) lgkmcnt(0)\n"
  "H_FIX1P_%=:\n"
  "buffer_load_dwordx4 v[104:107], v6, s[16:19], s57 offen sc1\n"
  "buffer_load_dwordx4 v[108:111], v6, s[16:19], s57 offen offset:1024 sc1\n"
  "buffer_load_dwordx2 v[54:55], v7, s[16:19], s57 offen sc1\n"
  "s_waitcnt vmcnt(2)\n"
  "v_max3_u32 v19, v104, v105, v106\n"
  "s_waitcnt vmcnt(1)\n"
  "v_max3_u32 v19, v107, v108, v19\n"
  "v_max3_u32 v19, v109, v110, v19\n"
  "s_waitcnt vmcnt(0)\n"
  "v_max3_u32 v19, v111, v54, v19\n"
  "v_max_u32 v19, v55, v19\n"
  "v_cmp_eq_u32 vcc, -1, v19\n"
  "s_nop 1\n"
  "s_cbranch_vccz H_D1_%=\n"
  "s_add_u32 s62, s62, 1\n"
  "s_cmp_gt_u32 s62, 300000\n"
  "s_cbranch_scc1 H_FAIL_%=\n"
  "s_and_b32 s60, s62, 255\n"
  "s_cmp_lg_u32 s60, 0\n"
  "s_cbranch_scc1 H_FIX1P_%=\n"
  "v_mov_b32 v19, 0\n"
  "buffer_load_dword v19, v19, s[40:43], 0 offen sc1\n"
  "s_waitcnt vmcnt(0)\n"
  "v_readfirstlane_b32 s60, v19\n"
  "s_cmp_lg_u32 s60, 0\n"
  "s_cbranch_scc1 H_EXIT_%=\n"
  "s_branch H_FIX1P_%=\n"
  "H_FIX2_%=:\n"
  "s_nop 7\n"
  "s_waitcnt vmcnt(0) lgkmcnt(0)\n"
  "H_FIX2P_%=:\n"
  "buffer_load_dwordx4 v[104:107], v6, s[16:19], s57 offen sc1\n"
  "buffer_load_dwordx4 v[108:111], v6, s[16:19], s57 offen offset:1024 sc1\n"
  "buffer_load_dwordx2 v[54:55], v7, s[16:19], s57 offen sc1\n"
  "s_waitcnt vmcnt(2)\n"
  "v_max3_u32 v19, v104, v105, v106\n"
  "s_waitcnt vmcnt(1)\n"
  "v_max3_u32 v19, v107, v108, v19\n"
  "v_max3_u32 v19, v109, v110, v19\n"
  "s_waitcnt vmcnt(0)\n"
  "v_max3_u32 v19, v111, v54, v19\n"
  "v_max_u32 v19, v55, v19\n"
  "v_cmp_eq_u32 vcc, -1, v19\n"
  "s_nop 1\n"
  "s_cbranch_vccz H_D2_%=\n"
  "s_add_u32 s62, s62, 1\n"
  "s_cmp_gt_u32 s62, 300000\n"
  "s_cbranch_scc1 H_FAIL_%=\n"
  "s_and_b32 s60, s62, 255\n"
  "s_cmp_lg_u32 s60, 0\n"
  "s_cbranch_scc1 H_FIX2P_%=\n"
  "v_mov_b32 v19, 0\n"
  "buffer_load_dword v19, v19, s[40:43], 0 offen sc1\n"
  "s_waitcnt vmcnt(0)\n"
  "v_readfirstlane_b32 s60, v19\n"
  "s_cmp_lg_u32 s60, 0\n"
  "s_cbranch_scc1 H_EXIT_%=\n"
  "s_branch H_FIX2P_%=\n"
  "H_FAIL_%=:\n"
  "s_nop 7\n"
  "v_mov_b32 v56, 0\n"
  "v_mov_b32 v57, 1\n"
  "buffer_store_dword v57, v56, s[40:43], 0 offen sc1\n"
  "H_EXIT_%=:\n"
  "s_nop 7\n"
  "s_waitcnt vmcnt(0)\n"
  "s_endpgm\n"
  "C_ENTRY_%=:\n"
  "s_mov_b32 s76, 0x3dd53b95\n"
  "s_mov_b32 s77, 0xceabfb8f\n"
  "s_mov_b32 s78, 0xfffffffe\n"
  "s_mov_b32 s56, s45\n"
  "s_mov_b32 s96, 0\n"
  "s_add_u32 s60, s56, 0\n"
  "s_mul_i32 s72, s60, 40960\n"
  "s_mul_i32 s73, s44, 10240\n"
  "s_add_u32 s72, s72, s73\n"
  "s_add_u32 s73, s73, s96\n"
  "s_mov_b32 m0, s73\n"
  "s_nop 0\n"
  "buffer_load_dwordx4 v0, s[24:27], s72 offen offset:0 lds\n"
  "buffer_load_dwordx4 v0, s[24:27], s72 offen offset:1024 lds\n"
  "buffer_load_dwordx4 v0, s[24:27], s72 offen offset:2048 lds\n"
  "buffer_load_dwordx4 v0, s[24:27], s72 offen offset:3072 lds\n"
  "s_add_u32 s72, s72, 4096\n"
  "s_add_u32 s73, s73, 4096\n"
  "s_mov_b32 m0, s73\n"
  "s_nop 0\n"
  "buffer_load_dwordx4 v0, s[24:27], s72 offen offset:0 lds\n"
  "buffer_load_dwordx4 v0, s[24:27], s72 offen offset:1024 lds\n"
  "buffer_load_dwordx4 v0, s[24:27], s72 offen offset:2048 lds\n"
  "buffer_load_dwordx4 v0, s[24:27], s72 offen offset:3072 lds\n"
  "s_add_u32 s72, s72, 4096\n"
  "s_add_u32 s73, s73, 4096\n"
  "s_mov_b32 m0, s73\n"
  "s_nop 0\n"
  "buffer_load_dwordx4 v0, s[24:27], s72 offen offset:0 lds\n"
  "buffer_load_dwordx4 v0, s[24:27], s72 offen offset:1024 lds\n"
  "s_mov_b32 s68, 0\n"
  "buffer_load_dwordx4 a[0:3], v0, s[28:31], s68 offen offset:0\n"
  "buffer_load_dwordx4 a[4:7], v0, s[28:31], s68 offen offset:1024\n"
  "s_mov_b32 s68, 2048\n"
  "buffer_load_dwordx4 a[8:11], v0, s[28:31], s68 offen offset:0\n"
  "buffer_load_dwordx4 a[12:15], v0, s[28:31], s68 offen offset:1024\n"
  "s_mov_b32 s68, 4096\n"
  "buffer_load_dwordx4 a[16:19], v0, s[28:31], s68 offen offset:0\n"
  "buffer_load_dwordx4 a[20:23], v0, s[28:31], s68 offen offset:1024\n"
  "s_mov_b32 s68, 6144\n"
  "buffer_load_dwordx4 a[24:27], v0, s[28:31], s68 offen offset:0\n"
  "buffer_load_dwordx4 a[28:31], v0, s[28:31], s68 offen offset:1024\n"
  "s_mov_b32 s68, 8192\n"
  "buffer_load_dwordx4 a[32:35], v0, s[28:31], s68 offen offset:0\n"
  "buffer_load_dwordx4 a[36:39], v0, s[28:31], s68 offen offset:1024\n"
  "s_mov_b32 s68, 10240\n"
  "buffer_load_dwordx4 a[40:43], v0, s[28:31], s68 offen offset:0\n"
  "buffer_load_dwordx4 a[44:47], v0, s[28:31], s68 offen offset:1024\n"
  "s_mov_b32 s68, 12288\n"
  "buffer_load_dwordx4 a[48:51], v0, s[28:31], s68 offen offset:0\n"
  "buffer_load_dwordx4 a[52:55], v0, s[28:31], s68 offen offset:1024\n"
  "s_mov_b32 s68, 14336\n"
  "buffer_load_dwordx4 a[56:59], v0, s[28:31], s68 offen offset:0\n"
  "buffer_load_dwordx4 a[60:63], v0, s[28:31], s68 offen offset:1024\n"
  "s_mov_b32 s68, 16384\n"
  "buffer_load_dwordx4 a[64:67], v0, s[28:31], s68 offen offset:0\n"
  "buffer_load_dwordx4 a[68:71], v0, s[28:31], s68 offen offset:1024\n"
  "s_mov_b32 s68, 18432\n"
  "buffer_load_dwordx4 a[72:75], v0, s[28:31], s68 offen offset:0\n"
  "buffer_load_dwordx4 a[76:79], v0, s[28:31], s68 offen offset:1024\n"
  "s_mov_b32 s68, 20480\n"
  "buffer_load_dwordx4 a[80:83], v0, s[28:31], s68 offen offset:0\n"
  "buffer_load_dwordx4 a[84:87], v0, s[28:31], s68 offen offset:1024\n"
  "s_mov_b32 s68, 22528\n"
  "buffer_load_dwordx4 a[88:91], v0, s[28:31], s68 offen offset:0\n"
  "buffer_load_dwordx4 a[92:95], v0, s[28:31], s68 offen offset:1024\n"
  "s_mov_b32 s68, 24576\n"
  "buffer_load_dwordx4 a[96:99], v0, s[28:31], s68 offen offset:0\n"
  "buffer_load_dwordx4 a[100:103], v0, s[28:31], s68 offen offset:1024\n"
  "s_mov_b32 s68, 26624\n"
  "buffer_load_dwordx4 a[104:107], v0, s[28:31], s68 offen offset:0\n"
  "buffer_load_dwordx4 a[108:111], v0, s[28:31], s68 offen offset:1024\n"
  "s_mov_b32 s68, 28672\n"
  "buffer_load_dwordx4 a[112:115], v0, s[28:31], s68 offen offset:0\n"
  "buffer_load_dwordx4 a[116:119], v0, s[28:31], s68 offen offset:1024\n"
  "s_mov_b32 s68, 30720\n"
  "buffer_load_dwordx4 a[120:123], v0, s[28:31], s68 offen offset:0\n"
  "buffer_load_dwordx4 a[124:127], v0, s[28:31], s68 offen offset:1024\n"
  "s_mov_b32 s68, 32768\n"
  "buffer_load_dwordx4 a[128:131], v0, s[28:31], s68 offen offset:0\n"
  "buffer_load_dwordx4 a[132:135], v0, s[28:31], s68 offen offset:1024\n"
  "s_mov_b32 s68, 34816\n"
  "buffer_load_dwordx4 a[136:139], v0, s[28:31], s68 offen offset:0\n"
  "buffer_load_dwordx4 a[140:143], v0, s[28:31], s68 offen offset:1024\n"
  "s_mov_b32 s68, 36864\n"
  "buffer_load_dwordx4 a[144:147], v0, s[28:31], s68 offen offset:0\n"
  "buffer_load_dwordx4 a[148:151], v0, s[28:31], s68 offen offset:1024\n"
  "s_mov_b32 s68, 38912\n"
  "buffer_load_dwordx4 a[152:155], v0, s[28:31], s68 offen offset:0\n"
  "buffer_load_dwordx4 a[156:159], v0, s[28:31], s68 offen offset:1024\n"
  "s_mov_b32 s68, 40960\n"
  "buffer_load_dwordx4 a[160:163], v0, s[28:31], s68 offen offset:0\n"
  "buffer_load_dwordx4 a[164:167], v0, s[28:31], s68 offen offset:1024\n"
  "s_mov_b32 s68, 43008\n"
  "buffer_load_dwordx4 a[168:171], v0, s[28:31], s68 offen offset:0\n"
  "buffer_load_dwordx4 a[172:175], v0, s[28:31], s68 offen offset:1024\n"
  "s_mov_b32 s68, 45056\n"
  "buffer_load_dwordx4 a[176:179], v0, s[28:31], s68 offen offset:0\n"
  "buffer_load_dwordx4 a[180:183], v0, s[28:31], s68 offen offset:1024\n"
  "s_mov_b32 s68, 47104\n"
  "buffer_load_dwordx4 a[184:187], v0, s[28:31], s68 offen offset:0\n"
  "buffer_load_dwordx4 a[188:191], v0, s[28:31], s68 offen offset:1024\n"
  "s_mov_b32 s68, 49152\n"
  "buffer_load_dwordx4 a[192:195], v0, s[28:31], s68 offen offset:0\n"
  "buffer_load_dwordx4 a[196:199], v0, s[28:31], s68 offen offset:1024\n"
  "s_mov_b32 s68, 51200\n"
  "s_waitcnt vmcnt(50)\n"
  "buffer_load_dwordx4 a[200:203], v0, s[28:31], s68 offen offset:0\n"
  "buffer_load_dwordx4 a[204:207], v0, s[28:31], s68 offen offset:1024\n"
  "s_mov_b32 s68, 53248\n"
  "buffer_load_dwordx4 a[208:211], v0, s[28:31], s68 offen offset:0\n"
  "buffer_load_dwordx4 a[212:215], v0, s[28:31], s68 offen offset:1024\n"
  "s_mov_b32 s68, 55296\n"
  "buffer_load_dwordx4 a[216:219], v0, s[28:31], s68 offen offset:0\n"
  "buffer_load_dwordx4 a[220:223], v0, s[28:31], s68 offen offset:1024\n"
  "s_mov_b32 s68, 57344\n"
  "buffer_load_dwordx4 a[224:227], v0, s[28:31], s68 offen offset:0\n"
  "buffer_load_dwordx4 a[228:231], v0, s[28:31], s68 offen offset:1024\n"
  "s_mov_b32 s68, 59392\n"
  "buffer_load_dwordx4 a[232:235], v0, s[28:31], s68 offen offset:0\n"
  "buffer_load_dwordx4 a[236:239], v0, s[28:31], s68 offen offset:1024\n"
  "s_mov_b32 s68, 61440\n"
  "s_waitcnt vmcnt(50)\n"
  "buffer_load_dwordx4 a[240:243], v0, s[28:31], s68 offen offset:0\n"
  "buffer_load_dwordx4 a[244:247], v0, s[28:31], s68 offen offset:1024\n"
  "s_mov_b32 s68, 63488\n"
  "buffer_load_dwordx4 a[248:251], v0, s[28:31], s68 offen offset:0\n"
  "buffer_load_dwordx4 a[252:255], v0, s[28:31], s68 offen offset:1024\n"
  "s_mov_b32 s68, 65536\n"
  "buffer_load_dwordx4 v[192:195], v0, s[28:31], s68 offen offset:0\n"
  "buffer_load_dwordx4 v[196:199], v0, s[28:31], s68 offen offset:1024\n"
  "s_mov_b32 s68, 67584\n"
  "buffer_load_dwordx4 v[200:203], v0, s[28:31], s68 offen offset:0\n"
  "buffer_load_dwordx4 v[204:207], v0, s[28:31], s68 offen offset:1024\n"
  "s_mov_b32 s68, 69632\n"
  "buffer_load_dwordx4 v[208:211], v0, s[28:31], s68 offen offset:0\n"
  "buffer_load_dwordx4 v[212:215], v0, s[28:31], s68 offen offset:1024\n"
  "s_mov_b32 s68, 71680\n"
  "s_waitcnt vmcnt(50)\n"
  "buffer_load_dwordx4 v[216:219], v0, s[28:31], s68 offen offset:0\n"
  "buffer_load_dwordx4 v[220:223], v0, s[28:31], s68 offen offset:1024\n"
  "s_mov_b32 s68, 73728\n"
  "buffer_load_dwordx4 v[224:227], v0, s[28:31], s68 offen offset:0\n"
  "buffer_load_dwordx4 v[228:231], v0, s[28:31], s68 offen offset:1024\n"
  "s_mov_b32 s68, 75776\n"
  "buffer_load_dwordx4 v[232:235], v0, s[28:31], s68 offen offset:0\n"
  "buffer_load_dwordx4 v[236:239], v0, s[28:31], s68 offen offset:1024\n"
  "s_mov_b32 s68, 77824\n"
  "buffer_load_dwordx4 v[240:243], v0, s[28:31], s68 offen offset:0\n"
  "buffer_load_dwordx4 v[244:247], v0, s[28:31], s68 offen offset:1024\n"
  "s_mov_b32 s68, 79872\n"
  "buffer_load_dwordx4 v[248:251], v0, s[28:31], s68 offen offset:0\n"
  "buffer_load_dwordx4 v[252:255], v0, s[28:31], s68 offen offset:1024\n"
  "s_mov_b32 s96, 40960\n"
  "s_add_u32 s60, s56, 2\n"
  "s_mul_i32 s72, s60, 40960\n"
  "s_mul_i32 s73, s44, 10240\n"
  "s_add_u32 s72, s72, s73\n"
  "s_add_u32 s73, s73, s96\n"
  "s_mov_b32 m0, s73\n"
  "s_waitcnt vmcnt(50)\n"
  "buffer_load_dwordx4 v0, s[24:27], s72 offen offset:0 lds\n"
  "buffer_load_dwordx4 v0, s[24:27], s72 offen offset:1024 lds\n"
  "buffer_load_dwordx4 v0, s[24:27], s72 offen offset:2048 lds\n"
  "buffer_load_dwordx4 v0, s[24:27], s72 offen offset:3072 lds\n"
  "s_add_u32 s72, s72, 4096\n"
  "s_add_u32 s73, s73, 4096\n"
  "s_mov_b32 m0, s73\n"
  "s_nop 0\n"
  "buffer_load_dwordx4 v0, s[24:27], s72 offen offset:0 lds\n"
  "buffer_load_dwordx4 v0, s[24:27], s72 offen offset:1024 lds\n"
  "buffer_load_dwordx4 v0, s[24:27], s72 offen offset:2048 lds\n"
  "buffer_load_dwordx4 v0, s[24:27], s72 offen offset:3072 lds\n"
  "s_add_u32 s72, s72, 4096\n"
  "s_add_u32 s73, s73, 4096\n"
  "s_mov_b32 m0, s73\n"
  "s_nop 0\n"
  "buffer_load_dwordx4 v0, s[24:27], s72 offen offset:0 lds\n"
  "buffer_load_dwordx4 v0, s[24:27], s72 offen offset:1024 lds\n"
  "v_add_u32 v20, 81920, v0\n"
  "s_waitcnt vmcnt(0)\n"
  "s_barrier\n"
  "s_mov_b32 s96, 0\n"
  "s_mov_b32 s97, 108544\n"
  "v_mov_b32 v17, v0\n"
  "s_mul_i32 s60, s44, 5120\n"
  "s_add_u32 s60, s60, s97\n"
  "v_add_u32 v19, s60, v0\n"
  "ds_read_b128 v[64:67], v17 offset:0\n"
  "ds_read_b128 v[68:71], v17 offset:10240\n"
  "ds_read_b128 v[72:75], v17 offset:20480\n"
  "ds_read_b128 v[76:79], v17 offset:30720\n"
  "s_waitcnt lgkmcnt(3)\n"
  "v_mfma_f32_16x16x32_bf16 v[32:35], v[64:67], a[0:3], 0\n"
  "s_waitcnt lgkmcnt(2)\n"
  "v_mfma_f32_16x16x32_bf16 v[36:39], v[68:71], a[0:3], 0\n"
  "ds_read_b128 v[80:83], v17 offset:1024\n"
  "v_mfma_f32_16x16x32_bf16 v[40:43], v[64:67], a[8:11], 0\n"
  "v_mfma_f32_16x16x32_bf16 v[44:47], v[68:71], a[8:11], 0\n"
  "ds_read_b128 v[84:87], v17 offset:11264\n"
  "v_mfma_f32_16x16x32_bf16 v[48:51], v[64:67], a[16:19], 0\n"
  "v_mfma_f32_16x16x32_bf16 v[52:55], v[68:71], a[16:19], 0\n"
  "ds_read_b128 v[88:91], v17 offset:21504\n"
  "v_mfma_f32_16x16x32_bf16 v[56:59], v[64:67], a[24:27], 0\n"
  "v_mfma_f32_16x16x32_bf16 v[60:63], v[68:71], a[24:27], 0\n"
  "ds_read_b128 v[92:95], v17 offset:31744\n"
  "s_waitcnt lgkmcnt(5)\n"
  "v_mfma_f32_16x16x32_bf16 v[32:35], v[72:75], a[4:7], v[32:35]\n"
  "s_waitcnt lgkmcnt(4)\n"
  "v_mfma_f32_16x16x32_bf16 v[36:39], v[76:79], a[4:7], v[36:39]\n"
  "v_mfma_f32_16x16x32_bf16 v[40:43], v[72:75], a[12:15], v[40:43]\n"
  "v_mfma_f32_16x16x32_bf16 v[44:47], v[76:79], a[12:15], v[44:47]\n"
  "v_mfma_f32_16x16x32_bf16 v[48:51], v[72:75], a[20:23], v[48:51]\n"
  "v_mfma_f32_16x16x32_bf16 v[52:55], v[76:79], a[20:23], v[52:55]\n"
  "v_mfma_f32_16x16x32_bf16 v[56:59], v[72:75], a[28:31], v[56:59]\n"
  "v_mfma_f32_16x16x32_bf16 v[60:63], v[76:79], a[28:31], v[60:63]\n"
  "s_waitcnt lgkmcnt(3)\n"
  "v_mfma_f32_16x16x32_bf16 v[32:35], v[80:83], a[32:35], v[32:35]\n"
  "s_waitcnt lgkmcnt(2)\n"
  "v_mfma_f32_16x16x32_bf16 v[36:39], v[84:87], a[32:35], v[36:39]\n"
  "ds_read_b128 v[64:67], v17 offset:2048\n"
  "v_mfma_f32_16x16x32_bf16 v[40:43], v[80:83], a[40:43], v[40:43]\n"
  "v_mfma_f32_16x16x32_bf16 v[44:47], v[84:87], a[40:43], v[44:47]\n"
  "ds_read_b128 v[68:71], v17 offset:12288\n"
  "v_mfma_f32_16x16x32_bf16 v[48:51], v[80:83], a[48:51], v[48:51]\n"
  "v_mfma_f32_16x16x32_bf16 v[52:55], v[84:87], a[48:51], v[52:55]\n"
  "ds_read_b128 v[72:75], v17 offset:22528\n"
  "v_mfma_f32_16x16x32_bf16 v[56:59], v[80:83], a[56:59], v[56:59]\n"
  "v_mfma_f32_16x16x32_bf16 v[60:63], v[84:87], a[56:59], v[60:63]\n"
  "ds_read_b128 v[76:79], v17 offset:32768\n"
  "s_waitcnt lgkmcnt(5)\n"
  "v_mfma_f32_16x16x32_bf16 v[32:35], v[88:91], a[36:39], v[32:35]\n"
  "s_waitcnt lgkmcnt(4)\n"
  "v_mfma_f32_16x16x32_bf16 v[36:39], v[92:95], a[36:39], v[36:39]\n"
  "v_mfma_f32_16x16x32_bf16 v[40:43], v[88:91], a[44:47], v[40:43]\n"
  "v_mfma_f32_16x16x32_bf16 v[44:47], v[92:95], a[44:47], v[44:47]\n"
  "v_mfma_f32_16x16x32_bf16 v[48:51], v[88:91], a[52:55], v[48:51]\n"
  "v_mfma_f32_16x16x32_bf16 v[52:55], v[92:95], a[52:55], v[52:55]\n"
  "v_mfma_f32_16x16x32_bf16 v[56:59], v[88:91], a[60:63], v[56:59]\n"
  "v_mfma_f32_16x16x32_bf16 v[60:63], v[92:95], a[60:63], v[60:63]\n"
  "s_waitcnt lgkmcnt(3)\n"
  "v_mfma_f32_16x16x32_bf16 v[32:35], v[64:67], a[64:67], v[32:35]\n"
  "s_waitcnt lgkmcnt(2)\n"
  "v_mfma_f32_16x16x32_bf16 v[36:39], v[68:71], a[64:67], v[36:39]\n"
  "ds_read_b128 v[80:83], v17 offset:3072\n"
  "v_mfma_f32_16x16x32_bf16 v[40:43], v[64:67], a[72:75], v[40:43]\n"
  "v_mfma_f32_16x16x32_bf16 v[44:47], v[68:71], a[72:75], v[44:47]\n"
  "ds_read_b128 v[84:87], v17 offset:13312\n"
  "v_mfma_f32_16x16x32_bf16 v[48:51], v[64:67], a[80:83], v[48:51]\n"
  "v_mfma_f32_16x16x32_bf16 v[52:55], v[68:71], a[80:83], v[52:55]\n"
  "ds_read_b128 v[88:91], v17 offset:23552\n"
  "v_mfma_f32_16x16x32_bf16 v[56:59], v[64:67], a[88:91], v[56:59]\n"
  "v_mfma_f32_16x16x32_bf16 v[60:63], v[68:71], a[88:91], v[60:63]\n"
  "ds_read_b128 v[92:95], v17 offset:33792\n"
  "s_waitcnt lgkmcnt(5)\n"
  "v_mfma_f32_16x16x32_bf16 v[32:35], v[72:75], a[68:71], v[32:35]\n"
  "s_waitcnt lgkmcnt(4)\n"
  "v_mfma_f32_16x16x32_bf16 v[36:39], v[76:79], a[68:71], v[36:39]\n"
  "v_mfma_f32_16x16x32_bf16 v[40:43], v[72:75], a[76:79], v[40:43]\n"
  "v_mfma_f32_16x16x32_bf16 v[44:47], v[76:79], a[76:79], v[44:47]\n"
  "v_mfma_f32_16x16x32_bf16 v[48:51], v[72:75], a[84:87], v[48:51]\n"
  "v_mfma_f32_16x16x32_bf16 v[52:55], v[76:79], a[84:87], v[52:55]\n"
  "v_mfma_f32_16x16x32_bf16 v[56:59], v[72:75], a[92:95], v[56:59]\n"
  "v_mfma_f32_16x16x32_bf16 v[60:63], v[76:79], a[92:95], v[60:63]\n"
  "s_waitcnt lgkmcnt(3)\n"
  "v_mfma_f32_16x16x32_bf16 v[32:35], v[80:83], a[96:99], v[32:35]\n"
  "s_waitcnt lgkmcnt(2)\n"
  "v_mfma_f32_16x16x32_bf16 v[36:39], v[84:87], a[96:99], v[36:39]\n"
  "ds_read_b128 v[64:67], v17 offset:4096\n"
  "v_mfma_f32_16x16x32_bf16 v[40:43], v[80:83], a[104:107], v[40:43]\n"
  "v_mfma_f32_16x16x32_bf16 v[44:47], v[84:87], a[104:107], v[44:47]\n"
  "ds_read_b128 v[68:71], v17 offset:14336\n"
  "v_mfma_f32_16x16x32_bf16 v[48:51], v[80:83], a[112:115], v[48:51]\n"
  "v_mfma_f32_16x16x32_bf16 v[52:55], v[84:87], a[112:115], v[52:55]\n"
  "ds_read_b128 v[72:75], v17 offset:24576\n"
  "v_mfma_f32_16x16x32_bf16 v[56:59], v[80:83], a[120:123], v[56:59]\n"
  "v_mfma_f32_16x16x32_bf16 v[60:63], v[84:87], a[120:123], v[60:63]\n"
  "ds_read_b128 v[76:79], v17 offset:34816\n"
  "s_waitcnt lgkmcnt(5)\n"
  "v_mfma_f32_16x16x32_bf16 v[32:35], v[88:91], a[100:103], v[32:35]\n"
  "s_waitcnt lgkmcnt(4)\n"
  "v_mfma_f32_16x16x32_bf16 v[36:39], v[92:95], a[100:103], v[36:39]\n"
  "v_mfma_f32_16x16x32_bf16 v[40:43], v[88:91], a[108:111], v[40:43]\n"
  "v_mfma_f32_16x16x32_bf16 v[44:47], v[92:95], a[108:111], v[44:47]\n"
  "v_mfma_f32_16x16x32_bf16 v[48:51], v[88:91], a[116:119], v[48:51]\n"
  "v_mfma_f32_16x16x32_bf16 v[52:55], v[92:95], a[116:119], v[52:55]\n"
  "v_mfma_f32_16x16x32_bf16 v[56:59], v[88:91], a[124:127], v[56:59]\n"
  "v_mfma_f32_16x16x32_bf16 v[60:63], v[92:95], a[124:127], v[60:63]\n"
  "s_waitcnt lgkmcnt(3)\n"
  "v_mfma_f32_16x16x32_bf16 v[32:35], v[64:67], a[128:131], v[32:35]\n"
  "s_waitcnt lgkmcnt(2)\n"
  "v_mfma_f32_16x16x32_bf16 v[36:39], v[68:71], a[128:131], v[36:39]\n"
  "ds_read_b128 v[80:83], v17 offset:5120\n"
  "v_mfma_f32_16x16x32_bf16 v[40:43], v[64:67], a[136:139], v[40:43]\n"
  "v_mfma_f32_16x16x32_bf16 v[44:47], v[68:71], a[136:139], v[44:47]\n"
  "ds_read_b128 v[84:87], v17 offset:15360\n"
  "v_mfma_f32_16x16x32_bf16 v[48:51], v[64:67], a[144:147], v[48:51]\n"
  "v_mfma_f32_16x16x32_bf16 v[52:55], v[68:71], a[144:147], v[52:55]\n"
  "ds_read_b128 v[88:91], v17 offset:25600\n"
  "v_mfma_f32_16x16x32_bf16 v[56:59], v[64:67], a[152:155], v[56:59]\n"
  "v_mfma_f32_16x16x32_bf16 v[60:63], v[68:71], a[152:155], v[60:63]\n"
  "ds_read_b128 v[92:95], v17 offset:35840\n"
  "s_waitcnt lgkmcnt(5)\n"
  "v_mfma_f32_16x16x32_bf16 v[32:35], v[72:75], a[132:135], v[32:35]\n"
  "s_waitcnt lgkmcnt(4)\n"
  "v_mfma_f32_16x16x32_bf16 v[36:39], v[76:79], a[132:135], v[36:39]\n"
  "v_mfma_f32_16x16x32_bf16 v[40:43], v[72:75], a[140:143], v[40:43]\n"
  "v_mfma_f32_16x16x32_bf16 v[44:47], v[76:79], a[140:143], v[44:47]\n"
  "v_mfma_f32_16x16x32_bf16 v[48:51], v[72:75], a[148:151], v[48:51]\n"
  "v_mfma_f32_16x16x32_bf16 v[52:55], v[76:79], a[148:151], v[52:55]\n"
  "v_mfma_f32_16x16x32_bf16 v[56:59], v[72:75], a[156:159], v[56:59]\n"
  "v_mfma_f32_16x16x32_bf16 v[60:63], v[76:79], a[156:159], v[60:63]\n"
  "s_waitcnt lgkmcnt(3)\n"
  "v_mfma_f32_16x16x32_bf16 v[32:35], v[80:83], a[160:163], v[32:35]\n"
  "s_waitcnt lgkmcnt(2)\n"
  "v_mfma_f32_16x16x32_bf16 v[36:39], v[84:87], a[160:163], v[36:39]\n"
  "ds_read_b128 v[64:67], v17 offset:6144\n"
  "v_mfma_f32_16x16x32_bf16 v[40:43], v[80:83], a[168:171], v[40:43]\n"
  "v_mfma_f32_16x16x32_bf16 v[44:47], v[84:87], a[168:171], v[44:47]\n"
  "ds_read_b128 v[68:71], v17 offset:16384\n"
  "v_mfma_f32_16x16x32_bf16 v[48:51], v[80:83], a[176:179], v[48:51]\n"
  "v_mfma_f32_16x16x32_bf16 v[52:55], v[84:87], a[176:179], v[52:55]\n"
  "ds_read_b128 v[72:75], v17 offset:26624\n"
  "v_mfma_f32_16x16x32_bf16 v[56:59], v[80:83], a[184:187], v[56:59]\n"
  "v_mfma_f32_16x16x32_bf16 v[60:63], v[84:87], a[184:187], v[60:63]\n"
  "ds_read_b128 v[76:79], v17 offset:36864\n"
  "s_waitcnt lgkmcnt(5)\n"
  "v_mfma_f32_16x16x32_bf16 v[32:35], v[88:91], a[164:167], v[32:35]\n"
  "s_waitcnt lgkmcnt(4)\n"
  "v_mfma_f32_16x16x32_bf16 v[36:39], v[92:95], a[164:167], v[36:39]\n"
  "v_mfma_f32_16x16x32_bf16 v[40:43], v[88:91], a[172:175], v[40:43]\n"
  "v_mfma_f32_16x16x32_bf16 v[44:47], v[92:95], a[172:175], v[44:47]\n"
  "v_mfma_f32_16x16x32_bf16 v[48:51], v[88:91], a[180:183], v[48:51]\n"
  "v_mfma_f32_16x16x32_bf16 v[52:55], v[92:95], a[180:183], v[52:55]\n"
  "v_mfma_f32_16x16x32_bf16 v[56:59], v[88:91], a[188:191], v[56:59]\n"
  "v_mfma_f32_16x16x32_bf16 v[60:63], v[92:95], a[188:191], v[60:63]\n"
  "s_waitcnt lgkmcnt(3)\n"
  "v_mfma_f32_16x16x32_bf16 v[32:35], v[64:67], a[192:195], v[32:35]\n"
  "s_waitcnt lgkmcnt(2)\n"
  "v_mfma_f32_16x16x32_bf16 v[36:39], v[68:71], a[192:195], v[36:39]\n"
  "ds_read_b128 v[80:83], v17 offset:7168\n"
  "v_mfma_f32_16x16x32_bf16 v[40:43], v[64:67], a[200:203], v[40:43]\n"
  "v_mfma_f32_16x16x32_bf16 v[44:47], v[68:71], a[200:203], v[44:47]\n"
  "ds_read_b128 v[84:87], v17 offset:17408\n"
  "v_mfma_f32_16x16x32_bf16 v[48:51], v[64:67], a[208:211], v[48:51]\n"
  "v_mfma_f32_16x16x32_bf16 v[52:55], v[68:71], a[208:211], v[52:55]\n"
  "ds_read_b128 v[88:91], v17 offset:27648\n"
  "v_mfma_f32_16x16x32_bf16 v[56:59], v[64:67], a[216:219], v[56:59]\n"
  "v_mfma_f32_16x16x32_bf16 v[60:63], v[68:71], a[216:219], v[60:63]\n"
  "ds_read_b128 v[92:95], v17 offset:37888\n"
  "s_waitcnt lgkmcnt(5)\n"
  "v_mfma_f32_16x16x32_bf16 v[32:35], v[72:75], a[196:199], v[32:35]\n"
  "s_waitcnt lgkmcnt(4)\n"
  "v_mfma_f32_16x16x32_bf16 v[36:39], v[76:79], a[196:199], v[36:39]\n"
  "v_mfma_f32_16x16x32_bf16 v[40:43], v[72:75], a[204:207], v[40:43]\n"
  "v_mfma_f32_16x16x32_bf16 v[44:47], v[76:79], a[204:207], v[44:47]\n"
  "v_mfma_f32_16x16x32_bf16 v[48:51], v[72:75], a[212:215], v[48:51]\n"
  "v_mfma_f32_16x16x32_bf16 v[52:55], v[76:79], a[212:215], v[52:55]\n"
  "v_mfma_f32_16x16x32_bf16 v[56:59], v[72:75], a[220:223], v[56:59]\n"
  "v_mfma_f32_16x16x32_bf16 v[60:63], v[76:79], a[220:223], v[60:63]\n"
  "s_waitcnt lgkmcnt(3)\n"
  "v_mfma_f32_16x16x32_bf16 v[32:35], v[80:83], a[224:227], v[32:35]\n"
  "s_waitcnt lgkmcnt(2)\n"
  "v_mfma_f32_16x16x32_bf16 v[36:39], v[84:87], a[224:227], v[36:39]\n"
  "ds_read_b128 v[64:67], v17 offset:8192\n"
  "v_mfma_f32_16x16x32_bf16 v[40:43], v[80:83], a[232:235], v[40:43]\n"
  "v_mfma_f32_16x16x32_bf16 v[44:47], v[84:87], a[232:235], v[44:47]\n"
  "ds_read_b128 v[68:71], v17 offset:18432\n"
  "v_mfma_f32_16x16x32_bf16 v[48:51], v[80:83], a[240:243], v[48:51]\n"
  "v_mfma_f32_16x16x32_bf16 v[52:55], v[84:87], a[240:243], v[52:55]\n"
  "ds_read_b128 v[72:75], v17 offset:28672\n"
  "v_mfma_f32_16x16x32_bf16 v[56:59], v[80:83], a[248:251], v[56:59]\n"
  "v_mfma_f32_16x16x32_bf16 v[60:63], v[84:87], a[248:251], v[60:63]\n"
  "ds_read_b128 v[76:79], v17 offset:38912\n"
  "s_waitcnt lgkmcnt(5)\n"
  "v_mfma_f32_16x16x32_bf16 v[32:35], v[88:91], a[228:231], v[32:35]\n"
  "s_waitcnt lgkmcnt(4)\n"
  "v_mfma_f32_16x16x32_bf16 v[36:39], v[92:95], a[228:231], v[36:39]\n"
  "v_mfma_f32_16x16x32_bf16 v[40:43], v[88:91], a[236:239], v[40:43]\n"
  "v_mfma_f32_16x16x32_bf16 v[44:47], v[92:95], a[236:239], v[44:47]\n"
  "v_mfma_f32_16x16x32_bf16 v[48:51], v[88:91], a[244:247], v[48:51]\n"
  "v_mfma_f32_16x16x32_bf16 v[52:55], v[92:95], a[244:247], v[52:55]\n"
  "v_mfma_f32_16x16x32_bf16 v[56:59], v[88:91], a[252:255], v[56:59]\n"
  "v_mfma_f32_16x16x32_bf16 v[60:63], v[92:95], a[252:255], v[60:63]\n"
  "s_waitcnt lgkmcnt(3)\n"
  "v_mfma_f32_16x16x32_bf16 v[32:35], v[64:67], v[192:195], v[32:35]\n"
  "s_waitcnt lgkmcnt(2)\n"
  "v_mfma_f32_16x16x32_bf16 v[36:39], v[68:71], v[192:195], v[36:39]\n"
  "ds_read_b128 v[80:83], v17 offset:9216\n"
  "v_mfma_f32_16x16x32_bf16 v[40:43], v[64:67], v[200:203], v[40:43]\n"
  "v_mfma_f32_16x16x32_bf16 v[44:47], v[68:71], v[200:203], v[44:47]\n"
  "ds_read_b128 v[84:87], v17 offset:19456\n"
  "v_mfma_f32_16x16x32_bf16 v[48:51], v[64:67], v[208:211], v[48:51]\n"
  "v_mfma_f32_16x16x32_bf16 v[52:55], v[68:71], v[208:211], v[52:55]\n"
  "ds_read_b128 v[88:91], v17 offset:29696\n"
  "v_mfma_f32_16x16x32_bf16 v[56:59], v[64:67], v[216:219], v[56:59]\n"
  "v_mfma_f32_16x16x32_bf16 v[60:63], v[68:71], v[216:219], v[60:63]\n"
  "ds_read_b128 v[92:95], v17 offset:39936\n"
  "s_waitcnt lgkmcnt(5)\n"
  "v_mfma_f32_16x16x32_bf16 v[32:35], v[72:75], v[196:199], v[32:35]\n"
  "s_waitcnt lgkmcnt(4)\n"
  "v_mfma_f32_16x16x32_bf16 v[36:39], v[76:79], v[196:199], v[36:39]\n"
  "v_mfma_f32_16x16x32_bf16 v[40:43], v[72:75], v[204:207], v[40:43]\n"
  "v_mfma_f32_16x16x32_bf16 v[44:47], v[76:79], v[204:207], v[44:47]\n"
  "v_mfma_f32_16x16x32_bf16 v[48:51], v[72:75], v[212:215], v[48:51]\n"
  "v_mfma_f32_16x16x32_bf16 v[52:55], v[76:79], v[212:215], v[52:55]\n"
  "v_mfma_f32_16x16x32_bf16 v[56:59], v[72:75], v[220:223], v[56:59]\n"
  "v_mfma_f32_16x16x32_bf16 v[60:63], v[76:79], v[220:223], v[60:63]\n"
  "s_waitcnt lgkmcnt(3)\n"
  "v_mfma_f32_16x16x32_bf16 v[32:35], v[80:83], v[224:227], v[32:35]\n"
  "s_waitcnt lgkmcnt(2)\n"
  "v_mfma_f32_16x16x32_bf16 v[36:39], v[84:87], v[224:227], v[36:39]\n"
  "v_mfma_f32_16x16x32_bf16 v[40:43], v[80:83], v[232:235], v[40:43]\n"
  "v_mfma_f32_16x16x32_bf16 v[44:47], v[84:87], v[232:235], v[44:47]\n"
  "v_mfma_f32_16x16x32_bf16 v[48:51], v[80:83], v[240:243], v[48:51]\n"
  "v_mfma_f32_16x16x32_bf16 v[52:55], v[84:87], v[240:243], v[52:55]\n"
  "v_mfma_f32_16x16x32_bf16 v[56:59], v[80:83], v[248:251], v[56:59]\n"
  "v_mfma_f32_16x16x32_bf16 v[60:63], v[84:87], v[248:251], v[60:63]\n"
  "s_waitcnt lgkmcnt(1)\n"
  "v_mfma_f32_16x16x32_bf16 v[32:35], v[88:91], v[228:231], v[32:35]\n"
  "s_waitcnt lgkmcnt(0)\n"
  "v_mfma_f32_16x16x32_bf16 v[36:39], v[92:95], v[228:231], v[36:39]\n"
  "v_mfma_f32_16x16x32_bf16 v[40:43], v[88:91], v[236:239], v[40:43]\n"
  "v_mfma_f32_16x16x32_bf16 v[44:47], v[92:95], v[236:239], v[44:47]\n"
  "v_mfma_f32_16x16x32_bf16 v[48:51], v[88:91], v[244:247], v[48:51]\n"
  "v_mfma_f32_16x16x32_bf16 v[52:55], v[92:95], v[244:247], v[52:55]\n"
  "v_mfma_f32_16x16x32_bf16 v[56:59], v[88:91], v[252:255], v[56:59]\n"
  "v_mfma_f32_16x16x32_bf16 v[60:63], v[92:95], v[252:255], v[60:63]\n"
  "v_cvt_pk_bf16_f32 v32, v32, v33\n"
  "v_cvt_pk_bf16_f32 v33, v34, v35\n"
  "v_cvt_pk_bf16_f32 v34, v36, v37\n"
  "v_cvt_pk_bf16_f32 v35, v38, v39\n"
  "ds_write_b128 v19, v[32:35] offset:0\n"
  "v_cvt_pk_bf16_f32 v40, v40, v41\n"
  "v_cvt_pk_bf16_f32 v41, v42, v43\n"
  "v_cvt_pk_bf16_f32 v42, v44, v45\n"
  "v_cvt_pk_bf16_f32 v43, v46, v47\n"
  "ds_write_b128 v19, v[40:43] offset:1024\n"
  "v_cvt_pk_bf16_f32 v48, v48, v49\n"
  "v_cvt_pk_bf16_f32 v49, v50, v51\n"
  "v_cvt_pk_bf16_f32 v50, v52, v53\n"
  "v_cvt_pk_bf16_f32 v51, v54, v55\n"
  "ds_write_b128 v19, v[48:51] offset:2048\n"
  "v_cvt_pk_bf16_f32 v56, v56, v57\n"
  "v_cvt_pk_bf16_f32 v57, v58, v59\n"
  "v_cvt_pk_bf16_f32 v58, v60, v61\n"
  "v_cvt_pk_bf16_f32 v59, v62, v63\n"
  "ds_write_b128 v19, v[56:59] offset:3072\n"
  "s_add_u32 s60, s56, 0\n"
  "s_lshr_b32 s61, s60, 1\n"
  "s_add_u32 s61, s61, s46\n"
  "s_lshl_b32 s57, s61, 18\n"
  "s_lshr_b32 s61, s61, 3\n"
  "s_lshr_b32 s61, s47, s61\n"
  "s_xor_b32 s60, s60, s61\n"
  "s_and_b32 s60, s60, 1\n"
  "s_lshl_b32 s60, s60, 13\n"
  "s_add_u32 s57, s57, s60\n"
  "v_mov_b32 v21, -1\n"
  "C_LOOP_%=:\n"
  "s_nop 7\n"
  "s_add_u32 s60, s56, 1\n"
  "s_lshr_b32 s61, s60, 1\n"
  "s_add_u32 s61, s61, s46\n"
  "s_lshl_b32 s58, s61, 18\n"
  "s_lshr_b32 s61, s61, 3\n"
  "s_lshr_b32 s61, s47, s61\n"
  "s_xor_b32 s60, s60, s61\n"
  "s_and_b32 s60, s60, 1\n"
  "s_lshl_b32 s60, s60, 13\n"
  "s_add_u32 s58, s58, s60\n"
  "s_mov_b32 s62, 0\n"
  "v_readfirstlane_b32 s59, v21\n"
  "s_cmp_lg_u32 s59, -1\n"
  "s_cbranch_scc1 C_SCHOK_%=\n"
  "C_SCHP_%=:\n"
  "s_nop 7\n"
  "buffer_load_dword v21, v10, s[16:19], s57 offen sc1\n"
  "s_waitcnt vmcnt(0)\n"
  "v_readfirstlane_b32 s59, v21\n"
  "s_cmp_lg_u32 s59, -1\n"
  "s_cbranch_scc1 C_SCHOK_%=\n"
  "s_sleep 2\n"
  "s_add_u32 s62, s62, 1\n"
  "s_cmp_gt_u32 s62, 300000\n"
  "s_cbranch_scc1 C_FAIL_%=\n"
  "s_and_b32 s60, s62, 255\n"
  "s_cmp_lg_u32 s60, 0\n"
  "s_cbranch_scc1 C_SCHP_%=\n"
  "v_mov_b32 v21, 0\n"
  "buffer_load_dword v21, v21, s[40:43], 0 offen sc1\n"
  "s_waitcnt vmcnt(0)\n"
  "v_readfirstlane_b32 s60, v21\n"
  "s_cmp_lg_u32 s60, 0\n"
  "s_cbranch_scc1 C_EXIT_%=\n"
  "s_branch C_SCHP_%=\n"
  "C_SCHOK_%=:\n"
  "s_nop 7\n"
  "s_waitcnt lgkmcnt(0)\n"
  "s_memrealtime s[64:65]\n"
  "s_waitcnt lgkmcnt(0)\n"
  "s_sub_i32 s60, s59, s64\n"
  "s_cmp_gt_i32 s60, 2000\n"
  "s_cselect_b32 s59, s64, s59\n"
  "s_cmp_lt_i32 s60, -100000\n"
  "s_cselect_b32 s59, s64, s59\n"
  "s_add_u32 s68, s59, s51\n"
  "v_mov_b32 v152, 0\n"
  "v_mov_b32 v153, 0\n"
  "v_mov_b32 v154, 0\n"
  "v_mov_b32 v155, 0\n"
  "v_mov_b32 v156, 0\n"
  "v_mov_b32 v157, 0\n"
  "v_mov_b32 v158, 0\n"
  "v_mov_b32 v159, 0\n"
  "v_mov_b32 v160, 0\n"
  "v_mov_b32 v161, 0\n"
  "v_mov_b32 v162, 0\n"
  "v_mov_b32 v163, 0\n"
  "v_mov_b32 v164, 0\n"
  "v_mov_b32 v165, 0\n"
  "v_mov_b32 v166, 0\n"
  "v_mov_b32 v167, 0\n"
  "v_add_u32 v18, s97, v0\n"
  "s_mov_b32 s62, 0\n"
  "C_WT_%=:\n"
  "s_memrealtime s[64:65]\n"
  "s_waitcnt lgkmcnt(0)\n"
  "s_sub_i32 s60, s64, s68\n"
  "s_cmp_lt_i32 s60, 0\n"
  "s_cbranch_scc1 C_WT_%=\n"
  "C_MP_%=:\n"
  "buffer_load_dwordx4 v[168:171], v2, s[16:19], s57 offen sc1\n"
  "buffer_load_dwordx4 v[172:175], v2, s[16:19], s57 offen offset:1024 sc1\n"
  "buffer_load_dwordx2 v[176:177], v3, s[16:19], s57 offen sc1\n"
  "s_waitcnt vmcnt(2)\n"
  "ds_write_b128 v1, v[168:171]\n"
  "v_max3_u32 v15, v168, v169, v170\n"
  "s_waitcnt vmcnt(1)\n"
  "ds_write_b128 v1, v[172:175] offset:4096\n"
  "v_max3_u32 v15, v171, v172, v15\n"
  "v_max3_u32 v15, v173, v174, v15\n"
  "s_waitcnt vmcnt(0)\n"
  "ds_write_b64 v4, v[176:177]\n"
  "v_max3_u32 v15, v175, v176, v15\n"
  "v_max_u32 v15, v177, v15\n"
  "v_cmp_eq_u32 vcc, -1, v15\n"
  "s_nop 1\n"
  "s_cbranch_vccnz C_MBAD_%=\n"
  "s_waitcnt lgkmcnt(0)\n"
  "s_barrier\n"
  "s_memrealtime s[64:65]\n"
  "ds_read_b128 v[112:115], v20 offset:0\n"
  "ds_read_b128 v[116:119], v20 offset:1024\n"
  "ds_read_b128 v[120:123], v20 offset:2048\n"
  "ds_read_b128 v[124:127], v20 offset:3072\n"
  "ds_read_b128 v[128:131], v20 offset:4096\n"
  "ds_read_b128 v[132:135], v20 offset:5120\n"
  "ds_read_b128 v[136:139], v20 offset:6144\n"
  "ds_read_b128 v[140:143], v20 offset:7168\n"
  "ds_read_b128 v[144:147], v20 offset:8192\n"
  "ds_read_b128 v[148:151], v20 offset:9216\n"
  "s_waitcnt lgkmcnt(0)\n"
  "v_smfmac_f32_16x16x64_bf16 v[152:155], v[112:115], a[0:7], v5\n"
  "s_sub_i32 s60, s64, s68\n"
  "s_add_u32 s61, s64, s50\n"
  "s_cmp_gt_i32 s60, s52\n"
  "s_cselect_b32 s59, s61, s59\n"
  "v_smfmac_f32_16x16x64_bf16 v[156:159], v[112:115], a[8:15], v5\n"
  "v_smfmac_f32_16x16x64_bf16 v[160:163], v[112:115], a[16:23], v5\n"
  "s_add_u32 s59, s59, s48\n"
  "s_add_u32 s60, s59, s49\n"
  "s_min_u32 s60, s60, s78\n"
  "v_mov_b32 v15, s60\n"
  "v_smfmac_f32_16x16x64_bf16 v[164:167], v[112:115], a[24:31], v5\n"
  "v_smfmac_f32_16x16x64_bf16 v[152:155], v[116:119], a[32:39], v5\n"
  "buffer_store_dword v15, v16, s[16:19], s58 offen sc1\n"
  "v_smfmac_f32_16x16x64_bf16 v[156:159], v[116:119], a[40:47], v5\n"
  "v_smfmac_f32_16x16x64_bf16 v[160:163], v[116:119], a[48:55], v5\n"
  "ds_read_b128 v[32:35], v18 offset:0\n"
  "v_smfmac_f32_16x16x64_bf16 v[164:167], v[116:119], a[56:63], v5\n"
  "v_smfmac_f32_16x16x64_bf16 v[152:155], v[120:123], a[64:71], v5\n"
  "ds_read_b128 v[36:39], v18 offset:1024\n"
  "v_smfmac_f32_16x16x64_bf16 v[156:159], v[120:123], a[72:79], v5\n"
  "v_smfmac_f32_16x16x64_bf16 v[160:163], v[120:123], a[80:87], v5\n"
  "ds_read_b128 v[40:43], v18 offset:2048\n"
  "v_smfmac_f32_16x16x64_bf16 v[164:167], v[120:123], a[88:95], v5\n"
  "v_smfmac_f32_16x16x64_bf16 v[152:155], v[124:127], a[96:103], v5\n"
  "ds_read_b128 v[44:47], v18 offset:3072\n"
  "v_smfmac_f32_16x16x64_bf16 v[156:159], v[124:127], a[104:111], v5\n"
  "v_smfmac_f32_16x16x64_bf16 v[160:163], v[124:127], a[112:119], v5\n"
  "ds_read_b128 v[52:55], v18 offset:5120\n"
  "v_smfmac_f32_16x16x64_bf16 v[164:167], v[124:127], a[120:127], v5\n"
  "v_smfmac_f32_16x16x64_bf16 v[152:155], v[128:131], a[128:135], v5\n"
  "ds_read_b128 v[56:59], v18 offset:6144\n"
  "v_smfmac_f32_16x16x64_bf16 v[156:159], v[128:131], a[136:143], v5\n"
  "v_smfmac_f32_16x16x64_bf16 v[160:163], v[128:131], a[144:151], v5\n"
  "ds_read_b128 v[60:63], v18 offset:7168\n"
  "v_smfmac_f32_16x16x64_bf16 v[164:167], v[128:131], a[152:159], v5\n"
  "v_smfmac_f32_16x16x64_bf16 v[152:155], v[132:135], a[160:167], v5\n"
  "ds_read_b128 v[64:67], v18 offset:8192\n"
  "v_smfmac_f32_16x16x64_bf16 v[156:159], v[132:135], a[168:175], v5\n"
  "v_smfmac_f32_16x16x64_bf16 v[160:163], v[132:135], a[176:183], v5\n"
  "ds_read_b128 v[72:75], v18 offset:10240\n"
  "v_smfmac_f32_16x16x64_bf16 v[164:167], v[132:135], a[184:191], v5\n"
  "v_smfmac_f32_16x16x64_bf16 v[152:155], v[136:139], a[192:199], v5\n"
  "ds_read_b128 v[76:79], v18 offset:11264\n"
  "v_smfmac_f32_16x16x64_bf16 v[156:159], v[136:139], a[200:207], v5\n"
  "v_smfmac_f32_16x16x64_bf16 v[160:163], v[136:139], a[208:215], v5\n"
  "ds_read_b128 v[80:83], v18 offset:12288\n"
  "v_smfmac_f32_16x16x64_bf16 v[164:167], v[136:139], a[216:223], v5\n"
  "v_smfmac_f32_16x16x64_bf16 v[152:155], v[140:143], a[224:231], v5\n"
  "ds_read_b128 v[84:87], v18 offset:13312\n"
  "v_smfmac_f32_16x16x64_bf16 v[156:159], v[140:143], a[232:239], v5\n"
  "v_smfmac_f32_16x16x64_bf16 v[160:163], v[140:143], a[240:247], v5\n"
  "ds_read_b128 v[92:95], v18 offset:15360\n"
  "v_smfmac_f32_16x16x64_bf16 v[164:167], v[140:143], a[248:255], v5\n"
  "v_smfmac_f32_16x16x64_bf16 v[152:155], v[144:147], v[192:199], v5\n"
  "ds_read_b128 v[96:99], v18 offset:16384\n"
  "v_smfmac_f32_16x16x64_bf16 v[156:159], v[144:147], v[200:207], v5\n"
  "v_smfmac_f32_16x16x64_bf16 v[160:163], v[144:147], v[208:215], v5\n"
  "s_waitcnt lgkmcnt(10)\n"
  "ds_read_b128 v[100:103], v18 offset:17408\n"
  "v_smfmac_f32_16x16x64_bf16 v[164:167], v[144:147], v[216:223], v5\n"
  "v_smfmac_f32_16x16x64_bf16 v[152:155], v[148:151], v[224:231], v5\n"
  "ds_read_b128 v[104:107], v18 offset:18432\n"
  "v_smfmac_f32_16x16x64_bf16 v[156:159], v[148:151], v[232:239], v5\n"
  "v_smfmac_f32_16x16x64_bf16 v[160:163], v[148:151], v[240:247], v5\n"
  "v_smfmac_f32_16x16x64_bf16 v[164:167], v[148:151], v[248:255], v5\n"
  "s_nop 6\n"
  "v_permlane32_swap_b32 v152, v160\n"
  "v_permlane32_swap_b32 v153, v161\n"
  "v_permlane32_swap_b32 v154, v162\n"
  "v_permlane32_swap_b32 v155, v163\n"
  "v_permlane32_swap_b32 v156, v164\n"
  "v_permlane32_swap_b32 v157, v165\n"
  "v_permlane32_swap_b32 v158, v166\n"
  "v_permlane32_swap_b32 v159, v167\n"
  "v_pk_add_f32 v[152:153], v[152:153], v[160:161]\n"
  "v_pk_add_f32 v[154:155], v[154:155], v[162:163]\n"
  "v_pk_add_f32 v[156:157], v[156:157], v[164:165]\n"
  "v_pk_add_f32 v[158:159], v[158:159], v[166:167]\n"
  "v_cvt_pk_bf16_f32 v152, v152, v153\n"
  "v_cvt_pk_bf16_f32 v153, v154, v155\n"
  "v_cvt_pk_bf16_f32 v154, v156, v157\n"
  "v_cvt_pk_bf16_f32 v155, v158, v159\n"
  "s_nop 0\n"
  "v_permlane16_swap_b32 v152, v154\n"
  "v_permlane16_swap_b32 v153, v155\n"
  "ds_write_b128 v19, v[152:155] offset:4096\n"
  "s_waitcnt lgkmcnt(0)\n"
  "s_barrier\n"
  "s_cmp_lg_u32 s44, 0\n"
  "s_cbranch_scc1 C_OTHER_%=\n"
  "s_cmp_eq_u32 s56, 63\n"
  "s_cbranch_scc1 C_SKIP2_%=\n"
  "ds_read_b128 v[48:51], v18 offset:4096\n"
  "ds_read_b128 v[68:71], v18 offset:9216\n"
  "ds_read_b128 v[88:91], v18 offset:14336\n"
  "ds_read_b128 v[108:111], v18 offset:19456\n"
  "s_add_u32 s68, s57, 0\n"
  "s_add_u32 s69, s57, 16384\n"
  "s_add_u32 s70, s57, 32768\n"
  "s_add_u32 s71, s57, 49152\n"
  "s_add_u32 s72, s58, 131072\n"
  "s_mov_b64 s[66:67], 0\n"
  "s_waitcnt lgkmcnt(0)\n"
  "C_WT2_%=:\n"
  "s_memrealtime s[64:65]\n"
  "s_waitcnt lgkmcnt(0)\n"
  "s_sub_i32 s60, s64, s59\n"
  "s_cmp_lt_i32 s60, 0\n"
  "s_cbranch_scc1 C_WT2_%=\n"
  "buffer_load_dwordx4 v[112:115], v6, s[16:19], s68 offen offset:0 sc1\n"
  "buffer_load_dwordx4 v[116:119], v6, s[16:19], s68 offen offset:1280 sc1\n"
  "buffer_load_dwordx4 v[120:123], v6, s[16:19], s68 offen offset:2560 sc1\n"
  "buffer_load_dwordx4 v[124:127], v6, s[16:19], s68 offen offset:3840 sc1\n"
  "buffer_load_dwordx4 v[128:131], v7, s[16:19], s68 offen sc1\n"
  "buffer_load_dwordx4 v[132:135], v6, s[16:19], s69 offen offset:0 sc1\n"
  "buffer_load_dwordx4 v[136:139], v6, s[16:19], s69 offen offset:1280 sc1\n"
  "buffer_load_dwordx4 v[140:143], v6, s[16:19], s69 offen offset:2560 sc1\n"
  "buffer_load_dwordx4 v[144:147], v6, s[16:19], s69 offen offset:3840 sc1\n"
  "buffer_load_dwordx4 v[148:151], v7, s[16:19], s69 offen sc1\n"
  "buffer_load_dwordx4 v[152:155], v6, s[16:19], s70 offen offset:0 sc1\n"
  "buffer_load_dwordx4 v[156:159], v6, s[16:19], s70 offen offset:1280 sc1\n"
  "buffer_load_dwordx4 v[160:163], v6, s[16:19], s70 offen offset:2560 sc1\n"
  "buffer_load_dwordx4 v[164:167], v6, s[16:19], s70 offen offset:3840 sc1\n"
  "buffer_load_dwordx4 v[168:171], v7, s[16:19], s70 offen sc1\n"
  "buffer_load_dwordx4 v[172:175], v6, s[16:19], s71 offen offset:0 sc1\n"
  "buffer_load_dwordx4 v[176:179], v6, s[16:19], s71 offen offset:1280 sc1\n"
  "buffer_load_dwordx4 v[180:183], v6, s[16:19], s71 offen offset:2560 sc1\n"
  "buffer_load_dwordx4 v[184:187], v6, s[16:19], s71 offen offset:3840 sc1\n"
  "buffer_load_dwordx4 v[188:191], v7, s[16:19], s71 offen sc1\n"
  "C_P2_%=:\n"
  "s_waitcnt vmcnt(19)\n"
  "v_mfma_f32_16x16x32_bf16 v[24:27], v[32:35], v[112:115], 0\n"
  "s_waitcnt vmcnt(18)\n"
  "v_mfma_f32_16x16x32_bf16 v[24:27], v[36:39], v[116:119], v[24:27]\n"
  "s_waitcnt vmcnt(17)\n"
  "v_mfma_f32_16x16x32_bf16 v[24:27], v[40:43], v[120:123], v[24:27]\n"
  "s_waitcnt vmcnt(16)\n"
  "v_mfma_f32_16x16x32_bf16 v[24:27], v[44:47], v[124:127], v[24:27]\n"
  "s_waitcnt vmcnt(15)\n"
  "v_mfma_f32_16x16x32_bf16 v[24:27], v[48:51], v[128:131], v[24:27]\n"
  "s_waitcnt vmcnt(14)\n"
  "v_mfma_f32_16x16x32_bf16 v[24:27], v[52:55], v[132:135], v[24:27]\n"
  "s_waitcnt vmcnt(13)\n"
  "v_mfma_f32_16x16x32_bf16 v[24:27], v[56:59], v[136:139], v[24:27]\n"
  "s_waitcnt vmcnt(12)\n"
  "v_mfma_f32_16x16x32_bf16 v[24:27], v[60:63], v[140:143], v[24:27]\n"
  "s_waitcnt vmcnt(11)\n"
  "v_mfma_f32_16x16x32_bf16 v[24:27], v[64:67], v[144:147], v[24:27]\n"
  "s_waitcnt vmcnt(10)\n"
  "v_mfma_f32_16x16x32_bf16 v[24:27], v[68:71], v[148:151], v[24:27]\n"
  "s_waitcnt vmcnt(9)\n"
  "v_mfma_f32_16x16x32_bf16 v[24:27], v[72:75], v[152:155], v[24:27]\n"
  "s_waitcnt vmcnt(8)\n"
  "v_mfma_f32_16x16x32_bf16 v[24:27], v[76:79], v[156:159], v[24:27]\n"
  "s_waitcnt vmcnt(7)\n"
  "v_mfma_f32_16x16x32_bf16 v[24:27], v[80:83], v[160:163], v[24:27]\n"
  "s_waitcnt vmcnt(6)\n"
  "v_mfma_f32_16x16x32_bf16 v[24:27], v[84:87], v[164:167], v[24:27]\n"
  "s_waitcnt vmcnt(5)\n"
  "v_mfma_f32_16x16x32_bf16 v[24:27], v[88:91], v[168:171], v[24:27]\n"
  "s_waitcnt vmcnt(4)\n"
  "v_mfma_f32_16x16x32_bf16 v[24:27], v[92:95], v[172:175], v[24:27]\n"
  "s_waitcnt vmcnt(3)\n"
  "v_mfma_f32_16x16x32_bf16 v[24:27], v[96:99], v[176:179], v[24:27]\n"
  "s_waitcnt vmcnt(2)\n"
  "v_mfma_f32_16x16x32_bf16 v[24:27], v[100:103], v[180:183], v[24:27]\n"
  "s_waitcnt vmcnt(1)\n"
  "v_mfma_f32_16x16x32_bf16 v[24:27], v[104:107], v[184:187], v[24:27]\n"
  "s_waitcnt vmcnt(0)\n"
  "v_mfma_f32_16x16x32_bf16 v[24:27], v[108:111], v[188:191], v[24:27]\n"
  "s_nop 7\n"
  "v_add_f32_dpp v24, v26, v24 row_ror:8 row_mask:0xf bank_mask:0xf\n"
  "v_add_f32_dpp v25, v27, v25 row_ror:8 row_mask:0xf bank_mask:0xf\n"
  "v_cmp_u_f32 vcc, v24, v24\n"
  "v_cvt_pk_bf16_f32 v24, v24, v25\n"
  "s_andn2_b64 vcc, vcc, s[66:67]\n"
  "v_min_u32 v24, s78, v24\n"
  "s_cbranch_vccnz C_REDO_%=\n"
  "s_nop 0\n"
  "v_mov_b32_dpp v24, v24 row_ror:8 row_mask:0xf bank_mask:0xc\n"
  "buffer_store_dword v24, v11, s[16:19], s58 offen sc1\n"
  "buffer_store_dword v24, v11, s[16:19], s72 offen sc1\n"
  "C_SKIP2_%=:\n"
  "s_nop 7\n"
  "s_add_u32 s60, s56, 2\n"
  "s_lshr_b32 s61, s60, 1\n"
  "s_add_u32 s61, s61, s46\n"
  "s_lshl_b32 s57, s61, 18\n"
  "s_lshr_b32 s61, s61, 3\n"
  "s_lshr_b32 s61, s47, s61\n"
  "s_xor_b32 s60, s60, s61\n"
  "s_and_b32 s60, s60, 1\n"
  "s_lshl_b32 s60, s60, 13\n"
  "s_add_u32 s57, s57, s60\n"
  "s_xor_b32 s75, s96, 40960\n"
  "v_add_u32 v17, s75, v0\n"
  "s_sub_u32 s97, 237568, s97\n"
  "s_mul_i32 s60, s44, 5120\n"
  "s_add_u32 s60, s60, s97\n"
  "v_add_u32 v19, s60, v0\n"
  "ds_read_b128 v[64:67], v17 offset:0\n"
  "ds_read_b128 v[68:71], v17 offset:10240\n"
  "ds_read_b128 v[72:75], v17 offset:20480\n"
  "ds_read_b128 v[76:79], v17 offset:30720\n"
  "s_waitcnt lgkmcnt(3)\n"
  "v_mfma_f32_16x16x32_bf16 v[32:35], v[64:67], a[0:3], 0\n"
  "s_waitcnt lgkmcnt(2)\n"
  "v_mfma_f32_16x16x32_bf16 v[36:39], v[68:71], a[0:3], 0\n"
  "ds_read_b128 v[80:83], v17 offset:1024\n"
  "v_mfma_f32_16x16x32_bf16 v[40:43], v[64:67], a[8:11], 0\n"
  "v_mfma_f32_16x16x32_bf16 v[44:47], v[68:71], a[8:11], 0\n"
  "ds_read_b128 v[84:87], v17 offset:11264\n"
  "v_mfma_f32_16x16x32_bf16 v[48:51], v[64:67], a[16:19], 0\n"
  "v_mfma_f32_16x16x32_bf16 v[52:55], v[68:71], a[16:19], 0\n"
  "ds_read_b128 v[88:91], v17 offset:21504\n"
  "v_mfma_f32_16x16x32_bf16 v[56:59], v[64:67], a[24:27], 0\n"
  "v_mfma_f32_16x16x32_bf16 v[60:63], v[68:71], a[24:27], 0\n"
  "ds_read_b128 v[92:95], v17 offset:31744\n"
  "s_waitcnt lgkmcnt(5)\n"
  "v_mfma_f32_16x16x32_bf16 v[32:35], v[72:75], a[4:7], v[32:35]\n"
  "s_waitcnt lgkmcnt(4)\n"
  "v_mfma_f32_16x16x32_bf16 v[36:39], v[76:79], a[4:7], v[36:39]\n"
  "v_mfma_f32_16x16x32_bf16 v[40:43], v[72:75], a[12:15], v[40:43]\n"
  "v_mfma_f32_16x16x32_bf16 v[44:47], v[76:79], a[12:15], v[44:47]\n"
  "v_mfma_f32_16x16x32_bf16 v[48:51], v[72:75], a[20:23], v[48:51]\n"
  "v_mfma_f32_16x16x32_bf16 v[52:55], v[76:79], a[20:23], v[52:55]\n"
  "v_mfma_f32_16x16x32_bf16 v[56:59], v[72:75], a[28:31], v[56:59]\n"
  "v_mfma_f32_16x16x32_bf16 v[60:63], v[76:79], a[28:31], v[60:63]\n"
  "s_waitcnt lgkmcnt(3)\n"
  "v_mfma_f32_16x16x32_bf16 v[32:35], v[80:83], a[32:35], v[32:35]\n"
  "s_waitcnt lgkmcnt(2)\n"
  "v_mfma_f32_16x16x32_bf16 v[36:39], v[84:87], a[32:35], v[36:39]\n"
  "ds_read_b128 v[64:67], v17 offset:2048\n"
  "v_mfma_f32_16x16x32_bf16 v[40:43], v[80:83], a[40:43], v[40:43]\n"
  "v_mfma_f32_16x16x32_bf16 v[44:47], v[84:87], a[40:43], v[44:47]\n"
  "ds_read_b128 v[68:71], v17 offset:12288\n"
  "v_mfma_f32_16x16x32_bf16 v[48:51], v[80:83], a[48:51], v[48:51]\n"
  "v_mfma_f32_16x16x32_bf16 v[52:55], v[84:87], a[48:51], v[52:55]\n"
  "ds_read_b128 v[72:75], v17 offset:22528\n"
  "v_mfma_f32_16x16x32_bf16 v[56:59], v[80:83], a[56:59], v[56:59]\n"
  "v_mfma_f32_16x16x32_bf16 v[60:63], v[84:87], a[56:59], v[60:63]\n"
  "ds_read_b128 v[76:79], v17 offset:32768\n"
  "s_waitcnt lgkmcnt(5)\n"
  "v_mfma_f32_16x16x32_bf16 v[32:35], v[88:91], a[36:39], v[32:35]\n"
  "s_waitcnt lgkmcnt(4)\n"
  "v_mfma_f32_16x16x32_bf16 v[36:39], v[92:95], a[36:39], v[36:39]\n"
  "v_mfma_f32_16x16x32_bf16 v[40:43], v[88:91], a[44:47], v[40:43]\n"
  "v_mfma_f32_16x16x32_bf16 v[44:47], v[92:95], a[44:47], v[44:47]\n"
  "v_mfma_f32_16x16x32_bf16 v[48:51], v[88:91], a[52:55], v[48:51]\n"
  "v_mfma_f32_16x16x32_bf16 v[52:55], v[92:95], a[52:55], v[52:55]\n"
  "v_mfma_f32_16x16x32_bf16 v[56:59], v[88:91], a[60:63], v[56:59]\n"
  "v_mfma_f32_16x16x32_bf16 v[60:63], v[92:95], a[60:63], v[60:63]\n"
  "s_waitcnt lgkmcnt(3)\n"
  "v_mfma_f32_16x16x32_bf16 v[32:35], v[64:67], a[64:67], v[32:35]\n"
  "s_waitcnt lgkmcnt(2)\n"
  "v_mfma_f32_16x16x32_bf16 v[36:39], v[68:71], a[64:67], v[36:39]\n"
  "ds_read_b128 v[80:83], v17 offset:3072\n"
  "v_mfma_f32_16x16x32_bf16 v[40:43], v[64:67], a[72:75], v[40:43]\n"
  "v_mfma_f32_16x16x32_bf16 v[44:47], v[68:71], a[72:75], v[44:47]\n"
  "ds_read_b128 v[84:87], v17 offset:13312\n"
  "v_mfma_f32_16x16x32_bf16 v[48:51], v[64:67], a[80:83], v[48:51]\n"
  "v_mfma_f32_16x16x32_bf16 v[52:55], v[68:71], a[80:83], v[52:55]\n"
  "ds_read_b128 v[88:91], v17 offset:23552\n"
  "v_mfma_f32_16x16x32_bf16 v[56:59], v[64:67], a[88:91], v[56:59]\n"
  "v_mfma_f32_16x16x32_bf16 v[60:63], v[68:71], a[88:91], v[60:63]\n"
  "ds_read_b128 v[92:95], v17 offset:33792\n"
  "s_waitcnt lgkmcnt(5)\n"
  "v_mfma_f32_16x16x32_bf16 v[32:35], v[72:75], a[68:71], v[32:35]\n"
  "s_waitcnt lgkmcnt(4)\n"
  "v_mfma_f32_16x16x32_bf16 v[36:39], v[76:79], a[68:71], v[36:39]\n"
  "v_mfma_f32_16x16x32_bf16 v[40:43], v[72:75], a[76:79], v[40:43]\n"
  "v_mfma_f32_16x16x32_bf16 v[44:47], v[76:79], a[76:79], v[44:47]\n"
  "v_mfma_f32_16x16x32_bf16 v[48:51], v[72:75], a[84:87], v[48:51]\n"
  "v_mfma_f32_16x16x32_bf16 v[52:55], v[76:79], a[84:87], v[52:55]\n"
  "v_mfma_f32_16x16x32_bf16 v[56:59], v[72:75], a[92:95], v[56:59]\n"
  "v_mfma_f32_16x16x32_bf16 v[60:63], v[76:79], a[92:95], v[60:63]\n"
  "s_waitcnt lgkmcnt(3)\n"
  "v_mfma_f32_16x16x32_bf16 v[32:35], v[80:83], a[96:99], v[32:35]\n"
  "s_waitcnt lgkmcnt(2)\n"
  "v_mfma_f32_16x16x32_bf16 v[36:39], v[84:87], a[96:99], v[36:39]\n"
  "ds_read_b128 v[64:67], v17 offset:4096\n"
  "v_mfma_f32_16x16x32_bf16 v[40:43], v[80:83], a[104:107], v[40:43]\n"
  "v_mfma_f32_16x16x32_bf16 v[44:47], v[84:87], a[104:107], v[44:47]\n"
  "ds_read_b128 v[68:71], v17 offset:14336\n"
  "v_mfma_f32_16x16x32_bf16 v[48:51], v[80:83], a[112:115], v[48:51]\n"
  "v_mfma_f32_16x16x32_bf16 v[52:55], v[84:87], a[112:115], v[52:55]\n"
  "ds_read_b128 v[72:75], v17 offset:24576\n"
  "v_mfma_f32_16x16x32_bf16 v[56:59], v[80:83], a[120:123], v[56:59]\n"
  "v_mfma_f32_16x16x32_bf16 v[60:63], v[84:87], a[120:123], v[60:63]\n"
  "ds_read_b128 v[76:79], v17 offset:34816\n"
  "s_waitcnt lgkmcnt(5)\n"
  "v_mfma_f32_16x16x32_bf16 v[32:35], v[88:91], a[100:103], v[32:35]\n"
  "s_waitcnt lgkmcnt(4)\n"
  "v_mfma_f32_16x16x32_bf16 v[36:39], v[92:95], a[100:103], v[36:39]\n"
  "v_mfma_f32_16x16x32_bf16 v[40:43], v[88:91], a[108:111], v[40:43]\n"
  "v_mfma_f32_16x16x32_bf16 v[44:47], v[92:95], a[108:111], v[44:47]\n"
  "v_mfma_f32_16x16x32_bf16 v[48:51], v[88:91], a[116:119], v[48:51]\n"
  "v_mfma_f32_16x16x32_bf16 v[52:55], v[92:95], a[116:119], v[52:55]\n"
  "v_mfma_f32_16x16x32_bf16 v[56:59], v[88:91], a[124:127], v[56:59]\n"
  "v_mfma_f32_16x16x32_bf16 v[60:63], v[92:95], a[124:127], v[60:63]\n"
  "s_waitcnt lgkmcnt(3)\n"
  "v_mfma_f32_16x16x32_bf16 v[32:35], v[64:67], a[128:131], v[32:35]\n"
  "s_waitcnt lgkmcnt(2)\n"
  "v_mfma_f32_16x16x32_bf16 v[36:39], v[68:71], a[128:131], v[36:39]\n"
  "ds_read_b128 v[80:83], v17 offset:5120\n"
  "v_mfma_f32_16x16x32_bf16 v[40:43], v[64:67], a[136:139], v[40:43]\n"
  "v_mfma_f32_16x16x32_bf16 v[44:47], v[68:71], a[136:139], v[44:47]\n"
  "ds_read_b128 v[84:87], v17 offset:15360\n"
  "v_mfma_f32_16x16x32_bf16 v[48:51], v[64:67], a[144:147], v[48:51]\n"
  "v_mfma_f32_16x16x32_bf16 v[52:55], v[68:71], a[144:147], v[52:55]\n"
  "ds_read_b128 v[88:91], v17 offset:25600\n"
  "v_mfma_f32_16x16x32_bf16 v[56:59], v[64:67], a[152:155], v[56:59]\n"
  "v_mfma_f32_16x16x32_bf16 v[60:63], v[68:71], a[152:155], v[60:63]\n"
  "ds_read_b128 v[92:95], v17 offset:35840\n"
  "s_waitcnt lgkmcnt(5)\n"
  "v_mfma_f32_16x16x32_bf16 v[32:35], v[72:75], a[132:135], v[32:35]\n"
  "s_waitcnt lgkmcnt(4)\n"
  "v_mfma_f32_16x16x32_bf16 v[36:39], v[76:79], a[132:135], v[36:39]\n"
  "v_mfma_f32_16x16x32_bf16 v[40:43], v[72:75], a[140:143], v[40:43]\n"
  "v_mfma_f32_16x16x32_bf16 v[44:47], v[76:79], a[140:143], v[44:47]\n"
  "v_mfma_f32_16x16x32_bf16 v[48:51], v[72:75], a[148:151], v[48:51]\n"
  "v_mfma_f32_16x16x32_bf16 v[52:55], v[76:79], a[148:151], v[52:55]\n"
  "v_mfma_f32_16x16x32_bf16 v[56:59], v[72:75], a[156:159], v[56:59]\n"
  "v_mfma_f32_16x16x32_bf16 v[60:63], v[76:79], a[156:159], v[60:63]\n"
  "s_waitcnt lgkmcnt(3)\n"
  "v_mfma_f32_16x16x32_bf16 v[32:35], v[80:83], a[160:163], v[32:35]\n"
  "s_waitcnt lgkmcnt(2)\n"
  "v_mfma_f32_16x16x32_bf16 v[36:39], v[84:87], a[160:163], v[36:39]\n"
  "ds_read_b128 v[64:67], v17 offset:6144\n"
  "v_mfma_f32_16x16x32_bf16 v[40:43], v[80:83], a[168:171], v[40:43]\n"
  "v_mfma_f32_16x16x32_bf16 v[44:47], v[84:87], a[168:171], v[44:47]\n"
  "ds_read_b128 v[68:71], v17 offset:16384\n"
  "v_mfma_f32_16x16x32_bf16 v[48:51], v[80:83], a[176:179], v[48:51]\n"
  "v_mfma_f32_16x16x32_bf16 v[52:55], v[84:87], a[176:179], v[52:55]\n"
  "ds_read_b128 v[72:75], v17 offset:26624\n"
  "v_mfma_f32_16x16x32_bf16 v[56:59], v[80:83], a[184:187], v[56:59]\n"
  "v_mfma_f32_16x16x32_bf16 v[60:63], v[84:87], a[184:187], v[60:63]\n"
  "ds_read_b128 v[76:79], v17 offset:36864\n"
  "s_waitcnt lgkmcnt(5)\n"
  "v_mfma_f32_16x16x32_bf16 v[32:35], v[88:91], a[164:167], v[32:35]\n"
  "s_waitcnt lgkmcnt(4)\n"
  "v_mfma_f32_16x16x32_bf16 v[36:39], v[92:95], a[164:167], v[36:39]\n"
  "v_mfma_f32_16x16x32_bf16 v[40:43], v[88:91], a[172:175], v[40:43]\n"
  "v_mfma_f32_16x16x32_bf16 v[44:47], v[92:95], a[172:175], v[44:47]\n"
  "v_mfma_f32_16x16x32_bf16 v[48:51], v[88:91], a[180:183], v[48:51]\n"
  "v_mfma_f32_16x16x32_bf16 v[52:55], v[92:95], a[180:183], v[52:55]\n"
  "v_mfma_f32_16x16x32_bf16 v[56:59], v[88:91], a[188:191], v[56:59]\n"
  "v_mfma_f32_16x16x32_bf16 v[60:63], v[92:95], a[188:191], v[60:63]\n"
  "s_waitcnt lgkmcnt(3)\n"
  "v_mfma_f32_16x16x32_bf16 v[32:35], v[64:67], a[192:195], v[32:35]\n"
  "s_waitcnt lgkmcnt(2)\n"
  "v_mfma_f32_16x16x32_bf16 v[36:39], v[68:71], a[192:195], v[36:39]\n"
  "ds_read_b128 v[80:83], v17 offset:7168\n"
  "v_mfma_f32_16x16x32_bf16 v[40:43], v[64:67], a[200:203], v[40:43]\n"
  "v_mfma_f32_16x16x32_bf16 v[44:47], v[68:71], a[200:203], v[44:47]\n"
  "ds_read_b128 v[84:87], v17 offset:17408\n"
  "v_mfma_f32_16x16x32_bf16 v[48:51], v[64:67], a[208:211], v[48:51]\n"
  "v_mfma_f32_16x16x32_bf16 v[52:55], v[68:71], a[208:211], v[52:55]\n"
  "ds_read_b128 v[88:91], v17 offset:27648\n"
  "v_mfma_f32_16x16x32_bf16 v[56:59], v[64:67], a[216:219], v[56:59]\n"
  "v_mfma_f32_16x16x32_bf16 v[60:63], v[68:71], a[216:219], v[60:63]\n"
  "ds_read_b128 v[92:95], v17 offset:37888\n"
  "s_waitcnt lgkmcnt(5)\n"
  "v_mfma_f32_16x16x32_bf16 v[32:35], v[72:75], a[196:199], v[32:35]\n"
  "s_waitcnt lgkmcnt(4)\n"
  "v_mfma_f32_16x16x32_bf16 v[36:39], v[76:79], a[196:199], v[36:39]\n"
  "v_mfma_f32_16x16x32_bf16 v[40:43], v[72:75], a[204:207], v[40:43]\n"
  "v_mfma_f32_16x16x32_bf16 v[44:47], v[76:79], a[204:207], v[44:47]\n"
  "v_mfma_f32_16x16x32_bf16 v[48:51], v[72:75], a[212:215], v[48:51]\n"
  "v_mfma_f32_16x16x32_bf16 v[52:55], v[76:79], a[212:215], v[52:55]\n"
  "v_mfma_f32_16x16x32_bf16 v[56:59], v[72:75], a[220:223], v[56:59]\n"
  "v_mfma_f32_16x16x32_bf16 v[60:63], v[76:79], a[220:223], v[60:63]\n"
  "s_waitcnt lgkmcnt(3)\n"
  "v_mfma_f32_16x16x32_bf16 v[32:35], v[80:83], a[224:227], v[32:35]\n"
  "s_waitcnt lgkmcnt(2)\n"
  "v_mfma_f32_16x16x32_bf16 v[36:39], v[84:87], a[224:227], v[36:39]\n"
  "ds_read_b128 v[64:67], v17 offset:8192\n"
  "v_mfma_f32_16x16x32_bf16 v[40:43], v[80:83], a[232:235], v[40:43]\n"
  "v_mfma_f32_16x16x32_bf16 v[44:47], v[84:87], a[232:235], v[44:47]\n"
  "ds_read_b128 v[68:71], v17 offset:18432\n"
  "v_mfma_f32_16x16x32_bf16 v[48:51], v[80:83], a[240:243], v[48:51]\n"
  "v_mfma_f32_16x16x32_bf16 v[52:55], v[84:87], a[240:243], v[52:55]\n"
  "ds_read_b128 v[72:75], v17 offset:28672\n"
  "v_mfma_f32_16x16x32_bf16 v[56:59], v[80:83], a[248:251], v[56:59]\n"
  "v_mfma_f32_16x16x32_bf16 v[60:63], v[84:87], a[248:251], v[60:63]\n"
  "ds_read_b128 v[76:79], v17 offset:38912\n"
  "s_waitcnt lgkmcnt(5)\n"
  "v_mfma_f32_16x16x32_bf16 v[32:35], v[88:91], a[228:231], v[32:35]\n"
  "s_waitcnt lgkmcnt(4)\n"
  "v_mfma_f32_16x16x32_bf16 v[36:39], v[92:95], a[228:231], v[36:39]\n"
  "v_mfma_f32_16x16x32_bf16 v[40:43], v[88:91], a[236:239], v[40:43]\n"
  "v_mfma_f32_16x16x32_bf16 v[44:47], v[92:95], a[236:239], v[44:47]\n"
  "v_mfma_f32_16x16x32_bf16 v[48:51], v[88:91], a[244:247], v[48:51]\n"
  "v_mfma_f32_16x16x32_bf16 v[52:55], v[92:95], a[244:247], v[52:55]\n"
  "v_mfma_f32_16x16x32_bf16 v[56:59], v[88:91], a[252:255], v[56:59]\n"
  "v_mfma_f32_16x16x32_bf16 v[60:63], v[92:95], a[252:255], v[60:63]\n"
  "s_waitcnt lgkmcnt(3)\n"
  "v_mfma_f32_16x16x32_bf16 v[32:35], v[64:67], v[192:195], v[32:35]\n"
  "s_waitcnt lgkmcnt(2)\n"
  "v_mfma_f32_16x16x32_bf16 v[36:39], v[68:71], v[192:195], v[36:39]\n"
  "ds_read_b128 v[80:83], v17 offset:9216\n"
  "v_mfma_f32_16x16x32_bf16 v[40:43], v[64:67], v[200:203], v[40:43]\n"
  "v_mfma_f32_16x16x32_bf16 v[44:47], v[68:71], v[200:203], v[44:47]\n"
  "ds_read_b128 v[84:87], v17 offset:19456\n"
  "v_mfma_f32_16x16x32_bf16 v[48:51], v[64:67], v[208:211], v[48:51]\n"
  "v_mfma_f32_16x16x32_bf16 v[52:55], v[68:71], v[208:211], v[52:55]\n"
  "ds_read_b128 v[88:91], v17 offset:29696\n"
  "v_mfma_f32_16x16x32_bf16 v[56:59], v[64:67], v[216:219], v[56:59]\n"
  "v_mfma_f32_16x16x32_bf16 v[60:63], v[68:71], v[216:219], v[60:63]\n"
  "ds_read_b128 v[92:95], v17 offset:39936\n"
  "s_waitcnt lgkmcnt(5)\n"
  "v_mfma_f32_16x16x32_bf16 v[32:35], v[72:75], v[196:199], v[32:35]\n"
  "s_waitcnt lgkmcnt(4)\n"
  "v_mfma_f32_16x16x32_bf16 v[36:39], v[76:79], v[196:199], v[36:39]\n"
  "v_mfma_f32_16x16x32_bf16 v[40:43], v[72:75], v[204:207], v[40:43]\n"
  "v_mfma_f32_16x16x32_bf16 v[44:47], v[76:79], v[204:207], v[44:47]\n"
  "v_mfma_f32_16x16x32_bf16 v[48:51], v[72:75], v[212:215], v[48:51]\n"
  "v_mfma_f32_16x16x32_bf16 v[52:55], v[76:79], v[212:215], v[52:55]\n"
  "v_mfma_f32_16x16x32_bf16 v[56:59], v[72:75], v[220:223], v[56:59]\n"
  "v_mfma_f32_16x16x32_bf16 v[60:63], v[76:79], v[220:223], v[60:63]\n"
  "s_waitcnt lgkmcnt(3)\n"
  "v_mfma_f32_16x16x32_bf16 v[32:35], v[80:83], v[224:227], v[32:35]\n"
  "s_waitcnt lgkmcnt(2)\n"
  "v_mfma_f32_16x16x32_bf16 v[36:39], v[84:87], v[224:227], v[36:39]\n"
  "v_mfma_f32_16x16x32_bf16 v[40:43], v[80:83], v[232:235], v[40:43]\n"
  "v_mfma_f32_16x16x32_bf16 v[44:47], v[84:87], v[232:235], v[44:47]\n"
  "v_mfma_f32_16x16x32_bf16 v[48:51], v[80:83], v[240:243], v[48:51]\n"
  "v_mfma_f32_16x16x32_bf16 v[52:55], v[84:87], v[240:243], v[52:55]\n"
  "v_mfma_f32_16x16x32_bf16 v[56:59], v[80:83], v[248:251], v[56:59]\n"
  "v_mfma_f32_16x16x32_bf16 v[60:63], v[84:87], v[248:251], v[60:63]\n"
  "s_waitcnt lgkmcnt(1)\n"
  "v_mfma_f32_16x16x32_bf16 v[32:35], v[88:91], v[228:231], v[32:35]\n"
  "s_waitcnt lgkmcnt(0)\n"
  "v_mfma_f32_16x16x32_bf16 v[36:39], v[92:95], v[228:231], v[36:39]\n"
  "v_mfma_f32_16x16x32_bf16 v[40:43], v[88:91], v[236:239], v[40:43]\n"
  "v_mfma_f32_16x16x32_bf16 v[44:47], v[92:95], v[236:239], v[44:47]\n"
  "v_mfma_f32_16x16x32_bf16 v[48:51], v[88:91], v[244:247], v[48:51]\n"
  "v_mfma_f32_16x16x32_bf16 v[52:55], v[92:95], v[244:247], v[52:55]\n"
  "v_mfma_f32_16x16x32_bf16 v[56:59], v[88:91], v[252:255], v[56:59]\n"
  "v_mfma_f32_16x16x32_bf16 v[60:63], v[92:95], v[252:255], v[60:63]\n"
  "v_cvt_pk_bf16_f32 v32, v32, v33\n"
  "v_cvt_pk_bf16_f32 v33, v34, v35\n"
  "v_cvt_pk_bf16_f32 v34, v36, v37\n"
  "v_cvt_pk_bf16_f32 v35, v38, v39\n"
  "ds_write_b128 v19, v[32:35] offset:0\n"
  "v_cvt_pk_bf16_f32 v40, v40, v41\n"
  "v_cvt_pk_bf16_f32 v41, v42, v43\n"
  "v_cvt_pk_bf16_f32 v42, v44, v45\n"
  "v_cvt_pk_bf16_f32 v43, v46, v47\n"
  "ds_write_b128 v19, v[40:43] offset:1024\n"
  "v_cvt_pk_bf16_f32 v48, v48, v49\n"
  "v_cvt_pk_bf16_f32 v49, v50, v51\n"
  "v_cvt_pk_bf16_f32 v50, v52, v53\n"
  "v_cvt_pk_bf16_f32 v51, v54, v55\n"
  "ds_write_b128 v19, v[48:51] offset:2048\n"
  "v_cvt_pk_bf16_f32 v56, v56, v57\n"
  "v_cvt_pk_bf16_f32 v57, v58, v59\n"
  "v_cvt_pk_bf16_f32 v58, v60, v61\n"
  "v_cvt_pk_bf16_f32 v59, v62, v63\n"
  "ds_write_b128 v19, v[56:59] offset:3072\n"
  "buffer_load_dword v21, v10, s[16:19], s57 offen sc1\n"
  "s_waitcnt vmcnt(0) lgkmcnt(0)\n"
  "s_branch C_JOIN_%=\n"
  "C_OTHER_%=:\n"
  "s_nop 7\n"
  "s_add_u32 s60, s56, 2\n"
  "s_lshr_b32 s61, s60, 1\n"
  "s_add_u32 s61, s61, s46\n"
  "s_lshl_b32 s57, s61, 18\n"
  "s_lshr_b32 s61, s61, 3\n"
  "s_lshr_b32 s61, s47, s61\n"
  "s_xor_b32 s60, s60, s61\n"
  "s_and_b32 s60, s60, 1\n"
  "s_lshl_b32 s60, s60, 13\n"
  "s_add_u32 s57, s57, s60\n"
  "s_xor_b32 s75, s96, 40960\n"
  "v_add_u32 v17, s75, v0\n"
  "s_sub_u32 s97, 237568, s97\n"
  "s_mul_i32 s60, s44, 5120\n"
  "s_add_u32 s60, s60, s97\n"
  "v_add_u32 v19, s60, v0\n"
  "s_add_u32 s59, s59, s55\n"
  "s_cmp_eq_u32 s44, 1\n"
  "s_cselect_b32 s98, 0, s44\n"
  "s_mul_i32 s68, s56, 81920\n"
  "s_mul_i32 s60, s98, 20480\n"
  "s_add_u32 s68, s68, s60\n"
  "s_add_u32 s69, s68, 5120\n"
  "s_add_u32 s70, s68, 10240\n"
  "s_add_u32 s71, s68, 15360\n"
  "s_mul_i32 s74, s56, 40960\n"
  "s_mul_i32 s60, s98, 10240\n"
  "s_add_u32 s74, s74, s60\n"
  "s_mov_b64 s[66:67], 0\n"
  "ds_read_b128 v[64:67], v17 offset:0\n"
  "ds_read_b128 v[68:71], v17 offset:10240\n"
  "ds_read_b128 v[72:75], v17 offset:20480\n"
  "ds_read_b128 v[76:79], v17 offset:30720\n"
  "s_waitcnt lgkmcnt(3)\n"
  "v_mfma_f32_16x16x32_bf16 v[32:35], v[64:67], a[0:3], 0\n"
  "s_waitcnt lgkmcnt(2)\n"
  "v_mfma_f32_16x16x32_bf16 v[36:39], v[68:71], a[0:3], 0\n"
  "ds_read_b128 v[80:83], v17 offset:1024\n"
  "v_mfma_f32_16x16x32_bf16 v[40:43], v[64:67], a[8:11], 0\n"
  "v_mfma_f32_16x16x32_bf16 v[44:47], v[68:71], a[8:11], 0\n"
  "ds_read_b128 v[84:87], v17 offset:11264\n"
  "v_mfma_f32_16x16x32_bf16 v[48:51], v[64:67], a[16:19], 0\n"
  "v_mfma_f32_16x16x32_bf16 v[52:55], v[68:71], a[16:19], 0\n"
  "ds_read_b128 v[88:91], v17 offset:21504\n"
  "v_mfma_f32_16x16x32_bf16 v[56:59], v[64:67], a[24:27], 0\n"
  "v_mfma_f32_16x16x32_bf16 v[60:63], v[68:71], a[24:27], 0\n"
  "ds_read_b128 v[92:95], v17 offset:31744\n"
  "s_waitcnt lgkmcnt(5)\n"
  "v_mfma_f32_16x16x32_bf16 v[32:35], v[72:75], a[4:7], v[32:35]\n"
  "s_waitcnt lgkmcnt(4)\n"
  "v_mfma_f32_16x16x32_bf16 v[36:39], v[76:79], a[4:7], v[36:39]\n"
  "v_mfma_f32_16x16x32_bf16 v[40:43], v[72:75], a[12:15], v[40:43]\n"
  "v_mfma_f32_16x16x32_bf16 v[44:47], v[76:79], a[12:15], v[44:47]\n"
  "v_mfma_f32_16x16x32_bf16 v[48:51], v[72:75], a[20:23], v[48:51]\n"
  "v_mfma_f32_16x16x32_bf16 v[52:55], v[76:79], a[20:23], v[52:55]\n"
  "v_mfma_f32_16x16x32_bf16 v[56:59], v[72:75], a[28:31], v[56:59]\n"
  "v_mfma_f32_16x16x32_bf16 v[60:63], v[76:79], a[28:31], v[60:63]\n"
  "s_waitcnt lgkmcnt(3)\n"
  "v_mfma_f32_16x16x32_bf16 v[32:35], v[80:83], a[32:35], v[32:35]\n"
  "s_waitcnt lgkmcnt(2)\n"
  "v_mfma_f32_16x16x32_bf16 v[36:39], v[84:87], a[32:35], v[36:39]\n"
  "ds_read_b128 v[64:67], v17 offset:2048\n"
  "v_mfma_f32_16x16x32_bf16 v[40:43], v[80:83], a[40:43], v[40:43]\n"
  "v_mfma_f32_16x16x32_bf16 v[44:47], v[84:87], a[40:43], v[44:47]\n"
  "ds_read_b128 v[68:71], v17 offset:12288\n"
  "v_mfma_f32_16x16x32_bf16 v[48:51], v[80:83], a[48:51], v[48:51]\n"
  "v_mfma_f32_16x16x32_bf16 v[52:55], v[84:87], a[48:51], v[52:55]\n"
  "ds_read_b128 v[72:75], v17 offset:22528\n"
  "v_mfma_f32_16x16x32_bf16 v[56:59], v[80:83], a[56:59], v[56:59]\n"
  "v_mfma_f32_16x16x32_bf16 v[60:63], v[84:87], a[56:59], v[60:63]\n"
  "ds_read_b128 v[76:79], v17 offset:32768\n"
  "s_waitcnt lgkmcnt(5)\n"
  "v_mfma_f32_16x16x32_bf16 v[32:35], v[88:91], a[36:39], v[32:35]\n"
  "s_waitcnt lgkmcnt(4)\n"
  "v_mfma_f32_16x16x32_bf16 v[36:39], v[92:95], a[36:39], v[36:39]\n"
  "v_mfma_f32_16x16x32_bf16 v[40:43], v[88:91], a[44:47], v[40:43]\n"
  "v_mfma_f32_16x16x32_bf16 v[44:47], v[92:95], a[44:47], v[44:47]\n"
  "v_mfma_f32_16x16x32_bf16 v[48:51], v[88:91], a[52:55], v[48:51]\n"
  "v_mfma_f32_16x16x32_bf16 v[52:55], v[92:95], a[52:55], v[52:55]\n"
  "v_mfma_f32_16x16x32_bf16 v[56:59], v[88:91], a[60:63], v[56:59]\n"
  "v_mfma_f32_16x16x32_bf16 v[60:63], v[92:95], a[60:63], v[60:63]\n"
  "s_waitcnt lgkmcnt(3)\n"
  "v_mfma_f32_16x16x32_bf16 v[32:35], v[64:67], a[64:67], v[32:35]\n"
  "s_waitcnt lgkmcnt(2)\n"
  "v_mfma_f32_16x16x32_bf16 v[36:39], v[68:71], a[64:67], v[36:39]\n"
  "ds_read_b128 v[80:83], v17 offset:3072\n"
  "v_mfma_f32_16x16x32_bf16 v[40:43], v[64:67], a[72:75], v[40:43]\n"
  "v_mfma_f32_16x16x32_bf16 v[44:47], v[68:71], a[72:75], v[44:47]\n"
  "ds_read_b128 v[84:87], v17 offset:13312\n"
  "v_mfma_f32_16x16x32_bf16 v[48:51], v[64:67], a[80:83], v[48:51]\n"
  "v_mfma_f32_16x16x32_bf16 v[52:55], v[68:71], a[80:83], v[52:55]\n"
  "ds_read_b128 v[88:91], v17 offset:23552\n"
  "v_mfma_f32_16x16x32_bf16 v[56:59], v[64:67], a[88:91], v[56:59]\n"
  "v_mfma_f32_16x16x32_bf16 v[60:63], v[68:71], a[88:91], v[60:63]\n"
  "ds_read_b128 v[92:95], v17 offset:33792\n"
  "s_waitcnt lgkmcnt(5)\n"
  "v_mfma_f32_16x16x32_bf16 v[32:35], v[72:75], a[68:71], v[32:35]\n"
  "s_waitcnt lgkmcnt(4)\n"
  "v_mfma_f32_16x16x32_bf16 v[36:39], v[76:79], a[68:71], v[36:39]\n"
  "v_mfma_f32_16x16x32_bf16 v[40:43], v[72:75], a[76:79], v[40:43]\n"
  "v_mfma_f32_16x16x32_bf16 v[44:47], v[76:79], a[76:79], v[44:47]\n"
  "v_mfma_f32_16x16x32_bf16 v[48:51], v[72:75], a[84:87], v[48:51]\n"
  "v_mfma_f32_16x16x32_bf16 v[52:55], v[76:79], a[84:87], v[52:55]\n"
  "v_mfma_f32_16x16x32_bf16 v[56:59], v[72:75], a[92:95], v[56:59]\n"
  "v_mfma_f32_16x16x32_bf16 v[60:63], v[76:79], a[92:95], v[60:63]\n"
  "s_waitcnt lgkmcnt(3)\n"
  "v_mfma_f32_16x16x32_bf16 v[32:35], v[80:83], a[96:99], v[32:35]\n"
  "s_waitcnt lgkmcnt(2)\n"
  "v_mfma_f32_16x16x32_bf16 v[36:39], v[84:87], a[96:99], v[36:39]\n"
  "ds_read_b128 v[64:67], v17 offset:4096\n"
  "v_mfma_f32_16x16x32_bf16 v[40:43], v[80:83], a[104:107], v[40:43]\n"
  "v_mfma_f32_16x16x32_bf16 v[44:47], v[84:87], a[104:107], v[44:47]\n"
  "ds_read_b128 v[68:71], v17 offset:14336\n"
  "v_mfma_f32_16x16x32_bf16 v[48:51], v[80:83], a[112:115], v[48:51]\n"
  "v_mfma_f32_16x16x32_bf16 v[52:55], v[84:87], a[112:115], v[52:55]\n"
  "ds_read_b128 v[72:75], v17 offset:24576\n"
  "v_mfma_f32_16x16x32_bf16 v[56:59], v[80:83], a[120:123], v[56:59]\n"
  "v_mfma_f32_16x16x32_bf16 v[60:63], v[84:87], a[120:123], v[60:63]\n"
  "ds_read_b128 v[76:79], v17 offset:34816\n"
  "s_waitcnt lgkmcnt(5)\n"
  "v_mfma_f32_16x16x32_bf16 v[32:35], v[88:91], a[100:103], v[32:35]\n"
  "s_waitcnt lgkmcnt(4)\n"
  "v_mfma_f32_16x16x32_bf16 v[36:39], v[92:95], a[100:103], v[36:39]\n"
  "v_mfma_f32_16x16x32_bf16 v[40:43], v[88:91], a[108:111], v[40:43]\n"
  "v_mfma_f32_16x16x32_bf16 v[44:47], v[92:95], a[108:111], v[44:47]\n"
  "v_mfma_f32_16x16x32_bf16 v[48:51], v[88:91], a[116:119], v[48:51]\n"
  "v_mfma_f32_16x16x32_bf16 v[52:55], v[92:95], a[116:119], v[52:55]\n"
  "v_mfma_f32_16x16x32_bf16 v[56:59], v[88:91], a[124:127], v[56:59]\n"
  "v_mfma_f32_16x16x32_bf16 v[60:63], v[92:95], a[124:127], v[60:63]\n"
  "s_waitcnt lgkmcnt(3)\n"
  "v_mfma_f32_16x16x32_bf16 v[32:35], v[64:67], a[128:131], v[32:35]\n"
  "s_waitcnt lgkmcnt(2)\n"
  "v_mfma_f32_16x16x32_bf16 v[36:39], v[68:71], a[128:131], v[36:39]\n"
  "ds_read_b128 v[80:83], v17 offset:5120\n"
  "v_mfma_f32_16x16x32_bf16 v[40:43], v[64:67], a[136:139], v[40:43]\n"
  "v_mfma_f32_16x16x32_bf16 v[44:47], v[68:71], a[136:139], v[44:47]\n"
  "ds_read_b128 v[84:87], v17 offset:15360\n"
  "v_mfma_f32_16x16x32_bf16 v[48:51], v[64:67], a[144:147], v[48:51]\n"
  "v_mfma_f32_16x16x32_bf16 v[52:55], v[68:71], a[144:147], v[52:55]\n"
  "ds_read_b128 v[88:91], v17 offset:25600\n"
  "v_mfma_f32_16x16x32_bf16 v[56:59], v[64:67], a[152:155], v[56:59]\n"
  "v_mfma_f32_16x16x32_bf16 v[60:63], v[68:71], a[152:155], v[60:63]\n"
  "ds_read_b128 v[92:95], v17 offset:35840\n"
  "s_waitcnt lgkmcnt(5)\n"
  "v_mfma_f32_16x16x32_bf16 v[32:35], v[72:75], a[132:135], v[32:35]\n"
  "s_waitcnt lgkmcnt(4)\n"
  "v_mfma_f32_16x16x32_bf16 v[36:39], v[76:79], a[132:135], v[36:39]\n"
  "v_mfma_f32_16x16x32_bf16 v[40:43], v[72:75], a[140:143], v[40:43]\n"
  "v_mfma_f32_16x16x32_bf16 v[44:47], v[76:79], a[140:143], v[44:47]\n"
  "v_mfma_f32_16x16x32_bf16 v[48:51], v[72:75], a[148:151], v[48:51]\n"
  "v_mfma_f32_16x16x32_bf16 v[52:55], v[76:79], a[148:151], v[52:55]\n"
  "v_mfma_f32_16x16x32_bf16 v[56:59], v[72:75], a[156:159], v[56:59]\n"
  "v_mfma_f32_16x16x32_bf16 v[60:63], v[76:79], a[156:159], v[60:63]\n"
  "s_waitcnt lgkmcnt(0)\n"
  "C_WT3_%=:\n"
  "s_memrealtime s[64:65]\n"
  "s_waitcnt lgkmcnt(0)\n"
  "s_sub_i32 s60, s64, s59\n"
  "s_cmp_lt_i32 s60, 0\n"
  "s_cbranch_scc1 C_WT3_%=\n"
  "v_mfma_f32_16x16x32_bf16 v[32:35], v[80:83], a[160:163], v[32:35]\n"
  "buffer_load_dword v22, v14, s[32:35], s74 offen\n"
  "v_mfma_f32_16x16x32_bf16 v[36:39], v[84:87], a[160:163], v[36:39]\n"
  "ds_read_b128 v[64:67], v17 offset:6144\n"
  "v_mfma_f32_16x16x32_bf16 v[40:43], v[80:83], a[168:171], v[40:43]\n"
  "v_mfma_f32_16x16x32_bf16 v[44:47], v[84:87], a[168:171], v[44:47]\n"
  "ds_read_b128 v[68:71], v17 offset:16384\n"
  "buffer_load_dwordx4 v[112:115], v8, s[20:23], s68 offen offset:0 sc1\n"
  "v_mfma_f32_16x16x32_bf16 v[48:51], v[80:83], a[176:179], v[48:51]\n"
  "v_mfma_f32_16x16x32_bf16 v[52:55], v[84:87], a[176:179], v[52:55]\n"
  "ds_read_b128 v[72:75], v17 offset:26624\n"
  "v_mfma_f32_16x16x32_bf16 v[56:59], v[80:83], a[184:187], v[56:59]\n"
  "buffer_load_dwordx4 v[116:119], v8, s[20:23], s68 offen offset:1280 sc1\n"
  "v_mfma_f32_16x16x32_bf16 v[60:63], v[84:87], a[184:187], v[60:63]\n"
  "ds_read_b128 v[76:79], v17 offset:36864\n"
  "v_mfma_f32_16x16x32_bf16 v[32:35], v[88:91], a[164:167], v[32:35]\n"
  "v_mfma_f32_16x16x32_bf16 v[36:39], v[92:95], a[164:167], v[36:39]\n"
  "buffer_load_dwordx4 v[120:123], v8, s[20:23], s68 offen offset:2560 sc1\n"
  "v_mfma_f32_16x16x32_bf16 v[40:43], v[88:91], a[172:175], v[40:43]\n"
  "v_mfma_f32_16x16x32_bf16 v[44:47], v[92:95], a[172:175], v[44:47]\n"
  "v_mfma_f32_16x16x32_bf16 v[48:51], v[88:91], a[180:183], v[48:51]\n"
  "buffer_load_dwordx4 v[124:127], v8, s[20:23], s68 offen offset:3840 sc1\n"
  "v_mfma_f32_16x16x32_bf16 v[52:55], v[92:95], a[180:183], v[52:55]\n"
  "v_mfma_f32_16x16x32_bf16 v[56:59], v[88:91], a[188:191], v[56:59]\n"
  "v_mfma_f32_16x16x32_bf16 v[60:63], v[92:95], a[188:191], v[60:63]\n"
  "buffer_load_dwordx4 v[128:131], v9, s[20:23], s68 offen sc1\n"
  "s_waitcnt lgkmcnt(3)\n"
  "v_mfma_f32_16x16x32_bf16 v[32:35], v[64:67], a[192:195], v[32:35]\n"
  "s_waitcnt lgkmcnt(2)\n"
  "v_mfma_f32_16x16x32_bf16 v[36:39], v[68:71], a[192:195], v[36:39]\n"
  "ds_read_b128 v[80:83], v17 offset:7168\n"
  "v_mfma_f32_16x16x32_bf16 v[40:43], v[64:67], a[200:203], v[40:43]\n"
  "buffer_load_dwordx4 v[132:135], v8, s[20:23], s69 offen offset:0 sc1\n"
  "v_mfma_f32_16x16x32_bf16 v[44:47], v[68:71], a[200:203], v[44:47]\n"
  "ds_read_b128 v[84:87], v17 offset:17408\n"
  "v_mfma_f32_16x16x32_bf16 v[48:51], v[64:67], a[208:211], v[48:51]\n"
  "v_mfma_f32_16x16x32_bf16 v[52:55], v[68:71], a[208:211], v[52:55]\n"
  "ds_read_b128 v[88:91], v17 offset:27648\n"
  "buffer_load_dwordx4 v[136:139], v8, s[20:23], s69 offen offset:1280 sc1\n"
  "v_mfma_f32_16x16x32_bf16 v[56:59], v[64:67], a[216:219], v[56:59]\n"
  "v_mfma_f32_16x16x32_bf16 v[60:63], v[68:71], a[216:219], v[60:63]\n"
  "ds_read_b128 v[92:95], v17 offset:37888\n"
  "s_waitcnt lgkmcnt(5)\n"
  "v_mfma_f32_16x16x32_bf16 v[32:35], v[72:75], a[196:199], v[32:35]\n"
  "buffer_load_dwordx4 v[140:143], v8, s[20:23], s69 offen offset:2560 sc1\n"
  "s_waitcnt lgkmcnt(4)\n"
  "v_mfma_f32_16x16x32_bf16 v[36:39], v[76:79], a[196:199], v[36:39]\n"
  "v_mfma_f32_16x16x32_bf16 v[40:43], v[72:75], a[204:207], v[40:43]\n"
  "v_mfma_f32_16x16x32_bf16 v[44:47], v[76:79], a[204:207], v[44:47]\n"
  "buffer_load_dwordx4 v[144:147], v8, s[20:23], s69 offen offset:3840 sc1\n"
  "v_mfma_f32_16x16x32_bf16 v[48:51], v[72:75], a[212:215], v[48:51]\n"
  "v_mfma_f32_16x16x32_bf16 v[52:55], v[76:79], a[212:215], v[52:55]\n"
  "v_mfma_f32_16x16x32_bf16 v[56:59], v[72:75], a[220:223], v[56:59]\n"
  "buffer_load_dwordx4 v[148:151], v9, s[20:23], s69 offen sc1\n"
  "v_mfma_f32_16x16x32_bf16 v[60:63], v[76:79], a[220:223], v[60:63]\n"
  "s_waitcnt lgkmcnt(3)\n"
  "v_mfma_f32_16x16x32_bf16 v[32:35], v[80:83], a[224:227], v[32:35]\n"
  "s_waitcnt lgkmcnt(2)\n"
  "v_mfma_f32_16x16x32_bf16 v[36:39], v[84:87], a[224:227], v[36:39]\n"
  "ds_read_b128 v[64:67], v17 offset:8192\n"
  "buffer_load_dwordx4 v[152:155], v8, s[20:23], s70 offen offset:0 sc1\n"
  "v_mfma_f32_16x16x32_bf16 v[40:43], v[80:83], a[232:235], v[40:43]\n"
  "v_mfma_f32_16x16x32_bf16 v[44:47], v[84:87], a[232:235], v[44:47]\n"
  "ds_read_b128 v[68:71], v17 offset:18432\n"
  "v_mfma_f32_16x16x32_bf16 v[48:51], v[80:83], a[240:243], v[48:51]\n"
  "buffer_load_dwordx4 v[156:159], v8, s[20:23], s70 offen offset:1280 sc1\n"
  "v_mfma_f32_16x16x32_bf16 v[52:55], v[84:87], a[240:243], v[52:55]\n"
  "ds_read_b128 v[72:75], v17 offset:28672\n"
  "v_mfma_f32_16x16x32_bf16 v[56:59], v[80:83], a[248:251], v[56:59]\n"
  "v_mfma_f32_16x16x32_bf16 v[60:63], v[84:87], a[248:251], v[60:63]\n"
  "ds_read_b128 v[76:79], v17 offset:38912\n"
  "buffer_load_dwordx4 v[160:163], v8, s[20:23], s70 offen offset:2560 sc1\n"
  "s_waitcnt lgkmcnt(5)\n"
  "v_mfma_f32_16x16x32_bf16 v[32:35], v[88:91], a[228:231], v[32:35]\n"
  "s_waitcnt lgkmcnt(4)\n"
  "v_mfma_f32_16x16x32_bf16 v[36:39], v[92:95], a[228:231], v[36:39]\n"
  "v_mfma_f32_16x16x32_bf16 v[40:43], v[88:91], a[236:239], v[40:43]\n"
  "buffer_load_dwordx4 v[164:167], v8, s[20:23], s70 offen offset:3840 sc1\n"
  "v_mfma_f32_16x16x32_bf16 v[44:47], v[92:95], a[236:239], v[44:47]\n"
  "v_mfma_f32_16x16x32_bf16 v[48:51], v[88:91], a[244:247], v[48:51]\n"
  "v_mfma_f32_16x16x32_bf16 v[52:55], v[92:95], a[244:247], v[52:55]\n"
  "buffer_load_dwordx4 v[168:171], v9, s[20:23], s70 offen sc1\n"
  "v_mfma_f32_16x16x32_bf16 v[56:59], v[88:91], a[252:255], v[56:59]\n"
  "v_mfma_f32_16x16x32_bf16 v[60:63], v[92:95], a[252:255], v[60:63]\n"
  "s_waitcnt lgkmcnt(3)\n"
  "v_mfma_f32_16x16x32_bf16 v[32:35], v[64:67], v[192:195], v[32:35]\n"
  "buffer_load_dwordx4 v[172:175], v8, s[20:23], s71 offen offset:0 sc1\n"
  "s_waitcnt lgkmcnt(2)\n"
  "v_mfma_f32_16x16x32_bf16 v[36:39], v[68:71], v[192:195], v[36:39]\n"
  "ds_read_b128 v[80:83], v17 offset:9216\n"
  "v_mfma_f32_16x16x32_bf16 v[40:43], v[64:67], v[200:203], v[40:43]\n"
  "v_mfma_f32_16x16x32_bf16 v[44:47], v[68:71], v[200:203], v[44:47]\n"
  "ds_read_b128 v[84:87], v17 offset:19456\n"
  "buffer_load_dwordx4 v[176:179], v8, s[20:23], s71 offen offset:1280 sc1\n"
  "v_mfma_f32_16x16x32_bf16 v[48:51], v[64:67], v[208:211], v[48:51]\n"
  "v_mfma_f32_16x16x32_bf16 v[52:55], v[68:71], v[208:211], v[52:55]\n"
  "ds_read_b128 v[88:91], v17 offset:29696\n"
  "v_mfma_f32_16x16x32_bf16 v[56:59], v[64:67], v[216:219], v[56:59]\n"
  "buffer_load_dwordx4 v[180:183], v8, s[20:23], s71 offen offset:2560 sc1\n"
  "v_mfma_f32_16x16x32_bf16 v[60:63], v[68:71], v[216:219], v[60:63]\n"
  "ds_read_b128 v[92:95], v17 offset:39936\n"
  "s_waitcnt lgkmcnt(5)\n"
  "v_mfma_f32_16x16x32_bf16 v[32:35], v[72:75], v[196:199], v[32:35]\n"
  "s_waitcnt lgkmcnt(4)\n"
  "v_mfma_f32_16x16x32_bf16 v[36:39], v[76:79], v[196:199], v[36:39]\n"
  "buffer_load_dwordx4 v[184:187], v8, s[20:23], s71 offen offset:3840 sc1\n"
  "v_mfma_f32_16x16x32_bf16 v[40:43], v[72:75], v[204:207], v[40:43]\n"
  "v_mfma_f32_16x16x32_bf16 v[44:47], v[76:79], v[204:207], v[44:47]\n"
  "v_mfma_f32_16x16x32_bf16 v[48:51], v[72:75], v[212:215], v[48:51]\n"
  "buffer_load_dwordx4 v[188:191], v9, s[20:23], s71 offen sc1\n"
  "v_mfma_f32_16x16x32_bf16 v[52:55], v[76:79], v[212:215], v[52:55]\n"
  "v_mfma_f32_16x16x32_bf16 v[56:59], v[72:75], v[220:223], v[56:59]\n"
  "v_mfma_f32_16x16x32_bf16 v[60:63], v[76:79], v[220:223], v[60:63]\n"
  "buffer_load_dwordx4 v[96:99], v13, s[20:23], s68 offen offset:0 sc1\n"
  "s_waitcnt lgkmcnt(3)\n"
  "v_mfma_f32_16x16x32_bf16 v[32:35], v[80:83], v[224:227], v[32:35]\n"
  "s_waitcnt lgkmcnt(2)\n"
  "v_mfma_f32_16x16x32_bf16 v[36:39], v[84:87], v[224:227], v[36:39]\n"
  "v_mfma_f32_16x16x32_bf16 v[40:43], v[80:83], v[232:235], v[40:43]\n"
  "buffer_load_dwordx4 v[100:103], v13, s[20:23], s68 offen offset:1024 sc1\n"
  "v_mfma_f32_16x16x32_bf16 v[44:47], v[84:87], v[232:235], v[44:47]\n"
  "v_mfma_f32_16x16x32_bf16 v[48:51], v[80:83], v[240:243], v[48:51]\n"
  "v_mfma_f32_16x16x32_bf16 v[52:55], v[84:87], v[240:243], v[52:55]\n"
  "buffer_load_dwordx4 v[104:107], v13, s[20:23], s68 offen offset:2048 sc1\n"
  "v_mfma_f32_16x16x32_bf16 v[56:59], v[80:83], v[248:251], v[56:59]\n"
  "v_mfma_f32_16x16x32_bf16 v[60:63], v[84:87], v[248:251], v[60:63]\n"
  "s_waitcnt lgkmcnt(1)\n"
  "v_mfma_f32_16x16x32_bf16 v[32:35], v[88:91], v[228:231], v[32:35]\n"
  "buffer_load_dwordx4 v[108:111], v13, s[20:23], s68 offen offset:3072 sc1\n"
  "s_waitcnt lgkmcnt(0)\n"
  "v_mfma_f32_16x16x32_bf16 v[36:39], v[92:95], v[228:231], v[36:39]\n"
  "v_mfma_f32_16x16x32_bf16 v[40:43], v[88:91], v[236:239], v[40:43]\n"
  "v_mfma_f32_16x16x32_bf16 v[44:47], v[92:95], v[236:239], v[44:47]\n"
  "v_mfma_f32_16x16x32_bf16 v[48:51], v[88:91], v[244:247], v[48:51]\n"
  "v_mfma_f32_16x16x32_bf16 v[52:55], v[92:95], v[244:247], v[52:55]\n"
  "v_mfma_f32_16x16x32_bf16 v[56:59], v[88:91], v[252:255], v[56:59]\n"
  "v_mfma_f32_16x16x32_bf16 v[60:63], v[92:95], v[252:255], v[60:63]\n"
  "v_cvt_pk_bf16_f32 v32, v32, v33\n"
  "v_cvt_pk_bf16_f32 v33, v34, v35\n"
  "v_cvt_pk_bf16_f32 v34, v36, v37\n"
  "v_cvt_pk_bf16_f32 v35, v38, v39\n"
  "ds_write_b128 v19, v[32:35] offset:0\n"
  "v_cvt_pk_bf16_f32 v40, v40, v41\n"
  "v_cvt_pk_bf16_f32 v41, v42, v43\n"
  "v_cvt_pk_bf16_f32 v42, v44, v45\n"
  "v_cvt_pk_bf16_f32 v43, v46, v47\n"
  "ds_write_b128 v19, v[40:43] offset:1024\n"
  "v_cvt_pk_bf16_f32 v48, v48, v49\n"
  "v_cvt_pk_bf16_f32 v49, v50, v51\n"
  "v_cvt_pk_bf16_f32 v50, v52, v53\n"
  "v_cvt_pk_bf16_f32 v51, v54, v55\n"
  "ds_write_b128 v19, v[48:51] offset:2048\n"
  "v_cvt_pk_bf16_f32 v56, v56, v57\n"
  "v_cvt_pk_bf16_f32 v57, v58, v59\n"
  "v_cvt_pk_bf16_f32 v58, v60, v61\n"
  "v_cvt_pk_bf16_f32 v59, v62, v63\n"
  "ds_write_b128 v19, v[56:59] offset:3072\n"
  "s_sub_u32 s98, s44, 1\n"
  "s_add_u32 s60, s56, 4\n"
  "s_mul_i32 s72, s60, 40960\n"
  "s_mul_i32 s73, s98, 13312\n"
  "s_add_u32 s72, s72, s73\n"
  "s_add_u32 s73, s73, s96\n"
  "s_mov_b32 m0, s73\n"
  "s_nop 0\n"
  "buffer_load_dwordx4 v0, s[24:27], s72 offen offset:0 lds\n"
  "buffer_load_dwordx4 v0, s[24:27], s72 offen offset:1024 lds\n"
  "buffer_load_dwordx4 v0, s[24:27], s72 offen offset:2048 lds\n"
  "buffer_load_dwordx4 v0, s[24:27], s72 offen offset:3072 lds\n"
  "s_add_u32 s72, s72, 4096\n"
  "s_add_u32 s73, s73, 4096\n"
  "s_mov_b32 m0, s73\n"
  "s_nop 0\n"
  "buffer_load_dwordx4 v0, s[24:27], s72 offen offset:0 lds\n"
  "buffer_load_dwordx4 v0, s[24:27], s72 offen offset:1024 lds\n"
  "buffer_load_dwordx4 v0, s[24:27], s72 offen offset:2048 lds\n"
  "buffer_load_dwordx4 v0, s[24:27], s72 offen offset:3072 lds\n"
  "s_add_u32 s72, s72, 4096\n"
  "s_add_u32 s73, s73, 4096\n"
  "s_mov_b32 m0, s73\n"
  "s_nop 0\n"
  "buffer_load_dwordx4 v0, s[24:27], s72 offen offset:0 lds\n"
  "buffer_load_dwordx4 v0, s[24:27], s72 offen offset:1024 lds\n"
  "buffer_load_dwordx4 v0, s[24:27], s72 offen offset:2048 lds\n"
  "buffer_load_dwordx4 v0, s[24:27], s72 offen offset:3072 lds\n"
  "s_add_u32 s72, s72, 4096\n"
  "s_add_u32 s73, s73, 4096\n"
  "s_mov_b32 m0, s73\n"
  "s_nop 0\n"
  "buffer_load_dwordx4 v0, s[24:27], s72 offen offset:0 lds\n"
  "buffer_load_dwordx4 v0, s[24:27], s72 offen offset:1024 lds\n"
  "buffer_load_dword v21, v10, s[16:19], s57 offen sc1\n"
  "s_cmp_eq_u32 s44, 1\n"
  "s_cbranch_scc1 C_W1_%=\n"
  "ds_read_b128 v[32:35], v18 offset:0\n"
  "ds_read_b128 v[36:39], v18 offset:1024\n"
  "ds_read_b128 v[40:43], v18 offset:2048\n"
  "ds_read_b128 v[44:47], v18 offset:3072\n"
  "ds_read_b128 v[48:51], v18 offset:4096\n"
  "ds_read_b128 v[52:55], v18 offset:5120\n"
  "ds_read_b128 v[56:59], v18 offset:6144\n"
  "ds_read_b128 v[60:63], v18 offset:7168\n"
  "ds_read_b128 v[64:67], v18 offset:8192\n"
  "ds_read_b128 v[68:71], v18 offset:9216\n"
  "s_waitcnt lgkmcnt(10)\n"
  "ds_read_b128 v[72:75], v18 offset:10240\n"
  "ds_read_b128 v[76:79], v18 offset:11264\n"
  "ds_read_b128 v[80:83], v18 offset:12288\n"
  "ds_read_b128 v[84:87], v18 offset:13312\n"
  "s_waitcnt lgkmcnt(10)\n"
  "ds_read_b128 v[88:91], v18 offset:14336\n"
  "ds_read_b128 v[92:95], v18 offset:15360\n"
  "s_waitcnt vmcnt(18)\n"
  "ds_read_b128 v[96:99], v18 offset:16384\n"
  "s_waitcnt vmcnt(17)\n"
  "ds_read_b128 v[100:103], v18 offset:17408\n"
  "s_waitcnt vmcnt(16) lgkmcnt(10)\n"
  "ds_read_b128 v[104:107], v18 offset:18432\n"
  "s_waitcnt vmcnt(15)\n"
  "ds_read_b128 v[108:111], v18 offset:19456\n"
  "s_waitcnt lgkmcnt(0)\n"
  "C_X2_%=:\n"
  "v_mfma_f32_16x16x32_bf16 v[24:27], v[32:35], v[112:115], 0\n"
  "v_mfma_f32_16x16x32_bf16 v[24:27], v[36:39], v[116:119], v[24:27]\n"
  "v_mfma_f32_16x16x32_bf16 v[24:27], v[40:43], v[120:123], v[24:27]\n"
  "v_mfma_f32_16x16x32_bf16 v[24:27], v[44:47], v[124:127], v[24:27]\n"
  "v_mfma_f32_16x16x32_bf16 v[24:27], v[48:51], v[128:131], v[24:27]\n"
  "v_mfma_f32_16x16x32_bf16 v[24:27], v[52:55], v[132:135], v[24:27]\n"
  "v_mfma_f32_16x16x32_bf16 v[24:27], v[56:59], v[136:139], v[24:27]\n"
  "v_mfma_f32_16x16x32_bf16 v[24:27], v[60:63], v[140:143], v[24:27]\n"
  "v_mfma_f32_16x16x32_bf16 v[24:27], v[64:67], v[144:147], v[24:27]\n"
  "v_mfma_f32_16x16x32_bf16 v[24:27], v[68:71], v[148:151], v[24:27]\n"
  "v_mfma_f32_16x16x32_bf16 v[24:27], v[72:75], v[152:155], v[24:27]\n"
  "v_mfma_f32_16x16x32_bf16 v[24:27], v[76:79], v[156:159], v[24:27]\n"
  "v_mfma_f32_16x16x32_bf16 v[24:27], v[80:83], v[160:163], v[24:27]\n"
  "v_mfma_f32_16x16x32_bf16 v[24:27], v[84:87], v[164:167], v[24:27]\n"
  "v_mfma_f32_16x16x32_bf16 v[24:27], v[88:91], v[168:171], v[24:27]\n"
  "v_mfma_f32_16x16x32_bf16 v[24:27], v[92:95], v[172:175], v[24:27]\n"
  "v_mfma_f32_16x16x32_bf16 v[24:27], v[96:99], v[176:179], v[24:27]\n"
  "v_mfma_f32_16x16x32_bf16 v[24:27], v[100:103], v[180:183], v[24:27]\n"
  "v_mfma_f32_16x16x32_bf16 v[24:27], v[104:107], v[184:187], v[24:27]\n"
  "v_mfma_f32_16x16x32_bf16 v[24:27], v[108:111], v[188:191], v[24:27]\n"
  "s_nop 7\n"
  "v_add_f32_dpp v24, v26, v24 row_ror:8 row_mask:0xf bank_mask:0xf\n"
  "v_add_f32_dpp v25, v27, v25 row_ror:8 row_mask:0xf bank_mask:0xf\n"
  "v_cmp_u_f32 vcc, v24, v24\n"
  "v_lshlrev_b32 v28, 16, v22\n"
  "v_and_b32 v29, 0xffff0000, v22\n"
  "s_andn2_b64 vcc, vcc, s[66:67]\n"
  "v_add_f32 v24, v24, v28\n"
  "v_add_f32 v25, v25, v29\n"
  "s_cbranch_vccnz C_XREDO_%=\n"
  "v_cvt_pk_bf16_f32 v24, v24, v25\n"
  "buffer_store_dword v24, v14, s[36:39], s74 offen\n"
  "s_waitcnt vmcnt(1)\n"
  "s_branch C_JOIN_%=\n"
  "C_W1_%=:\n"
  "ds_read_b64 v[32:33], v18 offset:0\n"
  "ds_read_b64 v[34:35], v18 offset:1024\n"
  "ds_read_b64 v[36:37], v18 offset:2048\n"
  "ds_read_b64 v[38:39], v18 offset:3072\n"
  "ds_read_b128 v[64:67], v18 offset:4096\n"
  "ds_read_b64 v[40:41], v18 offset:5120\n"
  "ds_read_b64 v[42:43], v18 offset:6144\n"
  "ds_read_b64 v[44:45], v18 offset:7168\n"
  "ds_read_b64 v[46:47], v18 offset:8192\n"
  "ds_read_b128 v[68:71], v18 offset:9216\n"
  "s_waitcnt lgkmcnt(10)\n"
  "ds_read_b64 v[48:49], v18 offset:10240\n"
  "ds_read_b64 v[50:51], v18 offset:11264\n"
  "ds_read_b64 v[52:53], v18 offset:12288\n"
  "ds_read_b64 v[54:55], v18 offset:13312\n"
  "s_waitcnt lgkmcnt(10)\n"
  "ds_read_b128 v[72:75], v18 offset:14336\n"
  "ds_read_b64 v[56:57], v18 offset:15360\n"
  "ds_read_b64 v[58:59], v18 offset:16384\n"
  "ds_read_b64 v[60:61], v18 offset:17408\n"
  "s_waitcnt lgkmcnt(10)\n"
  "ds_read_b64 v[62:63], v18 offset:18432\n"
  "ds_read_b128 v[76:79], v18 offset:19456\n"
  "s_waitcnt lgkmcnt(0)\n"
  "C_X1_%=:\n"
  "s_waitcnt vmcnt(38)\n"
  "v_mfma_f32_16x16x16_bf16 v[80:83], v[32:33], v[112:113], 0\n"
  "v_mfma_f32_16x16x16_bf16 v[84:87], v[32:33], v[114:115], 0\n"
  "s_waitcnt vmcnt(37)\n"
  "v_mfma_f32_16x16x16_bf16 v[80:83], v[34:35], v[116:117], v[80:83]\n"
  "v_mfma_f32_16x16x16_bf16 v[84:87], v[34:35], v[118:119], v[84:87]\n"
  "s_waitcnt vmcnt(36)\n"
  "v_mfma_f32_16x16x16_bf16 v[80:83], v[36:37], v[120:121], v[80:83]\n"
  "v_mfma_f32_16x16x16_bf16 v[84:87], v[36:37], v[122:123], v[84:87]\n"
  "s_waitcnt vmcnt(35)\n"
  "v_mfma_f32_16x16x16_bf16 v[80:83], v[38:39], v[124:125], v[80:83]\n"
  "v_mfma_f32_16x16x16_bf16 v[84:87], v[38:39], v[126:127], v[84:87]\n"
  "s_waitcnt vmcnt(33)\n"
  "v_mfma_f32_16x16x16_bf16 v[80:83], v[40:41], v[132:133], v[80:83]\n"
  "v_mfma_f32_16x16x16_bf16 v[84:87], v[40:41], v[134:135], v[84:87]\n"
  "s_waitcnt vmcnt(32)\n"
  "v_mfma_f32_16x16x16_bf16 v[80:83], v[42:43], v[136:137], v[80:83]\n"
  "v_mfma_f32_16x16x16_bf16 v[84:87], v[42:43], v[138:139], v[84:87]\n"
  "s_waitcnt vmcnt(31)\n"
  "v_mfma_f32_16x16x16_bf16 v[80:83], v[44:45], v[140:141], v[80:83]\n"
  "v_mfma_f32_16x16x16_bf16 v[84:87], v[44:45], v[142:143], v[84:87]\n"
  "s_waitcnt vmcnt(30)\n"
  "v_mfma_f32_16x16x16_bf16 v[80:83], v[46:47], v[144:145], v[80:83]\n"
  "v_mfma_f32_16x16x16_bf16 v[84:87], v[46:47], v[146:147], v[84:87]\n"
  "s_waitcnt vmcnt(28)\n"
  "v_mfma_f32_16x16x16_bf16 v[80:83], v[48:49], v[152:153], v[80:83]\n"
  "v_mfma_f32_16x16x16_bf16 v[84:87], v[48:49], v[154:155], v[84:87]\n"
  "s_waitcnt vmcnt(27)\n"
  "v_mfma_f32_16x16x16_bf16 v[80:83], v[50:51], v[156:157], v[80:83]\n"
  "v_mfma_f32_16x16x16_bf16 v[84:87], v[50:51], v[158:159], v[84:87]\n"
  "s_waitcnt vmcnt(26)\n"
  "v_mfma_f32_16x16x16_bf16 v[80:83], v[52:53], v[160:161], v[80:83]\n"
  "v_mfma_f32_16x16x16_bf16 v[84:87], v[52:53], v[162:163], v[84:87]\n"
  "s_waitcnt vmcnt(25)\n"
  "v_mfma_f32_16x16x16_bf16 v[80:83], v[54:55], v[164:165], v[80:83]\n"
  "v_mfma_f32_16x16x16_bf16 v[84:87], v[54:55], v[166:167], v[84:87]\n"
  "s_waitcnt vmcnt(23)\n"
  "v_mfma_f32_16x16x16_bf16 v[80:83], v[56:57], v[172:173], v[80:83]\n"
  "v_mfma_f32_16x16x16_bf16 v[84:87], v[56:57], v[174:175], v[84:87]\n"
  "s_waitcnt vmcnt(22)\n"
  "v_mfma_f32_16x16x16_bf16 v[80:83], v[58:59], v[176:177], v[80:83]\n"
  "v_mfma_f32_16x16x16_bf16 v[84:87], v[58:59], v[178:179], v[84:87]\n"
  "s_waitcnt vmcnt(21)\n"
  "v_mfma_f32_16x16x16_bf16 v[80:83], v[60:61], v[180:181], v[80:83]\n"
  "v_mfma_f32_16x16x16_bf16 v[84:87], v[60:61], v[182:183], v[84:87]\n"
  "s_waitcnt vmcnt(20)\n"
  "v_mfma_f32_16x16x16_bf16 v[80:83], v[62:63], v[184:185], v[80:83]\n"
  "v_mfma_f32_16x16x16_bf16 v[84:87], v[62:63], v[186:187], v[84:87]\n"
  "v_mfma_f32_16x16x32_bf16 v[88:91], v[64:67], v[128:131], 0\n"
  "s_waitcnt vmcnt(18)\n"
  "v_mfma_f32_16x16x32_bf16 v[92:95], v[64:67], v[96:99], 0\n"
  "v_mfma_f32_16x16x32_bf16 v[88:91], v[68:71], v[148:151], v[88:91]\n"
  "s_waitcnt vmcnt(17)\n"
  "v_mfma_f32_16x16x32_bf16 v[92:95], v[68:71], v[100:103], v[92:95]\n"
  "v_mfma_f32_16x16x32_bf16 v[88:91], v[72:75], v[168:171], v[88:91]\n"
  "s_waitcnt vmcnt(16)\n"
  "v_mfma_f32_16x16x32_bf16 v[92:95], v[72:75], v[104:107], v[92:95]\n"
  "v_mfma_f32_16x16x32_bf16 v[88:91], v[76:79], v[188:191], v[88:91]\n"
  "s_waitcnt vmcnt(15)\n"
  "v_mfma_f32_16x16x32_bf16 v[92:95], v[76:79], v[108:111], v[92:95]\n"
  "s_nop 5\n"
  "v_pk_add_f32 v[80:81], v[80:81], v[88:89]\n"
  "v_pk_add_f32 v[82:83], v[82:83], v[90:91]\n"
  "v_pk_add_f32 v[84:85], v[84:85], v[92:93]\n"
  "v_pk_add_f32 v[86:87], v[86:87], v[94:95]\n"
  "v_add_f32_dpp v24, v82, v80 row_ror:8 row_mask:0xf bank_mask:0x3\n"
  "v_add_f32_dpp v25, v83, v81 row_ror:8 row_mask:0xf bank_mask:0x3\n"
  "v_add_f32_dpp v24, v84, v86 row_ror:8 row_mask:0xf bank_mask:0xc\n"
  "v_add_f32_dpp v25, v85, v87 row_ror:8 row_mask:0xf bank_mask:0xc\n"
  "v_cmp_u_f32 vcc, v24, v24\n"
  "v_lshlrev_b32 v28, 16, v22\n"
  "v_and_b32 v29, 0xffff0000, v22\n"
  "s_andn2_b64 vcc, vcc, s[66:67]\n"
  "v_add_f32 v24, v24, v28\n"
  "v_add_f32 v25, v25, v29\n"
  "s_cbranch_vccnz C_X1REDO_%=\n"
  "v_cvt_pk_bf16_f32 v24, v24, v25\n"
  "buffer_store_dword v24, v14, s[36:39], s74 offen\n"
  "s_waitcnt vmcnt(1)\n"
  "C_JOIN_%=:\n"
  "s_mov_b32 s96, s75\n"
  "s_add_u32 s56, s56, 2\n"
  "s_cmp_lt_u32 s56, 64\n"
  "s_cbranch_scc1 C_LOOP_%=\n"
  "s_branch C_EXIT_%=\n"
  "C_MBAD_%=:\n"
  "s_nop 7\n"
  "s_add_u32 s62, s62, 1\n"
  "s_cmp_gt_u32 s62, 300000\n"
  "s_cbranch_scc1 C_FAIL_%=\n"
  "s_and_b32 s60, s62, 255\n"
  "s_cmp_lg_u32 s60, 0\n"
  "s_cbranch_scc1 C_MP_%=\n"
  "v_mov_b32 v15, 0\n"
  "buffer_load_dword v15, v15, s[40:43], 0 offen sc1\n"
  "s_waitcnt vmcnt(0)\n"
  "v_readfirstlane_b32 s60, v15\n"
  "s_cmp_lg_u32 s60, 0\n"
  "s_cbranch_scc1 C_EXIT_%=\n"
  "s_branch C_MP_%=\n"
  "C_REDO_%=:\n"
  "s_nop 7\n"
  "s_waitcnt vmcnt(0) lgkmcnt(0)\n"
  "s_mov_b32 s62, 0\n"
  "C_REDOP_%=:\n"
  "s_nop 7\n"
  "buffer_load_dwordx4 v[112:115], v6, s[16:19], s68 offen offset:0 sc1\n"
  "buffer_load_dwordx4 v[116:119], v6, s[16:19], s68 offen offset:1280 sc1\n"
  "buffer_load_dwordx4 v[120:123], v6, s[16:19], s68 offen offset:2560 sc1\n"
  "buffer_load_dwordx4 v[124:127], v6, s[16:19], s68 offen offset:3840 sc1\n"
  "buffer_load_dwordx4 v[128:131], v7, s[16:19], s68 offen sc1\n"
  "buffer_load_dwordx4 v[132:135], v6, s[16:19], s69 offen offset:0 sc1\n"
  "buffer_load_dwordx4 v[136:139], v6, s[16:19], s69 offen offset:1280 sc1\n"
  "buffer_load_dwordx4 v[140:143], v6, s[16:19], s69 offen offset:2560 sc1\n"
  "buffer_load_dwordx4 v[144:147], v6, s[16:19], s69 offen offset:3840 sc1\n"
  "buffer_load_dwordx4 v[148:151], v7, s[16:19], s69 offen sc1\n"
  "buffer_load_dwordx4 v[152:155], v6, s[16:19], s70 offen offset:0 sc1\n"
  "buffer_load_dwordx4 v[156:159], v6, s[16:19], s70 offen offset:1280 sc1\n"
  "buffer_load_dwordx4 v[160:163], v6, s[16:19], s70 offen offset:2560 sc1\n"
  "buffer_load_dwordx4 v[164:167], v6, s[16:19], s70 offen offset:3840 sc1\n"
  "buffer_load_dwordx4 v[168:171], v7, s[16:19], s70 offen sc1\n"
  "buffer_load_dwordx4 v[172:175], v6, s[16:19], s71 offen offset:0 sc1\n"
  "buffer_load_dwordx4 v[176:179], v6, s[16:19], s71 offen offset:1280 sc1\n"
  "buffer_load_dwordx4 v[180:183], v6, s[16:19], s71 offen offset:2560 sc1\n"
  "buffer_load_dwordx4 v[184:187], v6, s[16:19], s71 offen offset:3840 sc1\n"
  "buffer_load_dwordx4 v[188:191], v7, s[16:19], s71 offen sc1\n"
  "s_waitcnt vmcnt(19)\n"
  "v_max3_u32 v15, v112, v113, v114\n"
  "s_waitcnt vmcnt(18)\n"
  "v_max3_u32 v15, v115, v116, v15\n"
  "v_max3_u32 v15, v117, v118, v15\n"
  "s_waitcnt vmcnt(17)\n"
  "v_max3_u32 v15, v119, v120, v15\n"
  "v_max3_u32 v15, v121, v122, v15\n"
  "s_waitcnt vmcnt(16)\n"
  "v_max3_u32 v15, v123, v124, v15\n"
  "v_max3_u32 v15, v125, v126, v15\n"
  "s_waitcnt vmcnt(15)\n"
  "v_max3_u32 v15, v127, v128, v15\n"
  "v_max3_u32 v15, v129, v130, v15\n"
  "s_waitcnt vmcnt(14)\n"
  "v_max3_u32 v15, v131, v132, v15\n"
  "v_max3_u32 v15, v133, v134, v15\n"
  "s_waitcnt vmcnt(13)\n"
  "v_max3_u32 v15, v135, v136, v15\n"
  "v_max3_u32 v15, v137, v138, v15\n"
  "s_waitcnt vmcnt(12)\n"
  "v_max3_u32 v15, v139, v140, v15\n"
  "v_max3_u32 v15, v141, v142, v15\n"
  "s_waitcnt vmcnt(11)\n"
  "v_max3_u32 v15, v143, v144, v15\n"
  "v_max3_u32 v15, v145, v146, v15\n"
  "s_waitcnt vmcnt(10)\n"
  "v_max3_u32 v15, v147, v148, v15\n"
  "v_max3_u32 v15, v149, v150, v15\n"
  "s_waitcnt vmcnt(9)\n"
  "v_max3_u32 v15, v151, v152, v15\n"
  "v_max3_u32 v15, v153, v154, v15\n"
  "s_waitcnt vmcnt(8)\n"
  "v_max3_u32 v15, v155, v156, v15\n"
  "v_max3_u32 v15, v157, v158, v15\n"
  "s_waitcnt vmcnt(7)\n"
  "v_max3_u32 v15, v159, v160, v15\n"
  "v_max3_u32 v15, v161, v162, v15\n"
  "s_waitcnt vmcnt(6)\n"
  "v_max3_u32 v15, v163, v164, v15\n"
  "v_max3_u32 v15, v165, v166, v15\n"
  "s_waitcnt vmcnt(5)\n"
  "v_max3_u32 v15, v167, v168, v15\n"
  "v_max3_u32 v15, v169, v170, v15\n"
  "s_waitcnt vmcnt(4)\n"
  "v_max3_u32 v15, v171, v172, v15\n"
  "v_max3_u32 v15, v173, v174, v15\n"
  "s_waitcnt vmcnt(3)\n"
  "v_max3_u32 v15, v175, v176, v15\n"
  "v_max3_u32 v15, v177, v178, v15\n"
  "s_waitcnt vmcnt(2)\n"
  "v_max3_u32 v15, v179, v180, v15\n"
  "v_max3_u32 v15, v181, v182, v15\n"
  "s_waitcnt vmcnt(1)\n"
  "v_max3_u32 v15, v183, v184, v15\n"
  "v_max3_u32 v15, v185, v186, v15\n"
  "s_waitcnt vmcnt(0)\n"
  "v_max3_u32 v15, v187, v188, v15\n"
  "v_max3_u32 v15, v189, v190, v15\n"
  "v_max_u32 v15, v191, v15\n"
  "s_waitcnt vmcnt(0) lgkmcnt(0)\n"
  "v_cmp_eq_u32 vcc, -1, v15\n"
  "s_nop 1\n"
  "s_mov_b64 s[66:67], -1\n"
  "s_cbranch_vccz C_P2_%=\n"
  "s_add_u32 s62, s62, 1\n"
  "s_cmp_gt_u32 s62, 300000\n"
  "s_cbranch_scc1 C_FAIL_%=\n"
  "s_and_b32 s60, s62, 255\n"
  "s_cmp_lg_u32 s60, 0\n"
  "s_cbranch_scc1 C_REDOP_%=\n"
  "v_mov_b32 v15, 0\n"
  "buffer_load_dword v15, v15, s[40:43], 0 offen sc1\n"
  "s_waitcnt vmcnt(0)\n"
  "v_readfirstlane_b32 s60, v15\n"
  "s_cmp_lg_u32 s60, 0\n"
  "s_cbranch_scc1 C_EXIT_%=\n"
  "s_branch C_REDOP_%=\n"
  "C_XREDO_%=:\n"
  "s_nop 7\n"
  "s_waitcnt vmcnt(0) lgkmcnt(0)\n"
  "s_mov_b32 s62, 0\n"
  "C_XREDOP_%=:\n"
  "s_nop 7\n"
  "buffer_load_dwordx4 v[112:115], v8, s[20:23], s68 offen offset:0 sc1\n"
  "buffer_load_dwordx4 v[116:119], v8, s[20:23], s68 offen offset:1280 sc1\n"
  "buffer_load_dwordx4 v[120:123], v8, s[20:23], s68 offen offset:2560 sc1\n"
  "buffer_load_dwordx4 v[124:127], v8, s[20:23], s68 offen offset:3840 sc1\n"
  "buffer_load_dwordx4 v[128:131], v9, s[20:23], s68 offen sc1\n"
  "buffer_load_dwordx4 v[132:135], v8, s[20:23], s69 offen offset:0 sc1\n"
  "buffer_load_dwordx4 v[136:139], v8, s[20:23], s69 offen offset:1280 sc1\n"
  "buffer_load_dwordx4 v[140:143], v8, s[20:23], s69 offen offset:2560 sc1\n"
  "buffer_load_dwordx4 v[144:147], v8, s[20:23], s69 offen offset:3840 sc1\n"
  "buffer_load_dwordx4 v[148:151], v9, s[20:23], s69 offen sc1\n"
  "buffer_load_dwordx4 v[152:155], v8, s[20:23], s70 offen offset:0 sc1\n"
  "buffer_load_dwordx4 v[156:159], v8, s[20:23], s70 offen offset:1280 sc1\n"
  "buffer_load_dwordx4 v[160:163], v8, s[20:23], s70 offen offset:2560 sc1\n"
  "buffer_load_dwordx4 v[164:167], v8, s[20:23], s70 offen offset:3840 sc1\n"
  "buffer_load_dwordx4 v[168:171], v9, s[20:23], s70 offen sc1\n"
  "buffer_load_dwordx4 v[172:175], v8, s[20:23], s71 offen offset:0 sc1\n"
  "buffer_load_dwordx4 v[176:179], v8, s[20:23], s71 offen offset:1280 sc1\n"
  "buffer_load_dwordx4 v[180:183], v8, s[20:23], s71 offen offset:2560 sc1\n"
  "buffer_load_dwordx4 v[184:187], v8, s[20:23], s71 offen offset:3840 sc1\n"
  "buffer_load_dwordx4 v[188:191], v9, s[20:23], s71 offen sc1\n"
  "s_waitcnt vmcnt(19)\n"
  "v_max3_u32 v15, v112, v113, v114\n"
  "s_waitcnt vmcnt(18)\n"
  "v_max3_u32 v15, v115, v116, v15\n"
  "v_max3_u32 v15, v117, v118, v15\n"
  "s_waitcnt vmcnt(17)\n"
  "v_max3_u32 v15, v119, v120, v15\n"
  "v_max3_u32 v15, v121, v122, v15\n"
  "s_waitcnt vmcnt(16)\n"
  "v_max3_u32 v15, v123, v124, v15\n"
  "v_max3_u32 v15, v125, v126, v15\n"
  "s_waitcnt vmcnt(15)\n"
  "v_max3_u32 v15, v127, v128, v15\n"
  "v_max3_u32 v15, v129, v130, v15\n"
  "s_waitcnt vmcnt(14)\n"
  "v_max3_u32 v15, v131, v132, v15\n"
  "v_max3_u32 v15, v133, v134, v15\n"
  "s_waitcnt vmcnt(13)\n"
  "v_max3_u32 v15, v135, v136, v15\n"
  "v_max3_u32 v15, v137, v138, v15\n"
  "s_waitcnt vmcnt(12)\n"
  "v_max3_u32 v15, v139, v140, v15\n"
  "v_max3_u32 v15, v141, v142, v15\n"
  "s_waitcnt vmcnt(11)\n"
  "v_max3_u32 v15, v143, v144, v15\n"
  "v_max3_u32 v15, v145, v146, v15\n"
  "s_waitcnt vmcnt(10)\n"
  "v_max3_u32 v15, v147, v148, v15\n"
  "v_max3_u32 v15, v149, v150, v15\n"
  "s_waitcnt vmcnt(9)\n"
  "v_max3_u32 v15, v151, v152, v15\n"
  "v_max3_u32 v15, v153, v154, v15\n"
  "s_waitcnt vmcnt(8)\n"
  "v_max3_u32 v15, v155, v156, v15\n"
  "v_max3_u32 v15, v157, v158, v15\n"
  "s_waitcnt vmcnt(7)\n"
  "v_max3_u32 v15, v159, v160, v15\n"
  "v_max3_u32 v15, v161, v162, v15\n"
  "s_waitcnt vmcnt(6)\n"
  "v_max3_u32 v15, v163, v164, v15\n"
  "v_max3_u32 v15, v165, v166, v15\n"
  "s_waitcnt vmcnt(5)\n"
  "v_max3_u32 v15, v167, v168, v15\n"
  "v_max3_u32 v15, v169, v170, v15\n"
  "s_waitcnt vmcnt(4)\n"
  "v_max3_u32 v15, v171, v172, v15\n"
  "v_max3_u32 v15, v173, v174, v15\n"
  "s_waitcnt vmcnt(3)\n"
  "v_max3_u32 v15, v175, v176, v15\n"
  "v_max3_u32 v15, v177, v178, v15\n"
  "s_waitcnt vmcnt(2)\n"
  "v_max3_u32 v15, v179, v180, v15\n"
  "v_max3_u32 v15, v181, v182, v15\n"
  "s_waitcnt vmcnt(1)\n"
  "v_max3_u32 v15, v183, v184, v15\n"
  "v_max3_u32 v15, v185, v186, v15\n"
  "s_waitcnt vmcnt(0)\n"
  "v_max3_u32 v15, v187, v188, v15\n"
  "v_max3_u32 v15, v189, v190, v15\n"
  "v_max_u32 v15, v191, v15\n"
  "s_waitcnt vmcnt(0) lgkmcnt(0)\n"
  "v_cmp_eq_u32 vcc, -1, v15\n"
  "s_nop 1\n"
  "s_mov_b64 s[66:67], -1\n"
  "s_cbranch_vccz C_X2_%=\n"
  "s_add_u32 s62, s62, 1\n"
  "s_cmp_gt_u32 s62, 300000\n"
  "s_cbranch_scc1 C_FAIL_%=\n"
  "s_and_b32 s60, s62, 255\n"
  "s_cmp_lg_u32 s60, 0\n"
  "s_cbranch_scc1 C_XREDOP_%=\n"
  "v_mov_b32 v15, 0\n"
  "buffer_load_dword v15, v15, s[40:43], 0 offen sc1\n"
  "s_waitcnt vmcnt(0)\n"
  "v_readfirstlane_b32 s60, v15\n"
  "s_cmp_lg_u32 s60, 0\n"
  "s_cbranch_scc1 C_EXIT_%=\n"
  "s_branch C_XREDOP_%=\n"
  "C_X1REDO_%=:\n"
  "s_nop 7\n"
  "s_waitcnt vmcnt(0) lgkmcnt(0)\n"
  "s_mov_b32 s62, 0\n"
  "C_X1REDOP_%=:\n"
  "s_nop 7\n"
  "buffer_load_dwordx4 v[112:115], v8, s[20:23], s68 offen offset:0 sc1\n"
  "buffer_load_dwordx4 v[116:119], v8, s[20:23], s68 offen offset:1280 sc1\n"
  "buffer_load_dwordx4 v[120:123], v8, s[20:23], s68 offen offset:2560 sc1\n"
  "buffer_load_dwordx4 v[124:127], v8, s[20:23], s68 offen offset:3840 sc1\n"
  "buffer_load_dwordx4 v[128:131], v9, s[20:23], s68 offen sc1\n"
  "buffer_load_dwordx4 v[132:135], v8, s[20:23], s69 offen offset:0 sc1\n"
  "buffer_load_dwordx4 v[136:139], v8, s[20:23], s69 offen offset:1280 sc1\n"
  "buffer_load_dwordx4 v[140:143], v8, s[20:23], s69 offen offset:2560 sc1\n"
  "buffer_load_dwordx4 v[144:147], v8, s[20:23], s69 offen offset:3840 sc1\n"
  "buffer_load_dwordx4 v[148:151], v9, s[20:23], s69 offen sc1\n"
  "buffer_load_dwordx4 v[152:155], v8, s[20:23], s70 offen offset:0 sc1\n"
  "buffer_load_dwordx4 v[156:159], v8, s[20:23], s70 offen offset:1280 sc1\n"
  "buffer_load_dwordx4 v[160:163], v8, s[20:23], s70 offen offset:2560 sc1\n"
  "buffer_load_dwordx4 v[164:167], v8, s[20:23], s70 offen offset:3840 sc1\n"
  "buffer_load_dwordx4 v[168:171], v9, s[20:23], s70 offen sc1\n"
  "buffer_load_dwordx4 v[172:175], v8, s[20:23], s71 offen offset:0 sc1\n"
  "buffer_load_dwordx4 v[176:179], v8, s[20:23], s71 offen offset:1280 sc1\n"
  "buffer_load_dwordx4 v[180:183], v8, s[20:23], s71 offen offset:2560 sc1\n"
  "buffer_load_dwordx4 v[184:187], v8, s[20:23], s71 offen offset:3840 sc1\n"
  "buffer_load_dwordx4 v[188:191], v9, s[20:23], s71 offen sc1\n"
  "buffer_load_dwordx4 v[96:99], v13, s[20:23], s68 offen offset:0 sc1\n"
  "buffer_load_dwordx4 v[100:103], v13, s[20:23], s68 offen offset:1024 sc1\n"
  "buffer_load_dwordx4 v[104:107], v13, s[20:23], s68 offen offset:2048 sc1\n"
  "buffer_load_dwordx4 v[108:111], v13, s[20:23], s68 offen offset:3072 sc1\n"
  "s_waitcnt vmcnt(3)\n"
  "v_max3_u32 v15, v96, v97, v98\n"
  "s_waitcnt vmcnt(2)\n"
  "v_max3_u32 v15, v99, v100, v15\n"
  "v_max3_u32 v15, v101, v102, v15\n"
  "s_waitcnt vmcnt(1)\n"
  "v_max3_u32 v15, v103, v104, v15\n"
  "v_max3_u32 v15, v105, v106, v15\n"
  "s_waitcnt vmcnt(0)\n"
  "v_max3_u32 v15, v107, v108, v15\n"
  "v_max3_u32 v15, v109, v110, v15\n"
  "v_max3_u32 v15, v111, v112, v15\n"
  "v_max3_u32 v15, v113, v114, v15\n"
  "v_max3_u32 v15, v115, v116, v15\n"
  "v_max3_u32 v15, v117, v118, v15\n"
  "v_max3_u32 v15, v119, v120, v15\n"
  "v_max3_u32 v15, v121, v122, v15\n"
  "v_max3_u32 v15, v123, v124, v15\n"
  "v_max3_u32 v15, v125, v126, v15\n"
  "v_max3_u32 v15, v127, v128, v15\n"
  "v_max3_u32 v15, v129, v130, v15\n"
  "v_max3_u32 v15, v131, v132, v15\n"
  "v_max3_u32 v15, v133, v134, v15\n"
  "v_max3_u32 v15, v135, v136, v15\n"
  "v_max3_u32 v15, v137, v138, v15\n"
  "v_max3_u32 v15, v139, v140, v15\n"
  "v_max3_u32 v15, v141, v142, v15\n"
  "v_max3_u32 v15, v143, v144, v15\n"
  "v_max3_u32 v15, v145, v146, v15\n"
  "v_max3_u32 v15, v147, v148, v15\n"
  "v_max3_u32 v15, v149, v150, v15\n"
  "v_max3_u32 v15, v151, v152, v15\n"
  "v_max3_u32 v15, v153, v154, v15\n"
  "v_max3_u32 v15, v155, v156, v15\n"
  "v_max3_u32 v15, v157, v158, v15\n"
  "v_max3_u32 v15, v159, v160, v15\n"
  "v_max3_u32 v15, v161, v162, v15\n"
  "v_max3_u32 v15, v163, v164, v15\n"
  "v_max3_u32 v15, v165, v166, v15\n"
  "v_max3_u32 v15, v167, v168, v15\n"
  "v_max3_u32 v15, v169, v170, v15\n"
  "v_max3_u32 v15, v171, v172, v15\n"
  "v_max3_u32 v15, v173, v174, v15\n"
  "v_max3_u32 v15, v175, v176, v15\n"
  "v_max3_u32 v15, v177, v178, v15\n"
  "v_max3_u32 v15, v179, v180, v15\n"
  "v_max3_u32 v15, v181, v182, v15\n"
  "v_max3_u32 v15, v183, v184, v15\n"
  "v_max3_u32 v15, v185, v186, v15\n"
  "v_max3_u32 v15, v187, v188, v15\n"
  "v_max3_u32 v15, v189, v190, v15\n"
  "v_max_u32 v15, v191, v15\n"
  "s_waitcnt vmcnt(0) lgkmcnt(0)\n"
  "v_cmp_eq_u32 vcc, -1, v15\n"
  "s_nop 1\n"
  "s_mov_b64 s[66:67], -1\n"
  "s_cbranch_vccz C_X1_%=\n"
  "s_add_u32 s62, s62, 1\n"
  "s_cmp_gt_u32 s62, 300000\n"
  "s_cbranch_scc1 C_FAIL_%=\n"
  "s_and_b32 s60, s62, 255\n"
  "s_cmp_lg_u32 s60, 0\n"
  "s_cbranch_scc1 C_X1REDOP_%=\n"
  "v_mov_b32 v15, 0\n"
  "buffer_load_dword v15, v15, s[40:43], 0 offen sc1\n"
  "s_waitcnt vmcnt(0)\n"
  "v_readfirstlane_b32 s60, v15\n"
  "s_cmp_lg_u32 s60, 0\n"
  "s_cbranch_scc1 C_EXIT_%=\n"
  "s_branch C_X1REDOP_%=\n"
  "C_FAIL_%=:\n"
  "s_nop 7\n"
  "v_mov_b32 v32, 0\n"
  "v_mov_b32 v33, 1\n"
  "buffer_store_dword v33, v32, s[40:43], 0 offen sc1\n"
  "C_EXIT_%=:\n"
  "s_nop 7\n"
  "s_waitcnt vmcnt(0)\n"
  "s_endpgm\n"
      :: "{s[16:31]}"(sa), "{s[32:47]}"(sb), "{s[48:55]}"(sc), "{s79}"(dbgoff), "{v[0:15]}"(va), "{v[16:23]}"(vb) : "v24", "v25", "v26", "v27", "v28", "v29", "v30", "v31", "v32", "v33", "v34", "v35", "v36", "v37", "v38", "v39", "v40", "v41", "v42", "v43", "v44", "v45", "v46", "v47", "v48", "v49", "v50", "v51", "v52", "v53", "v54", "v55", "v56", "v57", "v58", "v59", "v60", "v61", "v62", "v63", "v64", "v65", "v66", "v67", "v68", "v69", "v70", "v71", "v72", "v73", "v74", "v75", "v76", "v77", "v78", "v79", "v80", "v81", "v82", "v83", "v84", "v85", "v86", "v87", "v88", "v89", "v90", "v91", "v92", "v93", "v94", "v95", "v96", "v97", "v98", "v99", "v100", "v101", "v102", "v103", "v104", "v105", "v106", "v107", "v108", "v109", "v110", "v111", "v112", "v113", "v114", "v115", "v116", "v117", "v118", "v119", "v120", "v121", "v122", "v123", "v124", "v125", "v126", "v127", "v128", "v129", "v130", "v131", "v132", "v133", "v134", "v135", "v136", "v137", "v138", "v139", "v140", "v141", "v142", "v143", "v144", "v145", "v146", "v147", "v148", "v149", "v150", "v151", "v152", "v153", "v154", "v155", "v156", "v157", "v158", "v159", "v160", "v161", "v162", "v163", "v164", "v165", "v166", "v167", "v168", "v169", "v170", "v171", "v172", "v173", "v174", "v175", "v176", "v177", "v178", "v179", "v180", "v181", "v182", "v183", "v184", "v185", "v186", "v187", "v188", "v189", "v190", "v191", "v192", "v193", "v194", "v195", "v196", "v197", "v198", "v199", "v200", "v201", "v202", "v203", "v204", "v205", "v206", "v207", "v208", "v209", "v210", "v211", "v212", "v213", "v214", "v215", "v216", "v217", "v218", "v219", "v220", "v221", "v222", "v223", "v224", "v225", "v226", "v227", "v228", "v229", "v230", "v231", "v232", "v233", "v234", "v235", "v236", "v237", "v238", "v239", "v240", "v241", "v242", "v243", "v244", "v245", "v246", "v247", "v248", "v249", "v250", "v251", "v252", "v253", "v254", "v255", "a0", "a1", "a2", "a3", "a4", "a5", "a6", "a7", "a8", "a9", "a10", "a11", "a12", "a13", "a14", "a15", "a16", "a17", "a18", "a19", "a20", "a21", "a22", "a23", "a24", "a25", "a26", "a27", "a28", "a29", "a30", "a31", "a32", "a33", "a34", "a35", "a36", "a37", "a38", "a39", "a40", "a41", "a42", "a43", "a44", "a45", "a46", "a47", "a48", "a49", "a50", "a51", "a52", "a53", "a54", "a55", "a56", "a57", "a58", "a59", "a60", "a61", "a62", "a63", "a64", "a65", "a66", "a67", "a68", "a69", "a70", "a71", "a72", "a73", "a74", "a75", "a76", "a77", "a78", "a79", "a80", "a81", "a82", "a83", "a84", "a85", "a86", "a87", "a88", "a89", "a90", "a91", "a92", "a93", "a94", "a95", "a96", "a97", "a98", "a99", "a100", "a101", "a102", "a103", "a104", "a105", "a106", "a107", "a108", "a109", "a110", "a111", "a112", "a113", "a114", "a115", "a116", "a117", "a118", "a119", "a120", "a121", "a122", "a123", "a124", "a125", "a126", "a127", "a128", "a129", "a130", "a131", "a132", "a133", "a134", "a135", "a136", "a137", "a138", "a139", "a140", "a141", "a142", "a143", "a144", "a145", "a146", "a147", "a148", "a149", "a150", "a151", "a152", "a153", "a154", "a155", "a156", "a157", "a158", "a159", "a160", "a161", "a162", "a163", "a164", "a165", "a166", "a167", "a168", "a169", "a170", "a171", "a172", "a173", "a174", "a175", "a176", "a177", "a178", "a179", "a180", "a181", "a182", "a183", "a184", "a185", "a186", "a187", "a188", "a189", "a190", "a191", "a192", "a193", "a194", "a195", "a196", "a197", "a198", "a199", "a200", "a201", "a202", "a203", "a204", "a205", "a206", "a207", "a208", "a209", "a210", "a211", "a212", "a213", "a214", "a215", "a216", "a217", "a218", "a219", "a220", "a221", "a222", "a223", "a224", "a225", "a226", "a227", "a228", "a229", "a230", "a231", "a232", "a233", "a234", "a235", "a236", "a237", "a238", "a239", "a240", "a241", "a242", "a243", "a244", "a245", "a246", "a247", "a248", "a249", "a250", "a251", "a252", "a253", "a254", "a255", "s8", "s9", "s10", "s11", "s12", "s13", "s14", "s15", "s56", "s57", "s58", "s59", "s60", "s61", "s62", "s63", "s64", "s65", "s66", "s67", "s68", "s69", "s70", "s71", "s72", "s73", "s74", "s75", "s76", "s77", "s78", "s80", "s81", "s82", "s83", "s84", "s85", "s86", "s87", "s88", "s89", "s90", "s91", "s92", "s93", "s94", "s95", "s96", "s97", "s98", "s99", "vcc", "scc", "memory");
}

static int envi(const char*, int d) { return d; }
extern "C" void kernel_launch(void* const* d_in, const int* in_sizes, int n_in, void* d_out, int out_size, void* d_ws, size_t ws_size, hipStream_t stream) {
  const u16* x0 = (const u16*)d_in[0]; const u16* wqkv = (const u16*)d_in[1]; const u16* wo = (const u16*)d_in[2];
  const u16* mem0 = (const u16*)d_in[4]; const u16* Wq = (const u16*)d_in[5]; const u16* Wk = (const u16*)d_in[6]; const u16* Wv = (const u16*)d_in[7]; const u16* Wo = (const u16*)d_in[8];
  unsigned char* ws = (unsigned char*)(((uintptr_t)d_ws + (2u << 20) - 1) & ~(uintptr_t)((2u << 20) - 1));
  hipFuncSetAttribute((const void*)rec_kernel, hipFuncAttributeMaxDynamicSharedMemorySize, 163840);
  hipMemsetAsync(ws + OFF_COMM, 0xFF, SZ_COMM + 2 * SZ_PXL, stream);
  prep_misc<<<1, 256, 0, stream>>>((const u32x4*)mem0, ws + OFF_COMM, (unsigned*)(ws + OFF_CTL));
  prep_hf<<<32 * 4 * 10 * 6 * 2 * 64 / 256, 256, 0, stream>>>(Wq, Wk, (u32x4*)(ws + OFF_HF));
  prep_cf<<<256, 256, 0, stream>>>(Wv, Wo, (u32x4*)(ws + OFF_CF));
  const u16* xin = x0;
  for (int layer = 0; layer < 2; ++layer) {
    u16* x1 = (u16*)(ws + OFF_X1);
    u16* xo = layer == 0 ? (u16*)(ws + OFF_X2) : (u16*)d_out;
    ln_rows<<<512, 256, 0, stream>>>(xin, (u16*)(ws + OFF_XN), 0);
    gemm_naive<<<dim3(30, 128), 256, 0, stream>>>((const u16*)(ws + OFF_XN), wqkv, nullptr, (u16*)(ws + OFF_QKV), 1920, 0);
    kv_naive<<<32, 448, 0, stream>>>((const u16*)(ws + OFF_QKV), (float*)(ws + OFF_KV));
    o_naive<<<2048, 640, 0, stream>>>((const u16*)(ws + OFF_QKV), (const float*)(ws + OFF_KV), (u16*)(ws + OFF_O));
    gemm_naive<<<dim3(10, 128), 256, 0, stream>>>((const u16*)(ws + OFF_O), wo, xin, x1, 640, 1);
    ln_rows<<<512, 256, 0, stream>>>(x1, (u16*)(ws + OFF_XNF), 1);
    RecArgs a;
    a.hf = ws + OFF_HF; a.cf = ws + OFF_CF; a.xnf = ws + OFF_XNF; a.xres = x1; a.xout = xo;
    a.comm = ws + OFF_COMM; a.px = ws + OFF_PX + layer * SZ_PXL; a.ctl = (unsigned*)(ws + OFF_CTL); a.layer = layer;
    a.dp = envi("R_DP", 122); a.dm = envi("R_DM", 64); a.skew = envi("R_SKEW", 20); a.delta = envi("R_DELTA", 10); a.lat = envi("R_LAT", 50); a.pubw = envi("R_PUBW", 50); a.pad = 0;
    rec_kernel<<<256, 256, 163840, stream>>>(a);
    xin = xo;
  }
}
```

```cpp
#include <hip/hip_runtime.h>
#include <hip/hip_bf16.h>
#include <stdint.h>

typedef __attribute__((ext_vector_type(8))) __bf16 bf16x8;
typedef __attribute__((ext_vector_type(4))) __bf16 bf16x4;
typedef __attribute__((ext_vector_type(2))) __bf16 bf16x2;
typedef __attribute__((ext_vector_type(4))) short s16x4;
typedef __attribute__((ext_vector_type(4))) float f32x4;
typedef __attribute__((ext_vector_type(4))) unsigned u32x4;
typedef __attribute__((ext_vector_type(2))) unsigned u32x2;
typedef __attribute__((ext_vector_type(16))) unsigned u32x16;
typedef __attribute__((ext_vector_type(8))) unsigned u32x8;
typedef unsigned short u16;
typedef unsigned char u8;
#define GAS __attribute__((address_space(1)))
#define LAS __attribute__((address_space(3)))
#define CAT4(a, b) __builtin_shufflevector(a, b, 0,1,2,3,4,5,6,7)
#define MFMA32(a, b, c) __builtin_amdgcn_mfma_f32_16x16x32_bf16(a, b, c, 0, 0, 0)
#define Z4 ((f32x4){0.f, 0.f, 0.f, 0.f})
#define DEV __device__ __forceinline__
#define SB() __builtin_amdgcn_sched_barrier(0)

constexpr unsigned SZ_X = 2048 * 640 * 2, SEGB = 40960;
constexpr unsigned SZ_RING = 2u << 20, PXB = 1u << 20, FLB = 7u << 18, RING_FILL = FLB + 400 * 128;
constexpr size_t OFF_O = SZ_RING;
constexpr size_t OFF_XR = OFF_O + 2 * SZ_X;
constexpr size_t OFF_X2 = OFF_XR + 2 * SZ_X;
constexpr size_t OFF_XNF = OFF_X2 + SZ_X;
constexpr size_t OFF_PQ = OFF_XNF + 2 * SZ_X + 4 * SEGB;
constexpr size_t OFF_KVP = OFF_PQ + (4u << 20);
constexpr size_t OFF_HF = OFF_KVP + (1u << 20);
constexpr size_t OFF_CF = OFF_HF + (size_t)2 * 640 * 6144 * 2;
constexpr size_t OFF_CTL = OFF_CF + (size_t)32 * 640 * 640 * 2;

#define TL(i)
#define TP(i)
struct Args { const u16* x0; const u16* wqkv; const u16* wo; const u16* mem0; const u16* Wq; const u16* Wk; const u16* Wv; const u16* Wo; u16* out; u8* ws; int dp, dm, skew, delta, lat, pubw, marg, pad; };

DEV float rbf(float f) { return (float)(__bf16)f; }
DEV bf16x4 cvt4(f32x4 v) { bf16x4 r; r[0] = (__bf16)v[0]; r[1] = (__bf16)v[1]; r[2] = (__bf16)v[2]; r[3] = (__bf16)v[3]; return r; }
DEV int pi16(int p) { return 8 * ((p >> 1) & 1) + 2 * (p >> 2) + (p & 1); }
DEV int drow(int u, int gb, int j) { return 16 * (2 * u + (gb >> 1)) + pi16(8 * (gb & 1) + j); }
DEV bf16x8 tr8(const u8* p0, const u8* p1) {
  s16x4 a = __builtin_amdgcn_ds_read_tr16_b64_v4i16((LAS s16x4*)p0), b = __builtin_amdgcn_ds_read_tr16_b64_v4i16((LAS s16x4*)p1);
  return __builtin_bit_cast(bf16x8, CAT4(a, b));
}
DEV unsigned mx4(u32x4 v) { return max(max(v.x, v.y), max(v.z, v.w)); }
DEV u32x4 canon4(u32x4 v) { return __builtin_elementwise_min(v, (u32x4){0xFFFFFFFEu, 0xFFFFFFFEu, 0xFFFFFFFEu, 0xFFFFFFFEu}); }
DEV float lo16(unsigned v) { return __uint_as_float(v << 16); }
DEV float hi16(unsigned v) { return __uint_as_float(v & 0xFFFF0000u); }
DEV unsigned pk2(float a, float b) { bf16x2 p; p[0] = (__bf16)a; p[1] = (__bf16)b; return __builtin_bit_cast(unsigned, p); }
DEV unsigned pkc(float a, float b) { return min(pk2(a, b), 0xFFFFFFFEu); }
DEV __amdgpu_buffer_rsrc_t rsrc(const void* p, unsigned bytes) { return __builtin_amdgcn_make_buffer_rsrc((void*)p, 0, (int)bytes, 0x00020000); }
DEV bool spin_fail(unsigned& spins, unsigned* ctl) {
  if (++spins > 300000u) { __hip_atomic_store((GAS unsigned*)ctl, 1u, __ATOMIC_RELAXED, __HIP_MEMORY_SCOPE_AGENT); return true; }
  return (spins & 63u) == 0u && __builtin_amdgcn_readfirstlane(__hip_atomic_load((GAS unsigned*)ctl, __ATOMIC_RELAXED, __HIP_MEMORY_SCOPE_AGENT));
}
DEV float phi(float t) { return (t > 0.f ? t + 1.f : __expf(t)) + 1e-6f; }

DEV unsigned wimg(int k, int tile) { return k * 128 + ((tile ^ (((k >> 1) & 1) | (((k >> 3) & 1) << 1))) * 32); }
template <bool FRAG>
DEV void lin_stage_w(const u16* __restrict__ wqkv, int h, u8* WI, int tid) {
  const int pc = tid & 15, tile = pc >> 2, p = pc & 3;
  const int col = 20 * h + (tile == 0 ? 640 + 4 * p : tile == 1 ? 1280 + 4 * p : tile == 3 ? 4 * p : (p == 0 ? 656 : p == 1 ? 1296 : 16));
  u32x2 v[40];
#pragma unroll
  for (int i = 0; i < 40; ++i) {
    const int k = 16 * i, r = tid >> 4;
    v[i] = __builtin_amdgcn_raw_buffer_load_b64(rsrc(wqkv, 640 * 3840), (FRAG ? r : (r >> 3) * 32 + (r & 7)) * 3840 + col * 2, (FRAG ? k : (k & ~127) + ((k >> 3) & 3) * 32 + ((k >> 5) & 3) * 8) * 3840, 0);
  }
  u8* d = WI + wimg(tid >> 4, tile) + p * 8;
#pragma unroll
  for (int i = 0; i < 40; ++i) *(u32x2*)(d + i * 2048) = pc == 11 ? (u32x2){0x3F80u, 0u} : v[i];
}
struct LinW { unsigned a[4]; float sN[3]; f32x4 sT[2]; };
DEV bf16x8 lin_wfrag(const u8* WI, const LinW& w, int tile, int ks) { return tr8(WI + w.a[tile] + ks * 4096, WI + w.a[tile] + ks * 4096 + 512); }
DEV void lin_init(const u8* WI, LinW& w, int lane) {
  const int g = lane >> 4, q = (lane & 15) >> 2, p = lane & 3;
#pragma unroll
  for (int t = 0; t < 4; ++t) w.a[t] = wimg(8 * g + q, t) + p * 8;
  bf16x8 one;
#pragma unroll
  for (int j = 0; j < 8; ++j) one[j] = (__bf16)1.f;
  f32x4 n0 = Z4, n1 = Z4, n2 = Z4, t0 = Z4, t1 = Z4;
#pragma unroll 1
  for (int k0 = 0; k0 < 20; k0 += 4) {
    bf16x8 wf[4][4];
#pragma unroll
    for (int i = 0; i < 4; ++i)
#pragma unroll
      for (int t = 0; t < 4; ++t) wf[i][t] = lin_wfrag(WI, w, t, k0 + i);
    SB();
#pragma unroll
    for (int i = 0; i < 4; ++i) { n0 = MFMA32(one, wf[i][0], n0); n1 = MFMA32(one, wf[i][1], n1); n2 = MFMA32(one, wf[i][2], n2); t0 = MFMA32(wf[i][3], one, t0); t1 = MFMA32(wf[i][2], one, t1); }
  }
  w.sN[0] = n0[0]; w.sN[1] = n1[0]; w.sN[2] = n2[0]; w.sT[0] = t0; w.sT[1] = t1;
}
DEV u32x4 lin_ld_rm(__amdgpu_buffer_rsrc_t rx, int tile, int i, int lane) {
  return __builtin_amdgcn_raw_buffer_load_b128(rx, ((lane >> 2) & 3) * 5120 + (lane >> 4) * 64 + (lane & 3) * 16 + (i & 3) * 1280, tile * 20480 + (i >> 2) * 256, 0);
}
#define QP(x, c) (unsigned)__builtin_amdgcn_update_dpp(0, (int)(x), c, 0xF, 0xF, true)
DEV void quad_tr(u32x4& a, u32x4& b, u32x4& c, u32x4& d, int lane) {
  const bool o1 = lane & 1, o2 = lane & 2;
#pragma unroll
  for (int k = 0; k < 4; ++k) {
    const unsigned a0 = a[k], b0 = b[k], c0 = c[k], d0 = d[k];
    const unsigned s1 = QP(o1 ? a0 : b0, 0xB1), s2 = QP(o1 ? c0 : d0, 0xB1);
    const unsigned a1 = o1 ? s1 : a0, b1 = o1 ? b0 : s1, c1 = o1 ? s2 : c0, d1 = o1 ? d0 : s2;
    const unsigned s3 = QP(o2 ? a1 : c1, 0x4E), s4 = QP(o2 ? b1 : d1, 0x4E);
    a[k] = o2 ? s3 : a1; c[k] = o2 ? c1 : s3; b[k] = o2 ? s4 : b1; d[k] = o2 ? d1 : s4;
  }
}
template <bool FRAG>
DEV void lin_load(__amdgpu_buffer_rsrc_t rx, int tile, u32x4 (&x)[20], int lane) {
#pragma unroll
  for (int i = 0; i < 20; ++i) { x[i] = FRAG ? __builtin_amdgcn_raw_buffer_load_b128(rx, lane * 16 + (i & 3) * 1024, tile * 20480 + (i >> 2) * 4096, 16) : lin_ld_rm(rx, tile, i, lane); SB(); }
}
template <bool FRAG, bool PF>
DEV bool lin_tile(u32x4 (&x)[20], __amdgpu_buffer_rsrc_t rx, int tile, const u8* WI, const LinW& w, f32x4 (&kv)[4], u8* pq, int h, int lane, bool check, unsigned pfo = 0) {
  const int g = lane >> 4, n16 = lane & 15;
  f32x4 n0 = Z4, n1 = Z4, n2 = Z4, t0 = Z4, t1 = Z4, gr = Z4;
  bf16x8 wf[2][4];
#pragma unroll
  for (int t = 0; t < 4; ++t) wf[0][t] = lin_wfrag(WI, w, t, 0);
#pragma unroll
  for (int i = 0; i < 20; ++i) {
    if (i < 19) {
#pragma unroll
      for (int t = 0; t < 4; ++t) wf[(i + 1) & 1][t] = lin_wfrag(WI, w, t, i + 1);
    }
    if (!FRAG && i == 0) quad_tr(x[0], x[1], x[2], x[3], lane);
    SB();
    if (!FRAG && (i & 3) == 3 && i < 19) quad_tr(x[i + 1], x[i + 2], x[i + 3], x[i + 4], lane);
    const bf16x8 xf = __builtin_bit_cast(bf16x8, x[i]);
    gr = MFMA32(xf, xf, gr); n0 = MFMA32(xf, wf[i & 1][0], n0); n1 = MFMA32(xf, wf[i & 1][1], n1); n2 = MFMA32(xf, wf[i & 1][2], n2);
    t1 = MFMA32(wf[i & 1][2], xf, t1); t0 = MFMA32(wf[i & 1][3], xf, t0);
    if (PF) x[i] = FRAG ? __builtin_amdgcn_raw_buffer_load_b128(rx, pfo + (i & 3) * 1024, (tile + 4) * 20480 + (i >> 2) * 4096, 16) : lin_ld_rm(rx, tile + 4, i, lane);
    SB();
  }
  const float s1 = __shfl(t1[0], 48 + n16);
  if (check && __any(s1 != s1)) return false;
  const float dg = (n16 & 2) ? ((n16 & 1) ? gr[3] : gr[2]) : ((n16 & 1) ? gr[1] : gr[0]);
  const float s2 = __shfl(dg, (n16 >> 2) * 16 + n16);
  const float mu = s1 * (1.f / 640.f), rs = rsqrtf(fmaxf(s2 * (1.f / 640.f) - mu * mu, 0.f) + 1e-5f);
  f32x4 q0, q1, k0, v0, mx;
#pragma unroll
  for (int i = 0; i < 4; ++i) {
    const float mi = __shfl(mu, 4 * g + i), ri = __shfl(rs, 4 * g + i);
    k0[i] = phi(rbf(ri * (n0[i] - mi * w.sN[0])));
    v0[i] = ri * (n1[i] - mi * w.sN[1]);
    const float t = rbf(ri * (n2[i] - mi * w.sN[2]));
    mx[i] = n16 < 4 ? phi(t) : (n16 < 8 ? t : (n16 == 12 ? 1.f : 0.f));
    q0[i] = phi(rbf(rs * (t0[i] - mu * w.sT[0][i])));
    q1[i] = g == 2 ? phi(rbf(rs * (t1[i] - mu * w.sT[1][i]))) : 0.f;
  }
  const bf16x4 z = cvt4(Z4);
  const bf16x8 K = CAT4(cvt4(k0), z), V = CAT4(cvt4(v0), z), M = CAT4(cvt4(mx), z);
  kv[0] = MFMA32(K, V, kv[0]); kv[1] = MFMA32(K, M, kv[1]); kv[2] = MFMA32(M, V, kv[2]); kv[3] = MFMA32(M, M, kv[3]);
  *(bf16x8*)(pq + ((size_t)tile * 32 + h) * 1024 + lane * 16) = CAT4(cvt4(q0), cvt4(q1));
  return true;
}

constexpr unsigned FILL_END = OFF_XNF;
__global__ void __launch_bounds__(256, 2) k_pre(Args a) {
  extern __shared__ __attribute__((aligned(16))) u8 smem[];
  const int tid = threadIdx.x, wave = __builtin_amdgcn_readfirstlane(tid >> 6), lane = tid & 63, g = lane >> 4, n16 = lane & 15;
  const int b = blockIdx.x & 255;
  TP(0);
  if (blockIdx.x < 256) {
    const int h = b >> 3, rg = b & 7;
    if (b == 0 && tid == 0) *(unsigned*)(a.ws + OFF_CTL) = 0u;
    const __amdgpu_buffer_rsrc_t rx = rsrc(a.x0, SZ_X);
    u32x4 xa[20];
    lin_load<false>(rx, rg * 16 + wave, xa, lane);
    lin_stage_w<false>(a.wqkv, h, smem, tid);
    __syncthreads();
    TP(1);
    LinW w; lin_init(smem, w, lane);
    TP(2);
    f32x4 kv[4] = {Z4, Z4, Z4, Z4};
    lin_tile<false, true>(xa, rx, rg * 16 + wave, smem, w, kv, a.ws + OFF_PQ, h, lane, false);
    TP(3);
    lin_tile<false, true>(xa, rx, rg * 16 + 4 + wave, smem, w, kv, a.ws + OFF_PQ, h, lane, false);
    lin_tile<false, true>(xa, rx, rg * 16 + 8 + wave, smem, w, kv, a.ws + OFF_PQ, h, lane, false);
    lin_tile<false, false>(xa, rx, rg * 16 + 12 + wave, smem, w, kv, a.ws + OFF_PQ, h, lane, false);
    __syncthreads();
#pragma unroll
    for (int t = 0; t < 4; ++t) *(f32x4*)(smem + wave * 4096 + t * 1024 + lane * 16) = kv[t];
    __syncthreads();
    const f32x4 s = (*(const f32x4*)(smem + tid * 16) + *(const f32x4*)(smem + 4096 + tid * 16)) + (*(const f32x4*)(smem + 8192 + tid * 16) + *(const f32x4*)(smem + 12288 + tid * 16));
    *(f32x4*)(a.ws + OFF_KVP + (size_t)b * 4096 + tid * 16) = s;
    TP(4);
    {
      const u32x4 f = {0xFFFFFFFFu, 0xFFFFFFFFu, 0xFFFFFFFFu, 0xFFFFFFFFu};
#pragma unroll
      for (int i = 0; i < 15; ++i) { const unsigned p = (i * 256 + b) * 4096 + tid * 16; if ((p < RING_FILL || p >= SZ_RING) && p < FILL_END) *(u32x4*)(a.ws + p) = f; }
    }
    TP(5);
    return;
  }
  const int pr = (b & 7) * 2 + ((b >> 3) & 1), ch = (b >> 4) & 7, hf2 = b >> 7, e = wave >> 1, h = pr * 2 + e, u = wave & 1;
  u32x4 vo[15];
  u32x4 va[12];
  const __amdgpu_buffer_rsrc_t rv = rsrc(a.Wv + (size_t)320 * hf2 * 6144 + pr * 384, 320 * 12288);
  unsigned ga[3], la[3];
#pragma unroll
  for (int j = 0; j < 3; ++j) { const unsigned n = tid + 256 * j; ga[j] = (n / 48) * 12288 + (n % 48) * 16; la[j] = (n / 48) * 784 + (n % 48) * 16; }
  {
    const int qd = b & 3, m = (b >> 2) & 1, hh = b >> 3;
    const u16* W = (m ? a.Wk : a.Wq) + (size_t)(160 * qd) * 6144 + hh * 192;
    u32x4 v[15];
#pragma unroll
    for (int i = 0; i < 15; ++i) { const unsigned n = tid + 256 * (i % 3); v[i] = __builtin_amdgcn_raw_buffer_load_b128(rsrc(W, 160 * 12288), (n / 24) * 12288 + (n % 24) * 16, (i / 3) * 32 * 12288, 0); }
#pragma unroll
    for (int i = 0; i < 15; ++i) { const unsigned n = tid + 256 * (i % 5); vo[i] = __builtin_amdgcn_raw_buffer_load_b128(rsrc(a.Wo + (size_t)pr * 384 * 640 + ch * 80, 384 * 1280), (n / 10) * 1280 + (n % 10) * 16, (i / 5) * 128 * 1280, 0); }
#pragma unroll
    for (int j = 0; j < 12; ++j) va[j] = __builtin_amdgcn_raw_buffer_load_b128(rv, ga[j % 3], (j / 3) * 16 * 12288, 0);
#pragma unroll
    for (int i = 0; i < 15; ++i) { const unsigned n = tid + 256 * (i % 3); *(u32x4*)(smem + (n / 24) * 416 + (n % 24) * 16 + (i / 3) * 32 * 416) = v[i]; }
    __syncthreads();
    TP(1);
    const u8* p0 = smem + wave * 96 + (lane & 3) * 8 + drow(0, g, n16 >> 2) * 416;
    const u8* p1 = smem + wave * 96 + (lane & 3) * 8 + drow(0, g, 4 + (n16 >> 2)) * 416;
#pragma unroll
    for (int j = 0; j < 5; ++j) {
      const int tu = qd * 5 + j, t = tu >> 1;
      const int slot = t >= 8 ? t : (t < 4 ? (t == wave ? 0 : 1 + (t ^ wave)) : ((t & 3) == wave ? 1 : 4 + ((t & 3) ^ wave)));
      u32x4* hf = (u32x4*)(a.ws + OFF_HF) + ((size_t)(hh * 4 + wave) * 60 + slot * 6 + m * 3) * 128 + (tu & 1) * 64 + lane;
#pragma unroll
      for (int n = 0; n < 3; ++n) hf[n * 128] = __builtin_bit_cast(u32x4, tr8(p0 + j * 32 * 416 + n * 32, p1 + j * 32 * 416 + n * 32));
    }
    __syncthreads();
  }
  TP(2);
#pragma unroll
  for (int i = 0; i < 15; ++i) { const unsigned n = tid + 256 * (i % 5); *(u32x4*)(smem + (n / 10) * 176 + (n % 10) * 16 + (i / 5) * 128 * 176) = vo[i]; }
  __syncthreads();
  const u8* bp = smem + (e * 192 + 8 * g + (n16 >> 2)) * 176 + (lane & 3) * 8;
  const __amdgpu_buffer_rsrc_t rcf = rsrc(a.ws + OFF_CF, 32 * 640 * 640 * 2);
  const unsigned cfo = (((((ch * 10 + (n16 >> 3)) * 4 + (h >> 3)) * 10 + hf2 * 5) * 4 + ((h & 7) >> 1)) * 128 + u * 64 + g * 16 + ((n16 & 7) >> 1) * 4 + e * 2 + (n16 & 1)) * 16;
  bf16x8 bfr[5][6];
#pragma unroll
  for (int ct = 0; ct < 5; ++ct)
#pragma unroll
    for (int ks = 0; ks < 6; ++ks) bfr[ct][ks] = tr8(bp + ks * 32 * 176 + ct * 32, bp + (ks * 32 + 4) * 176 + ct * 32);
  TP(3);
  const u8* ap0 = smem + drow(u, n16 >> 2, n16 & 3) * 784 + e * 384 + g * 16;
  const u8* ap1 = smem + drow(u, n16 >> 2, 4 + (n16 & 3)) * 784 + e * 384 + g * 16;
#pragma unroll
  for (int i = 0; i < 5; ++i) {
    __syncthreads();
#pragma unroll
    for (int j = 0; j < 12; ++j) *(u32x4*)(smem + la[j % 3] + (j / 3) * 16 * 784) = va[j];
    if (i < 4) {
#pragma unroll
      for (int j = 0; j < 12; ++j) va[j] = __builtin_amdgcn_raw_buffer_load_b128(rv, ga[j % 3], (64 * (i + 1) + (j / 3) * 16) * 12288, 0);
    }
    __syncthreads();
    bf16x8 af[6]; bf16x4 lo[5];
#pragma unroll
    for (int ks = 0; ks < 6; ++ks) af[ks] = *(const bf16x8*)(ap0 + ks * 64);
    {
      f32x4 c[5] = {Z4, Z4, Z4, Z4, Z4};
#pragma unroll
      for (int ks = 0; ks < 6; ++ks) {
#pragma unroll
        for (int ct = 0; ct < 5; ++ct) c[ct] = MFMA32(af[ks], bfr[ct][ks], c[ct]);
        af[ks] = *(const bf16x8*)(ap1 + ks * 64);
        SB();
      }
#pragma unroll
      for (int ct = 0; ct < 5; ++ct) lo[ct] = cvt4(c[ct]);
    }
    f32x4 c[5] = {Z4, Z4, Z4, Z4, Z4};
#pragma unroll
    for (int ks = 0; ks < 6; ++ks)
#pragma unroll
      for (int ct = 0; ct < 5; ++ct) c[ct] = MFMA32(af[ks], bfr[ct][ks], c[ct]);
#pragma unroll
    for (int ct = 0; ct < 5; ++ct) __builtin_amdgcn_raw_buffer_store_b128(__builtin_bit_cast(u32x4, CAT4(lo[ct], cvt4(c[ct]))), rcf, cfo, (ct * 2 * 20480 + i * 512) * 16, 0);
    if (i == 0) TP(4);
  }
  TP(5);
}

template <int N, int STRIDE>
DEV bool poll_frags(__amdgpu_buffer_rsrc_t r, unsigned vo, unsigned so, u32x4 (&v)[N], unsigned* ctl) {
  for (unsigned spins = 0;;) {
    unsigned m = 0;
#pragma unroll
    for (int i = 0; i < N; ++i) v[i] = __builtin_amdgcn_raw_buffer_load_b128(r, vo + ((i * STRIDE) & 4095), so + ((i * STRIDE) & ~4095), 16);
#pragma unroll
    for (int i = 0; i < N; ++i) m = max(m, mx4(v[i]));
    if (__all(m != 0xFFFFFFFFu)) return true;
    __builtin_amdgcn_s_sleep(N == 1 ? 8 : 2);
    if (spin_fail(spins, ctl)) return false;
  }
}
DEV void lin_role(const Args& a, u8* smem, const int id) {
  const int tid = threadIdx.x, wave = __builtin_amdgcn_readfirstlane(tid >> 6), lane = tid & 63, g = lane >> 4, n16 = lane & 15;
  unsigned* ctl = (unsigned*)(a.ws + OFF_CTL);
  u8* WI = smem; u8* KS = smem + 81920; u8* OI = smem + 102400;
  const __amdgpu_buffer_rsrc_t rO = rsrc(a.ws + OFF_O, 2 * SZ_X), rR = rsrc(a.ws + OFF_XR, 2 * SZ_X), rX2 = rsrc(a.ws + OFF_X2, SZ_X), rN = rsrc(a.ws + OFF_XNF, 2 * SZ_X), rx0 = rsrc(a.x0, SZ_X);
  TL(0);
  const u8* pqp = a.ws + OFF_PQ + (size_t)(wave * 32 + id) * 1024 + lane * 16;
  bf16x8 pq0 = *(const bf16x8*)pqp, pq1 = *(const bf16x8*)(pqp + 131072);
  f32x4 ks = Z4;
#pragma unroll
  for (int r = 0; r < 8; ++r) ks += *(const f32x4*)(a.ws + OFF_KVP + (size_t)(id * 8 + r) * 4096 + tid * 16);
  if (id < 20) {
    u32x4 v[10];
#pragma unroll
    for (int i = 0; i < 10; ++i) v[i] = *(const u32x4*)(a.wo + (size_t)((tid >> 2) + 64 * i) * 640 + id * 32 + (tid & 3) * 8);
#pragma unroll
    for (int i = 0; i < 10; ++i) *(u32x4*)(OI + ((tid >> 2) + 64 * i) * 80 + (tid & 3) * 16) = v[i];
  }
  const unsigned f0 = 20 * id + 4 * g, f4 = 20 * id + 16;
  const unsigned oo0 = (f0 >> 3) * 256 + n16 * 16 + ((f0 >> 2) & 1) * 8, oo4 = g == 0 ? (f4 >> 3) * 256 + n16 * 16 + ((f4 >> 2) & 1) * 8 : 0x80000000u;
  const u8* ob = OI + (8 * g + (n16 >> 2)) * 80 + (lane & 3) * 16;
#pragma unroll 1
  for (int layer = 0; layer < 2; ++layer) {
    *(f32x4*)(KS + 16384 + tid * 16) = ks;
    __syncthreads();
    bf16x8 E0, E1, EZ;
    {
      const u8* S = KS + 16384;
      const int c1 = 4 + (n16 & 3);
      f32x4 a0 = *(const f32x4*)(S + lane * 16), a1 = *(const f32x4*)(S + 2048 + n16 * 16);
      f32x4 b0 = *(const f32x4*)(S + 1024 + (g * 16 + c1) * 16), b1 = *(const f32x4*)(S + 3072 + c1 * 16);
      f32x4 z0 = *(const f32x4*)(S + 1024 + (g * 16 + 12) * 16), z1 = *(const f32x4*)(S + 3072 + 12 * 16);
      if (g != 2) { a1 = Z4; b1 = Z4; z1 = Z4; }
      E0 = CAT4(cvt4(a0), cvt4(a1)); E1 = CAT4(cvt4(b0), cvt4(b1)); EZ = CAT4(cvt4(z0), cvt4(z1));
    }
    TL(1 + layer * 8);
    auto attn_out = [&](bf16x8 pq, int rd) {
      const f32x4 n0 = MFMA32(E0, pq, Z4), n1 = MFMA32(E1, pq, Z4), dn = MFMA32(EZ, pq, Z4);
      const float r = __builtin_amdgcn_rcpf(dn[0] + 1e-6f);
      const unsigned so = (layer * 128 + rd * 4 + wave) * 20480;
      __builtin_amdgcn_raw_buffer_store_b64((u32x2){pkc(n0[0] * r, n0[1] * r), pkc(n0[2] * r, n0[3] * r)}, rO, oo0, so, 16);
      __builtin_amdgcn_raw_buffer_store_b64((u32x2){pkc(n1[0] * r, n1[1] * r), pkc(n1[2] * r, n1[3] * r)}, rO, oo4, so, 16);
    };
    attn_out(pq0, 0); attn_out(pq1, 1);
    SB();
    bf16x8 wo[20][2];
#pragma unroll
    for (int k = 0; k < 20; ++k) { wo[k][0] = tr8(ob + k * 2560, ob + k * 2560 + 320); wo[k][1] = tr8(ob + k * 2560 + 8, ob + k * 2560 + 328); }
#pragma unroll 1
    for (int rd = 0; rd < 32; ++rd) {
      const bf16x8 pqn = *(const bf16x8*)(pqp + (size_t)min(rd + 2, 31) * 131072);
      if (id == 24 && rd == 0 && layer == 0) {
        unsigned best[2] = {1000000u, 1000000u};
        asm volatile("s_waitcnt vmcnt(0)" ::: "memory");
        for (int rep = 0; rep < 4; ++rep)
          for (int cc = 0; cc < 2; ++cc) {
            unsigned* p = (unsigned*)(a.ws + SZ_RING - 16384 + cc * 8192 + wave * 2048 + rep * 256);
            unsigned v, z = 0;
            const unsigned c0 = (unsigned)__builtin_amdgcn_s_memtime();
            if (lane == 0) asm volatile("global_atomic_or %0, %1, %2, off sc0\n s_waitcnt vmcnt(0)" : "=&v"(v) : "v"(p), "v"(z) : "memory");
            const unsigned c1 = (unsigned)__builtin_amdgcn_s_memtime();
            best[cc] = min(best[cc], c1 - c0);
          }
        if (lane == 0) *(u32x2*)(KS + wave * 8) = (u32x2){best[0], best[1]};
        __syncthreads();
        if (tid == 0) {
          const u32x4 p0 = *(const u32x4*)KS, p1 = *(const u32x4*)(KS + 16);
          const unsigned b0 = min(min(p0.x, p0.z), min(p1.x, p1.z)), b1 = min(min(p0.y, p0.w), min(p1.y, p1.w));
          __hip_atomic_store((GAS unsigned*)(a.ws + FLB + 130 * 128), b1 < b0 ? 1u : 0u, __ATOMIC_RELAXED, __HIP_MEMORY_SCOPE_AGENT);
        }
      }
      if (rd == 0) TL(2 + layer * 8);
      if (rd == 1) TL(3 + layer * 8);
      if (rd == 8) TL(4 + layer * 8);
      if (id >= 20 && rd < 30) attn_out(pqn, rd + 2);
      if (id < 20) {
        const unsigned tile = rd * 4 + wave, so = (layer * 128 + tile) * 20480;
        const u32x4 res = layer == 0 ? __builtin_amdgcn_raw_buffer_load_b128(rx0, n16 * 1280 + id * 64 + g * 16, tile * 20480, 0)
                                     : __builtin_amdgcn_raw_buffer_load_b128(rX2, id * 1024 + lane * 16, tile * 20480, 16);
        u32x4 o[20];
        if (!poll_frags<20, 1024>(rO, lane * 16, so, o, ctl)) return;
        f32x4 c0 = Z4, c1 = Z4;
#pragma unroll
        for (int k = 0; k < 20; ++k) {
          c0 = MFMA32(wo[k][0], __builtin_bit_cast(bf16x8, o[k]), c0); c1 = MFMA32(wo[k][1], __builtin_bit_cast(bf16x8, o[k]), c1);
        }
        const u32x4 y = {pk2(lo16(res.x) + c0[0], hi16(res.x) + c0[1]), pk2(lo16(res.y) + c0[2], hi16(res.y) + c0[3]), pk2(lo16(res.z) + c1[0], hi16(res.z) + c1[1]), pk2(lo16(res.w) + c1[2], hi16(res.w) + c1[3])};
        __builtin_amdgcn_raw_buffer_store_b128(canon4(y), rR, id * 1024 + lane * 16, so, 16);
      } else if ((rd % 6) == ((id - 20) >> 1)) {
        const unsigned sg = layer * 64 + rd * 2 + (id & 1), tok = (wave & 1) * 8 + (lane & 7), part = lane >> 3;
        u32x4 x[10];
        if (!poll_frags<10, 256>(rR, part * 2560 + tok * 16, sg * SEGB + (wave >> 1) * 20480, x, ctl)) return;
        float s = 0.f;
#pragma unroll
        for (int i = 0; i < 10; ++i)
#pragma unroll
          for (int d = 0; d < 4; ++d) { const unsigned xd = x[i][d]; s += lo16(xd) + hi16(xd); }
        s += __shfl_xor(s, 8); s += __shfl_xor(s, 16); s += __shfl_xor(s, 32);
        const float mu = s * (1.f / 640.f);
        float q = 0.f;
#pragma unroll
        for (int i = 0; i < 10; ++i)
#pragma unroll
          for (int d = 0; d < 4; ++d) { const unsigned xd = x[i][d]; const float d0 = lo16(xd) - mu, d1 = hi16(xd) - mu; q += d0 * d0 + d1 * d1; }
        q += __shfl_xor(q, 8); q += __shfl_xor(q, 16); q += __shfl_xor(q, 32);
        const float rs = rsqrtf(q * (1.f / 640.f) + 1e-5f);
#pragma unroll
        for (int i = 0; i < 5; ++i) {
          unsigned y[8];
#pragma unroll
          for (int d = 0; d < 8; ++d) { const unsigned xd = x[2 * i + (d >> 2)][d & 3]; y[d] = pk2((lo16(xd) - mu) * rs, (hi16(xd) - mu) * rs); }
          const unsigned gr = part * 5 + i, t = gr >> 2, u = (gr >> 1) & 1;
          const unsigned vo = ((2 * u + (wave >> 1)) * 10 + t) * 1024 + (gr & 1) * 512 + tok * 16;
          __builtin_amdgcn_raw_buffer_store_b128((u32x4){y[0], y[4], y[1], y[5]}, rN, vo, sg * SEGB, 16);
          __builtin_amdgcn_raw_buffer_store_b128((u32x4){y[2], y[6], y[3], y[7]}, rN, vo + 256, sg * SEGB, 16);
        }
        asm volatile("s_waitcnt vmcnt(0)" ::: "memory");
        __syncthreads();
        if (tid == 0) __hip_atomic_store((GAS unsigned*)(a.ws + FLB + sg * 128), 1u, __ATOMIC_RELAXED, __HIP_MEMORY_SCOPE_AGENT);
      }
      if (id < 20 && rd < 30) attn_out(pqn, rd + 2);
    }
    TL(5 + layer * 8);
    if (layer == 1) break;
    lin_stage_w<true>(a.wqkv, id, WI, tid);
    __syncthreads();
    LinW w; lin_init(WI, w, lane);
    f32x4 kv[4] = {Z4, Z4, Z4, Z4};
    u32x4 x[20];
    int st = 0;
#pragma unroll 1
    for (int tile = wave; tile < 128;) {
      bool ahead = st == 1;
      if (st == 0) {
        u32x4 one[1];
        one[0] = __builtin_amdgcn_raw_buffer_load_b128(rX2, (id & 15) * 1024 + lane * 16, tile * 20480, 16);
        ahead = __all(mx4(one[0]) != 0xFFFFFFFFu);
        if (!ahead && !poll_frags<1, 0>(rX2, (id & 15) * 1024 + lane * 16, tile * 20480, one, ctl)) return;
        if (tile >= 124) TL(6);
        lin_load<true>(rX2, tile, x, lane);
      } else if (st == 2) {
        if (!poll_frags<20, 1024>(rX2, lane * 16, tile * 20480, x, ctl)) return;
      }
      ahead = ahead && tile < 124;
      if (lin_tile<true, true>(x, rX2, tile, WI, w, kv, a.ws + OFF_PQ, id, lane, st != 2, ahead ? lane * 16 : 0x80000000u)) { st = ahead ? 1 : 0; tile += 4; }
      else st = st == 1 ? 0 : 2;
    }
    pq0 = *(const bf16x8*)pqp; pq1 = *(const bf16x8*)(pqp + 131072);
#pragma unroll
    for (int t = 0; t < 4; ++t) *(f32x4*)(KS + wave * 4096 + t * 1024 + lane * 16) = kv[t];
    TL(7);
    asm volatile("s_waitcnt vmcnt(0)" ::: "memory");
    __syncthreads();
    ks = (*(const f32x4*)(KS + tid * 16) + *(const f32x4*)(KS + 4096 + tid * 16)) + (*(const f32x4*)(KS + 8192 + tid * 16) + *(const f32x4*)(KS + 12288 + tid * 16));
  }
}

#define SRD(vec, i, ptr, bytes) do { unsigned long long p_ = (unsigned long long)(ptr); vec[i] = (unsigned)p_; vec[i + 1] = (unsigned)(p_ >> 32) & 0xFFFFu; vec[i + 2] = (unsigned)(bytes); vec[i + 3] = 0x00020000u; } while (0)
__global__ void __launch_bounds__(256, 1) k_main(Args a) {
  extern __shared__ __attribute__((aligned(16))) u8 smem[];
  const unsigned b = blockIdx.x, wave = __builtin_amdgcn_readfirstlane(threadIdx.x >> 6), lane = threadIdx.x & 63;
  const unsigned par = (b & 7) >> 2, sub = b & 3, jj = b >> 3;
  if (jj >= 28) { lin_role(a, smem, b - 224); return; }
  const unsigned g = lane >> 4, n16 = lane & 15, L16 = lane * 16, OOB = 0x80000000u;
  const bool isH = jj < 8;
  const unsigned h = sub * 8 + jj, c = sub * 20 + (jj - 8);
  u32x16 sa, sb; u32x8 sc;
  SRD(sa, 0, a.ws, SZ_RING); SRD(sa, 4, a.ws + OFF_X2, SZ_X); SRD(sa, 8, a.ws + OFF_XNF, 2 * SZ_X + 4 * SEGB);
  if (isH) SRD(sa, 12, a.ws + OFF_HF + (size_t)(h * 4 + wave) * 122880, 122880); else SRD(sa, 12, a.ws + OFF_CF + (size_t)(c * 4 + wave) * 81920, 81920);
  SRD(sb, 0, a.ws + OFF_XR, 2 * SZ_X); SRD(sb, 4, a.out, SZ_X); SRD(sb, 8, a.ws + OFF_CTL, 131072);
  sb[12] = wave; sb[13] = par; sb[14] = 0; sb[15] = 0;
  sc[0] = isH ? (wave < 2 ? OOB : 0u) : (unsigned)a.dp; sc[1] = isH ? PXB + (wave == 1 ? 20480 - 512 + h * 128 : wave * 20480 + h * 640) : (unsigned)a.dm;
  sc[2] = a.skew; sc[3] = a.delta; sc[4] = a.lat; sc[5] = isH ? PXB + (wave == 1 ? 8 : wave * 20480) + h * 640 : (unsigned)a.marg | (c == 0 ? OOB : 0u); sc[6] = jj;
  sc[7] = isH ? wave * 65536 + (h >> 3) * 16384 + (h & 7) * 640 : (unsigned)a.pubw;
  const unsigned sto = (isH ? wave != 3 : wave > 1) ? OOB : FLB + (136 + par * 128 + (isH ? h : 32 + c)) * 128;
  const unsigned dbgoff = 256 + (b * 4 + wave) * 32;
  const unsigned mo = sub * 65536 + wave * 16384 + 5120 + L16, mo2 = sub * 65536 + (wave & 1) * 16384 + 7168 + (wave >> 1) * 512 + lane * 8;
  const unsigned m2w = (8 + (wave & 1)) * 1024 + (wave >> 1) * 512 + lane * 8, idx = (lane & 8) ? 0xEEEEu : 0x4444u, sch = sub * 65536 + 3 * 16384 + 7168;
  u32x16 va; u32x8 vb;
  if (isH) {
    const unsigned r8 = lane >> 3, q8 = lane & 7, dump = 62464 + L16;
    va[0] = L16; va[1] = L16 + (wave ^ 1) * 1024; va[2] = L16 + (wave ^ 2) * 1024; va[3] = L16 + (wave ^ 3) * 1024; va[4] = L16 + wave * 1024;
    va[5] = 65536 + wave * 24576 + L16; va[6] = mo; va[7] = mo2; va[8] = 40960 + m2w; va[9] = idx;
    const unsigned sw = 57344 + wave * 1024 + n16 * 128 + (((2 * g) ^ n16) & 7) * 16;
    va[10] = n16 < 8 ? sw : dump; va[11] = n16 < 8 ? (sw ^ 16) : dump;
    va[12] = (n16 < 8 && g >= 2) ? 61440 + wave * 256 + n16 * 32 + (g - 2) * 16 : dump;
    va[13] = 57344 + r8 * 128 + (q8 ^ r8) * 16; va[14] = 61440 + lane * 4;
    va[15] = (q8 >> 1) * 128 + r8 * 16 + (q8 & 1) * 8;
    vb[0] = (q8 & 1) ? OOB : 512 + r8 * 16 + q8 * 2; vb[1] = sch;
    vb[2] = (q8 & 1) * 16 + (q8 >> 1) * 4 - (wave * 8 + r8); vb[3] = 0;
#pragma unroll
    for (int j = 0; j < 4; ++j) {
      const unsigned base = (j < 2 ? 0 : 8) + 4 * (g & 1) + 2 * (j & 1);
      vb[4 + j] = (n16 == base ? 0x3F80u : 0u) | (n16 == base + 1 ? 0x3F800000u : 0u);
    }
  } else {
    const unsigned e1 = (lane >> 3) & 1, r7 = lane & 7, t = c >> 3;
    va[0] = L16; va[1] = 81920 + wave * 1024 + L16; va[2] = mo; va[3] = mo2; va[4] = 81920 + m2w; va[5] = idx;
    va[6] = sub * 65536 + e1 * 640 + g * 128 + r7 * 16; va[7] = sub * 65536 + (2 * g + e1) * 640 + 512 + r7 * 16;
    va[8] = PXB + e1 * 640 + g * 128 + r7 * 16; va[9] = PXB + (2 * g + e1) * 640 + 512 + r7 * 16;
    va[10] = sch;
    va[11] = (t & 3) * 16384 + 5120 + (t >> 2) * 1024 + (c & 7) * 128 + r7 * 16 + g * 4 + e1 * 65536;
    const unsigned tok = wave == 1 ? e1 * 8 + r7 : wave * 8 + r7;
    const bool off = e1 && wave != 1;
    va[12] = off ? OOB : tok * 1280 + c * 16 + g * 4;
    va[13] = wave == 1 ? PXB + 20480 + (2 * g + e1) * 128 + r7 * 16 : OOB;
    va[14] = off ? OOB : (tok >> 4) * 20480 + c * 256 + (tok & 15) * 16 + g * 4; va[15] = 0;
    vb[0] = (c == 0 && wave == 0 && lane < 4) ? lane * 65536 + 3 * 16384 + 7168 : OOB;
    vb[1] = vb[2] = vb[3] = vb[4] = vb[5] = vb[6] = 0;
    vb[7] = min(*(const unsigned*)(a.mem0 + r7 * 640 + c * 8 + g * 2), 0xFFFFFFFEu);
  }
  asm volatile(
  "s_waitcnt vmcnt(0) lgkmcnt(0)\n s_nop 7\n s_cmp_ge_u32 s54, 8\n s_cbranch_scc1 C_ENTRY_%=\n"
  "s_mov_b32 s76, 0x3dd53b95\n"
  "s_mov_b32 s77, 0xceabfb8f\n"
  "s_mov_b32 s78, 0xfffffffe\n"
  "s_mov_b32 s46, 0\n"
  "v_cmp_ge_i32 s[8:9], 0, v18\n"
  "v_cmp_ge_i32 s[10:11], -1, v18\n"
  "v_cmp_ge_i32 s[12:13], -2, v18\n"
  "v_cmp_ge_i32 s[14:15], -3, v18\n"
  "s_mul_i32 s69, s44, 24576\n"
  "s_add_u32 s69, s69, 65536\n"
  "s_mov_b32 s68, 0\n"
  "buffer_load_dwordx4 a[0:3], v0, s[28:31], s68 offen offset:0\n"
  "buffer_load_dwordx4 a[4:7], v0, s[28:31], s68 offen offset:1024\n"
  "s_mov_b32 s68, 2048\n"
  "buffer_load_dwordx4 a[8:11], v0, s[28:31], s68 offen offset:0\n"
  "buffer_load_dwordx4 a[12:15], v0, s[28:31], s68 offen offset:1024\n"
  "s_mov_b32 s68, 4096\n"
  "buffer_load_dwordx4 a[16:19], v0, s[28:31], s68 offen offset:0\n"
  "buffer_load_dwordx4 a[20:23], v0, s[28:31], s68 offen offset:1024\n"
  "s_mov_b32 s68, 6144\n"
  "buffer_load_dwordx4 a[24:27], v0, s[28:31], s68 offen offset:0\n"
  "buffer_load_dwordx4 a[28:31], v0, s[28:31], s68 offen offset:1024\n"
  "s_mov_b32 s68, 8192\n"
  "buffer_load_dwordx4 a[32:35], v0, s[28:31], s68 offen offset:0\n"
  "buffer_load_dwordx4 a[36:39], v0, s[28:31], s68 offen offset:1024\n"
  "s_mov_b32 s68, 10240\n"
  "s_add_u32 m0, s69, 0\n"
  "s_nop 0\n"
  "buffer_load_dwordx4 v0, s[28:31], s68 offen offset:0 lds\n"
  "buffer_load_dwordx4 v0, s[28:31], s68 offen offset:1024 lds\n"
  "s_mov_b32 s68, 12288\n"
  "buffer_load_dwordx4 a[40:43], v0, s[28:31], s68 offen offset:0\n"
  "buffer_load_dwordx4 a[44:47], v0, s[28:31], s68 offen offset:1024\n"
  "s_mov_b32 s68, 14336\n"
  "buffer_load_dwordx4 a[48:51], v0, s[28:31], s68 offen offset:0\n"
  "buffer_load_dwordx4 a[52:55], v0, s[28:31], s68 offen offset:1024\n"
  "s_mov_b32 s68, 16384\n"
  "buffer_load_dwordx4 a[56:59], v0, s[28:31], s68 offen offset:0\n"
  "buffer_load_dwordx4 a[60:63], v0, s[28:31], s68 offen offset:1024\n"
  "s_mov_b32 s68, 18432\n"
  "buffer_load_dwordx4 a[64:67], v0, s[28:31], s68 offen offset:0\n"
  "buffer_load_dwordx4 a[68:71], v0, s[28:31], s68 offen offset:1024\n"
  "s_mov_b32 s68, 20480\n"
  "buffer_load_dwordx4 a[72:75], v0, s[28:31], s68 offen offset:0\n"
  "buffer_load_dwordx4 a[76:79], v0, s[28:31], s68 offen offset:1024\n"
  "s_mov_b32 s68, 22528\n"
  "s_add_u32 m0, s69, 2048\n"
  "s_nop 0\n"
  "buffer_load_dwordx4 v0, s[28:31], s68 offen offset:0 lds\n"
  "buffer_load_dwordx4 v0, s[28:31], s68 offen offset:1024 lds\n"
  "s_mov_b32 s68, 24576\n"
  "buffer_load_dwordx4 a[80:83], v0, s[28:31], s68 offen offset:0\n"
  "buffer_load_dwordx4 a[84:87], v0, s[28:31], s68 offen offset:1024\n"
  "s_mov_b32 s68, 26624\n"
  "buffer_load_dwordx4 a[88:91], v0, s[28:31], s68 offen offset:0\n"
  "buffer_load_dwordx4 a[92:95], v0, s[28:31], s68 offen offset:1024\n"
  "s_mov_b32 s68, 28672\n"
  "buffer_load_dwordx4 a[96:99], v0, s[28:31], s68 offen offset:0\n"
  "buffer_load_dwordx4 a[100:103], v0, s[28:31], s68 offen offset:1024\n"
  "s_mov_b32 s68, 30720\n"
  "buffer_load_dwordx4 a[104:107], v0, s[28:31], s68 offen offset:0\n"
  "buffer_load_dwordx4 a[108:111], v0, s[28:31], s68 offen offset:1024\n"
  "s_mov_b32 s68, 32768\n"
  "s_add_u32 m0, s69, 4096\n"
  "s_nop 0\n"
  "buffer_load_dwordx4 v0, s[28:31], s68 offen offset:0 lds\n"
  "buffer_load_dwordx4 v0, s[28:31], s68 offen offset:1024 lds\n"
  "s_mov_b32 s68, 34816\n"
  "s_add_u32 m0, s69, 6144\n"
  "s_nop 0\n"
  "buffer_load_dwordx4 v0, s[28:31], s68 offen offset:0 lds\n"
  "buffer_load_dwordx4 v0, s[28:31], s68 offen offset:1024 lds\n"
  "s_mov_b32 s68, 36864\n"
  "buffer_load_dwordx4 a[112:115], v0, s[28:31], s68 offen offset:0\n"
  "buffer_load_dwordx4 a[116:119], v0, s[28:31], s68 offen offset:1024\n"
  "s_mov_b32 s68, 38912\n"
  "buffer_load_dwordx4 a[120:123], v0, s[28:31], s68 offen offset:0\n"
  "buffer_load_dwordx4 a[124:127], v0, s[28:31], s68 offen offset:1024\n"
  "s_mov_b32 s68, 40960\n"
  "buffer_load_dwordx4 a[128:131], v0, s[28:31], s68 offen offset:0\n"
  "buffer_load_dwordx4 a[132:135], v0, s[28:31], s68 offen offset:1024\n"
  "s_mov_b32 s68, 43008\n"
  "buffer_load_dwordx4 a[136:139], v0, s[28:31], s68 offen offset:0\n"
  "buffer_load_dwordx4 a[140:143], v0, s[28:31], s68 offen offset:1024\n"
  "s_mov_b32 s68, 45056\n"
  "buffer_load_dwordx4 a[144:147], v0, s[28:31], s68 offen offset:0\n"
  "buffer_load_dwordx4 a[148:151], v0, s[28:31], s68 offen offset:1024\n"
  "s_mov_b32 s68, 47104\n"
  "s_add_u32 m0, s69, 8192\n"
  "s_nop 0\n"
  "buffer_load_dwordx4 v0, s[28:31], s68 offen offset:0 lds\n"
  "buffer_load_dwordx4 v0, s[28:31], s68 offen offset:1024 lds\n"
  "s_mov_b32 s68, 49152\n"
  "buffer_load_dwordx4 a[152:155], v0, s[28:31], s68 offen offset:0\n"
  "buffer_load_dwordx4 a[156:159], v0, s[28:31], s68 offen offset:1024\n"
  "s_mov_b32 s68, 51200\n"
  "buffer_load_dwordx4 a[160:163], v0, s[28:31], s68 offen offset:0\n"
  "buffer_load_dwordx4 a[164:167], v0, s[28:31], s68 offen offset:1024\n"
  "s_mov_b32 s68, 53248\n"
  "buffer_load_dwordx4 a[168:171], v0, s[28:31], s68 offen offset:0\n"
  "buffer_load_dwordx4 a[172:175], v0, s[28:31], s68 offen offset:1024\n"
  "s_mov_b32 s68, 55296\n"
  "buffer_load_dwordx4 a[176:179], v0, s[28:31], s68 offen offset:0\n"
  "buffer_load_dwordx4 a[180:183], v0, s[28:31], s68 offen offset:1024\n"
  "s_mov_b32 s68, 57344\n"
  "s_add_u32 m0, s69, 10240\n"
  "s_nop 0\n"
  "buffer_load_dwordx4 v0, s[28:31], s68 offen offset:0 lds\n"
  "buffer_load_dwordx4 v0, s[28:31], s68 offen offset:1024 lds\n"
  "s_mov_b32 s68, 59392\n"
  "s_add_u32 m0, s69, 12288\n"
  "s_nop 0\n"
  "buffer_load_dwordx4 v0, s[28:31], s68 offen offset:0 lds\n"
  "buffer_load_dwordx4 v0, s[28:31], s68 offen offset:1024 lds\n"
  "s_mov_b32 s68, 61440\n"
  "s_waitcnt vmcnt(50)\n"
  "buffer_load_dwordx4 a[184:187], v0, s[28:31], s68 offen offset:0\n"
  "buffer_load_dwordx4 a[188:191], v0, s[28:31], s68 offen offset:1024\n"
  "s_mov_b32 s68, 63488\n"
  "buffer_load_dwordx4 a[192:195], v0, s[28:31], s68 offen offset:0\n"
  "buffer_load_dwordx4 a[196:199], v0, s[28:31], s68 offen offset:1024\n"
  "s_mov_b32 s68, 65536\n"
  "buffer_load_dwordx4 a[200:203], v0, s[28:31], s68 offen offset:0\n"
  "buffer_load_dwordx4 a[204:207], v0, s[28:31], s68 offen offset:1024\n"
  "s_mov_b32 s68, 67584\n"
  "s_add_u32 m0, s69, 14336\n"
  "s_nop 0\n"
  "buffer_load_dwordx4 v0, s[28:31], s68 offen offset:0 lds\n"
  "buffer_load_dwordx4 v0, s[28:31], s68 offen offset:1024 lds\n"
  "s_mov_b32 s68, 69632\n"
  "buffer_load_dwordx4 a[208:211], v0, s[28:31], s68 offen offset:0\n"
  "buffer_load_dwordx4 a[212:215], v0, s[28:31], s68 offen offset:1024\n"
  "s_mov_b32 s68, 71680\n"
  "s_waitcnt vmcnt(50)\n"
  "buffer_load_dwordx4 a[216:219], v0, s[28:31], s68 offen offset:0\n"
  "buffer_load_dwordx4 a[220:223], v0, s[28:31], s68 offen offset:1024\n"
  "s_mov_b32 s68, 73728\n"
  "buffer_load_dwordx4 a[224:227], v0, s[28:31], s68 offen offset:0\n"
  "buffer_load_dwordx4 a[228:231], v0, s[28:31], s68 offen offset:1024\n"
  "s_mov_b32 s68, 75776\n"
  "buffer_load_dwordx4 a[232:235], v0, s[28:31], s68 offen offset:0\n"
  "buffer_load_dwordx4 a[236:239], v0, s[28:31], s68 offen offset:1024\n"
  "s_mov_b32 s68, 77824\n"
  "buffer_load_dwordx4 a[240:243], v0, s[28:31], s68 offen offset:0\n"
  "buffer_load_dwordx4 a[244:247], v0, s[28:31], s68 offen offset:1024\n"
  "s_mov_b32 s68, 79872\n"
  "buffer_load_dwordx4 a[248:251], v0, s[28:31], s68 offen offset:0\n"
  "buffer_load_dwordx4 a[252:255], v0, s[28:31], s68 offen offset:1024\n"
  "s_mov_b32 s68, 81920\n"
  "s_add_u32 m0, s69, 16384\n"
  "s_waitcnt vmcnt(50)\n"
  "buffer_load_dwordx4 v0, s[28:31], s68 offen offset:0 lds\n"
  "buffer_load_dwordx4 v0, s[28:31], s68 offen offset:1024 lds\n"
  "s_mov_b32 s68, 83968\n"
  "buffer_load_dwordx4 v[128:131], v0, s[28:31], s68 offen offset:0\n"
  "buffer_load_dwordx4 v[132:135], v0, s[28:31], s68 offen offset:1024\n"
  "s_mov_b32 s68, 86016\n"
  "buffer_load_dwordx4 v[136:139], v0, s[28:31], s68 offen offset:0\n"
  "buffer_load_dwordx4 v[140:143], v0, s[28:31], s68 offen offset:1024\n"
  "s_mov_b32 s68, 88064\n"
  "buffer_load_dwordx4 v[144:147], v0, s[28:31], s68 offen offset:0\n"
  "buffer_load_dwordx4 v[148:151], v0, s[28:31], s68 offen offset:1024\n"
  "s_mov_b32 s68, 90112\n"
  "buffer_load_dwordx4 v[152:155], v0, s[28:31], s68 offen offset:0\n"
  "buffer_load_dwordx4 v[156:159], v0, s[28:31], s68 offen offset:1024\n"
  "s_mov_b32 s68, 92160\n"
  "s_add_u32 m0, s69, 18432\n"
  "s_waitcnt vmcnt(50)\n"
  "buffer_load_dwordx4 v0, s[28:31], s68 offen offset:0 lds\n"
  "buffer_load_dwordx4 v0, s[28:31], s68 offen offset:1024 lds\n"
  "s_mov_b32 s68, 94208\n"
  "buffer_load_dwordx4 v[160:163], v0, s[28:31], s68 offen offset:0\n"
  "buffer_load_dwordx4 v[164:167], v0, s[28:31], s68 offen offset:1024\n"
  "s_mov_b32 s68, 96256\n"
  "buffer_load_dwordx4 v[168:171], v0, s[28:31], s68 offen offset:0\n"
  "buffer_load_dwordx4 v[172:175], v0, s[28:31], s68 offen offset:1024\n"
  "s_mov_b32 s68, 98304\n"
  "buffer_load_dwordx4 v[176:179], v0, s[28:31], s68 offen offset:0\n"
  "buffer_load_dwordx4 v[180:183], v0, s[28:31], s68 offen offset:1024\n"
  "s_mov_b32 s68, 100352\n"
  "buffer_load_dwordx4 v[184:187], v0, s[28:31], s68 offen offset:0\n"
  "buffer_load_dwordx4 v[188:191], v0, s[28:31], s68 offen offset:1024\n"
  "s_mov_b32 s68, 102400\n"
  "s_waitcnt vmcnt(50)\n"
  "buffer_load_dwordx4 v[192:195], v0, s[28:31], s68 offen offset:0\n"
  "buffer_load_dwordx4 v[196:199], v0, s[28:31], s68 offen offset:1024\n"
  "s_mov_b32 s68, 104448\n"
  "buffer_load_dwordx4 v[200:203], v0, s[28:31], s68 offen offset:0\n"
  "buffer_load_dwordx4 v[204:207], v0, s[28:31], s68 offen offset:1024\n"
  "s_mov_b32 s68, 106496\n"
  "s_add_u32 m0, s69, 20480\n"
  "s_nop 0\n"
  "buffer_load_dwordx4 v0, s[28:31], s68 offen offset:0 lds\n"
  "buffer_load_dwordx4 v0, s[28:31], s68 offen offset:1024 lds\n"
  "s_mov_b32 s68, 108544\n"
  "buffer_load_dwordx4 v[208:211], v0, s[28:31], s68 offen offset:0\n"
  "buffer_load_dwordx4 v[212:215], v0, s[28:31], s68 offen offset:1024\n"
  "s_mov_b32 s68, 110592\n"
  "buffer_load_dwordx4 v[216:219], v0, s[28:31], s68 offen offset:0\n"
  "buffer_load_dwordx4 v[220:223], v0, s[28:31], s68 offen offset:1024\n"
  "s_mov_b32 s68, 112640\n"
  "s_waitcnt vmcnt(50)\n"
  "buffer_load_dwordx4 v[224:227], v0, s[28:31], s68 offen offset:0\n"
  "buffer_load_dwordx4 v[228:231], v0, s[28:31], s68 offen offset:1024\n"
  "s_mov_b32 s68, 114688\n"
  "buffer_load_dwordx4 v[232:235], v0, s[28:31], s68 offen offset:0\n"
  "buffer_load_dwordx4 v[236:239], v0, s[28:31], s68 offen offset:1024\n"
  "s_mov_b32 s68, 116736\n"
  "buffer_load_dwordx4 v[240:243], v0, s[28:31], s68 offen offset:0\n"
  "buffer_load_dwordx4 v[244:247], v0, s[28:31], s68 offen offset:1024\n"
  "s_mov_b32 s68, 118784\n"
  "buffer_load_dwordx4 v[248:251], v0, s[28:31], s68 offen offset:0\n"
  "buffer_load_dwordx4 v[252:255], v0, s[28:31], s68 offen offset:1024\n"
  "s_mov_b32 s68, 120832\n"
  "s_add_u32 m0, s69, 22528\n"
  "s_nop 0\n"
  "buffer_load_dwordx4 v0, s[28:31], s68 offen offset:0 lds\n"
  "buffer_load_dwordx4 v0, s[28:31], s68 offen offset:1024 lds\n"
  "s_waitcnt vmcnt(0)\n"
  "s_mov_b32 s62, 0\n"
  "s_mov_b32 s74, 1851648\n"
  "H_CALP_%=:\n"
  "s_nop 7\n"
  "buffer_load_dword v19, off, s[16:19], s74 sc1\n"
  "s_waitcnt vmcnt(0)\n"
  "v_readfirstlane_b32 s47, v19\n"
  "s_cmp_lg_u32 s47, -1\n"
  "s_cbranch_scc1 H_CALK_%=\n"
  "s_sleep 4\n"
  "s_add_u32 s62, s62, 1\n"
  "s_cmp_gt_u32 s62, 300000\n"
  "s_cbranch_scc1 H_FAIL_%=\n"
  "s_and_b32 s60, s62, 255\n"
  "s_cmp_lg_u32 s60, 0\n"
  "s_cbranch_scc1 H_CALP_%=\n"
  "v_mov_b32 v19, 0\n"
  "buffer_load_dword v19, v19, s[40:43], 0 offen sc1\n"
  "s_waitcnt vmcnt(0)\n"
  "v_readfirstlane_b32 s60, v19\n"
  "s_cmp_lg_u32 s60, 0\n"
  "s_cbranch_scc1 H_EXIT_%=\n"
  "s_branch H_CALP_%=\n"
  "H_CALK_%=:\n"
  "s_nop 7\n"
  "v_mov_b32 v24, 63488\n"
  "v_mov_b32 v25, -1\n"
  "ds_write_b32 v24, v25\n"
  "H_LAYER_%=:\n"
  "s_nop 7\n"
  "s_mov_b32 s56, s45\n"
  "s_add_u32 s99, s46, s45\n"
  "s_add_u32 s74, s99, 0\n"
  "s_lshl_b32 s74, s74, 7\n"
  "s_add_u32 s74, s74, 1835008\n"
  "s_mov_b32 s62, 0\n"
  "s_cmp_ge_u32 s56, 64\n"
  "s_cbranch_scc1 H_F0K_%=\n"
  "s_waitcnt vmcnt(0) lgkmcnt(0)\n"
  "H_F0P_%=:\n"
  "buffer_load_dword v19, off, s[16:19], s74 sc1\n"
  "s_waitcnt vmcnt(0)\n"
  "v_readfirstlane_b32 s60, v19\n"
  "s_cmp_lg_u32 s60, -1\n"
  "s_cbranch_scc1 H_F0K_%=\n"
  "s_sleep 4\n"
  "s_add_u32 s62, s62, 1\n"
  "s_cmp_gt_u32 s62, 300000\n"
  "s_cbranch_scc1 H_FAIL_%=\n"
  "s_and_b32 s60, s62, 255\n"
  "s_cmp_lg_u32 s60, 0\n"
  "s_cbranch_scc1 H_F0P_%=\n"
  "v_mov_b32 v19, 0\n"
  "buffer_load_dword v19, v19, s[40:43], 0 offen sc1\n"
  "s_waitcnt vmcnt(0)\n"
  "v_readfirstlane_b32 s60, v19\n"
  "s_cmp_lg_u32 s60, 0\n"
  "s_cbranch_scc1 H_EXIT_%=\n"
  "s_branch H_F0P_%=\n"
  "H_F0K_%=:\n"
  "s_waitcnt lgkmcnt(0)\n"
  "s_barrier\n"
  "s_add_u32 s60, s99, 0\n"
  "s_mul_i32 s72, s60, 40960\n"
  "s_mul_i32 s73, s44, 10240\n"
  "s_add_u32 s72, s72, s73\n"
  "s_mov_b32 m0, s73\n"
  "s_nop 0\n"
  "buffer_load_dwordx4 v0, s[24:27], s72 offen offset:0 sc1 lds\n"
  "buffer_load_dwordx4 v0, s[24:27], s72 offen offset:1024 sc1 lds\n"
  "buffer_load_dwordx4 v0, s[24:27], s72 offen offset:2048 sc1 lds\n"
  "buffer_load_dwordx4 v0, s[24:27], s72 offen offset:3072 sc1 lds\n"
  "s_waitcnt vmcnt(0)\n"
  "s_barrier\n"
  "ds_read_b128 v[24:27], v4 offset:0\n"
  "ds_read_b128 v[28:31], v4 offset:10240\n"
  "ds_read_b128 v[32:35], v4 offset:20480\n"
  "ds_read_b128 v[36:39], v4 offset:30720\n"
  "ds_read_b128 v[80:83], v5 offset:0\n"
  "ds_read_b128 v[84:87], v5 offset:1024\n"
  "s_waitcnt lgkmcnt(0)\n"
  "H_LOOP_%=:\n"
  "s_add_u32 s60, s56, 0\n"
  "s_bfe_u32 s57, s60, 131073\n"
  "s_lshl_b32 s57, s57, 18\n"
  "s_xor_b32 s60, s60, s47\n"
  "s_and_b32 s60, s60, 1\n"
  "s_lshl_b32 s60, s60, 13\n"
  "s_add_u32 s57, s57, s60\n"
  "s_add_u32 s63, s57, s55\n"
  "s_add_u32 s74, s99, 2\n"
  "s_lshl_b32 s74, s74, 7\n"
  "s_add_u32 s74, s74, 1835008\n"
  "buffer_load_dword v19, off, s[16:19], s74 sc1\n"
  "v_mfma_f32_16x16x32_bf16 v[56:59], a[0:3], v[24:27], 0\n"
  "v_mfma_f32_16x16x32_bf16 v[104:107], a[0:3], v[28:31], 0\n"
  "ds_read_b128 v[88:91], v5 offset:4096\n"
  "ds_read_b128 v[92:95], v5 offset:5120\n"
  "v_mfma_f32_16x16x32_bf16 v[60:63], a[8:11], v[24:27], 0\n"
  "s_add_u32 s60, s99, 0\n"
  "s_mul_i32 s72, s60, 40960\n"
  "s_mul_i32 s73, s44, 10240\n"
  "s_add_u32 s73, s73, 4096\n"
  "s_add_u32 s72, s72, s73\n"
  "s_mov_b32 m0, s73\n"
  "s_nop 0\n"
  "buffer_load_dwordx4 v0, s[24:27], s72 offen offset:0 sc1 lds\n"
  "v_mfma_f32_16x16x32_bf16 v[108:111], a[8:11], v[28:31], 0\n"
  "ds_read_b128 v[96:99], v5 offset:6144\n"
  "ds_read_b128 v[100:103], v5 offset:7168\n"
  "v_mfma_f32_16x16x32_bf16 v[64:67], a[16:19], v[24:27], 0\n"
  "v_mfma_f32_16x16x32_bf16 v[112:115], a[16:19], v[28:31], 0\n"
  "ds_read_b128 v[40:43], v1 offset:0\n"
  "v_mfma_f32_16x16x32_bf16 v[68:71], a[24:27], v[24:27], 0\n"
  "v_mfma_f32_16x16x32_bf16 v[116:119], a[24:27], v[28:31], 0\n"
  "ds_read_b128 v[44:47], v1 offset:10240\n"
  "v_mfma_f32_16x16x32_bf16 v[72:75], a[32:35], v[24:27], 0\n"
  "v_mfma_f32_16x16x32_bf16 v[120:123], a[32:35], v[28:31], 0\n"
  "ds_read_b128 v[48:51], v1 offset:20480\n"
  "v_mfma_f32_16x16x32_bf16 v[76:79], v[80:83], v[24:27], 0\n"
  "buffer_load_dwordx4 v0, s[24:27], s72 offen offset:1024 sc1 lds\n"
  "v_mfma_f32_16x16x32_bf16 v[124:127], v[80:83], v[28:31], 0\n"
  "ds_read_b128 v[52:55], v1 offset:30720\n"
  "v_mfma_f32_16x16x32_bf16 v[56:59], a[4:7], v[32:35], v[56:59]\n"
  "v_mfma_f32_16x16x32_bf16 v[104:107], a[4:7], v[36:39], v[104:107]\n"
  "v_mfma_f32_16x16x32_bf16 v[60:63], a[12:15], v[32:35], v[60:63]\n"
  "v_mfma_f32_16x16x32_bf16 v[108:111], a[12:15], v[36:39], v[108:111]\n"
  "v_mfma_f32_16x16x32_bf16 v[64:67], a[20:23], v[32:35], v[64:67]\n"
  "v_mfma_f32_16x16x32_bf16 v[112:115], a[20:23], v[36:39], v[112:115]\n"
  "v_mfma_f32_16x16x32_bf16 v[68:71], a[28:31], v[32:35], v[68:71]\n"
  "buffer_load_dwordx4 v0, s[24:27], s72 offen offset:2048 sc1 lds\n"
  "v_mfma_f32_16x16x32_bf16 v[116:119], a[28:31], v[36:39], v[116:119]\n"
  "v_mfma_f32_16x16x32_bf16 v[72:75], a[36:39], v[32:35], v[72:75]\n"
  "v_mfma_f32_16x16x32_bf16 v[120:123], a[36:39], v[36:39], v[120:123]\n"
  "v_mfma_f32_16x16x32_bf16 v[76:79], v[84:87], v[32:35], v[76:79]\n"
  "v_mfma_f32_16x16x32_bf16 v[124:127], v[84:87], v[36:39], v[124:127]\n"
  "s_waitcnt lgkmcnt(3)\n"
  "v_mfma_f32_16x16x32_bf16 v[56:59], a[80:83], v[40:43], v[56:59]\n"
  "s_waitcnt lgkmcnt(2)\n"
  "v_mfma_f32_16x16x32_bf16 v[104:107], a[80:83], v[44:47], v[104:107]\n"
  "ds_read_b128 v[80:83], v5 offset:8192\n"
  "ds_read_b128 v[84:87], v5 offset:9216\n"
  "v_mfma_f32_16x16x32_bf16 v[60:63], a[88:91], v[40:43], v[60:63]\n"
  "buffer_load_dwordx4 v0, s[24:27], s72 offen offset:3072 sc1 lds\n"
  "v_mfma_f32_16x16x32_bf16 v[108:111], a[88:91], v[44:47], v[108:111]\n"
  "ds_read_b128 v[24:27], v2 offset:0\n"
  "v_mfma_f32_16x16x32_bf16 v[64:67], a[96:99], v[40:43], v[64:67]\n"
  "v_mfma_f32_16x16x32_bf16 v[112:115], a[96:99], v[44:47], v[112:115]\n"
  "ds_read_b128 v[28:31], v2 offset:10240\n"
  "v_mfma_f32_16x16x32_bf16 v[68:71], a[104:107], v[40:43], v[68:71]\n"
  "v_mfma_f32_16x16x32_bf16 v[116:119], a[104:107], v[44:47], v[116:119]\n"
  "ds_read_b128 v[32:35], v2 offset:20480\n"
  "v_mfma_f32_16x16x32_bf16 v[72:75], v[88:91], v[40:43], v[72:75]\n"
  "v_mfma_f32_16x16x32_bf16 v[120:123], v[88:91], v[44:47], v[120:123]\n"
  "ds_read_b128 v[36:39], v2 offset:30720\n"
  "v_mfma_f32_16x16x32_bf16 v[76:79], v[96:99], v[40:43], v[76:79]\n"
  "s_add_u32 s72, s72, 4096\n"
  "s_add_u32 s73, s73, 4096\n"
  "s_mov_b32 m0, s73\n"
  "s_nop 0\n"
  "buffer_load_dwordx4 v0, s[24:27], s72 offen offset:0 sc1 lds\n"
  "v_mfma_f32_16x16x32_bf16 v[124:127], v[96:99], v[44:47], v[124:127]\n"
  "s_waitcnt lgkmcnt(7)\n"
  "v_mfma_f32_16x16x32_bf16 v[56:59], a[84:87], v[48:51], v[56:59]\n"
  "s_waitcnt lgkmcnt(6)\n"
  "v_mfma_f32_16x16x32_bf16 v[104:107], a[84:87], v[52:55], v[104:107]\n"
  "v_mfma_f32_16x16x32_bf16 v[60:63], a[92:95], v[48:51], v[60:63]\n"
  "v_mfma_f32_16x16x32_bf16 v[108:111], a[92:95], v[52:55], v[108:111]\n"
  "v_mfma_f32_16x16x32_bf16 v[64:67], a[100:103], v[48:51], v[64:67]\n"
  "v_mfma_f32_16x16x32_bf16 v[112:115], a[100:103], v[52:55], v[112:115]\n"
  "v_mfma_f32_16x16x32_bf16 v[68:71], a[108:111], v[48:51], v[68:71]\n"
  "buffer_load_dwordx4 v0, s[24:27], s72 offen offset:1024 sc1 lds\n"
  "v_mfma_f32_16x16x32_bf16 v[116:119], a[108:111], v[52:55], v[116:119]\n"
  "v_mfma_f32_16x16x32_bf16 v[72:75], v[92:95], v[48:51], v[72:75]\n"
  "v_mfma_f32_16x16x32_bf16 v[120:123], v[92:95], v[52:55], v[120:123]\n"
  "v_mfma_f32_16x16x32_bf16 v[76:79], v[100:103], v[48:51], v[76:79]\n"
  "v_mfma_f32_16x16x32_bf16 v[124:127], v[100:103], v[52:55], v[124:127]\n"
  "s_waitcnt lgkmcnt(3)\n"
  "v_mfma_f32_16x16x32_bf16 v[56:59], a[112:115], v[24:27], v[56:59]\n"
  "s_waitcnt lgkmcnt(2)\n"
  "v_mfma_f32_16x16x32_bf16 v[104:107], a[112:115], v[28:31], v[104:107]\n"
  "ds_read_b128 v[88:91], v5 offset:10240\n"
  "ds_read_b128 v[92:95], v5 offset:11264\n"
  "v_mfma_f32_16x16x32_bf16 v[60:63], a[120:123], v[24:27], v[60:63]\n"
  "v_mfma_f32_16x16x32_bf16 v[108:111], a[120:123], v[28:31], v[108:111]\n"
  "ds_read_b128 v[96:99], v5 offset:12288\n"
  "ds_read_b128 v[100:103], v5 offset:13312\n"
  "v_mfma_f32_16x16x32_bf16 v[64:67], a[128:131], v[24:27], v[64:67]\n"
  "v_mfma_f32_16x16x32_bf16 v[112:115], a[128:131], v[28:31], v[112:115]\n"
  "ds_read_b128 v[40:43], v3 offset:0\n"
  "v_mfma_f32_16x16x32_bf16 v[68:71], a[136:139], v[24:27], v[68:71]\n"
  "v_mfma_f32_16x16x32_bf16 v[116:119], a[136:139], v[28:31], v[116:119]\n"
  "ds_read_b128 v[44:47], v3 offset:10240\n"
  "v_mfma_f32_16x16x32_bf16 v[72:75], a[144:147], v[24:27], v[72:75]\n"
  "v_mfma_f32_16x16x32_bf16 v[120:123], a[144:147], v[28:31], v[120:123]\n"
  "ds_read_b128 v[48:51], v3 offset:20480\n"
  "v_mfma_f32_16x16x32_bf16 v[76:79], v[80:83], v[24:27], v[76:79]\n"
  "v_mfma_f32_16x16x32_bf16 v[124:127], v[80:83], v[28:31], v[124:127]\n"
  "ds_read_b128 v[52:55], v3 offset:30720\n"
  "s_waitcnt lgkmcnt(9)\n"
  "v_mfma_f32_16x16x32_bf16 v[56:59], a[116:119], v[32:35], v[56:59]\n"
  "s_waitcnt lgkmcnt(8)\n"
  "v_mfma_f32_16x16x32_bf16 v[104:107], a[116:119], v[36:39], v[104:107]\n"
  "v_mfma_f32_16x16x32_bf16 v[60:63], a[124:127], v[32:35], v[60:63]\n"
  "v_mfma_f32_16x16x32_bf16 v[108:111], a[124:127], v[36:39], v[108:111]\n"
  "v_mfma_f32_16x16x32_bf16 v[64:67], a[132:135], v[32:35], v[64:67]\n"
  "v_mfma_f32_16x16x32_bf16 v[112:115], a[132:135], v[36:39], v[112:115]\n"
  "v_mfma_f32_16x16x32_bf16 v[68:71], a[140:143], v[32:35], v[68:71]\n"
  "v_mfma_f32_16x16x32_bf16 v[116:119], a[140:143], v[36:39], v[116:119]\n"
  "v_mfma_f32_16x16x32_bf16 v[72:75], a[148:151], v[32:35], v[72:75]\n"
  "v_mfma_f32_16x16x32_bf16 v[120:123], a[148:151], v[36:39], v[120:123]\n"
  "v_mfma_f32_16x16x32_bf16 v[76:79], v[84:87], v[32:35], v[76:79]\n"
  "v_mfma_f32_16x16x32_bf16 v[124:127], v[84:87], v[36:39], v[124:127]\n"
  "s_waitcnt lgkmcnt(3)\n"
  "v_mfma_f32_16x16x32_bf16 v[56:59], a[152:155], v[40:43], v[56:59]\n"
  "s_waitcnt lgkmcnt(2)\n"
  "v_mfma_f32_16x16x32_bf16 v[104:107], a[152:155], v[44:47], v[104:107]\n"
  "ds_read_b128 v[80:83], v5 offset:2048\n"
  "ds_read_b128 v[84:87], v5 offset:3072\n"
  "v_mfma_f32_16x16x32_bf16 v[60:63], a[160:163], v[40:43], v[60:63]\n"
  "v_mfma_f32_16x16x32_bf16 v[108:111], a[160:163], v[44:47], v[108:111]\n"
  "v_mfma_f32_16x16x32_bf16 v[64:67], a[168:171], v[40:43], v[64:67]\n"
  "v_mfma_f32_16x16x32_bf16 v[112:115], a[168:171], v[44:47], v[112:115]\n"
  "v_mfma_f32_16x16x32_bf16 v[68:71], a[176:179], v[40:43], v[68:71]\n"
  "v_mfma_f32_16x16x32_bf16 v[116:119], a[176:179], v[44:47], v[116:119]\n"
  "v_mfma_f32_16x16x32_bf16 v[72:75], v[88:91], v[40:43], v[72:75]\n"
  "v_mfma_f32_16x16x32_bf16 v[120:123], v[88:91], v[44:47], v[120:123]\n"
  "v_mfma_f32_16x16x32_bf16 v[76:79], v[96:99], v[40:43], v[76:79]\n"
  "v_mfma_f32_16x16x32_bf16 v[124:127], v[96:99], v[44:47], v[124:127]\n"
  "s_waitcnt lgkmcnt(3)\n"
  "v_mfma_f32_16x16x32_bf16 v[56:59], a[156:159], v[48:51], v[56:59]\n"
  "s_waitcnt lgkmcnt(2)\n"
  "v_mfma_f32_16x16x32_bf16 v[104:107], a[156:159], v[52:55], v[104:107]\n"
  "s_waitcnt vmcnt(0)\n"
  "s_waitcnt lgkmcnt(0)\n"
  "s_barrier\n"
  "s_mov_b32 s62, 0\n"
  "v_readfirstlane_b32 s60, v19\n"
  "s_cmp_lg_u32 s60, -1\n"
  "s_cbranch_scc1 H_F2K_%=\n"
  "s_cmp_ge_u32 s56, 62\n"
  "s_cbranch_scc1 H_F2K_%=\n"
  "s_waitcnt vmcnt(0) lgkmcnt(0)\n"
  "H_F2P_%=:\n"
  "buffer_load_dword v19, off, s[16:19], s74 sc1\n"
  "s_waitcnt vmcnt(0)\n"
  "v_readfirstlane_b32 s60, v19\n"
  "s_cmp_lg_u32 s60, -1\n"
  "s_cbranch_scc1 H_F2K_%=\n"
  "s_sleep 4\n"
  "s_add_u32 s62, s62, 1\n"
  "s_cmp_gt_u32 s62, 300000\n"
  "s_cbranch_scc1 H_FAIL_%=\n"
  "s_and_b32 s60, s62, 255\n"
  "s_cmp_lg_u32 s60, 0\n"
  "s_cbranch_scc1 H_F2P_%=\n"
  "v_mov_b32 v19, 0\n"
  "buffer_load_dword v19, v19, s[40:43], 0 offen sc1\n"
  "s_waitcnt vmcnt(0)\n"
  "v_readfirstlane_b32 s60, v19\n"
  "s_cmp_lg_u32 s60, 0\n"
  "s_cbranch_scc1 H_EXIT_%=\n"
  "s_branch H_F2P_%=\n"
  "H_F2K_%=:\n"
  "v_mfma_f32_16x16x32_bf16 v[60:63], a[164:167], v[48:51], v[60:63]\n"
  "v_mfma_f32_16x16x32_bf16 v[108:111], a[164:167], v[52:55], v[108:111]\n"
  "ds_read_b128 v[24:27], v4 offset:4096\n"
  "v_mfma_f32_16x16x32_bf16 v[64:67], a[172:175], v[48:51], v[64:67]\n"
  "v_mfma_f32_16x16x32_bf16 v[112:115], a[172:175], v[52:55], v[112:115]\n"
  "ds_read_b128 v[28:31], v4 offset:14336\n"
  "v_mfma_f32_16x16x32_bf16 v[68:71], a[180:183], v[48:51], v[68:71]\n"
  "v_mfma_f32_16x16x32_bf16 v[116:119], a[180:183], v[52:55], v[116:119]\n"
  "ds_read_b128 v[32:35], v4 offset:24576\n"
  "v_mfma_f32_16x16x32_bf16 v[72:75], v[92:95], v[48:51], v[72:75]\n"
  "v_mfma_f32_16x16x32_bf16 v[120:123], v[92:95], v[52:55], v[120:123]\n"
  "ds_read_b128 v[36:39], v4 offset:34816\n"
  "v_mfma_f32_16x16x32_bf16 v[76:79], v[100:103], v[48:51], v[76:79]\n"
  "v_mfma_f32_16x16x32_bf16 v[124:127], v[100:103], v[52:55], v[124:127]\n"
  "s_waitcnt lgkmcnt(3)\n"
  "v_mfma_f32_16x16x32_bf16 v[56:59], a[40:43], v[24:27], v[56:59]\n"
  "s_waitcnt lgkmcnt(2)\n"
  "v_mfma_f32_16x16x32_bf16 v[104:107], a[40:43], v[28:31], v[104:107]\n"
  "ds_read_b128 v[88:91], v5 offset:14336\n"
  "ds_read_b128 v[92:95], v5 offset:15360\n"
  "v_mfma_f32_16x16x32_bf16 v[60:63], a[48:51], v[24:27], v[60:63]\n"
  "s_add_u32 s60, s99, 2\n"
  "s_mul_i32 s72, s60, 40960\n"
  "s_mul_i32 s73, s44, 10240\n"
  "s_add_u32 s72, s72, s73\n"
  "s_mov_b32 m0, s73\n"
  "s_nop 0\n"
  "buffer_load_dwordx4 v0, s[24:27], s72 offen offset:0 sc1 lds\n"
  "v_mfma_f32_16x16x32_bf16 v[108:111], a[48:51], v[28:31], v[108:111]\n"
  "ds_read_b128 v[40:43], v1 offset:4096\n"
  "v_mfma_f32_16x16x32_bf16 v[64:67], a[56:59], v[24:27], v[64:67]\n"
  "v_mfma_f32_16x16x32_bf16 v[112:115], a[56:59], v[28:31], v[112:115]\n"
  "ds_read_b128 v[44:47], v1 offset:14336\n"
  "v_mfma_f32_16x16x32_bf16 v[68:71], a[64:67], v[24:27], v[68:71]\n"
  "v_mfma_f32_16x16x32_bf16 v[116:119], a[64:67], v[28:31], v[116:119]\n"
  "ds_read_b128 v[48:51], v1 offset:24576\n"
  "v_mfma_f32_16x16x32_bf16 v[72:75], a[72:75], v[24:27], v[72:75]\n"
  "v_mfma_f32_16x16x32_bf16 v[120:123], a[72:75], v[28:31], v[120:123]\n"
  "ds_read_b128 v[52:55], v1 offset:34816\n"
  "v_mfma_f32_16x16x32_bf16 v[76:79], v[80:83], v[24:27], v[76:79]\n"
  "v_mfma_f32_16x16x32_bf16 v[124:127], v[80:83], v[28:31], v[124:127]\n"
  "s_waitcnt lgkmcnt(7)\n"
  "v_mfma_f32_16x16x32_bf16 v[56:59], a[44:47], v[32:35], v[56:59]\n"
  "s_waitcnt lgkmcnt(6)\n"
  "v_mfma_f32_16x16x32_bf16 v[104:107], a[44:47], v[36:39], v[104:107]\n"
  "v_mfma_f32_16x16x32_bf16 v[60:63], a[52:55], v[32:35], v[60:63]\n"
  "buffer_load_dwordx4 v0, s[24:27], s72 offen offset:1024 sc1 lds\n"
  "v_mfma_f32_16x16x32_bf16 v[108:111], a[52:55], v[36:39], v[108:111]\n"
  "v_mfma_f32_16x16x32_bf16 v[64:67], a[60:63], v[32:35], v[64:67]\n"
  "v_mfma_f32_16x16x32_bf16 v[112:115], a[60:63], v[36:39], v[112:115]\n"
  "v_mfma_f32_16x16x32_bf16 v[68:71], a[68:71], v[32:35], v[68:71]\n"
  "v_mfma_f32_16x16x32_bf16 v[116:119], a[68:71], v[36:39], v[116:119]\n"
  "v_mfma_f32_16x16x32_bf16 v[72:75], a[76:79], v[32:35], v[72:75]\n"
  "v_mfma_f32_16x16x32_bf16 v[120:123], a[76:79], v[36:39], v[120:123]\n"
  "v_mfma_f32_16x16x32_bf16 v[76:79], v[84:87], v[32:35], v[76:79]\n"
  "v_mfma_f32_16x16x32_bf16 v[124:127], v[84:87], v[36:39], v[124:127]\n"
  "s_waitcnt lgkmcnt(3)\n"
  "v_mfma_f32_16x16x32_bf16 v[56:59], a[184:187], v[40:43], v[56:59]\n"
  "s_waitcnt lgkmcnt(2)\n"
  "v_mfma_f32_16x16x32_bf16 v[104:107], a[184:187], v[44:47], v[104:107]\n"
  "ds_read_b128 v[96:99], v5 offset:16384\n"
  "ds_read_b128 v[100:103], v5 offset:17408\n"
  "v_mfma_f32_16x16x32_bf16 v[60:63], a[192:195], v[40:43], v[60:63]\n"
  "buffer_load_dwordx4 v0, s[24:27], s72 offen offset:2048 sc1 lds\n"
  "v_mfma_f32_16x16x32_bf16 v[108:111], a[192:195], v[44:47], v[108:111]\n"
  "ds_read_b128 v[24:27], v2 offset:4096\n"
  "v_mfma_f32_16x16x32_bf16 v[64:67], a[200:203], v[40:43], v[64:67]\n"
  "v_mfma_f32_16x16x32_bf16 v[112:115], a[200:203], v[44:47], v[112:115]\n"
  "ds_read_b128 v[28:31], v2 offset:14336\n"
  "v_mfma_f32_16x16x32_bf16 v[68:71], v[88:91], v[40:43], v[68:71]\n"
  "v_mfma_f32_16x16x32_bf16 v[116:119], v[88:91], v[44:47], v[116:119]\n"
  "ds_read_b128 v[32:35], v2 offset:24576\n"
  "v_mfma_f32_16x16x32_bf16 v[72:75], a[208:211], v[40:43], v[72:75]\n"
  "v_mfma_f32_16x16x32_bf16 v[120:123], a[208:211], v[44:47], v[120:123]\n"
  "ds_read_b128 v[36:39], v2 offset:34816\n"
  "v_mfma_f32_16x16x32_bf16 v[76:79], a[216:219], v[40:43], v[76:79]\n"
  "v_mfma_f32_16x16x32_bf16 v[124:127], a[216:219], v[44:47], v[124:127]\n"
  "s_waitcnt lgkmcnt(7)\n"
  "v_mfma_f32_16x16x32_bf16 v[56:59], a[188:191], v[48:51], v[56:59]\n"
  "s_waitcnt lgkmcnt(6)\n"
  "v_mfma_f32_16x16x32_bf16 v[104:107], a[188:191], v[52:55], v[104:107]\n"
  "v_mfma_f32_16x16x32_bf16 v[60:63], a[196:199], v[48:51], v[60:63]\n"
  "buffer_load_dwordx4 v0, s[24:27], s72 offen offset:3072 sc1 lds\n"
  "v_mfma_f32_16x16x32_bf16 v[108:111], a[196:199], v[52:55], v[108:111]\n"
  "v_mfma_f32_16x16x32_bf16 v[64:67], a[204:207], v[48:51], v[64:67]\n"
  "v_mfma_f32_16x16x32_bf16 v[112:115], a[204:207], v[52:55], v[112:115]\n"
  "v_mfma_f32_16x16x32_bf16 v[68:71], v[92:95], v[48:51], v[68:71]\n"
  "v_mfma_f32_16x16x32_bf16 v[116:119], v[92:95], v[52:55], v[116:119]\n"
  "v_mfma_f32_16x16x32_bf16 v[72:75], a[212:215], v[48:51], v[72:75]\n"
  "v_mfma_f32_16x16x32_bf16 v[120:123], a[212:215], v[52:55], v[120:123]\n"
  "v_mfma_f32_16x16x32_bf16 v[76:79], a[220:223], v[48:51], v[76:79]\n"
  "v_mfma_f32_16x16x32_bf16 v[124:127], a[220:223], v[52:55], v[124:127]\n"
  "s_waitcnt lgkmcnt(3)\n"
  "v_mfma_f32_16x16x32_bf16 v[56:59], a[224:227], v[24:27], v[56:59]\n"
  "s_waitcnt lgkmcnt(2)\n"
  "v_mfma_f32_16x16x32_bf16 v[104:107], a[224:227], v[28:31], v[104:107]\n"
  "ds_read_b128 v[80:83], v5 offset:18432\n"
  "ds_read_b128 v[84:87], v5 offset:19456\n"
  "v_mfma_f32_16x16x32_bf16 v[60:63], a[232:235], v[24:27], v[60:63]\n"
  "v_mfma_f32_16x16x32_bf16 v[108:111], a[232:235], v[28:31], v[108:111]\n"
  "ds_read_b128 v[40:43], v3 offset:4096\n"
  "v_mfma_f32_16x16x32_bf16 v[64:67], a[240:243], v[24:27], v[64:67]\n"
  "v_mfma_f32_16x16x32_bf16 v[112:115], a[240:243], v[28:31], v[112:115]\n"
  "ds_read_b128 v[44:47], v3 offset:14336\n"
  "v_mfma_f32_16x16x32_bf16 v[68:71], a[248:251], v[24:27], v[68:71]\n"
  "v_mfma_f32_16x16x32_bf16 v[116:119], a[248:251], v[28:31], v[116:119]\n"
  "ds_read_b128 v[48:51], v3 offset:24576\n"
  "v_mfma_f32_16x16x32_bf16 v[72:75], v[96:99], v[24:27], v[72:75]\n"
  "v_mfma_f32_16x16x32_bf16 v[120:123], v[96:99], v[28:31], v[120:123]\n"
  "ds_read_b128 v[52:55], v3 offset:34816\n"
  "v_mfma_f32_16x16x32_bf16 v[76:79], v[128:131], v[24:27], v[76:79]\n"
  "v_mfma_f32_16x16x32_bf16 v[124:127], v[128:131], v[28:31], v[124:127]\n"
  "s_waitcnt lgkmcnt(7)\n"
  "v_mfma_f32_16x16x32_bf16 v[56:59], a[228:231], v[32:35], v[56:59]\n"
  "s_waitcnt lgkmcnt(6)\n"
  "v_mfma_f32_16x16x32_bf16 v[104:107], a[228:231], v[36:39], v[104:107]\n"
  "v_mfma_f32_16x16x32_bf16 v[60:63], a[236:239], v[32:35], v[60:63]\n"
  "v_mfma_f32_16x16x32_bf16 v[108:111], a[236:239], v[36:39], v[108:111]\n"
  "v_mfma_f32_16x16x32_bf16 v[64:67], a[244:247], v[32:35], v[64:67]\n"
  "v_mfma_f32_16x16x32_bf16 v[112:115], a[244:247], v[36:39], v[112:115]\n"
  "v_mfma_f32_16x16x32_bf16 v[68:71], a[252:255], v[32:35], v[68:71]\n"
  "v_mfma_f32_16x16x32_bf16 v[116:119], a[252:255], v[36:39], v[116:119]\n"
  "v_mfma_f32_16x16x32_bf16 v[72:75], v[100:103], v[32:35], v[72:75]\n"
  "v_mfma_f32_16x16x32_bf16 v[120:123], v[100:103], v[36:39], v[120:123]\n"
  "v_mfma_f32_16x16x32_bf16 v[76:79], v[132:135], v[32:35], v[76:79]\n"
  "v_mfma_f32_16x16x32_bf16 v[124:127], v[132:135], v[36:39], v[124:127]\n"
  "s_waitcnt lgkmcnt(3)\n"
  "v_mfma_f32_16x16x32_bf16 v[56:59], v[136:139], v[40:43], v[56:59]\n"
  "s_waitcnt lgkmcnt(2)\n"
  "v_mfma_f32_16x16x32_bf16 v[104:107], v[136:139], v[44:47], v[104:107]\n"
  "ds_read_b128 v[88:91], v5 offset:20480\n"
  "ds_read_b128 v[92:95], v5 offset:21504\n"
  "v_mfma_f32_16x16x32_bf16 v[60:63], v[144:147], v[40:43], v[60:63]\n"
  "v_mfma_f32_16x16x32_bf16 v[108:111], v[144:147], v[44:47], v[108:111]\n"
  "ds_read_b128 v[24:27], v0 offset:8192\n"
  "v_mfma_f32_16x16x32_bf16 v[64:67], v[152:155], v[40:43], v[64:67]\n"
  "v_mfma_f32_16x16x32_bf16 v[112:115], v[152:155], v[44:47], v[112:115]\n"
  "ds_read_b128 v[28:31], v0 offset:18432\n"
  "v_mfma_f32_16x16x32_bf16 v[68:71], v[80:83], v[40:43], v[68:71]\n"
  "v_mfma_f32_16x16x32_bf16 v[116:119], v[80:83], v[44:47], v[116:119]\n"
  "ds_read_b128 v[32:35], v0 offset:28672\n"
  "v_mfma_f32_16x16x32_bf16 v[72:75], v[160:163], v[40:43], v[72:75]\n"
  "v_mfma_f32_16x16x32_bf16 v[120:123], v[160:163], v[44:47], v[120:123]\n"
  "ds_read_b128 v[36:39], v0 offset:38912\n"
  "v_mfma_f32_16x16x32_bf16 v[76:79], v[168:171], v[40:43], v[76:79]\n"
  "v_mfma_f32_16x16x32_bf16 v[124:127], v[168:171], v[44:47], v[124:127]\n"
  "s_waitcnt lgkmcnt(7)\n"
  "v_mfma_f32_16x16x32_bf16 v[56:59], v[140:143], v[48:51], v[56:59]\n"
  "s_waitcnt lgkmcnt(6)\n"
  "v_mfma_f32_16x16x32_bf16 v[104:107], v[140:143], v[52:55], v[104:107]\n"
  "v_mfma_f32_16x16x32_bf16 v[60:63], v[148:151], v[48:51], v[60:63]\n"
  "v_mfma_f32_16x16x32_bf16 v[108:111], v[148:151], v[52:55], v[108:111]\n"
  "v_mfma_f32_16x16x32_bf16 v[64:67], v[156:159], v[48:51], v[64:67]\n"
  "v_mfma_f32_16x16x32_bf16 v[112:115], v[156:159], v[52:55], v[112:115]\n"
  "v_mfma_f32_16x16x32_bf16 v[68:71], v[84:87], v[48:51], v[68:71]\n"
  "v_mfma_f32_16x16x32_bf16 v[116:119], v[84:87], v[52:55], v[116:119]\n"
  "v_mfma_f32_16x16x32_bf16 v[72:75], v[164:167], v[48:51], v[72:75]\n"
  "v_mfma_f32_16x16x32_bf16 v[120:123], v[164:167], v[52:55], v[120:123]\n"
  "v_mfma_f32_16x16x32_bf16 v[76:79], v[172:175], v[48:51], v[76:79]\n"
  "v_mfma_f32_16x16x32_bf16 v[124:127], v[172:175], v[52:55], v[124:127]\n"
  "s_waitcnt lgkmcnt(3)\n"
  "v_mfma_f32_16x16x32_bf16 v[56:59], v[176:179], v[24:27], v[56:59]\n"
  "s_waitcnt lgkmcnt(2)\n"
  "v_mfma_f32_16x16x32_bf16 v[104:107], v[176:179], v[28:31], v[104:107]\n"
  "ds_read_b128 v[96:99], v5 offset:22528\n"
  "ds_read_b128 v[100:103], v5 offset:23552\n"
  "v_mfma_f32_16x16x32_bf16 v[60:63], v[184:187], v[24:27], v[60:63]\n"
  "v_mfma_f32_16x16x32_bf16 v[108:111], v[184:187], v[28:31], v[108:111]\n"
  "ds_read_b128 v[40:43], v0 offset:9216\n"
  "v_mfma_f32_16x16x32_bf16 v[64:67], v[192:195], v[24:27], v[64:67]\n"
  "v_mfma_f32_16x16x32_bf16 v[112:115], v[192:195], v[28:31], v[112:115]\n"
  "ds_read_b128 v[44:47], v0 offset:19456\n"
  "v_mfma_f32_16x16x32_bf16 v[68:71], v[200:203], v[24:27], v[68:71]\n"
  "v_mfma_f32_16x16x32_bf16 v[116:119], v[200:203], v[28:31], v[116:119]\n"
  "ds_read_b128 v[48:51], v0 offset:29696\n"
  "v_mfma_f32_16x16x32_bf16 v[72:75], v[88:91], v[24:27], v[72:75]\n"
  "v_mfma_f32_16x16x32_bf16 v[120:123], v[88:91], v[28:31], v[120:123]\n"
  "ds_read_b128 v[52:55], v0 offset:39936\n"
  "v_mfma_f32_16x16x32_bf16 v[76:79], v[208:211], v[24:27], v[76:79]\n"
  "v_mfma_f32_16x16x32_bf16 v[124:127], v[208:211], v[28:31], v[124:127]\n"
  "buffer_load_dword v19, v17, s[16:19], s57 offen sc1\n"
  "s_waitcnt lgkmcnt(7)\n"
  "v_mfma_f32_16x16x32_bf16 v[56:59], v[180:183], v[32:35], v[56:59]\n"
  "s_waitcnt lgkmcnt(6)\n"
  "v_mfma_f32_16x16x32_bf16 v[104:107], v[180:183], v[36:39], v[104:107]\n"
  "v_mfma_f32_16x16x32_bf16 v[60:63], v[188:191], v[32:35], v[60:63]\n"
  "v_mfma_f32_16x16x32_bf16 v[108:111], v[188:191], v[36:39], v[108:111]\n"
  "v_mfma_f32_16x16x32_bf16 v[64:67], v[196:199], v[32:35], v[64:67]\n"
  "v_mfma_f32_16x16x32_bf16 v[112:115], v[196:199], v[36:39], v[112:115]\n"
  "v_mfma_f32_16x16x32_bf16 v[68:71], v[204:207], v[32:35], v[68:71]\n"
  "v_mfma_f32_16x16x32_bf16 v[116:119], v[204:207], v[36:39], v[116:119]\n"
  "v_mfma_f32_16x16x32_bf16 v[72:75], v[92:95], v[32:35], v[72:75]\n"
  "v_mfma_f32_16x16x32_bf16 v[120:123], v[92:95], v[36:39], v[120:123]\n"
  "v_mfma_f32_16x16x32_bf16 v[76:79], v[212:215], v[32:35], v[76:79]\n"
  "v_mfma_f32_16x16x32_bf16 v[124:127], v[212:215], v[36:39], v[124:127]\n"
  "s_waitcnt lgkmcnt(3)\n"
  "v_mfma_f32_16x16x32_bf16 v[56:59], v[216:219], v[40:43], v[56:59]\n"
  "s_waitcnt lgkmcnt(2)\n"
  "v_mfma_f32_16x16x32_bf16 v[104:107], v[216:219], v[44:47], v[104:107]\n"
  "v_mfma_f32_16x16x32_bf16 v[60:63], v[224:227], v[40:43], v[60:63]\n"
  "v_mfma_f32_16x16x32_bf16 v[108:111], v[224:227], v[44:47], v[108:111]\n"
  "v_mfma_f32_16x16x32_bf16 v[64:67], v[232:235], v[40:43], v[64:67]\n"
  "v_mfma_f32_16x16x32_bf16 v[112:115], v[232:235], v[44:47], v[112:115]\n"
  "v_mfma_f32_16x16x32_bf16 v[68:71], v[240:243], v[40:43], v[68:71]\n"
  "v_mfma_f32_16x16x32_bf16 v[116:119], v[240:243], v[44:47], v[116:119]\n"
  "v_mfma_f32_16x16x32_bf16 v[72:75], v[248:251], v[40:43], v[72:75]\n"
  "v_mfma_f32_16x16x32_bf16 v[120:123], v[248:251], v[44:47], v[120:123]\n"
  "v_mfma_f32_16x16x32_bf16 v[76:79], v[96:99], v[40:43], v[76:79]\n"
  "v_mfma_f32_16x16x32_bf16 v[124:127], v[96:99], v[44:47], v[124:127]\n"
  "s_waitcnt lgkmcnt(1)\n"
  "v_mfma_f32_16x16x32_bf16 v[56:59], v[220:223], v[48:51], v[56:59]\n"
  "s_waitcnt lgkmcnt(0)\n"
  "v_mfma_f32_16x16x32_bf16 v[104:107], v[220:223], v[52:55], v[104:107]\n"
  "v_mfma_f32_16x16x32_bf16 v[60:63], v[228:231], v[48:51], v[60:63]\n"
  "v_mfma_f32_16x16x32_bf16 v[108:111], v[228:231], v[52:55], v[108:111]\n"
  "v_mfma_f32_16x16x32_bf16 v[64:67], v[236:239], v[48:51], v[64:67]\n"
  "v_mfma_f32_16x16x32_bf16 v[112:115], v[236:239], v[52:55], v[112:115]\n"
  "v_mfma_f32_16x16x32_bf16 v[68:71], v[244:247], v[48:51], v[68:71]\n"
  "v_mfma_f32_16x16x32_bf16 v[116:119], v[244:247], v[52:55], v[116:119]\n"
  "v_mfma_f32_16x16x32_bf16 v[72:75], v[252:255], v[48:51], v[72:75]\n"
  "v_mfma_f32_16x16x32_bf16 v[120:123], v[252:255], v[52:55], v[120:123]\n"
  "v_mfma_f32_16x16x32_bf16 v[76:79], v[100:103], v[48:51], v[76:79]\n"
  "v_mfma_f32_16x16x32_bf16 v[124:127], v[100:103], v[52:55], v[124:127]\n"
  "s_mov_b32 s62, 0\n"
  "s_waitcnt vmcnt(0)\n"
  "v_readfirstlane_b32 s59, v19\n"
  "s_cmp_lg_u32 s59, -1\n"
  "s_cbranch_scc1 H_SCHOK_%=\n"
  "H_SCHP_%=:\n"
  "s_nop 7\n"
  "buffer_load_dword v19, v17, s[16:19], s57 offen sc1\n"
  "s_waitcnt vmcnt(0)\n"
  "v_readfirstlane_b32 s59, v19\n"
  "s_cmp_lg_u32 s59, -1\n"
  "s_cbranch_scc1 H_SCHOK_%=\n"
  "s_sleep 2\n"
  "s_add_u32 s62, s62, 1\n"
  "s_cmp_gt_u32 s62, 300000\n"
  "s_cbranch_scc1 H_FAIL_%=\n"
  "s_and_b32 s60, s62, 255\n"
  "s_cmp_lg_u32 s60, 0\n"
  "s_cbranch_scc1 H_SCHP_%=\n"
  "v_mov_b32 v19, 0\n"
  "buffer_load_dword v19, v19, s[40:43], 0 offen sc1\n"
  "s_waitcnt vmcnt(0)\n"
  "v_readfirstlane_b32 s60, v19\n"
  "s_cmp_lg_u32 s60, 0\n"
  "s_cbranch_scc1 H_EXIT_%=\n"
  "s_branch H_SCHP_%=\n"
  "H_SCHOK_%=:\n"
  "s_nop 7\n"
  "s_memrealtime s[64:65]\n"
  "s_waitcnt lgkmcnt(0)\n"
  "s_sub_i32 s60, s59, s64\n"
  "s_cmp_gt_i32 s60, 2000\n"
  "s_cselect_b32 s59, s64, s59\n"
  "s_cmp_lt_i32 s60, -100000\n"
  "s_cselect_b32 s59, s64, s59\n"
  "s_sub_i32 s60, s64, s59\n"
  "s_cmp_gt_i32 s60, 0\n"
  "s_cselect_b32 s59, s64, s59\n"
  "s_add_i32 s60, s60, 100\n"
  "s_max_i32 s60, s60, 0\n"
  "s_min_i32 s3, s60, 200\n"
  "s_cmp_lt_u32 s56, 4\n"
  "s_cselect_b32 s3, 0, s3\n"
  "v_cvt_pk_bf16_f32 v40, v104, v105\n"
  "v_cvt_pk_bf16_f32 v41, v106, v107\n"
  "v_cvt_pk_bf16_f32 v42, v108, v109\n"
  "v_cvt_pk_bf16_f32 v43, v110, v111\n"
  "s_mov_b32 s62, 0\n"
  "H_WT_%=:\n"
  "s_memrealtime s[64:65]\n"
  "s_waitcnt lgkmcnt(0)\n"
  "s_sub_i32 s60, s64, s59\n"
  "s_cmp_lt_i32 s60, 0\n"
  "s_cbranch_scc1 H_WT_%=\n"
  "buffer_load_dwordx4 v[104:107], v6, s[16:19], s57 offen sc1\n"
  "v_cvt_pk_bf16_f32 v46, v112, v113\n"
  "v_cvt_pk_bf16_f32 v47, v114, v115\n"
  "v_cvt_pk_bf16_f32 v28, v116, v117\n"
  "v_cvt_pk_bf16_f32 v29, v118, v119\n"
  "v_cvt_pk_bf16_f32 v30, v120, v121\n"
  "v_cvt_pk_bf16_f32 v31, v122, v123\n"
  "v_cvt_pk_bf16_f32 v34, v124, v125\n"
  "v_cvt_pk_bf16_f32 v35, v126, v127\n"
  "v_cvt_pk_bf16_f32 v36, v56, v57\n"
  "v_cvt_pk_bf16_f32 v37, v58, v59\n"
  "buffer_load_dwordx4 v[108:111], v6, s[16:19], s57 offen offset:1024 sc1\n"
  "v_cvt_pk_bf16_f32 v38, v60, v61\n"
  "v_cvt_pk_bf16_f32 v39, v62, v63\n"
  "v_cvt_pk_bf16_f32 v44, v64, v65\n"
  "v_cvt_pk_bf16_f32 v45, v66, v67\n"
  "v_cvt_pk_bf16_f32 v24, v68, v69\n"
  "v_cvt_pk_bf16_f32 v25, v70, v71\n"
  "v_cvt_pk_bf16_f32 v26, v72, v73\n"
  "v_cvt_pk_bf16_f32 v27, v74, v75\n"
  "v_cvt_pk_bf16_f32 v32, v76, v77\n"
  "v_cvt_pk_bf16_f32 v33, v78, v79\n"
  "buffer_load_dwordx2 v[54:55], v7, s[16:19], s57 offen sc1\n"
  "ds_read_b128 v[80:83], v5 offset:0\n"
  "ds_read_b128 v[84:87], v5 offset:1024\n"
  "ds_read_b128 v[88:91], v5 offset:2048\n"
  "ds_read_b128 v[92:95], v5 offset:3072\n"
  "ds_read_b128 v[96:99], v5 offset:4096\n"
  "ds_read_b128 v[100:103], v5 offset:5120\n"
  "v_mov_b32 v56, 0\n"
  "v_mov_b32 v57, 0\n"
  "v_mov_b32 v58, 0\n"
  "v_mov_b32 v59, 0\n"
  "v_mov_b32 v60, 0\n"
  "v_mov_b32 v61, 0\n"
  "v_mov_b32 v62, 0\n"
  "v_mov_b32 v63, 0\n"
  "v_mov_b32 v64, 0\n"
  "v_mov_b32 v65, 0\n"
  "v_mov_b32 v66, 0\n"
  "v_mov_b32 v67, 0\n"
  "v_mov_b32 v68, 0\n"
  "v_mov_b32 v69, 0\n"
  "v_mov_b32 v70, 0\n"
  "v_mov_b32 v71, 0\n"
  "v_mov_b32 v72, 0\n"
  "v_mov_b32 v73, 0\n"
  "v_mov_b32 v74, 0\n"
  "v_mov_b32 v75, 0\n"
  "v_mov_b32 v76, 0\n"
  "v_mov_b32 v77, 0\n"
  "v_mov_b32 v78, 0\n"
  "v_mov_b32 v79, 0\n"
  "s_waitcnt lgkmcnt(0)\n"
  "H_D0_%=:\n"
  "s_waitcnt vmcnt(2)\n"
  "v_smfmac_f32_16x16x64_bf16 v[56:59], v[104:107], a[0:7], v9\n"
  "ds_write_b128 v4, v[104:107] offset:40960\n"
  "v_smfmac_f32_16x16x64_bf16 v[60:63], v[104:107], a[8:15], v9\n"
  "v_max3_u32 v19, v104, v105, v106\n"
  "v_max_u32 v19, v107, v19\n"
  "v_smfmac_f32_16x16x64_bf16 v[64:67], v[104:107], a[16:23], v9\n"
  "v_cmp_eq_u32 vcc, -1, v19\n"
  "v_smfmac_f32_16x16x64_bf16 v[68:71], v[104:107], a[24:31], v9\n"
  "s_cbranch_vccnz H_FIX0_%=\n"
  "v_smfmac_f32_16x16x64_bf16 v[72:75], v[104:107], a[32:39], v9\n"
  "s_waitcnt lgkmcnt(0)\n"
  "s_barrier\n"
  "ds_read_b128 v[112:115], v1 offset:40960\n"
  "s_waitcnt vmcnt(1)\n"
  "v_max3_u32 v19, v108, v109, v110\n"
  "v_max_u32 v19, v111, v19\n"
  "v_smfmac_f32_16x16x64_bf16 v[76:79], v[104:107], v[80:87], v9\n"
  "v_cmp_eq_u32 vcc, -1, v19\n"
  "s_cbranch_vccnz H_FIX1_%=\n"
  "H_D1_%=:\n"
  "v_smfmac_f32_16x16x64_bf16 v[56:59], v[108:111], a[40:47], v9\n"
  "ds_read_b128 v[116:119], v2 offset:40960\n"
  "v_smfmac_f32_16x16x64_bf16 v[60:63], v[108:111], a[48:55], v9\n"
  "ds_read_b128 v[80:83], v5 offset:6144\n"
  "ds_read_b128 v[84:87], v5 offset:7168\n"
  "v_smfmac_f32_16x16x64_bf16 v[64:67], v[108:111], a[56:63], v9\n"
  "ds_write_b128 v4, v[108:111] offset:45056\n"
  "v_smfmac_f32_16x16x64_bf16 v[68:71], v[108:111], a[64:71], v9\n"
  "ds_read_b128 v[120:123], v3 offset:40960\n"
  "v_smfmac_f32_16x16x64_bf16 v[72:75], v[108:111], a[72:79], v9\n"
  "s_waitcnt vmcnt(0)\n"
  "v_max_u32 v19, v54, v55\n"
  "v_smfmac_f32_16x16x64_bf16 v[76:79], v[108:111], v[88:95], v9\n"
  "v_cmp_eq_u32 vcc, -1, v19\n"
  "s_waitcnt lgkmcnt(5)\n"
  "v_smfmac_f32_16x16x64_bf16 v[56:59], v[112:115], a[80:87], v9\n"
  "s_cbranch_vccnz H_FIX2_%=\n"
  "H_D2_%=:\n"
  "v_smfmac_f32_16x16x64_bf16 v[60:63], v[112:115], a[88:95], v9\n"
  "ds_write_b64 v8, v[54:55]\n"
  "v_smfmac_f32_16x16x64_bf16 v[64:67], v[112:115], a[96:103], v9\n"
  "ds_read_b128 v[88:91], v5 offset:8192\n"
  "ds_read_b128 v[92:95], v5 offset:9216\n"
  "v_smfmac_f32_16x16x64_bf16 v[68:71], v[112:115], a[104:111], v9\n"
  "v_smfmac_f32_16x16x64_bf16 v[72:75], v[112:115], v[96:103], v9\n"
  "s_waitcnt lgkmcnt(5)\n"
  "v_smfmac_f32_16x16x64_bf16 v[76:79], v[112:115], v[80:87], v9\n"
  "s_waitcnt lgkmcnt(2)\n"
  "s_barrier\n"
  "v_smfmac_f32_16x16x64_bf16 v[56:59], v[116:119], a[112:119], v9\n"
  "ds_read_b128 v[96:99], v5 offset:10240\n"
  "v_smfmac_f32_16x16x64_bf16 v[60:63], v[116:119], a[120:127], v9\n"
  "ds_read_b128 v[100:103], v5 offset:11264\n"
  "v_smfmac_f32_16x16x64_bf16 v[64:67], v[116:119], a[128:135], v9\n"
  "ds_read_b128 v[80:83], v5 offset:12288\n"
  "v_smfmac_f32_16x16x64_bf16 v[68:71], v[116:119], a[136:143], v9\n"
  "ds_read_b128 v[84:87], v5 offset:13312\n"
  "v_smfmac_f32_16x16x64_bf16 v[72:75], v[116:119], a[144:151], v9\n"
  "ds_read_b128 v[124:127], v1 offset:45056\n"
  "s_waitcnt lgkmcnt(5)\n"
  "v_smfmac_f32_16x16x64_bf16 v[76:79], v[116:119], v[88:95], v9\n"
  "ds_read_b128 v[104:107], v2 offset:45056\n"
  "v_smfmac_f32_16x16x64_bf16 v[56:59], v[120:123], a[152:159], v9\n"
  "ds_read_b128 v[88:91], v5 offset:14336\n"
  "v_smfmac_f32_16x16x64_bf16 v[60:63], v[120:123], a[160:167], v9\n"
  "ds_read_b128 v[92:95], v5 offset:15360\n"
  "v_smfmac_f32_16x16x64_bf16 v[64:67], v[120:123], a[168:175], v9\n"
  "ds_read_b128 v[108:111], v3 offset:45056\n"
  "v_smfmac_f32_16x16x64_bf16 v[68:71], v[120:123], a[176:183], v9\n"
  "ds_read_b128 v[112:115], v0 offset:49152\n"
  "s_waitcnt lgkmcnt(8)\n"
  "v_smfmac_f32_16x16x64_bf16 v[72:75], v[120:123], v[96:103], v9\n"
  "ds_read_b128 v[116:119], v0 offset:50176\n"
  "s_waitcnt lgkmcnt(7)\n"
  "v_smfmac_f32_16x16x64_bf16 v[76:79], v[120:123], v[80:87], v9\n"
  "s_waitcnt lgkmcnt(6)\n"
  "v_smfmac_f32_16x16x64_bf16 v[56:59], v[124:127], a[184:191], v9\n"
  "ds_read_b128 v[96:99], v5 offset:18432\n"
  "s_waitcnt lgkmcnt(4)\n"
  "v_smfmac_f32_16x16x64_bf16 v[68:71], v[124:127], v[88:95], v9\n"
  "ds_read_b128 v[100:103], v5 offset:19456\n"
  "v_smfmac_f32_16x16x64_bf16 v[56:59], v[104:107], a[224:231], v9\n"
  "ds_read_b128 v[80:83], v5 offset:16384\n"
  "v_smfmac_f32_16x16x64_bf16 v[68:71], v[104:107], a[248:255], v9\n"
  "ds_read_b128 v[84:87], v5 offset:17408\n"
  "s_waitcnt lgkmcnt(6)\n"
  "v_smfmac_f32_16x16x64_bf16 v[56:59], v[108:111], v[136:143], v9\n"
  "ds_read_b128 v[88:91], v5 offset:20480\n"
  "s_waitcnt lgkmcnt(3)\n"
  "v_smfmac_f32_16x16x64_bf16 v[68:71], v[108:111], v[96:103], v9\n"
  "ds_read_b128 v[92:95], v5 offset:21504\n"
  "v_smfmac_f32_16x16x64_bf16 v[56:59], v[112:115], v[176:183], v9\n"
  "ds_read_b128 v[96:99], v5 offset:22528\n"
  "v_smfmac_f32_16x16x64_bf16 v[68:71], v[112:115], v[200:207], v9\n"
  "ds_read_b128 v[100:103], v5 offset:23552\n"
  "v_smfmac_f32_16x16x64_bf16 v[56:59], v[116:119], v[216:223], v9\n"
  "v_smfmac_f32_16x16x64_bf16 v[68:71], v[116:119], v[240:247], v9\n"
  "v_smfmac_f32_16x16x64_bf16 v[60:63], v[124:127], a[192:199], v9\n"
  "s_nop 1\n"
  "v_smfmac_f32_16x16x64_bf16 v[72:75], v[124:127], a[208:215], v9\n"
  "s_nop 1\n"
  "v_smfmac_f32_16x16x64_bf16 v[60:63], v[104:107], a[232:239], v9\n"
  "s_nop 1\n"
  "s_waitcnt lgkmcnt(4)\n"
  "v_smfmac_f32_16x16x64_bf16 v[72:75], v[104:107], v[80:87], v9\n"
  "v_cvt_pk_bf16_f32 v56, v56, v57\n"
  "v_cvt_pk_bf16_f32 v57, v58, v59\n"
  "v_smfmac_f32_16x16x64_bf16 v[60:63], v[108:111], v[144:151], v9\n"
  "v_cvt_pk_bf16_f32 v58, v68, v69\n"
  "v_cvt_pk_bf16_f32 v59, v70, v71\n"
  "v_smfmac_f32_16x16x64_bf16 v[72:75], v[108:111], v[160:167], v9\n"
  "s_nop 1\n"
  "v_mfma_f32_16x16x32_bf16 v[68:71], v[56:59], v[20:23], 0\n"
  "v_smfmac_f32_16x16x64_bf16 v[60:63], v[112:115], v[184:191], v9\n"
  "s_nop 1\n"
  "s_waitcnt lgkmcnt(2)\n"
  "v_smfmac_f32_16x16x64_bf16 v[72:75], v[112:115], v[88:95], v9\n"
  "s_nop 1\n"
  "v_smfmac_f32_16x16x64_bf16 v[60:63], v[116:119], v[224:231], v9\n"
  "s_nop 1\n"
  "v_smfmac_f32_16x16x64_bf16 v[72:75], v[116:119], v[248:255], v9\n"
  "v_cvt_pk_bf16_f32 v48, v68, v69\n"
  "v_cvt_pk_bf16_f32 v49, v70, v71\n"
  "v_smfmac_f32_16x16x64_bf16 v[64:67], v[124:127], a[200:207], v9\n"
  "s_nop 1\n"
  "v_smfmac_f32_16x16x64_bf16 v[76:79], v[124:127], a[216:223], v9\n"
  "s_nop 1\n"
  "v_smfmac_f32_16x16x64_bf16 v[64:67], v[104:107], a[240:247], v9\n"
  "v_cvt_pk_bf16_f32 v60, v60, v61\n"
  "v_cvt_pk_bf16_f32 v61, v62, v63\n"
  "v_smfmac_f32_16x16x64_bf16 v[76:79], v[104:107], v[128:135], v9\n"
  "v_cvt_pk_bf16_f32 v62, v72, v73\n"
  "v_cvt_pk_bf16_f32 v63, v74, v75\n"
  "v_smfmac_f32_16x16x64_bf16 v[64:67], v[108:111], v[152:159], v9\n"
  "s_nop 1\n"
  "v_mfma_f32_16x16x32_bf16 v[72:75], v[60:63], v[20:23], 0\n"
  "v_smfmac_f32_16x16x64_bf16 v[76:79], v[108:111], v[168:175], v9\n"
  "s_nop 1\n"
  "v_smfmac_f32_16x16x64_bf16 v[64:67], v[112:115], v[192:199], v9\n"
  "s_nop 1\n"
  "v_smfmac_f32_16x16x64_bf16 v[76:79], v[112:115], v[208:215], v9\n"
  "s_nop 1\n"
  "v_smfmac_f32_16x16x64_bf16 v[64:67], v[116:119], v[232:239], v9\n"
  "v_cvt_pk_bf16_f32 v50, v72, v73\n"
  "v_cvt_pk_bf16_f32 v51, v74, v75\n"
  "s_waitcnt lgkmcnt(0)\n"
  "v_smfmac_f32_16x16x64_bf16 v[76:79], v[116:119], v[96:103], v9\n"
  "v_mfma_f32_16x16x32_bf16 v[120:123], v[24:27], v[48:51], 0\n"
  "s_nop 1\n"
  "v_mfma_f32_16x16x32_bf16 v[56:59], v[28:31], v[48:51], 0\n"
  "s_nop 1\n"
  "v_cvt_pk_bf16_f32 v64, v64, v65\n"
  "v_cvt_pk_bf16_f32 v65, v66, v67\n"
  "v_cvt_pk_bf16_f32 v66, v76, v77\n"
  "v_cvt_pk_bf16_f32 v67, v78, v79\n"
  "v_mfma_f32_16x16x32_bf16 v[68:71], v[48:51], v[48:51], 0\n"
  "s_nop 0\n"
  "v_mfma_f32_16x16x32_bf16 v[76:79], v[64:67], v[20:23], 0\n"
  "s_nop 7\n"
  "v_cvt_pk_bf16_f32 v52, v76, v77\n"
  "v_cvt_pk_bf16_f32 v53, v78, v79\n"
  "s_nop 1\n"
  "v_mfma_f32_16x16x16_bf16 v[120:123], v[32:33], v[52:53], v[120:123]\n"
  "s_nop 1\n"
  "v_mfma_f32_16x16x16_bf16 v[56:59], v[34:35], v[52:53], v[56:59]\n"
  "s_nop 1\n"
  "v_mfma_f32_16x16x16_bf16 v[68:71], v[52:53], v[52:53], v[68:71]\n"
  "s_nop 1\n"
  "ds_write_b128 v10, v[120:123]\n"
  "s_nop 1\n"
  "ds_write_b128 v11, v[56:59]\n"
  "s_nop 1\n"
  "ds_write_b128 v12, v[68:71]\n"
  "s_waitcnt lgkmcnt(0)\n"
  "s_barrier\n"
  "ds_read_b128 v[60:63], v13\n"
  "ds_read_b128 v[64:67], v13 offset:1024\n"
  "ds_read2st64_b32 v[96:97], v14 offset0:0 offset1:1\n"
  "ds_read2st64_b32 v[98:99], v14 offset0:2 offset1:3\n"
  "ds_read_b128 v[72:75], v13 offset:2048\n"
  "ds_read_b128 v[76:79], v13 offset:3072\n"
  "s_waitcnt lgkmcnt(4)\n"
  "v_pk_add_f32 v[60:61], v[60:61], v[64:65]\n"
  "v_pk_add_f32 v[62:63], v[62:63], v[66:67]\n"
  "s_waitcnt lgkmcnt(2)\n"
  "v_pk_add_f32 v[96:97], v[96:97], v[98:99]\n"
  "v_add_f32 v96, v96, v97\n"
  "s_waitcnt lgkmcnt(1)\n"
  "v_pk_add_f32 v[60:61], v[60:61], v[72:73]\n"
  "v_pk_add_f32 v[62:63], v[62:63], v[74:75]\n"
  "s_waitcnt lgkmcnt(0)\n"
  "v_pk_add_f32 v[60:61], v[60:61], v[76:77]\n"
  "v_pk_add_f32 v[62:63], v[62:63], v[78:79]\n"
  "v_max3_f32 v83, v60, v61, v96\n"
  "v_max3_f32 v83, v62, v63, v83\n"
  "v_lshrrev_b32 v92, 4, v0\n"
  "s_lshl_b32 s68, s44, 12\n"
  "v_max_f32_dpp v83, v83, v83 quad_perm:[1,0,3,2] row_mask:0xf bank_mask:0xf\n"
  "v_and_b32 v93, 15, v92\n"
  "v_lshrrev_b32 v94, 4, v92\n"
  "v_max_f32_dpp v83, v83, v83 quad_perm:[2,3,0,1] row_mask:0xf bank_mask:0xf\n"
  "v_and_b32 v92, 7, v93\n"
  "v_lshlrev_b32 v95, 1, v94\n"
  "v_max_f32_dpp v83, v83, v83 row_half_mirror row_mask:0xf bank_mask:0xf\n"
  "v_mul_f32 v83, s76, v83\n"
  "v_fma_f32 v84, v60, s76, -v83\n"
  "v_fma_f32 v85, v61, s76, -v83\n"
  "v_fma_f32 v86, v62, s76, -v83\n"
  "v_fma_f32 v87, v63, s76, -v83\n"
  "v_fma_f32 v88, v96, s76, -v83\n"
  "v_exp_f32 v84, v84\n"
  "v_exp_f32 v85, v85\n"
  "v_exp_f32 v86, v86\n"
  "v_exp_f32 v87, v87\n"
  "v_exp_f32 v88, v88\n"
  "v_add_f32 v80, v84, v85\n"
  "v_add_f32 v81, v86, v87\n"
  "v_add_f32 v80, v80, v88\n"
  "v_add_f32 v83, v80, v81\n"
  "v_xor_b32 v92, v92, v95\n"
  "v_lshlrev_b32 v94, 4, v94\n"
  "v_add_f32_dpp v83, v83, v83 quad_perm:[1,0,3,2] row_mask:0xf bank_mask:0xf\n"
  "v_lshlrev_b32 v92, 4, v92\n"
  "s_lshl_b32 s69, s44, 10\n"
  "v_add_f32_dpp v83, v83, v83 quad_perm:[2,3,0,1] row_mask:0xf bank_mask:0xf\n"
  "v_lshl_add_u32 v92, v93, 7, v92\n"
  "v_lshl_add_u32 v93, v93, 5, v94\n"
  "v_add_f32_dpp v83, v83, v83 row_half_mirror row_mask:0xf bank_mask:0xf\n"
  "v_rcp_f32 v83, v83\n"
  "v_add_u32 v92, s68, v92\n"
  "v_mul_f32 v88, v88, v83\n"
  "v_mul_f32 v84, v84, v83\n"
  "v_mul_f32 v85, v85, v83\n"
  "v_mul_f32 v86, v86, v83\n"
  "v_mul_f32 v87, v87, v83\n"
  "v_mov_b32_dpp v89, v88 quad_perm:[1,0,3,2] row_mask:0xf bank_mask:0xf\n"
  "v_cvt_pk_bf16_f32 v80, v84, v85\n"
  "v_cvt_pk_bf16_f32 v81, v86, v87\n"
  "v_min_u32 v80, s78, v80\n"
  "v_min_u32 v81, s78, v81\n"
  "v_cvt_pk_bf16_f32 v82, v88, v89\n"
  "buffer_store_dwordx2 v[80:81], v15, s[16:19], s63 offen sc1\n"
  "v_min_u32 v82, s78, v82\n"
  "buffer_store_dword v82, v16, s[16:19], s63 offen sc1\n"
  "s_memrealtime s[64:65]\n"
  "v_mov_b32 v102, 63488\n"
  "ds_read_b32 v102, v102\n"
  "v_mfma_f32_16x16x32_bf16 v[104:107], v[24:27], v[36:39], 0\n"
  "v_xor_b32 v95, 16, v92\n"
  "v_add_u32 v93, s69, v93\n"
  "v_mfma_f32_16x16x32_bf16 v[108:111], v[24:27], v[40:43], 0\n"
  "s_lshl_b32 s70, s44, 8\n"
  "s_sub_u32 s69, s69, 16384\n"
  "v_mfma_f32_16x16x32_bf16 v[112:115], v[28:31], v[40:43], 0\n"
  "s_sub_u32 s70, s70, 4096\n"
  "v_add_u32 v100, s69, v13\n"
  "v_mfma_f32_16x16x32_bf16 v[116:119], v[48:51], v[36:39], 0\n"
  "v_add_u32 v101, s70, v14\n"
  "s_and_b32 s71, s56, 7\n"
  "v_mfma_f32_16x16x32_bf16 v[120:123], v[48:51], v[40:43], 0\n"
  "s_xor_b32 s75, s71, 4\n"
  "s_mul_i32 s71, s71, 81920\n"
  "s_barrier\n"
  "v_mfma_f32_16x16x16_bf16 v[104:107], v[32:33], v[44:45], v[104:107]\n"
  "s_mul_i32 s75, s75, 81920\n"
  "s_add_u32 s68, s71, s49\n"
  "v_mfma_f32_16x16x16_bf16 v[108:111], v[32:33], v[46:47], v[108:111]\n"
  "s_add_u32 s71, s71, s53\n"
  "s_add_u32 s74, s75, s49\n"
  "v_mfma_f32_16x16x16_bf16 v[112:115], v[34:35], v[46:47], v[112:115]\n"
  "s_add_u32 s75, s75, s53\n"
  "s_xor_b32 s63, s63, 524288\n"
  "v_mfma_f32_16x16x16_bf16 v[116:119], v[52:53], v[44:45], v[116:119]\n"
  "v_and_b32 v94, s48, v16\n"
  "v_or_b32 v94, v94, v15\n"
  "v_mfma_f32_16x16x16_bf16 v[120:123], v[52:53], v[46:47], v[120:123]\n"
  "s_nop 1\n"
  "ds_write_b128 v92, v[104:107] offset:40960\n"
  "ds_write_b128 v92, v[108:111] offset:43008\n"
  "ds_write_b128 v95, v[112:115] offset:43008\n"
  "s_mov_b32 exec_lo, 0\n"
  "ds_write_b128 v93, v[116:119] offset:57312\n"
  "s_nop 0\n"
  "ds_write_b128 v93, v[120:123] offset:57824\n"
  "s_mov_b32 exec_lo, -1\n"
  "s_waitcnt lgkmcnt(0)\n"
  "s_barrier\n"
  "ds_read_b128 v[60:63], v100\n"
  "ds_read_b128 v[64:67], v100 offset:4096\n"
  "ds_read2st64_b32 v[96:97], v101 offset0:0 offset1:4\n"
  "ds_read2st64_b32 v[98:99], v101 offset0:8 offset1:12\n"
  "ds_read_b128 v[72:75], v100 offset:8192\n"
  "ds_read_b128 v[76:79], v100 offset:12288\n"
  "ds_read_b128 v[24:27], v4 offset:0\n"
  "ds_read_b128 v[28:31], v4 offset:10240\n"
  "ds_read_b128 v[32:35], v4 offset:20480\n"
  "ds_read_b128 v[36:39], v4 offset:30720\n"
  "ds_read_b128 v[80:83], v5 offset:0\n"
  "ds_read_b128 v[84:87], v5 offset:1024\n"
  "s_waitcnt lgkmcnt(10)\n"
  "v_pk_add_f32 v[60:61], v[60:61], v[64:65]\n"
  "v_pk_add_f32 v[62:63], v[62:63], v[66:67]\n"
  "s_waitcnt lgkmcnt(8)\n"
  "v_pk_add_f32 v[96:97], v[96:97], v[98:99]\n"
  "v_add_f32 v96, v96, v97\n"
  "s_waitcnt lgkmcnt(7)\n"
  "v_pk_add_f32 v[60:61], v[60:61], v[72:73]\n"
  "v_pk_add_f32 v[62:63], v[62:63], v[74:75]\n"
  "s_waitcnt lgkmcnt(6)\n"
  "v_pk_add_f32 v[60:61], v[60:61], v[76:77]\n"
  "v_pk_add_f32 v[62:63], v[62:63], v[78:79]\n"
  "v_mul_f32 v60, s76, v60\n"
  "v_mul_f32 v61, s76, v61\n"
  "v_mul_f32 v62, s76, v62\n"
  "v_mul_f32 v63, s76, v63\n"
  "v_mul_f32 v96, s76, v96\n"
  "v_mov_b32 v104, s77\n"
  "v_cndmask_b32 v60, v104, v60, s[8:9]\n"
  "v_cndmask_b32 v61, v104, v61, s[10:11]\n"
  "v_cndmask_b32 v62, v104, v62, s[12:13]\n"
  "v_cndmask_b32 v63, v104, v63, s[14:15]\n"
  "v_max3_f32 v107, v60, v61, v96\n"
  "v_max3_f32 v107, v62, v63, v107\n"
  "s_nop 1\n"
  "v_max_f32_dpp v107, v107, v107 quad_perm:[1,0,3,2] row_mask:0xf bank_mask:0xf\n"
  "s_nop 1\n"
  "v_max_f32_dpp v107, v107, v107 quad_perm:[2,3,0,1] row_mask:0xf bank_mask:0xf\n"
  "s_nop 1\n"
  "v_max_f32_dpp v107, v107, v107 row_half_mirror row_mask:0xf bank_mask:0xf\n"
  "v_sub_f32 v108, v60, v107\n"
  "v_sub_f32 v109, v61, v107\n"
  "v_sub_f32 v110, v62, v107\n"
  "v_sub_f32 v111, v63, v107\n"
  "v_sub_f32 v112, v96, v107\n"
  "v_exp_f32 v108, v108\n"
  "v_exp_f32 v109, v109\n"
  "v_exp_f32 v110, v110\n"
  "v_exp_f32 v111, v111\n"
  "v_exp_f32 v112, v112\n"
  "v_add_f32 v104, v108, v109\n"
  "v_add_f32 v105, v110, v111\n"
  "v_add_f32 v104, v104, v112\n"
  "v_add_f32 v107, v104, v105\n"
  "s_nop 1\n"
  "v_add_f32_dpp v107, v107, v107 quad_perm:[1,0,3,2] row_mask:0xf bank_mask:0xf\n"
  "s_nop 1\n"
  "v_add_f32_dpp v107, v107, v107 quad_perm:[2,3,0,1] row_mask:0xf bank_mask:0xf\n"
  "s_nop 1\n"
  "v_add_f32_dpp v107, v107, v107 row_half_mirror row_mask:0xf bank_mask:0xf\n"
  "v_rcp_f32 v107, v107\n"
  "s_nop 0\n"
  "v_mul_f32 v112, v112, v107\n"
  "v_mul_f32 v108, v108, v107\n"
  "v_mul_f32 v109, v109, v107\n"
  "v_mul_f32 v110, v110, v107\n"
  "v_mul_f32 v111, v111, v107\n"
  "v_mov_b32_dpp v113, v112 quad_perm:[1,0,3,2] row_mask:0xf bank_mask:0xf\n"
  "v_cvt_pk_bf16_f32 v104, v108, v109\n"
  "v_cvt_pk_bf16_f32 v105, v110, v111\n"
  "v_min_u32 v104, s78, v104\n"
  "v_min_u32 v105, s78, v105\n"
  "v_cvt_pk_bf16_f32 v106, v112, v113\n"
  "buffer_store_dwordx2 v[104:105], v94, s[16:19], s71 offen sc1\n"
  "v_min_u32 v106, s78, v106\n"
  "buffer_store_dword v106, v16, s[16:19], s68 offen sc1\n"
  "s_sub_i32 s60, s64, s59\n"
  "v_mov_b32 v110, s60\n"
  "v_mov_b32 v111, s3\n"
  "v_readfirstlane_b32 s60, v102\n"
  "s_cmp_eq_u32 s60, s99\n"
  "s_cselect_b32 s60, 0x80000000, s2\n"
  "v_mov_b32 v102, s60\n"
  "s_mov_b64 exec, 1\n"
  "buffer_store_dwordx2 v[110:111], v102, s[16:19], 0 offen sc1\n"
  "s_mov_b64 exec, -1\n"
  "v_mov_b32 v108, -1\n"
  "v_mov_b32 v109, -1\n"
  "buffer_store_dwordx2 v[108:109], v94, s[16:19], s75 offen sc1\n"
  "buffer_store_dword v108, v16, s[16:19], s74 offen sc1\n"
  "buffer_store_dwordx2 v[108:109], v15, s[16:19], s63 offen sc1\n"
  "buffer_store_dword v108, v16, s[16:19], s63 offen sc1\n"
  "s_waitcnt lgkmcnt(0)\n"
  "s_add_u32 s56, s56, 2\n"
  "s_add_u32 s99, s99, 2\n"
  "s_cmp_lt_u32 s56, 64\n"
  "s_cbranch_scc1 H_LOOP_%=\n"
  "s_waitcnt vmcnt(0)\n"
  "s_add_u32 s46, s46, 64\n"
  "s_cmp_lt_u32 s46, 128\n"
  "s_cbranch_scc1 H_LAYER_%=\n"
  "s_branch H_EXIT_%=\n"
  "H_FIX0_%=:\n"
  "s_nop 7\n"
  "s_waitcnt vmcnt(0) lgkmcnt(0)\n"
  "v_mov_b32 v19, 63488\n"
  "v_mov_b32 v54, s99\n"
  "ds_write_b32 v19, v54\n"
  "s_waitcnt lgkmcnt(0)\n"
  "v_mov_b32 v56, 0\n"
  "v_mov_b32 v57, 0\n"
  "v_mov_b32 v58, 0\n"
  "v_mov_b32 v59, 0\n"
  "v_mov_b32 v60, 0\n"
  "v_mov_b32 v61, 0\n"
  "v_mov_b32 v62, 0\n"
  "v_mov_b32 v63, 0\n"
  "v_mov_b32 v64, 0\n"
  "v_mov_b32 v65, 0\n"
  "v_mov_b32 v66, 0\n"
  "v_mov_b32 v67, 0\n"
  "v_mov_b32 v68, 0\n"
  "v_mov_b32 v69, 0\n"
  "v_mov_b32 v70, 0\n"
  "v_mov_b32 v71, 0\n"
  "H_FIX0P_%=:\n"
  "buffer_load_dwordx4 v[104:107], v6, s[16:19], s57 offen sc1\n"
  "buffer_load_dwordx4 v[108:111], v6, s[16:19], s57 offen offset:1024 sc1\n"
  "buffer_load_dwordx2 v[54:55], v7, s[16:19], s57 offen sc1\n"
  "s_waitcnt vmcnt(2)\n"
  "v_max3_u32 v19, v104, v105, v106\n"
  "s_waitcnt vmcnt(1)\n"
  "v_max3_u32 v19, v107, v108, v19\n"
  "v_max3_u32 v19, v109, v110, v19\n"
  "s_waitcnt vmcnt(0)\n"
  "v_max3_u32 v19, v111, v54, v19\n"
  "v_max_u32 v19, v55, v19\n"
  "v_cmp_eq_u32 vcc, -1, v19\n"
  "s_nop 1\n"
  "s_cbranch_vccz H_D0_%=\n"
  "s_add_u32 s62, s62, 1\n"
  "s_cmp_gt_u32 s62, 300000\n"
  "s_cbranch_scc1 H_FAIL_%=\n"
  "s_and_b32 s60, s62, 255\n"
  "s_cmp_lg_u32 s60, 0\n"
  "s_cbranch_scc1 H_FIX0P_%=\n"
  "v_mov_b32 v19, 0\n"
  "buffer_load_dword v19, v19, s[40:43], 0 offen sc1\n"
  "s_waitcnt vmcnt(0)\n"
  "v_readfirstlane_b32 s60, v19\n"
  "s_cmp_lg_u32 s60, 0\n"
  "s_cbranch_scc1 H_EXIT_%=\n"
  "s_branch H_FIX0P_%=\n"
  "H_FIX1_%=:\n"
  "s_nop 7\n"
  "s_waitcnt vmcnt(0) lgkmcnt(0)\n"
  "v_mov_b32 v19, 63488\n"
  "v_mov_b32 v54, s99\n"
  "ds_write_b32 v19, v54\n"
  "s_waitcnt lgkmcnt(0)\n"
  "H_FIX1P_%=:\n"
  "buffer_load_dwordx4 v[104:107], v6, s[16:19], s57 offen sc1\n"
  "buffer_load_dwordx4 v[108:111], v6, s[16:19], s57 offen offset:1024 sc1\n"
  "buffer_load_dwordx2 v[54:55], v7, s[16:19], s57 offen sc1\n"
  "s_waitcnt vmcnt(2)\n"
  "v_max3_u32 v19, v104, v105, v106\n"
  "s_waitcnt vmcnt(1)\n"
  "v_max3_u32 v19, v107, v108, v19\n"
  "v_max3_u32 v19, v109, v110, v19\n"
  "s_waitcnt vmcnt(0)\n"
  "v_max3_u32 v19, v111, v54, v19\n"
  "v_max_u32 v19, v55, v19\n"
  "v_cmp_eq_u32 vcc, -1, v19\n"
  "s_nop 1\n"
  "s_cbranch_vccz H_D1_%=\n"
  "s_add_u32 s62, s62, 1\n"
  "s_cmp_gt_u32 s62, 300000\n"
  "s_cbranch_scc1 H_FAIL_%=\n"
  "s_and_b32 s60, s62, 255\n"
  "s_cmp_lg_u32 s60, 0\n"
  "s_cbranch_scc1 H_FIX1P_%=\n"
  "v_mov_b32 v19, 0\n"
  "buffer_load_dword v19, v19, s[40:43], 0 offen sc1\n"
  "s_waitcnt vmcnt(0)\n"
  "v_readfirstlane_b32 s60, v19\n"
  "s_cmp_lg_u32 s60, 0\n"
  "s_cbranch_scc1 H_EXIT_%=\n"
  "s_branch H_FIX1P_%=\n"
  "H_FIX2_%=:\n"
  "s_nop 7\n"
  "s_waitcnt vmcnt(0) lgkmcnt(0)\n"
  "v_mov_b32 v19, 63488\n"
  "v_mov_b32 v54, s99\n"
  "ds_write_b32 v19, v54\n"
  "s_waitcnt lgkmcnt(0)\n"
  "H_FIX2P_%=:\n"
  "buffer_load_dwordx4 v[104:107], v6, s[16:19], s57 offen sc1\n"
  "buffer_load_dwordx4 v[108:111], v6, s[16:19], s57 offen offset:1024 sc1\n"
  "buffer_load_dwordx2 v[54:55], v7, s[16:19], s57 offen sc1\n"
  "s_waitcnt vmcnt(2)\n"
  "v_max3_u32 v19, v104, v105, v106\n"
  "s_waitcnt vmcnt(1)\n"
  "v_max3_u32 v19, v107, v108, v19\n"
  "v_max3_u32 v19, v109, v110, v19\n"
  "s_waitcnt vmcnt(0)\n"
  "v_max3_u32 v19, v111, v54, v19\n"
  "v_max_u32 v19, v55, v19\n"
  "v_cmp_eq_u32 vcc, -1, v19\n"
  "s_nop 1\n"
  "s_cbranch_vccz H_D2_%=\n"
  "s_add_u32 s62, s62, 1\n"
  "s_cmp_gt_u32 s62, 300000\n"
  "s_cbranch_scc1 H_FAIL_%=\n"
  "s_and_b32 s60, s62, 255\n"
  "s_cmp_lg_u32 s60, 0\n"
  "s_cbranch_scc1 H_FIX2P_%=\n"
  "v_mov_b32 v19, 0\n"
  "buffer_load_dword v19, v19, s[40:43], 0 offen sc1\n"
  "s_waitcnt vmcnt(0)\n"
  "v_readfirstlane_b32 s60, v19\n"
  "s_cmp_lg_u32 s60, 0\n"
  "s_cbranch_scc1 H_EXIT_%=\n"
  "s_branch H_FIX2P_%=\n"
  "H_FAIL_%=:\n"
  "s_nop 7\n"
  "v_mov_b32 v56, 0\n"
  "v_mov_b32 v57, 1\n"
  "buffer_store_dword v57, v56, s[40:43], 0 offen sc1\n"
  "H_EXIT_%=:\n"
  "s_nop 7\n"
  "s_waitcnt vmcnt(0)\n"
  "s_endpgm\n"
  "C_ENTRY_%=:\n"
  "s_mov_b32 s76, 0x3dd53b95\n"
  "s_mov_b32 s77, 0xceabfb8f\n"
  "s_mov_b32 s78, 0xfffffffe\n"
  "s_mov_b32 s46, 0\n"
  "s_mov_b32 s68, 0\n"
  "buffer_load_dwordx4 a[0:3], v0, s[28:31], s68 offen offset:0\n"
  "buffer_load_dwordx4 a[4:7], v0, s[28:31], s68 offen offset:1024\n"
  "s_mov_b32 s68, 2048\n"
  "buffer_load_dwordx4 a[8:11], v0, s[28:31], s68 offen offset:0\n"
  "buffer_load_dwordx4 a[12:15], v0, s[28:31], s68 offen offset:1024\n"
  "s_mov_b32 s68, 4096\n"
  "buffer_load_dwordx4 a[16:19], v0, s[28:31], s68 offen offset:0\n"
  "buffer_load_dwordx4 a[20:23], v0, s[28:31], s68 offen offset:1024\n"
  "s_mov_b32 s68, 6144\n"
  "buffer_load_dwordx4 a[24:27], v0, s[28:31], s68 offen offset:0\n"
  "buffer_load_dwordx4 a[28:31], v0, s[28:31], s68 offen offset:1024\n"
  "s_mov_b32 s68, 8192\n"
  "buffer_load_dwordx4 a[32:35], v0, s[28:31], s68 offen offset:0\n"
  "buffer_load_dwordx4 a[36:39], v0, s[28:31], s68 offen offset:1024\n"
  "s_mov_b32 s68, 10240\n"
  "buffer_load_dwordx4 a[40:43], v0, s[28:31], s68 offen offset:0\n"
  "buffer_load_dwordx4 a[44:47], v0, s[28:31], s68 offen offset:1024\n"
  "s_mov_b32 s68, 12288\n"
  "buffer_load_dwordx4 a[48:51], v0, s[28:31], s68 offen offset:0\n"
  "buffer_load_dwordx4 a[52:55], v0, s[28:31], s68 offen offset:1024\n"
  "s_mov_b32 s68, 14336\n"
  "buffer_load_dwordx4 a[56:59], v0, s[28:31], s68 offen offset:0\n"
  "buffer_load_dwordx4 a[60:63], v0, s[28:31], s68 offen offset:1024\n"
  "s_mov_b32 s68, 16384\n"
  "buffer_load_dwordx4 a[64:67], v0, s[28:31], s68 offen offset:0\n"
  "buffer_load_dwordx4 a[68:71], v0, s[28:31], s68 offen offset:1024\n"
  "s_mov_b32 s68, 18432\n"
  "buffer_load_dwordx4 a[72:75], v0, s[28:31], s68 offen offset:0\n"
  "buffer_load_dwordx4 a[76:79], v0, s[28:31], s68 offen offset:1024\n"
  "s_mov_b32 s68, 20480\n"
  "buffer_load_dwordx4 a[80:83], v0, s[28:31], s68 offen offset:0\n"
  "buffer_load_dwordx4 a[84:87], v0, s[28:31], s68 offen offset:1024\n"
  "s_mov_b32 s68, 22528\n"
  "buffer_load_dwordx4 a[88:91], v0, s[28:31], s68 offen offset:0\n"
  "buffer_load_dwordx4 a[92:95], v0, s[28:31], s68 offen offset:1024\n"
  "s_mov_b32 s68, 24576\n"
  "buffer_load_dwordx4 a[96:99], v0, s[28:31], s68 offen offset:0\n"
  "buffer_load_dwordx4 a[100:103], v0, s[28:31], s68 offen offset:1024\n"
  "s_mov_b32 s68, 26624\n"
  "buffer_load_dwordx4 a[104:107], v0, s[28:31], s68 offen offset:0\n"
  "buffer_load_dwordx4 a[108:111], v0, s[28:31], s68 offen offset:1024\n"
  "s_mov_b32 s68, 28672\n"
  "buffer_load_dwordx4 a[112:115], v0, s[28:31], s68 offen offset:0\n"
  "buffer_load_dwordx4 a[116:119], v0, s[28:31], s68 offen offset:1024\n"
  "s_mov_b32 s68, 30720\n"
  "buffer_load_dwordx4 a[120:123], v0, s[28:31], s68 offen offset:0\n"
  "buffer_load_dwordx4 a[124:127], v0, s[28:31], s68 offen offset:1024\n"
  "s_mov_b32 s68, 32768\n"
  "buffer_load_dwordx4 a[128:131], v0, s[28:31], s68 offen offset:0\n"
  "buffer_load_dwordx4 a[132:135], v0, s[28:31], s68 offen offset:1024\n"
  "s_mov_b32 s68, 34816\n"
  "buffer_load_dwordx4 a[136:139], v0, s[28:31], s68 offen offset:0\n"
  "buffer_load_dwordx4 a[140:143], v0, s[28:31], s68 offen offset:1024\n"
  "s_mov_b32 s68, 36864\n"
  "buffer_load_dwordx4 a[144:147], v0, s[28:31], s68 offen offset:0\n"
  "buffer_load_dwordx4 a[148:151], v0, s[28:31], s68 offen offset:1024\n"
  "s_mov_b32 s68, 38912\n"
  "buffer_load_dwordx4 a[152:155], v0, s[28:31], s68 offen offset:0\n"
  "buffer_load_dwordx4 a[156:159], v0, s[28:31], s68 offen offset:1024\n"
  "s_mov_b32 s68, 40960\n"
  "buffer_load_dwordx4 a[160:163], v0, s[28:31], s68 offen offset:0\n"
  "buffer_load_dwordx4 a[164:167], v0, s[28:31], s68 offen offset:1024\n"
  "s_mov_b32 s68, 43008\n"
  "buffer_load_dwordx4 a[168:171], v0, s[28:31], s68 offen offset:0\n"
  "buffer_load_dwordx4 a[172:175], v0, s[28:31], s68 offen offset:1024\n"
  "s_mov_b32 s68, 45056\n"
  "buffer_load_dwordx4 a[176:179], v0, s[28:31], s68 offen offset:0\n"
  "buffer_load_dwordx4 a[180:183], v0, s[28:31], s68 offen offset:1024\n"
  "s_mov_b32 s68, 47104\n"
  "buffer_load_dwordx4 a[184:187], v0, s[28:31], s68 offen offset:0\n"
  "buffer_load_dwordx4 a[188:191], v0, s[28:31], s68 offen offset:1024\n"
  "s_mov_b32 s68, 49152\n"
  "buffer_load_dwordx4 a[192:195], v0, s[28:31], s68 offen offset:0\n"
  "buffer_load_dwordx4 a[196:199], v0, s[28:31], s68 offen offset:1024\n"
  "s_mov_b32 s68, 51200\n"
  "buffer_load_dwordx4 a[200:203], v0, s[28:31], s68 offen offset:0\n"
  "buffer_load_dwordx4 a[204:207], v0, s[28:31], s68 offen offset:1024\n"
  "s_mov_b32 s68, 53248\n"
  "buffer_load_dwordx4 a[208:211], v0, s[28:31], s68 offen offset:0\n"
  "buffer_load_dwordx4 a[212:215], v0, s[28:31], s68 offen offset:1024\n"
  "s_mov_b32 s68, 55296\n"
  "buffer_load_dwordx4 a[216:219], v0, s[28:31], s68 offen offset:0\n"
  "buffer_load_dwordx4 a[220:223], v0, s[28:31], s68 offen offset:1024\n"
  "s_mov_b32 s68, 57344\n"
  "buffer_load_dwordx4 a[224:227], v0, s[28:31], s68 offen offset:0\n"
  "buffer_load_dwordx4 a[228:231], v0, s[28:31], s68 offen offset:1024\n"
  "s_mov_b32 s68, 59392\n"
  "buffer_load_dwordx4 a[232:235], v0, s[28:31], s68 offen offset:0\n"
  "buffer_load_dwordx4 a[236:239], v0, s[28:31], s68 offen offset:1024\n"
  "s_mov_b32 s68, 61440\n"
  "s_waitcnt vmcnt(50)\n"
  "buffer_load_dwordx4 a[240:243], v0, s[28:31], s68 offen offset:0\n"
  "buffer_load_dwordx4 a[244:247], v0, s[28:31], s68 offen offset:1024\n"
  "s_mov_b32 s68, 63488\n"
  "buffer_load_dwordx4 a[248:251], v0, s[28:31], s68 offen offset:0\n"
  "buffer_load_dwordx4 a[252:255], v0, s[28:31], s68 offen offset:1024\n"
  "s_mov_b32 s68, 65536\n"
  "buffer_load_dwordx4 v[192:195], v0, s[28:31], s68 offen offset:0\n"
  "buffer_load_dwordx4 v[196:199], v0, s[28:31], s68 offen offset:1024\n"
  "s_mov_b32 s68, 67584\n"
  "buffer_load_dwordx4 v[200:203], v0, s[28:31], s68 offen offset:0\n"
  "buffer_load_dwordx4 v[204:207], v0, s[28:31], s68 offen offset:1024\n"
  "s_mov_b32 s68, 69632\n"
  "buffer_load_dwordx4 v[208:211], v0, s[28:31], s68 offen offset:0\n"
  "buffer_load_dwordx4 v[212:215], v0, s[28:31], s68 offen offset:1024\n"
  "s_mov_b32 s68, 71680\n"
  "s_waitcnt vmcnt(50)\n"
  "buffer_load_dwordx4 v[216:219], v0, s[28:31], s68 offen offset:0\n"
  "buffer_load_dwordx4 v[220:223], v0, s[28:31], s68 offen offset:1024\n"
  "s_mov_b32 s68, 73728\n"
  "buffer_load_dwordx4 v[224:227], v0, s[28:31], s68 offen offset:0\n"
  "buffer_load_dwordx4 v[228:231], v0, s[28:31], s68 offen offset:1024\n"
  "s_mov_b32 s68, 75776\n"
  "buffer_load_dwordx4 v[232:235], v0, s[28:31], s68 offen offset:0\n"
  "buffer_load_dwordx4 v[236:239], v0, s[28:31], s68 offen offset:1024\n"
  "s_mov_b32 s68, 77824\n"
  "buffer_load_dwordx4 v[240:243], v0, s[28:31], s68 offen offset:0\n"
  "buffer_load_dwordx4 v[244:247], v0, s[28:31], s68 offen offset:1024\n"
  "s_mov_b32 s68, 79872\n"
  "buffer_load_dwordx4 v[248:251], v0, s[28:31], s68 offen offset:0\n"
  "buffer_load_dwordx4 v[252:255], v0, s[28:31], s68 offen offset:1024\n"
  "s_mov_b32 s38, 0\n"
  "v_add_u32 v20, 81920, v0\n"
  "s_waitcnt vmcnt(0)\n"
  "s_mov_b32 s62, 0\n"
  "s_mov_b32 s74, 1851648\n"
  "C_CALP_%=:\n"
  "s_nop 7\n"
  "buffer_load_dword v15, off, s[16:19], s74 sc1\n"
  "s_waitcnt vmcnt(0)\n"
  "v_readfirstlane_b32 s47, v15\n"
  "s_cmp_lg_u32 s47, -1\n"
  "s_cbranch_scc1 C_CALK_%=\n"
  "s_sleep 4\n"
  "s_add_u32 s62, s62, 1\n"
  "s_cmp_gt_u32 s62, 300000\n"
  "s_cbranch_scc1 C_FAIL_%=\n"
  "s_and_b32 s60, s62, 255\n"
  "s_cmp_lg_u32 s60, 0\n"
  "s_cbranch_scc1 C_CALP_%=\n"
  "v_mov_b32 v15, 0\n"
  "buffer_load_dword v15, v15, s[40:43], 0 offen sc1\n"
  "s_waitcnt vmcnt(0)\n"
  "v_readfirstlane_b32 s60, v15\n"
  "s_cmp_lg_u32 s60, 0\n"
  "s_cbranch_scc1 C_EXIT_%=\n"
  "s_branch C_CALP_%=\n"
  "C_CALK_%=:\n"
  "s_nop 7\n"
  "s_cmp_eq_u32 s45, 0\n"
  "s_cbranch_scc1 C_NOM0_%=\n"
  "s_mov_b32 s56, 0\n"
  "s_add_u32 s60, s56, 0\n"
  "s_bfe_u32 s58, s60, 131073\n"
  "s_lshl_b32 s58, s58, 18\n"
  "s_xor_b32 s60, s60, s47\n"
  "s_and_b32 s60, s60, 1\n"
  "s_lshl_b32 s60, s60, 13\n"
  "s_add_u32 s58, s58, s60\n"
  "v_mov_b32 v15, 0\n"
  "buffer_store_dword v15, v16, s[16:19], s58 offen sc1\n"
  "s_cmp_lg_u32 s44, 0\n"
  "s_cbranch_scc1 C_NOM0_%=\n"
  "s_add_u32 s72, s58, 131072\n"
  "buffer_store_dword v23, v11, s[16:19], s58 offen sc1\n"
  "buffer_store_dword v23, v11, s[16:19], s72 offen sc1\n"
  "C_NOM0_%=:\n"
  "s_nop 7\n"
  "s_mov_b32 s8, 0\n"
  "s_mov_b32 s9, 0\n"
  "s_mov_b32 s10, 0\n"
  "C_LAYER_%=:\n"
  "s_nop 7\n"
  "s_mov_b32 s56, s45\n"
  "s_add_u32 s99, s46, s45\n"
  "s_add_u32 s74, s99, 0\n"
  "s_lshl_b32 s74, s74, 7\n"
  "s_add_u32 s74, s74, 1835008\n"
  "s_mov_b32 s62, 0\n"
  "s_cmp_ge_u32 s56, 64\n"
  "s_cbranch_scc1 C_F0K_%=\n"
  "s_waitcnt vmcnt(0) lgkmcnt(0)\n"
  "C_F0P_%=:\n"
  "buffer_load_dword v15, off, s[16:19], s74 sc1\n"
  "s_waitcnt vmcnt(0)\n"
  "v_readfirstlane_b32 s60, v15\n"
  "s_cmp_lg_u32 s60, -1\n"
  "s_cbranch_scc1 C_F0K_%=\n"
  "s_sleep 4\n"
  "s_add_u32 s62, s62, 1\n"
  "s_cmp_gt_u32 s62, 300000\n"
  "s_cbranch_scc1 C_FAIL_%=\n"
  "s_and_b32 s60, s62, 255\n"
  "s_cmp_lg_u32 s60, 0\n"
  "s_cbranch_scc1 C_F0P_%=\n"
  "v_mov_b32 v15, 0\n"
  "buffer_load_dword v15, v15, s[40:43], 0 offen sc1\n"
  "s_waitcnt vmcnt(0)\n"
  "v_readfirstlane_b32 s60, v15\n"
  "s_cmp_lg_u32 s60, 0\n"
  "s_cbranch_scc1 C_EXIT_%=\n"
  "s_branch C_F0P_%=\n"
  "C_F0K_%=:\n"
  "s_barrier\n"
  "s_mov_b32 s96, 0\n"
  "s_mov_b32 s97, 108544\n"
  "s_add_u32 s60, s99, 0\n"
  "s_mul_i32 s72, s60, 40960\n"
  "s_mul_i32 s73, s44, 10240\n"
  "s_add_u32 s72, s72, s73\n"
  "s_add_u32 s73, s73, s96\n"
  "s_mov_b32 m0, s73\n"
  "s_nop 0\n"
  "buffer_load_dwordx4 v0, s[24:27], s72 offen offset:0 sc1 lds\n"
  "buffer_load_dwordx4 v0, s[24:27], s72 offen offset:1024 sc1 lds\n"
  "buffer_load_dwordx4 v0, s[24:27], s72 offen offset:2048 sc1 lds\n"
  "buffer_load_dwordx4 v0, s[24:27], s72 offen offset:3072 sc1 lds\n"
  "s_add_u32 s72, s72, 4096\n"
  "s_add_u32 s73, s73, 4096\n"
  "s_mov_b32 m0, s73\n"
  "s_nop 0\n"
  "buffer_load_dwordx4 v0, s[24:27], s72 offen offset:0 sc1 lds\n"
  "buffer_load_dwordx4 v0, s[24:27], s72 offen offset:1024 sc1 lds\n"
  "buffer_load_dwordx4 v0, s[24:27], s72 offen offset:2048 sc1 lds\n"
  "buffer_load_dwordx4 v0, s[24:27], s72 offen offset:3072 sc1 lds\n"
  "s_add_u32 s72, s72, 4096\n"
  "s_add_u32 s73, s73, 4096\n"
  "s_mov_b32 m0, s73\n"
  "s_nop 0\n"
  "buffer_load_dwordx4 v0, s[24:27], s72 offen offset:0 sc1 lds\n"
  "buffer_load_dwordx4 v0, s[24:27], s72 offen offset:1024 sc1 lds\n"
  "s_waitcnt vmcnt(0)\n"
  "s_barrier\n"
  "v_mov_b32 v17, v0\n"
  "s_mul_i32 s60, s44, 5120\n"
  "s_add_u32 s60, s60, s97\n"
  "v_add_u32 v19, s60, v0\n"
  "ds_read_b128 v[64:67], v17 offset:0\n"
  "ds_read_b128 v[68:71], v17 offset:10240\n"
  "ds_read_b128 v[72:75], v17 offset:20480\n"
  "ds_read_b128 v[76:79], v17 offset:30720\n"
  "s_waitcnt lgkmcnt(3)\n"
  "v_mfma_f32_16x16x32_bf16 v[32:35], v[64:67], a[0:3], 0\n"
  "s_waitcnt lgkmcnt(2)\n"
  "v_mfma_f32_16x16x32_bf16 v[36:39], v[68:71], a[0:3], 0\n"
  "ds_read_b128 v[80:83], v17 offset:1024\n"
  "v_mfma_f32_16x16x32_bf16 v[40:43], v[64:67], a[8:11], 0\n"
  "v_mfma_f32_16x16x32_bf16 v[44:47], v[68:71], a[8:11], 0\n"
  "ds_read_b128 v[84:87], v17 offset:11264\n"
  "v_mfma_f32_16x16x32_bf16 v[48:51], v[64:67], a[16:19], 0\n"
  "v_mfma_f32_16x16x32_bf16 v[52:55], v[68:71], a[16:19], 0\n"
  "ds_read_b128 v[88:91], v17 offset:21504\n"
  "v_mfma_f32_16x16x32_bf16 v[56:59], v[64:67], a[24:27], 0\n"
  "v_mfma_f32_16x16x32_bf16 v[60:63], v[68:71], a[24:27], 0\n"
  "ds_read_b128 v[92:95], v17 offset:31744\n"
  "s_waitcnt lgkmcnt(5)\n"
  "v_mfma_f32_16x16x32_bf16 v[32:35], v[72:75], a[4:7], v[32:35]\n"
  "s_waitcnt lgkmcnt(4)\n"
  "v_mfma_f32_16x16x32_bf16 v[36:39], v[76:79], a[4:7], v[36:39]\n"
  "v_mfma_f32_16x16x32_bf16 v[40:43], v[72:75], a[12:15], v[40:43]\n"
  "v_mfma_f32_16x16x32_bf16 v[44:47], v[76:79], a[12:15], v[44:47]\n"
  "v_mfma_f32_16x16x32_bf16 v[48:51], v[72:75], a[20:23], v[48:51]\n"
  "v_mfma_f32_16x16x32_bf16 v[52:55], v[76:79], a[20:23], v[52:55]\n"
  "v_mfma_f32_16x16x32_bf16 v[56:59], v[72:75], a[28:31], v[56:59]\n"
  "v_mfma_f32_16x16x32_bf16 v[60:63], v[76:79], a[28:31], v[60:63]\n"
  "s_waitcnt lgkmcnt(3)\n"
  "v_mfma_f32_16x16x32_bf16 v[32:35], v[80:83], a[32:35], v[32:35]\n"
  "s_waitcnt lgkmcnt(2)\n"
  "v_mfma_f32_16x16x32_bf16 v[36:39], v[84:87], a[32:35], v[36:39]\n"
  "ds_read_b128 v[64:67], v17 offset:2048\n"
  "v_mfma_f32_16x16x32_bf16 v[40:43], v[80:83], a[40:43], v[40:43]\n"
  "v_mfma_f32_16x16x32_bf16 v[44:47], v[84:87], a[40:43], v[44:47]\n"
  "ds_read_b128 v[68:71], v17 offset:12288\n"
  "v_mfma_f32_16x16x32_bf16 v[48:51], v[80:83], a[48:51], v[48:51]\n"
  "v_mfma_f32_16x16x32_bf16 v[52:55], v[84:87], a[48:51], v[52:55]\n"
  "ds_read_b128 v[72:75], v17 offset:22528\n"
  "v_mfma_f32_16x16x32_bf16 v[56:59], v[80:83], a[56:59], v[56:59]\n"
  "v_mfma_f32_16x16x32_bf16 v[60:63], v[84:87], a[56:59], v[60:63]\n"
  "ds_read_b128 v[76:79], v17 offset:32768\n"
  "s_waitcnt lgkmcnt(5)\n"
  "v_mfma_f32_16x16x32_bf16 v[32:35], v[88:91], a[36:39], v[32:35]\n"
  "s_waitcnt lgkmcnt(4)\n"
  "v_mfma_f32_16x16x32_bf16 v[36:39], v[92:95], a[36:39], v[36:39]\n"
  "v_mfma_f32_16x16x32_bf16 v[40:43], v[88:91], a[44:47], v[40:43]\n"
  "v_mfma_f32_16x16x32_bf16 v[44:47], v[92:95], a[44:47], v[44:47]\n"
  "v_mfma_f32_16x16x32_bf16 v[48:51], v[88:91], a[52:55], v[48:51]\n"
  "v_mfma_f32_16x16x32_bf16 v[52:55], v[92:95], a[52:55], v[52:55]\n"
  "v_mfma_f32_16x16x32_bf16 v[56:59], v[88:91], a[60:63], v[56:59]\n"
  "v_mfma_f32_16x16x32_bf16 v[60:63], v[92:95], a[60:63], v[60:63]\n"
  "s_waitcnt lgkmcnt(3)\n"
  "v_mfma_f32_16x16x32_bf16 v[32:35], v[64:67], a[64:67], v[32:35]\n"
  "s_waitcnt lgkmcnt(2)\n"
  "v_mfma_f32_16x16x32_bf16 v[36:39], v[68:71], a[64:67], v[36:39]\n"
  "ds_read_b128 v[80:83], v17 offset:3072\n"
  "v_mfma_f32_16x16x32_bf16 v[40:43], v[64:67], a[72:75], v[40:43]\n"
  "v_mfma_f32_16x16x32_bf16 v[44:47], v[68:71], a[72:75], v[44:47]\n"
  "ds_read_b128 v[84:87], v17 offset:13312\n"
  "v_mfma_f32_16x16x32_bf16 v[48:51], v[64:67], a[80:83], v[48:51]\n"
  "v_mfma_f32_16x16x32_bf16 v[52:55], v[68:71], a[80:83], v[52:55]\n"
  "ds_read_b128 v[88:91], v17 offset:23552\n"
  "v_mfma_f32_16x16x32_bf16 v[56:59], v[64:67], a[88:91], v[56:59]\n"
  "v_mfma_f32_16x16x32_bf16 v[60:63], v[68:71], a[88:91], v[60:63]\n"
  "ds_read_b128 v[92:95], v17 offset:33792\n"
  "s_waitcnt lgkmcnt(5)\n"
  "v_mfma_f32_16x16x32_bf16 v[32:35], v[72:75], a[68:71], v[32:35]\n"
  "s_waitcnt lgkmcnt(4)\n"
  "v_mfma_f32_16x16x32_bf16 v[36:39], v[76:79], a[68:71], v[36:39]\n"
  "v_mfma_f32_16x16x32_bf16 v[40:43], v[72:75], a[76:79], v[40:43]\n"
  "v_mfma_f32_16x16x32_bf16 v[44:47], v[76:79], a[76:79], v[44:47]\n"
  "v_mfma_f32_16x16x32_bf16 v[48:51], v[72:75], a[84:87], v[48:51]\n"
  "v_mfma_f32_16x16x32_bf16 v[52:55], v[76:79], a[84:87], v[52:55]\n"
  "v_mfma_f32_16x16x32_bf16 v[56:59], v[72:75], a[92:95], v[56:59]\n"
  "v_mfma_f32_16x16x32_bf16 v[60:63], v[76:79], a[92:95], v[60:63]\n"
  "s_waitcnt lgkmcnt(3)\n"
  "v_mfma_f32_16x16x32_bf16 v[32:35], v[80:83], a[96:99], v[32:35]\n"
  "s_waitcnt lgkmcnt(2)\n"
  "v_mfma_f32_16x16x32_bf16 v[36:39], v[84:87], a[96:99], v[36:39]\n"
  "ds_read_b128 v[64:67], v17 offset:4096\n"
  "v_mfma_f32_16x16x32_bf16 v[40:43], v[80:83], a[104:107], v[40:43]\n"
  "v_mfma_f32_16x16x32_bf16 v[44:47], v[84:87], a[104:107], v[44:47]\n"
  "ds_read_b128 v[68:71], v17 offset:14336\n"
  "v_mfma_f32_16x16x32_bf16 v[48:51], v[80:83], a[112:115], v[48:51]\n"
  "v_mfma_f32_16x16x32_bf16 v[52:55], v[84:87], a[112:115], v[52:55]\n"
  "ds_read_b128 v[72:75], v17 offset:24576\n"
  "v_mfma_f32_16x16x32_bf16 v[56:59], v[80:83], a[120:123], v[56:59]\n"
  "v_mfma_f32_16x16x32_bf16 v[60:63], v[84:87], a[120:123], v[60:63]\n"
  "ds_read_b128 v[76:79], v17 offset:34816\n"
  "s_waitcnt lgkmcnt(5)\n"
  "v_mfma_f32_16x16x32_bf16 v[32:35], v[88:91], a[100:103], v[32:35]\n"
  "s_waitcnt lgkmcnt(4)\n"
  "v_mfma_f32_16x16x32_bf16 v[36:39], v[92:95], a[100:103], v[36:39]\n"
  "v_mfma_f32_16x16x32_bf16 v[40:43], v[88:91], a[108:111], v[40:43]\n"
  "v_mfma_f32_16x16x32_bf16 v[44:47], v[92:95], a[108:111], v[44:47]\n"
  "v_mfma_f32_16x16x32_bf16 v[48:51], v[88:91], a[116:119], v[48:51]\n"
  "v_mfma_f32_16x16x32_bf16 v[52:55], v[92:95], a[116:119], v[52:55]\n"
  "v_mfma_f32_16x16x32_bf16 v[56:59], v[88:91], a[124:127], v[56:59]\n"
  "v_mfma_f32_16x16x32_bf16 v[60:63], v[92:95], a[124:127], v[60:63]\n"
  "s_waitcnt lgkmcnt(3)\n"
  "v_mfma_f32_16x16x32_bf16 v[32:35], v[64:67], a[128:131], v[32:35]\n"
  "s_waitcnt lgkmcnt(2)\n"
  "v_mfma_f32_16x16x32_bf16 v[36:39], v[68:71], a[128:131], v[36:39]\n"
  "ds_read_b128 v[80:83], v17 offset:5120\n"
  "v_mfma_f32_16x16x32_bf16 v[40:43], v[64:67], a[136:139], v[40:43]\n"
  "v_mfma_f32_16x16x32_bf16 v[44:47], v[68:71], a[136:139], v[44:47]\n"
  "ds_read_b128 v[84:87], v17 offset:15360\n"
  "v_mfma_f32_16x16x32_bf16 v[48:51], v[64:67], a[144:147], v[48:51]\n"
  "v_mfma_f32_16x16x32_bf16 v[52:55], v[68:71], a[144:147], v[52:55]\n"
  "ds_read_b128 v[88:91], v17 offset:25600\n"
  "v_mfma_f32_16x16x32_bf16 v[56:59], v[64:67], a[152:155], v[56:59]\n"
  "v_mfma_f32_16x16x32_bf16 v[60:63], v[68:71], a[152:155], v[60:63]\n"
  "ds_read_b128 v[92:95], v17 offset:35840\n"
  "s_waitcnt lgkmcnt(5)\n"
  "v_mfma_f32_16x16x32_bf16 v[32:35], v[72:75], a[132:135], v[32:35]\n"
  "s_waitcnt lgkmcnt(4)\n"
  "v_mfma_f32_16x16x32_bf16 v[36:39], v[76:79], a[132:135], v[36:39]\n"
  "v_mfma_f32_16x16x32_bf16 v[40:43], v[72:75], a[140:143], v[40:43]\n"
  "v_mfma_f32_16x16x32_bf16 v[44:47], v[76:79], a[140:143], v[44:47]\n"
  "v_mfma_f32_16x16x32_bf16 v[48:51], v[72:75], a[148:151], v[48:51]\n"
  "v_mfma_f32_16x16x32_bf16 v[52:55], v[76:79], a[148:151], v[52:55]\n"
  "v_mfma_f32_16x16x32_bf16 v[56:59], v[72:75], a[156:159], v[56:59]\n"
  "v_mfma_f32_16x16x32_bf16 v[60:63], v[76:79], a[156:159], v[60:63]\n"
  "s_waitcnt lgkmcnt(3)\n"
  "v_mfma_f32_16x16x32_bf16 v[32:35], v[80:83], a[160:163], v[32:35]\n"
  "s_waitcnt lgkmcnt(2)\n"
  "v_mfma_f32_16x16x32_bf16 v[36:39], v[84:87], a[160:163], v[36:39]\n"
  "ds_read_b128 v[64:67], v17 offset:6144\n"
  "v_mfma_f32_16x16x32_bf16 v[40:43], v[80:83], a[168:171], v[40:43]\n"
  "v_mfma_f32_16x16x32_bf16 v[44:47], v[84:87], a[168:171], v[44:47]\n"
  "ds_read_b128 v[68:71], v17 offset:16384\n"
  "v_mfma_f32_16x16x32_bf16 v[48:51], v[80:83], a[176:179], v[48:51]\n"
  "v_mfma_f32_16x16x32_bf16 v[52:55], v[84:87], a[176:179], v[52:55]\n"
  "ds_read_b128 v[72:75], v17 offset:26624\n"
  "v_mfma_f32_16x16x32_bf16 v[56:59], v[80:83], a[184:187], v[56:59]\n"
  "v_mfma_f32_16x16x32_bf16 v[60:63], v[84:87], a[184:187], v[60:63]\n"
  "ds_read_b128 v[76:79], v17 offset:36864\n"
  "s_waitcnt lgkmcnt(5)\n"
  "v_mfma_f32_16x16x32_bf16 v[32:35], v[88:91], a[164:167], v[32:35]\n"
  "s_waitcnt lgkmcnt(4)\n"
  "v_mfma_f32_16x16x32_bf16 v[36:39], v[92:95], a[164:167], v[36:39]\n"
  "v_mfma_f32_16x16x32_bf16 v[40:43], v[88:91], a[172:175], v[40:43]\n"
  "v_mfma_f32_16x16x32_bf16 v[44:47], v[92:95], a[172:175], v[44:47]\n"
  "v_mfma_f32_16x16x32_bf16 v[48:51], v[88:91], a[180:183], v[48:51]\n"
  "v_mfma_f32_16x16x32_bf16 v[52:55], v[92:95], a[180:183], v[52:55]\n"
  "v_mfma_f32_16x16x32_bf16 v[56:59], v[88:91], a[188:191], v[56:59]\n"
  "v_mfma_f32_16x16x32_bf16 v[60:63], v[92:95], a[188:191], v[60:63]\n"
  "s_waitcnt lgkmcnt(3)\n"
  "v_mfma_f32_16x16x32_bf16 v[32:35], v[64:67], a[192:195], v[32:35]\n"
  "s_waitcnt lgkmcnt(2)\n"
  "v_mfma_f32_16x16x32_bf16 v[36:39], v[68:71], a[192:195], v[36:39]\n"
  "ds_read_b128 v[80:83], v17 offset:7168\n"
  "v_mfma_f32_16x16x32_bf16 v[40:43], v[64:67], a[200:203], v[40:43]\n"
  "v_mfma_f32_16x16x32_bf16 v[44:47], v[68:71], a[200:203], v[44:47]\n"
  "ds_read_b128 v[84:87], v17 offset:17408\n"
  "v_mfma_f32_16x16x32_bf16 v[48:51], v[64:67], a[208:211], v[48:51]\n"
  "v_mfma_f32_16x16x32_bf16 v[52:55], v[68:71], a[208:211], v[52:55]\n"
  "ds_read_b128 v[88:91], v17 offset:27648\n"
  "v_mfma_f32_16x16x32_bf16 v[56:59], v[64:67], a[216:219], v[56:59]\n"
  "v_mfma_f32_16x16x32_bf16 v[60:63], v[68:71], a[216:219], v[60:63]\n"
  "ds_read_b128 v[92:95], v17 offset:37888\n"
  "s_waitcnt lgkmcnt(5)\n"
  "v_mfma_f32_16x16x32_bf16 v[32:35], v[72:75], a[196:199], v[32:35]\n"
  "s_waitcnt lgkmcnt(4)\n"
  "v_mfma_f32_16x16x32_bf16 v[36:39], v[76:79], a[196:199], v[36:39]\n"
  "v_mfma_f32_16x16x32_bf16 v[40:43], v[72:75], a[204:207], v[40:43]\n"
  "v_mfma_f32_16x16x32_bf16 v[44:47], v[76:79], a[204:207], v[44:47]\n"
  "v_mfma_f32_16x16x32_bf16 v[48:51], v[72:75], a[212:215], v[48:51]\n"
  "v_mfma_f32_16x16x32_bf16 v[52:55], v[76:79], a[212:215], v[52:55]\n"
  "v_mfma_f32_16x16x32_bf16 v[56:59], v[72:75], a[220:223], v[56:59]\n"
  "v_mfma_f32_16x16x32_bf16 v[60:63], v[76:79], a[220:223], v[60:63]\n"
  "s_waitcnt lgkmcnt(3)\n"
  "v_mfma_f32_16x16x32_bf16 v[32:35], v[80:83], a[224:227], v[32:35]\n"
  "s_waitcnt lgkmcnt(2)\n"
  "v_mfma_f32_16x16x32_bf16 v[36:39], v[84:87], a[224:227], v[36:39]\n"
  "ds_read_b128 v[64:67], v17 offset:8192\n"
  "v_mfma_f32_16x16x32_bf16 v[40:43], v[80:83], a[232:235], v[40:43]\n"
  "v_mfma_f32_16x16x32_bf16 v[44:47], v[84:87], a[232:235], v[44:47]\n"
  "ds_read_b128 v[68:71], v17 offset:18432\n"
  "v_mfma_f32_16x16x32_bf16 v[48:51], v[80:83], a[240:243], v[48:51]\n"
  "v_mfma_f32_16x16x32_bf16 v[52:55], v[84:87], a[240:243], v[52:55]\n"
  "ds_read_b128 v[72:75], v17 offset:28672\n"
  "v_mfma_f32_16x16x32_bf16 v[56:59], v[80:83], a[248:251], v[56:59]\n"
  "v_mfma_f32_16x16x32_bf16 v[60:63], v[84:87], a[248:251], v[60:63]\n"
  "ds_read_b128 v[76:79], v17 offset:38912\n"
  "s_waitcnt lgkmcnt(5)\n"
  "v_mfma_f32_16x16x32_bf16 v[32:35], v[88:91], a[228:231], v[32:35]\n"
  "s_waitcnt lgkmcnt(4)\n"
  "v_mfma_f32_16x16x32_bf16 v[36:39], v[92:95], a[228:231], v[36:39]\n"
  "v_mfma_f32_16x16x32_bf16 v[40:43], v[88:91], a[236:239], v[40:43]\n"
  "v_mfma_f32_16x16x32_bf16 v[44:47], v[92:95], a[236:239], v[44:47]\n"
  "v_mfma_f32_16x16x32_bf16 v[48:51], v[88:91], a[244:247], v[48:51]\n"
  "v_mfma_f32_16x16x32_bf16 v[52:55], v[92:95], a[244:247], v[52:55]\n"
  "v_mfma_f32_16x16x32_bf16 v[56:59], v[88:91], a[252:255], v[56:59]\n"
  "v_mfma_f32_16x16x32_bf16 v[60:63], v[92:95], a[252:255], v[60:63]\n"
  "s_waitcnt lgkmcnt(3)\n"
  "v_mfma_f32_16x16x32_bf16 v[32:35], v[64:67], v[192:195], v[32:35]\n"
  "s_waitcnt lgkmcnt(2)\n"
  "v_mfma_f32_16x16x32_bf16 v[36:39], v[68:71], v[192:195], v[36:39]\n"
  "ds_read_b128 v[80:83], v17 offset:9216\n"
  "v_mfma_f32_16x16x32_bf16 v[40:43], v[64:67], v[200:203], v[40:43]\n"
  "v_mfma_f32_16x16x32_bf16 v[44:47], v[68:71], v[200:203], v[44:47]\n"
  "ds_read_b128 v[84:87], v17 offset:19456\n"
  "v_mfma_f32_16x16x32_bf16 v[48:51], v[64:67], v[208:211], v[48:51]\n"
  "v_mfma_f32_16x16x32_bf16 v[52:55], v[68:71], v[208:211], v[52:55]\n"
  "ds_read_b128 v[88:91], v17 offset:29696\n"
  "v_mfma_f32_16x16x32_bf16 v[56:59], v[64:67], v[216:219], v[56:59]\n"
  "v_mfma_f32_16x16x32_bf16 v[60:63], v[68:71], v[216:219], v[60:63]\n"
  "ds_read_b128 v[92:95], v17 offset:39936\n"
  "s_waitcnt lgkmcnt(5)\n"
  "v_mfma_f32_16x16x32_bf16 v[32:35], v[72:75], v[196:199], v[32:35]\n"
  "s_waitcnt lgkmcnt(4)\n"
  "v_mfma_f32_16x16x32_bf16 v[36:39], v[76:79], v[196:199], v[36:39]\n"
  "v_mfma_f32_16x16x32_bf16 v[40:43], v[72:75], v[204:207], v[40:43]\n"
  "v_mfma_f32_16x16x32_bf16 v[44:47], v[76:79], v[204:207], v[44:47]\n"
  "v_mfma_f32_16x16x32_bf16 v[48:51], v[72:75], v[212:215], v[48:51]\n"
  "v_mfma_f32_16x16x32_bf16 v[52:55], v[76:79], v[212:215], v[52:55]\n"
  "v_mfma_f32_16x16x32_bf16 v[56:59], v[72:75], v[220:223], v[56:59]\n"
  "v_mfma_f32_16x16x32_bf16 v[60:63], v[76:79], v[220:223], v[60:63]\n"
  "s_waitcnt lgkmcnt(3)\n"
  "v_mfma_f32_16x16x32_bf16 v[32:35], v[80:83], v[224:227], v[32:35]\n"
  "s_waitcnt lgkmcnt(2)\n"
  "v_mfma_f32_16x16x32_bf16 v[36:39], v[84:87], v[224:227], v[36:39]\n"
  "v_mfma_f32_16x16x32_bf16 v[40:43], v[80:83], v[232:235], v[40:43]\n"
  "v_mfma_f32_16x16x32_bf16 v[44:47], v[84:87], v[232:235], v[44:47]\n"
  "v_mfma_f32_16x16x32_bf16 v[48:51], v[80:83], v[240:243], v[48:51]\n"
  "v_mfma_f32_16x16x32_bf16 v[52:55], v[84:87], v[240:243], v[52:55]\n"
  "v_mfma_f32_16x16x32_bf16 v[56:59], v[80:83], v[248:251], v[56:59]\n"
  "v_mfma_f32_16x16x32_bf16 v[60:63], v[84:87], v[248:251], v[60:63]\n"
  "s_waitcnt lgkmcnt(1)\n"
  "v_mfma_f32_16x16x32_bf16 v[32:35], v[88:91], v[228:231], v[32:35]\n"
  "s_waitcnt lgkmcnt(0)\n"
  "v_mfma_f32_16x16x32_bf16 v[36:39], v[92:95], v[228:231], v[36:39]\n"
  "v_mfma_f32_16x16x32_bf16 v[40:43], v[88:91], v[236:239], v[40:43]\n"
  "v_mfma_f32_16x16x32_bf16 v[44:47], v[92:95], v[236:239], v[44:47]\n"
  "v_mfma_f32_16x16x32_bf16 v[48:51], v[88:91], v[244:247], v[48:51]\n"
  "v_mfma_f32_16x16x32_bf16 v[52:55], v[92:95], v[244:247], v[52:55]\n"
  "v_mfma_f32_16x16x32_bf16 v[56:59], v[88:91], v[252:255], v[56:59]\n"
  "v_mfma_f32_16x16x32_bf16 v[60:63], v[92:95], v[252:255], v[60:63]\n"
  "v_cvt_pk_bf16_f32 v32, v32, v33\n"
  "v_cvt_pk_bf16_f32 v33, v34, v35\n"
  "v_cvt_pk_bf16_f32 v34, v36, v37\n"
  "v_cvt_pk_bf16_f32 v35, v38, v39\n"
  "ds_write_b128 v19, v[32:35] offset:0\n"
  "v_cvt_pk_bf16_f32 v40, v40, v41\n"
  "v_cvt_pk_bf16_f32 v41, v42, v43\n"
  "v_cvt_pk_bf16_f32 v42, v44, v45\n"
  "v_cvt_pk_bf16_f32 v43, v46, v47\n"
  "ds_write_b128 v19, v[40:43] offset:1024\n"
  "v_cvt_pk_bf16_f32 v48, v48, v49\n"
  "v_cvt_pk_bf16_f32 v49, v50, v51\n"
  "v_cvt_pk_bf16_f32 v50, v52, v53\n"
  "v_cvt_pk_bf16_f32 v51, v54, v55\n"
  "ds_write_b128 v19, v[48:51] offset:2048\n"
  "v_cvt_pk_bf16_f32 v56, v56, v57\n"
  "v_cvt_pk_bf16_f32 v57, v58, v59\n"
  "v_cvt_pk_bf16_f32 v58, v60, v61\n"
  "v_cvt_pk_bf16_f32 v59, v62, v63\n"
  "ds_write_b128 v19, v[56:59] offset:3072\n"
  "s_add_u32 s74, s99, 2\n"
  "s_lshl_b32 s74, s74, 7\n"
  "s_add_u32 s74, s74, 1835008\n"
  "s_mov_b32 s62, 0\n"
  "s_cmp_ge_u32 s56, 62\n"
  "s_cbranch_scc1 C_F2K_%=\n"
  "s_waitcnt vmcnt(0) lgkmcnt(0)\n"
  "C_F2P_%=:\n"
  "buffer_load_dword v15, off, s[16:19], s74 sc1\n"
  "s_waitcnt vmcnt(0)\n"
  "v_readfirstlane_b32 s60, v15\n"
  "s_cmp_lg_u32 s60, -1\n"
  "s_cbranch_scc1 C_F2K_%=\n"
  "s_sleep 4\n"
  "s_add_u32 s62, s62, 1\n"
  "s_cmp_gt_u32 s62, 300000\n"
  "s_cbranch_scc1 C_FAIL_%=\n"
  "s_and_b32 s60, s62, 255\n"
  "s_cmp_lg_u32 s60, 0\n"
  "s_cbranch_scc1 C_F2P_%=\n"
  "v_mov_b32 v15, 0\n"
  "buffer_load_dword v15, v15, s[40:43], 0 offen sc1\n"
  "s_waitcnt vmcnt(0)\n"
  "v_readfirstlane_b32 s60, v15\n"
  "s_cmp_lg_u32 s60, 0\n"
  "s_cbranch_scc1 C_EXIT_%=\n"
  "s_branch C_F2P_%=\n"
  "C_F2K_%=:\n"
  "s_mov_b32 s75, 40960\n"
  "s_add_u32 s60, s99, 2\n"
  "s_mul_i32 s72, s60, 40960\n"
  "s_mul_i32 s73, s44, 10240\n"
  "s_add_u32 s72, s72, s73\n"
  "s_add_u32 s73, s73, s75\n"
  "s_mov_b32 m0, s73\n"
  "s_nop 0\n"
  "buffer_load_dwordx4 v0, s[24:27], s72 offen offset:0 sc1 lds\n"
  "buffer_load_dwordx4 v0, s[24:27], s72 offen offset:1024 sc1 lds\n"
  "buffer_load_dwordx4 v0, s[24:27], s72 offen offset:2048 sc1 lds\n"
  "buffer_load_dwordx4 v0, s[24:27], s72 offen offset:3072 sc1 lds\n"
  "s_add_u32 s72, s72, 4096\n"
  "s_add_u32 s73, s73, 4096\n"
  "s_mov_b32 m0, s73\n"
  "s_nop 0\n"
  "buffer_load_dwordx4 v0, s[24:27], s72 offen offset:0 sc1 lds\n"
  "buffer_load_dwordx4 v0, s[24:27], s72 offen offset:1024 sc1 lds\n"
  "buffer_load_dwordx4 v0, s[24:27], s72 offen offset:2048 sc1 lds\n"
  "buffer_load_dwordx4 v0, s[24:27], s72 offen offset:3072 sc1 lds\n"
  "s_add_u32 s72, s72, 4096\n"
  "s_add_u32 s73, s73, 4096\n"
  "s_mov_b32 m0, s73\n"
  "s_nop 0\n"
  "buffer_load_dwordx4 v0, s[24:27], s72 offen offset:0 sc1 lds\n"
  "buffer_load_dwordx4 v0, s[24:27], s72 offen offset:1024 sc1 lds\n"
  "s_add_u32 s60, s56, 0\n"
  "s_bfe_u32 s57, s60, 131073\n"
  "s_lshl_b32 s57, s57, 18\n"
  "s_xor_b32 s60, s60, s47\n"
  "s_and_b32 s60, s60, 1\n"
  "s_lshl_b32 s60, s60, 13\n"
  "s_add_u32 s57, s57, s60\n"
  "v_mov_b32 v30, -1\n"
  "v_mov_b32 v31, -1\n"
  "C_LOOP_%=:\n"
  "s_nop 7\n"
  "s_add_u32 s60, s56, 1\n"
  "s_bfe_u32 s58, s60, 131073\n"
  "s_lshl_b32 s58, s58, 18\n"
  "s_xor_b32 s60, s60, s47\n"
  "s_and_b32 s60, s60, 1\n"
  "s_lshl_b32 s60, s60, 13\n"
  "s_add_u32 s58, s58, s60\n"
  "v_readfirstlane_b32 s61, v31\n"
  "s_cmp_lg_u32 s61, -1\n"
  "s_cselect_b32 s48, s61, s48\n"
  "s_mov_b32 s62, 0\n"
  "v_readfirstlane_b32 s59, v30\n"
  "s_cmp_lg_u32 s59, -1\n"
  "s_cbranch_scc1 C_SCHOK_%=\n"
  "C_SCHP_%=:\n"
  "s_nop 7\n"
  "buffer_load_dword v30, v10, s[16:19], s57 offen sc1\n"
  "s_waitcnt vmcnt(0)\n"
  "v_readfirstlane_b32 s59, v30\n"
  "s_cmp_lg_u32 s59, -1\n"
  "s_cbranch_scc1 C_SCHOK_%=\n"
  "s_sleep 2\n"
  "s_add_u32 s62, s62, 1\n"
  "s_cmp_gt_u32 s62, 300000\n"
  "s_cbranch_scc1 C_FAIL_%=\n"
  "s_and_b32 s60, s62, 255\n"
  "s_cmp_lg_u32 s60, 0\n"
  "s_cbranch_scc1 C_SCHP_%=\n"
  "v_mov_b32 v30, 0\n"
  "buffer_load_dword v30, v30, s[40:43], 0 offen sc1\n"
  "s_waitcnt vmcnt(0)\n"
  "v_readfirstlane_b32 s60, v30\n"
  "s_cmp_lg_u32 s60, 0\n"
  "s_cbranch_scc1 C_EXIT_%=\n"
  "s_branch C_SCHP_%=\n"
  "C_SCHOK_%=:\n"
  "s_nop 7\n"
  "s_waitcnt lgkmcnt(0)\n"
  "s_memrealtime s[64:65]\n"
  "s_waitcnt lgkmcnt(0)\n"
  "s_sub_i32 s60, s59, s64\n"
  "s_cmp_gt_i32 s60, 2000\n"
  "s_cselect_b32 s59, s64, s59\n"
  "s_cmp_lt_i32 s60, -100000\n"
  "s_cselect_b32 s59, s64, s59\n"
  "s_add_u32 s68, s59, s51\n"
  "s_mov_b32 s69, s68\n"
  "s_sub_i32 s60, s64, s69\n"
  "s_cmp_gt_i32 s60, 0\n"
  "s_cselect_b32 s69, s64, s69\n"
  "s_add_i32 s60, s60, 100\n"
  "s_max_i32 s60, s60, 0\n"
  "s_min_i32 s3, s60, 200\n"
  "s_cmp_lt_u32 s56, 4\n"
  "s_cselect_b32 s3, 0, s3\n"
  "v_mov_b32 v152, 0\n"
  "v_mov_b32 v153, 0\n"
  "v_mov_b32 v154, 0\n"
  "v_mov_b32 v155, 0\n"
  "v_mov_b32 v156, 0\n"
  "v_mov_b32 v157, 0\n"
  "v_mov_b32 v158, 0\n"
  "v_mov_b32 v159, 0\n"
  "v_mov_b32 v160, 0\n"
  "v_mov_b32 v161, 0\n"
  "v_mov_b32 v162, 0\n"
  "v_mov_b32 v163, 0\n"
  "v_mov_b32 v164, 0\n"
  "v_mov_b32 v165, 0\n"
  "v_mov_b32 v166, 0\n"
  "v_mov_b32 v167, 0\n"
  "v_add_u32 v18, s97, v0\n"
  "s_mov_b32 s62, 0\n"
  "C_WT_%=:\n"
  "s_memrealtime s[64:65]\n"
  "s_waitcnt lgkmcnt(0)\n"
  "s_sub_i32 s60, s64, s68\n"
  "s_cmp_lt_i32 s60, 0\n"
  "s_cbranch_scc1 C_WT_%=\n"
  "C_MP_%=:\n"
  "buffer_load_dwordx4 v[168:171], v2, s[16:19], s57 offen sc1\n"
  "buffer_load_dwordx4 v[172:175], v2, s[16:19], s57 offen offset:1024 sc1\n"
  "buffer_load_dwordx2 v[176:177], v3, s[16:19], s57 offen sc1\n"
  "s_waitcnt vmcnt(2)\n"
  "ds_write_b128 v1, v[168:171]\n"
  "v_max3_u32 v15, v168, v169, v170\n"
  "s_waitcnt vmcnt(1)\n"
  "ds_write_b128 v1, v[172:175] offset:4096\n"
  "v_max3_u32 v15, v171, v172, v15\n"
  "v_max3_u32 v15, v173, v174, v15\n"
  "s_waitcnt vmcnt(0)\n"
  "ds_write_b64 v4, v[176:177]\n"
  "v_max3_u32 v15, v175, v176, v15\n"
  "v_max_u32 v15, v177, v15\n"
  "v_cmp_eq_u32 vcc, -1, v15\n"
  "s_nop 1\n"
  "s_cbranch_vccnz C_MBAD_%=\n"
  "s_waitcnt lgkmcnt(0)\n"
  "s_barrier\n"
  "s_memrealtime s[64:65]\n"
  "ds_read_b128 v[112:115], v20 offset:0\n"
  "ds_read_b128 v[116:119], v20 offset:1024\n"
  "ds_read_b128 v[120:123], v20 offset:2048\n"
  "ds_read_b128 v[124:127], v20 offset:3072\n"
  "ds_read_b128 v[128:131], v20 offset:4096\n"
  "ds_read_b128 v[132:135], v20 offset:5120\n"
  "ds_read_b128 v[136:139], v20 offset:6144\n"
  "ds_read_b128 v[140:143], v20 offset:7168\n"
  "ds_read_b128 v[144:147], v20 offset:8192\n"
  "ds_read_b128 v[148:151], v20 offset:9216\n"
  "s_waitcnt lgkmcnt(0)\n"
  "v_smfmac_f32_16x16x64_bf16 v[152:155], v[112:115], a[0:7], v5\n"
  "s_sub_i32 s60, s64, s68\n"
  "s_add_u32 s61, s64, s50\n"
  "s_cmp_gt_i32 s60, s52\n"
  "s_cselect_b32 s59, s61, s59\n"
  "v_smfmac_f32_16x16x64_bf16 v[156:159], v[112:115], a[8:15], v5\n"
  "v_smfmac_f32_16x16x64_bf16 v[160:163], v[112:115], a[16:23], v5\n"
  "s_add_u32 s59, s59, s48\n"
  "s_add_u32 s60, s59, s49\n"
  "s_min_u32 s60, s60, s78\n"
  "v_mov_b32 v15, s60\n"
  "v_smfmac_f32_16x16x64_bf16 v[164:167], v[112:115], a[24:31], v5\n"
  "v_smfmac_f32_16x16x64_bf16 v[152:155], v[116:119], a[32:39], v5\n"
  "buffer_store_dword v15, v16, s[16:19], s58 offen sc1\n"
  "s_xor_b32 s73, s58, 524288\n"
  "v_mov_b32 v21, -1\n"
  "v_smfmac_f32_16x16x64_bf16 v[156:159], v[116:119], a[40:47], v5\n"
  "v_smfmac_f32_16x16x64_bf16 v[160:163], v[116:119], a[48:55], v5\n"
  "buffer_store_dword v21, v16, s[16:19], s73 offen sc1\n"
  "v_smfmac_f32_16x16x64_bf16 v[164:167], v[116:119], a[56:63], v5\n"
  "v_smfmac_f32_16x16x64_bf16 v[152:155], v[120:123], a[64:71], v5\n"
  "ds_read_b128 v[32:35], v18 offset:0\n"
  "v_smfmac_f32_16x16x64_bf16 v[156:159], v[120:123], a[72:79], v5\n"
  "v_smfmac_f32_16x16x64_bf16 v[160:163], v[120:123], a[80:87], v5\n"
  "ds_read_b128 v[36:39], v18 offset:1024\n"
  "v_smfmac_f32_16x16x64_bf16 v[164:167], v[120:123], a[88:95], v5\n"
  "v_smfmac_f32_16x16x64_bf16 v[152:155], v[124:127], a[96:103], v5\n"
  "ds_read_b128 v[40:43], v18 offset:2048\n"
  "v_smfmac_f32_16x16x64_bf16 v[156:159], v[124:127], a[104:111], v5\n"
  "v_smfmac_f32_16x16x64_bf16 v[160:163], v[124:127], a[112:119], v5\n"
  "ds_read_b128 v[44:47], v18 offset:3072\n"
  "v_smfmac_f32_16x16x64_bf16 v[164:167], v[124:127], a[120:127], v5\n"
  "v_smfmac_f32_16x16x64_bf16 v[152:155], v[128:131], a[128:135], v5\n"
  "ds_read_b128 v[52:55], v18 offset:5120\n"
  "v_smfmac_f32_16x16x64_bf16 v[156:159], v[128:131], a[136:143], v5\n"
  "v_smfmac_f32_16x16x64_bf16 v[160:163], v[128:131], a[144:151], v5\n"
  "ds_read_b128 v[56:59], v18 offset:6144\n"
  "v_smfmac_f32_16x16x64_bf16 v[164:167], v[128:131], a[152:159], v5\n"
  "v_smfmac_f32_16x16x64_bf16 v[152:155], v[132:135], a[160:167], v5\n"
  "ds_read_b128 v[60:63], v18 offset:7168\n"
  "v_smfmac_f32_16x16x64_bf16 v[156:159], v[132:135], a[168:175], v5\n"
  "v_smfmac_f32_16x16x64_bf16 v[160:163], v[132:135], a[176:183], v5\n"
  "ds_read_b128 v[64:67], v18 offset:8192\n"
  "v_smfmac_f32_16x16x64_bf16 v[164:167], v[132:135], a[184:191], v5\n"
  "v_smfmac_f32_16x16x64_bf16 v[152:155], v[136:139], a[192:199], v5\n"
  "ds_read_b128 v[72:75], v18 offset:10240\n"
  "v_smfmac_f32_16x16x64_bf16 v[156:159], v[136:139], a[200:207], v5\n"
  "v_smfmac_f32_16x16x64_bf16 v[160:163], v[136:139], a[208:215], v5\n"
  "ds_read_b128 v[76:79], v18 offset:11264\n"
  "v_smfmac_f32_16x16x64_bf16 v[164:167], v[136:139], a[216:223], v5\n"
  "v_smfmac_f32_16x16x64_bf16 v[152:155], v[140:143], a[224:231], v5\n"
  "ds_read_b128 v[80:83], v18 offset:12288\n"
  "v_smfmac_f32_16x16x64_bf16 v[156:159], v[140:143], a[232:239], v5\n"
  "v_smfmac_f32_16x16x64_bf16 v[160:163], v[140:143], a[240:247], v5\n"
  "ds_read_b128 v[84:87], v18 offset:13312\n"
  "v_smfmac_f32_16x16x64_bf16 v[164:167], v[140:143], a[248:255], v5\n"
  "v_smfmac_f32_16x16x64_bf16 v[152:155], v[144:147], v[192:199], v5\n"
  "ds_read_b128 v[92:95], v18 offset:15360\n"
  "v_smfmac_f32_16x16x64_bf16 v[156:159], v[144:147], v[200:207], v5\n"
  "v_smfmac_f32_16x16x64_bf16 v[160:163], v[144:147], v[208:215], v5\n"
  "ds_read_b128 v[96:99], v18 offset:16384\n"
  "v_smfmac_f32_16x16x64_bf16 v[164:167], v[144:147], v[216:223], v5\n"
  "v_smfmac_f32_16x16x64_bf16 v[152:155], v[148:151], v[224:231], v5\n"
  "s_waitcnt lgkmcnt(10)\n"
  "ds_read_b128 v[100:103], v18 offset:17408\n"
  "v_smfmac_f32_16x16x64_bf16 v[156:159], v[148:151], v[232:239], v5\n"
  "v_smfmac_f32_16x16x64_bf16 v[160:163], v[148:151], v[240:247], v5\n"
  "ds_read_b128 v[104:107], v18 offset:18432\n"
  "v_smfmac_f32_16x16x64_bf16 v[164:167], v[148:151], v[248:255], v5\n"
  "s_nop 5\n"
  "v_permlane32_swap_b32 v152, v160\n"
  "v_permlane32_swap_b32 v153, v161\n"
  "v_permlane32_swap_b32 v154, v162\n"
  "v_permlane32_swap_b32 v155, v163\n"
  "v_permlane32_swap_b32 v156, v164\n"
  "v_permlane32_swap_b32 v157, v165\n"
  "v_permlane32_swap_b32 v158, v166\n"
  "v_permlane32_swap_b32 v159, v167\n"
  "v_pk_add_f32 v[152:153], v[152:153], v[160:161]\n"
  "v_pk_add_f32 v[154:155], v[154:155], v[162:163]\n"
  "v_pk_add_f32 v[156:157], v[156:157], v[164:165]\n"
  "v_pk_add_f32 v[158:159], v[158:159], v[166:167]\n"
  "v_cvt_pk_bf16_f32 v152, v152, v153\n"
  "v_cvt_pk_bf16_f32 v153, v154, v155\n"
  "v_cvt_pk_bf16_f32 v154, v156, v157\n"
  "v_cvt_pk_bf16_f32 v155, v158, v159\n"
  "s_nop 0\n"
  "v_permlane16_swap_b32 v152, v154\n"
  "v_permlane16_swap_b32 v153, v155\n"
  "ds_write_b128 v19, v[152:155] offset:4096\n"
  "s_waitcnt lgkmcnt(0)\n"
  "s_barrier\n"
  "s_cmp_lg_u32 s44, 0\n"
  "s_cbranch_scc1 C_OTHER_%=\n"
  "s_cmp_eq_u32 s56, 63\n"
  "s_cbranch_scc1 C_LAST_%=\n"
  "ds_read_b128 v[48:51], v18 offset:4096\n"
  "ds_read_b128 v[68:71], v18 offset:9216\n"
  "ds_read_b128 v[88:91], v18 offset:14336\n"
  "ds_read_b128 v[108:111], v18 offset:19456\n"
  "s_add_u32 s68, s57, 0\n"
  "s_add_u32 s69, s57, 16384\n"
  "s_add_u32 s70, s57, 32768\n"
  "s_add_u32 s71, s57, 49152\n"
  "s_add_u32 s72, s58, 131072\n"
  "s_mov_b64 s[66:67], 0\n"
  "s_waitcnt lgkmcnt(0)\n"
  "s_memrealtime s[64:65]\n"
  "s_waitcnt lgkmcnt(0)\n"
  "s_sub_i32 s60, s59, s64\n"
  "s_cmp_lt_i32 s60, 0\n"
  "s_cselect_b32 s59, s64, s59\n"
  "C_WT2_%=:\n"
  "s_memrealtime s[64:65]\n"
  "s_waitcnt lgkmcnt(0)\n"
  "s_sub_i32 s60, s64, s59\n"
  "s_cmp_lt_i32 s60, 0\n"
  "s_cbranch_scc1 C_WT2_%=\n"
  "buffer_load_dwordx4 v[112:115], v6, s[16:19], s68 offen offset:0 sc1\n"
  "buffer_load_dwordx4 v[116:119], v6, s[16:19], s68 offen offset:1280 sc1\n"
  "buffer_load_dwordx4 v[120:123], v6, s[16:19], s68 offen offset:2560 sc1\n"
  "buffer_load_dwordx4 v[124:127], v6, s[16:19], s68 offen offset:3840 sc1\n"
  "buffer_load_dwordx4 v[128:131], v7, s[16:19], s68 offen sc1\n"
  "buffer_load_dwordx4 v[132:135], v6, s[16:19], s69 offen offset:0 sc1\n"
  "buffer_load_dwordx4 v[136:139], v6, s[16:19], s69 offen offset:1280 sc1\n"
  "buffer_load_dwordx4 v[140:143], v6, s[16:19], s69 offen offset:2560 sc1\n"
  "buffer_load_dwordx4 v[144:147], v6, s[16:19], s69 offen offset:3840 sc1\n"
  "buffer_load_dwordx4 v[148:151], v7, s[16:19], s69 offen sc1\n"
  "buffer_load_dwordx4 v[152:155], v6, s[16:19], s70 offen offset:0 sc1\n"
  "buffer_load_dwordx4 v[156:159], v6, s[16:19], s70 offen offset:1280 sc1\n"
  "buffer_load_dwordx4 v[160:163], v6, s[16:19], s70 offen offset:2560 sc1\n"
  "buffer_load_dwordx4 v[164:167], v6, s[16:19], s70 offen offset:3840 sc1\n"
  "buffer_load_dwordx4 v[168:171], v7, s[16:19], s70 offen sc1\n"
  "buffer_load_dwordx4 v[172:175], v6, s[16:19], s71 offen offset:0 sc1\n"
  "buffer_load_dwordx4 v[176:179], v6, s[16:19], s71 offen offset:1280 sc1\n"
  "buffer_load_dwordx4 v[180:183], v6, s[16:19], s71 offen offset:2560 sc1\n"
  "buffer_load_dwordx4 v[184:187], v6, s[16:19], s71 offen offset:3840 sc1\n"
  "buffer_load_dwordx4 v[188:191], v7, s[16:19], s71 offen sc1\n"
  "C_P2_%=:\n"
  "s_waitcnt vmcnt(19)\n"
  "v_mfma_f32_16x16x32_bf16 v[24:27], v[32:35], v[112:115], 0\n"
  "s_waitcnt vmcnt(18)\n"
  "v_mfma_f32_16x16x32_bf16 v[24:27], v[36:39], v[116:119], v[24:27]\n"
  "s_waitcnt vmcnt(17)\n"
  "v_mfma_f32_16x16x32_bf16 v[24:27], v[40:43], v[120:123], v[24:27]\n"
  "s_waitcnt vmcnt(16)\n"
  "v_mfma_f32_16x16x32_bf16 v[24:27], v[44:47], v[124:127], v[24:27]\n"
  "s_waitcnt vmcnt(15)\n"
  "v_mfma_f32_16x16x32_bf16 v[24:27], v[48:51], v[128:131], v[24:27]\n"
  "s_waitcnt vmcnt(14)\n"
  "v_mfma_f32_16x16x32_bf16 v[24:27], v[52:55], v[132:135], v[24:27]\n"
  "s_waitcnt vmcnt(13)\n"
  "v_mfma_f32_16x16x32_bf16 v[24:27], v[56:59], v[136:139], v[24:27]\n"
  "s_waitcnt vmcnt(12)\n"
  "v_mfma_f32_16x16x32_bf16 v[24:27], v[60:63], v[140:143], v[24:27]\n"
  "s_waitcnt vmcnt(11)\n"
  "v_mfma_f32_16x16x32_bf16 v[24:27], v[64:67], v[144:147], v[24:27]\n"
  "s_waitcnt vmcnt(10)\n"
  "v_mfma_f32_16x16x32_bf16 v[24:27], v[68:71], v[148:151], v[24:27]\n"
  "s_waitcnt vmcnt(9)\n"
  "v_mfma_f32_16x16x32_bf16 v[24:27], v[72:75], v[152:155], v[24:27]\n"
  "s_waitcnt vmcnt(8)\n"
  "v_mfma_f32_16x16x32_bf16 v[24:27], v[76:79], v[156:159], v[24:27]\n"
  "s_waitcnt vmcnt(7)\n"
  "v_mfma_f32_16x16x32_bf16 v[24:27], v[80:83], v[160:163], v[24:27]\n"
  "s_waitcnt vmcnt(6)\n"
  "v_mfma_f32_16x16x32_bf16 v[24:27], v[84:87], v[164:167], v[24:27]\n"
  "s_waitcnt vmcnt(5)\n"
  "v_mfma_f32_16x16x32_bf16 v[24:27], v[88:91], v[168:171], v[24:27]\n"
  "s_waitcnt vmcnt(4)\n"
  "v_mfma_f32_16x16x32_bf16 v[24:27], v[92:95], v[172:175], v[24:27]\n"
  "s_waitcnt vmcnt(3)\n"
  "v_mfma_f32_16x16x32_bf16 v[24:27], v[96:99], v[176:179], v[24:27]\n"
  "s_waitcnt vmcnt(2)\n"
  "v_mfma_f32_16x16x32_bf16 v[24:27], v[100:103], v[180:183], v[24:27]\n"
  "s_waitcnt vmcnt(1)\n"
  "v_mfma_f32_16x16x32_bf16 v[24:27], v[104:107], v[184:187], v[24:27]\n"
  "s_waitcnt vmcnt(0)\n"
  "v_mfma_f32_16x16x32_bf16 v[24:27], v[108:111], v[188:191], v[24:27]\n"
  "s_nop 7\n"
  "v_add_f32_dpp v24, v26, v24 row_ror:8 row_mask:0xf bank_mask:0xf\n"
  "v_add_f32_dpp v25, v27, v25 row_ror:8 row_mask:0xf bank_mask:0xf\n"
  "v_cmp_u_f32 vcc, v24, v24\n"
  "v_cvt_pk_bf16_f32 v24, v24, v25\n"
  "s_andn2_b64 vcc, vcc, s[66:67]\n"
  "v_min_u32 v24, s78, v24\n"
  "s_cbranch_vccnz C_REDO_%=\n"
  "s_nop 0\n"
  "v_mov_b32_dpp v24, v24 row_ror:8 row_mask:0xf bank_mask:0xc\n"
  "buffer_store_dword v24, v11, s[16:19], s58 offen sc1\n"
  "buffer_store_dword v24, v11, s[16:19], s72 offen sc1\n"
  "s_memrealtime s[64:65]\n"
  "s_waitcnt lgkmcnt(0)\n"
  "s_sub_i32 s60, s64, s59\n"
  "v_mov_b32 v28, s60\n"
  "s_cmp_eq_u64 s[66:67], 0\n"
  "s_cselect_b32 s60, s2, 0x80000000\n"
  "v_mov_b32 v29, s60\n"
  "s_mov_b64 exec, 1\n"
  "buffer_store_dword v28, v29, s[16:19], 0 offen sc1\n"
  "s_mov_b64 exec, -1\n"
  "C_SKIP2_%=:\n"
  "s_nop 7\n"
  "s_add_u32 s72, s73, 131072\n"
  "buffer_store_dword v21, v11, s[16:19], s73 offen sc1\n"
  "buffer_store_dword v21, v11, s[16:19], s72 offen sc1\n"
  "s_add_u32 s60, s56, 2\n"
  "s_bfe_u32 s57, s60, 131073\n"
  "s_lshl_b32 s57, s57, 18\n"
  "s_xor_b32 s60, s60, s47\n"
  "s_and_b32 s60, s60, 1\n"
  "s_lshl_b32 s60, s60, 13\n"
  "s_add_u32 s57, s57, s60\n"
  "s_xor_b32 s75, s96, 40960\n"
  "v_add_u32 v17, s75, v0\n"
  "s_sub_u32 s97, 237568, s97\n"
  "s_mul_i32 s60, s44, 5120\n"
  "s_add_u32 s60, s60, s97\n"
  "v_add_u32 v19, s60, v0\n"
  "s_cmp_ge_u32 s56, 62\n"
  "s_cbranch_scc1 C_JOIN_%=\n"
  "s_lshl_b32 s68, s45, 14\n"
  "s_add_u32 s68, s68, 1852416\n"
  "s_add_u32 s69, s68, 8192\n"
  "s_andn2_b32 s60, 0x80000000, s53\n"
  "v_lshlrev_b32 v96, 3, v0\n"
  "v_or_b32 v96, s60, v96\n"
  "ds_read_b128 v[64:67], v17 offset:0\n"
  "ds_read_b128 v[68:71], v17 offset:10240\n"
  "ds_read_b128 v[72:75], v17 offset:20480\n"
  "ds_read_b128 v[76:79], v17 offset:30720\n"
  "s_waitcnt lgkmcnt(3)\n"
  "v_mfma_f32_16x16x32_bf16 v[32:35], v[64:67], a[0:3], 0\n"
  "s_waitcnt lgkmcnt(2)\n"
  "v_mfma_f32_16x16x32_bf16 v[36:39], v[68:71], a[0:3], 0\n"
  "ds_read_b128 v[80:83], v17 offset:1024\n"
  "v_mfma_f32_16x16x32_bf16 v[40:43], v[64:67], a[8:11], 0\n"
  "v_mfma_f32_16x16x32_bf16 v[44:47], v[68:71], a[8:11], 0\n"
  "ds_read_b128 v[84:87], v17 offset:11264\n"
  "v_mfma_f32_16x16x32_bf16 v[48:51], v[64:67], a[16:19], 0\n"
  "v_mfma_f32_16x16x32_bf16 v[52:55], v[68:71], a[16:19], 0\n"
  "ds_read_b128 v[88:91], v17 offset:21504\n"
  "v_mfma_f32_16x16x32_bf16 v[56:59], v[64:67], a[24:27], 0\n"
  "v_mfma_f32_16x16x32_bf16 v[60:63], v[68:71], a[24:27], 0\n"
  "ds_read_b128 v[92:95], v17 offset:31744\n"
  "s_waitcnt lgkmcnt(5)\n"
  "v_mfma_f32_16x16x32_bf16 v[32:35], v[72:75], a[4:7], v[32:35]\n"
  "s_waitcnt lgkmcnt(4)\n"
  "v_mfma_f32_16x16x32_bf16 v[36:39], v[76:79], a[4:7], v[36:39]\n"
  "v_mfma_f32_16x16x32_bf16 v[40:43], v[72:75], a[12:15], v[40:43]\n"
  "v_mfma_f32_16x16x32_bf16 v[44:47], v[76:79], a[12:15], v[44:47]\n"
  "v_mfma_f32_16x16x32_bf16 v[48:51], v[72:75], a[20:23], v[48:51]\n"
  "v_mfma_f32_16x16x32_bf16 v[52:55], v[76:79], a[20:23], v[52:55]\n"
  "v_mfma_f32_16x16x32_bf16 v[56:59], v[72:75], a[28:31], v[56:59]\n"
  "v_mfma_f32_16x16x32_bf16 v[60:63], v[76:79], a[28:31], v[60:63]\n"
  "s_waitcnt lgkmcnt(3)\n"
  "v_mfma_f32_16x16x32_bf16 v[32:35], v[80:83], a[32:35], v[32:35]\n"
  "s_waitcnt lgkmcnt(2)\n"
  "v_mfma_f32_16x16x32_bf16 v[36:39], v[84:87], a[32:35], v[36:39]\n"
  "ds_read_b128 v[64:67], v17 offset:2048\n"
  "v_mfma_f32_16x16x32_bf16 v[40:43], v[80:83], a[40:43], v[40:43]\n"
  "v_mfma_f32_16x16x32_bf16 v[44:47], v[84:87], a[40:43], v[44:47]\n"
  "ds_read_b128 v[68:71], v17 offset:12288\n"
  "v_mfma_f32_16x16x32_bf16 v[48:51], v[80:83], a[48:51], v[48:51]\n"
  "v_mfma_f32_16x16x32_bf16 v[52:55], v[84:87], a[48:51], v[52:55]\n"
  "ds_read_b128 v[72:75], v17 offset:22528\n"
  "v_mfma_f32_16x16x32_bf16 v[56:59], v[80:83], a[56:59], v[56:59]\n"
  "v_mfma_f32_16x16x32_bf16 v[60:63], v[84:87], a[56:59], v[60:63]\n"
  "ds_read_b128 v[76:79], v17 offset:32768\n"
  "s_waitcnt lgkmcnt(5)\n"
  "v_mfma_f32_16x16x32_bf16 v[32:35], v[88:91], a[36:39], v[32:35]\n"
  "s_waitcnt lgkmcnt(4)\n"
  "v_mfma_f32_16x16x32_bf16 v[36:39], v[92:95], a[36:39], v[36:39]\n"
  "v_mfma_f32_16x16x32_bf16 v[40:43], v[88:91], a[44:47], v[40:43]\n"
  "v_mfma_f32_16x16x32_bf16 v[44:47], v[92:95], a[44:47], v[44:47]\n"
  "v_mfma_f32_16x16x32_bf16 v[48:51], v[88:91], a[52:55], v[48:51]\n"
  "v_mfma_f32_16x16x32_bf16 v[52:55], v[92:95], a[52:55], v[52:55]\n"
  "v_mfma_f32_16x16x32_bf16 v[56:59], v[88:91], a[60:63], v[56:59]\n"
  "v_mfma_f32_16x16x32_bf16 v[60:63], v[92:95], a[60:63], v[60:63]\n"
  "s_waitcnt lgkmcnt(3)\n"
  "v_mfma_f32_16x16x32_bf16 v[32:35], v[64:67], a[64:67], v[32:35]\n"
  "s_waitcnt lgkmcnt(2)\n"
  "v_mfma_f32_16x16x32_bf16 v[36:39], v[68:71], a[64:67], v[36:39]\n"
  "ds_read_b128 v[80:83], v17 offset:3072\n"
  "v_mfma_f32_16x16x32_bf16 v[40:43], v[64:67], a[72:75], v[40:43]\n"
  "v_mfma_f32_16x16x32_bf16 v[44:47], v[68:71], a[72:75], v[44:47]\n"
  "ds_read_b128 v[84:87], v17 offset:13312\n"
  "v_mfma_f32_16x16x32_bf16 v[48:51], v[64:67], a[80:83], v[48:51]\n"
  "v_mfma_f32_16x16x32_bf16 v[52:55], v[68:71], a[80:83], v[52:55]\n"
  "ds_read_b128 v[88:91], v17 offset:23552\n"
  "v_mfma_f32_16x16x32_bf16 v[56:59], v[64:67], a[88:91], v[56:59]\n"
  "v_mfma_f32_16x16x32_bf16 v[60:63], v[68:71], a[88:91], v[60:63]\n"
  "ds_read_b128 v[92:95], v17 offset:33792\n"
  "s_waitcnt lgkmcnt(5)\n"
  "v_mfma_f32_16x16x32_bf16 v[32:35], v[72:75], a[68:71], v[32:35]\n"
  "s_waitcnt lgkmcnt(4)\n"
  "v_mfma_f32_16x16x32_bf16 v[36:39], v[76:79], a[68:71], v[36:39]\n"
  "v_mfma_f32_16x16x32_bf16 v[40:43], v[72:75], a[76:79], v[40:43]\n"
  "v_mfma_f32_16x16x32_bf16 v[44:47], v[76:79], a[76:79], v[44:47]\n"
  "v_mfma_f32_16x16x32_bf16 v[48:51], v[72:75], a[84:87], v[48:51]\n"
  "v_mfma_f32_16x16x32_bf16 v[52:55], v[76:79], a[84:87], v[52:55]\n"
  "v_mfma_f32_16x16x32_bf16 v[56:59], v[72:75], a[92:95], v[56:59]\n"
  "v_mfma_f32_16x16x32_bf16 v[60:63], v[76:79], a[92:95], v[60:63]\n"
  "s_waitcnt lgkmcnt(3)\n"
  "v_mfma_f32_16x16x32_bf16 v[32:35], v[80:83], a[96:99], v[32:35]\n"
  "s_waitcnt lgkmcnt(2)\n"
  "v_mfma_f32_16x16x32_bf16 v[36:39], v[84:87], a[96:99], v[36:39]\n"
  "ds_read_b128 v[64:67], v17 offset:4096\n"
  "v_mfma_f32_16x16x32_bf16 v[40:43], v[80:83], a[104:107], v[40:43]\n"
  "v_mfma_f32_16x16x32_bf16 v[44:47], v[84:87], a[104:107], v[44:47]\n"
  "ds_read_b128 v[68:71], v17 offset:14336\n"
  "v_mfma_f32_16x16x32_bf16 v[48:51], v[80:83], a[112:115], v[48:51]\n"
  "v_mfma_f32_16x16x32_bf16 v[52:55], v[84:87], a[112:115], v[52:55]\n"
  "ds_read_b128 v[72:75], v17 offset:24576\n"
  "v_mfma_f32_16x16x32_bf16 v[56:59], v[80:83], a[120:123], v[56:59]\n"
  "v_mfma_f32_16x16x32_bf16 v[60:63], v[84:87], a[120:123], v[60:63]\n"
  "ds_read_b128 v[76:79], v17 offset:34816\n"
  "s_waitcnt lgkmcnt(5)\n"
  "v_mfma_f32_16x16x32_bf16 v[32:35], v[88:91], a[100:103], v[32:35]\n"
  "s_waitcnt lgkmcnt(4)\n"
  "v_mfma_f32_16x16x32_bf16 v[36:39], v[92:95], a[100:103], v[36:39]\n"
  "v_mfma_f32_16x16x32_bf16 v[40:43], v[88:91], a[108:111], v[40:43]\n"
  "v_mfma_f32_16x16x32_bf16 v[44:47], v[92:95], a[108:111], v[44:47]\n"
  "v_mfma_f32_16x16x32_bf16 v[48:51], v[88:91], a[116:119], v[48:51]\n"
  "v_mfma_f32_16x16x32_bf16 v[52:55], v[92:95], a[116:119], v[52:55]\n"
  "v_mfma_f32_16x16x32_bf16 v[56:59], v[88:91], a[124:127], v[56:59]\n"
  "v_mfma_f32_16x16x32_bf16 v[60:63], v[92:95], a[124:127], v[60:63]\n"
  "s_waitcnt lgkmcnt(3)\n"
  "v_mfma_f32_16x16x32_bf16 v[32:35], v[64:67], a[128:131], v[32:35]\n"
  "s_waitcnt lgkmcnt(2)\n"
  "v_mfma_f32_16x16x32_bf16 v[36:39], v[68:71], a[128:131], v[36:39]\n"
  "ds_read_b128 v[80:83], v17 offset:5120\n"
  "v_mfma_f32_16x16x32_bf16 v[40:43], v[64:67], a[136:139], v[40:43]\n"
  "v_mfma_f32_16x16x32_bf16 v[44:47], v[68:71], a[136:139], v[44:47]\n"
  "ds_read_b128 v[84:87], v17 offset:15360\n"
  "v_mfma_f32_16x16x32_bf16 v[48:51], v[64:67], a[144:147], v[48:51]\n"
  "v_mfma_f32_16x16x32_bf16 v[52:55], v[68:71], a[144:147], v[52:55]\n"
  "ds_read_b128 v[88:91], v17 offset:25600\n"
  "v_mfma_f32_16x16x32_bf16 v[56:59], v[64:67], a[152:155], v[56:59]\n"
  "v_mfma_f32_16x16x32_bf16 v[60:63], v[68:71], a[152:155], v[60:63]\n"
  "ds_read_b128 v[92:95], v17 offset:35840\n"
  "s_waitcnt lgkmcnt(5)\n"
  "v_mfma_f32_16x16x32_bf16 v[32:35], v[72:75], a[132:135], v[32:35]\n"
  "s_waitcnt lgkmcnt(4)\n"
  "v_mfma_f32_16x16x32_bf16 v[36:39], v[76:79], a[132:135], v[36:39]\n"
  "v_mfma_f32_16x16x32_bf16 v[40:43], v[72:75], a[140:143], v[40:43]\n"
  "v_mfma_f32_16x16x32_bf16 v[44:47], v[76:79], a[140:143], v[44:47]\n"
  "v_mfma_f32_16x16x32_bf16 v[48:51], v[72:75], a[148:151], v[48:51]\n"
  "v_mfma_f32_16x16x32_bf16 v[52:55], v[76:79], a[148:151], v[52:55]\n"
  "v_mfma_f32_16x16x32_bf16 v[56:59], v[72:75], a[156:159], v[56:59]\n"
  "v_mfma_f32_16x16x32_bf16 v[60:63], v[76:79], a[156:159], v[60:63]\n"
  "s_waitcnt lgkmcnt(3)\n"
  "v_mfma_f32_16x16x32_bf16 v[32:35], v[80:83], a[160:163], v[32:35]\n"
  "s_waitcnt lgkmcnt(2)\n"
  "v_mfma_f32_16x16x32_bf16 v[36:39], v[84:87], a[160:163], v[36:39]\n"
  "ds_read_b128 v[64:67], v17 offset:6144\n"
  "v_mfma_f32_16x16x32_bf16 v[40:43], v[80:83], a[168:171], v[40:43]\n"
  "v_mfma_f32_16x16x32_bf16 v[44:47], v[84:87], a[168:171], v[44:47]\n"
  "ds_read_b128 v[68:71], v17 offset:16384\n"
  "v_mfma_f32_16x16x32_bf16 v[48:51], v[80:83], a[176:179], v[48:51]\n"
  "v_mfma_f32_16x16x32_bf16 v[52:55], v[84:87], a[176:179], v[52:55]\n"
  "ds_read_b128 v[72:75], v17 offset:26624\n"
  "v_mfma_f32_16x16x32_bf16 v[56:59], v[80:83], a[184:187], v[56:59]\n"
  "v_mfma_f32_16x16x32_bf16 v[60:63], v[84:87], a[184:187], v[60:63]\n"
  "ds_read_b128 v[76:79], v17 offset:36864\n"
  "s_waitcnt lgkmcnt(5)\n"
  "v_mfma_f32_16x16x32_bf16 v[32:35], v[88:91], a[164:167], v[32:35]\n"
  "s_waitcnt lgkmcnt(4)\n"
  "v_mfma_f32_16x16x32_bf16 v[36:39], v[92:95], a[164:167], v[36:39]\n"
  "v_mfma_f32_16x16x32_bf16 v[40:43], v[88:91], a[172:175], v[40:43]\n"
  "v_mfma_f32_16x16x32_bf16 v[44:47], v[92:95], a[172:175], v[44:47]\n"
  "v_mfma_f32_16x16x32_bf16 v[48:51], v[88:91], a[180:183], v[48:51]\n"
  "v_mfma_f32_16x16x32_bf16 v[52:55], v[92:95], a[180:183], v[52:55]\n"
  "v_mfma_f32_16x16x32_bf16 v[56:59], v[88:91], a[188:191], v[56:59]\n"
  "v_mfma_f32_16x16x32_bf16 v[60:63], v[92:95], a[188:191], v[60:63]\n"
  "s_waitcnt lgkmcnt(3)\n"
  "v_mfma_f32_16x16x32_bf16 v[32:35], v[64:67], a[192:195], v[32:35]\n"
  "s_waitcnt lgkmcnt(2)\n"
  "v_mfma_f32_16x16x32_bf16 v[36:39], v[68:71], a[192:195], v[36:39]\n"
  "ds_read_b128 v[80:83], v17 offset:7168\n"
  "v_mfma_f32_16x16x32_bf16 v[40:43], v[64:67], a[200:203], v[40:43]\n"
  "v_mfma_f32_16x16x32_bf16 v[44:47], v[68:71], a[200:203], v[44:47]\n"
  "ds_read_b128 v[84:87], v17 offset:17408\n"
  "v_mfma_f32_16x16x32_bf16 v[48:51], v[64:67], a[208:211], v[48:51]\n"
  "v_mfma_f32_16x16x32_bf16 v[52:55], v[68:71], a[208:211], v[52:55]\n"
  "ds_read_b128 v[88:91], v17 offset:27648\n"
  "v_mfma_f32_16x16x32_bf16 v[56:59], v[64:67], a[216:219], v[56:59]\n"
  "v_mfma_f32_16x16x32_bf16 v[60:63], v[68:71], a[216:219], v[60:63]\n"
  "ds_read_b128 v[92:95], v17 offset:37888\n"
  "s_waitcnt lgkmcnt(5)\n"
  "v_mfma_f32_16x16x32_bf16 v[32:35], v[72:75], a[196:199], v[32:35]\n"
  "s_waitcnt lgkmcnt(4)\n"
  "v_mfma_f32_16x16x32_bf16 v[36:39], v[76:79], a[196:199], v[36:39]\n"
  "v_mfma_f32_16x16x32_bf16 v[40:43], v[72:75], a[204:207], v[40:43]\n"
  "v_mfma_f32_16x16x32_bf16 v[44:47], v[76:79], a[204:207], v[44:47]\n"
  "v_mfma_f32_16x16x32_bf16 v[48:51], v[72:75], a[212:215], v[48:51]\n"
  "v_mfma_f32_16x16x32_bf16 v[52:55], v[76:79], a[212:215], v[52:55]\n"
  "v_mfma_f32_16x16x32_bf16 v[56:59], v[72:75], a[220:223], v[56:59]\n"
  "buffer_load_dwordx2 v[98:99], v96, s[16:19], s68 offen sc1\n"
  "v_mfma_f32_16x16x32_bf16 v[60:63], v[76:79], a[220:223], v[60:63]\n"
  "s_waitcnt lgkmcnt(3)\n"
  "v_mfma_f32_16x16x32_bf16 v[32:35], v[80:83], a[224:227], v[32:35]\n"
  "s_waitcnt lgkmcnt(2)\n"
  "v_mfma_f32_16x16x32_bf16 v[36:39], v[84:87], a[224:227], v[36:39]\n"
  "ds_read_b128 v[64:67], v17 offset:8192\n"
  "v_mfma_f32_16x16x32_bf16 v[40:43], v[80:83], a[232:235], v[40:43]\n"
  "v_mfma_f32_16x16x32_bf16 v[44:47], v[84:87], a[232:235], v[44:47]\n"
  "ds_read_b128 v[68:71], v17 offset:18432\n"
  "v_mfma_f32_16x16x32_bf16 v[48:51], v[80:83], a[240:243], v[48:51]\n"
  "v_mfma_f32_16x16x32_bf16 v[52:55], v[84:87], a[240:243], v[52:55]\n"
  "ds_read_b128 v[72:75], v17 offset:28672\n"
  "v_mfma_f32_16x16x32_bf16 v[56:59], v[80:83], a[248:251], v[56:59]\n"
  "v_mfma_f32_16x16x32_bf16 v[60:63], v[84:87], a[248:251], v[60:63]\n"
  "ds_read_b128 v[76:79], v17 offset:38912\n"
  "s_waitcnt lgkmcnt(5)\n"
  "v_mfma_f32_16x16x32_bf16 v[32:35], v[88:91], a[228:231], v[32:35]\n"
  "s_waitcnt lgkmcnt(4)\n"
  "v_mfma_f32_16x16x32_bf16 v[36:39], v[92:95], a[228:231], v[36:39]\n"
  "v_mfma_f32_16x16x32_bf16 v[40:43], v[88:91], a[236:239], v[40:43]\n"
  "v_mfma_f32_16x16x32_bf16 v[44:47], v[92:95], a[236:239], v[44:47]\n"
  "v_mfma_f32_16x16x32_bf16 v[48:51], v[88:91], a[244:247], v[48:51]\n"
  "v_mfma_f32_16x16x32_bf16 v[52:55], v[92:95], a[244:247], v[52:55]\n"
  "v_mfma_f32_16x16x32_bf16 v[56:59], v[88:91], a[252:255], v[56:59]\n"
  "buffer_load_dwordx2 v[100:101], v96, s[16:19], s69 offen sc1\n"
  "v_mfma_f32_16x16x32_bf16 v[60:63], v[92:95], a[252:255], v[60:63]\n"
  "s_waitcnt lgkmcnt(3)\n"
  "v_mfma_f32_16x16x32_bf16 v[32:35], v[64:67], v[192:195], v[32:35]\n"
  "s_waitcnt lgkmcnt(2)\n"
  "v_mfma_f32_16x16x32_bf16 v[36:39], v[68:71], v[192:195], v[36:39]\n"
  "ds_read_b128 v[80:83], v17 offset:9216\n"
  "v_mfma_f32_16x16x32_bf16 v[40:43], v[64:67], v[200:203], v[40:43]\n"
  "v_mfma_f32_16x16x32_bf16 v[44:47], v[68:71], v[200:203], v[44:47]\n"
  "ds_read_b128 v[84:87], v17 offset:19456\n"
  "v_mfma_f32_16x16x32_bf16 v[48:51], v[64:67], v[208:211], v[48:51]\n"
  "v_mfma_f32_16x16x32_bf16 v[52:55], v[68:71], v[208:211], v[52:55]\n"
  "ds_read_b128 v[88:91], v17 offset:29696\n"
  "v_mfma_f32_16x16x32_bf16 v[56:59], v[64:67], v[216:219], v[56:59]\n"
  "v_mfma_f32_16x16x32_bf16 v[60:63], v[68:71], v[216:219], v[60:63]\n"
  "ds_read_b128 v[92:95], v17 offset:39936\n"
  "s_waitcnt lgkmcnt(5)\n"
  "v_mfma_f32_16x16x32_bf16 v[32:35], v[72:75], v[196:199], v[32:35]\n"
  "s_waitcnt lgkmcnt(4)\n"
  "v_mfma_f32_16x16x32_bf16 v[36:39], v[76:79], v[196:199], v[36:39]\n"
  "v_mfma_f32_16x16x32_bf16 v[40:43], v[72:75], v[204:207], v[40:43]\n"
  "v_mfma_f32_16x16x32_bf16 v[44:47], v[76:79], v[204:207], v[44:47]\n"
  "v_mfma_f32_16x16x32_bf16 v[48:51], v[72:75], v[212:215], v[48:51]\n"
  "v_mfma_f32_16x16x32_bf16 v[52:55], v[76:79], v[212:215], v[52:55]\n"
  "v_mfma_f32_16x16x32_bf16 v[56:59], v[72:75], v[220:223], v[56:59]\n"
  "buffer_load_dwordx2 v[30:31], v10, s[16:19], s57 offen sc1\n"
  "v_mfma_f32_16x16x32_bf16 v[60:63], v[76:79], v[220:223], v[60:63]\n"
  "s_waitcnt lgkmcnt(3)\n"
  "v_mfma_f32_16x16x32_bf16 v[32:35], v[80:83], v[224:227], v[32:35]\n"
  "s_waitcnt lgkmcnt(2)\n"
  "v_mfma_f32_16x16x32_bf16 v[36:39], v[84:87], v[224:227], v[36:39]\n"
  "v_mfma_f32_16x16x32_bf16 v[40:43], v[80:83], v[232:235], v[40:43]\n"
  "v_mfma_f32_16x16x32_bf16 v[44:47], v[84:87], v[232:235], v[44:47]\n"
  "v_mfma_f32_16x16x32_bf16 v[48:51], v[80:83], v[240:243], v[48:51]\n"
  "v_mfma_f32_16x16x32_bf16 v[52:55], v[84:87], v[240:243], v[52:55]\n"
  "v_mfma_f32_16x16x32_bf16 v[56:59], v[80:83], v[248:251], v[56:59]\n"
  "v_mfma_f32_16x16x32_bf16 v[60:63], v[84:87], v[248:251], v[60:63]\n"
  "s_waitcnt lgkmcnt(1)\n"
  "v_mfma_f32_16x16x32_bf16 v[32:35], v[88:91], v[228:231], v[32:35]\n"
  "s_waitcnt lgkmcnt(0)\n"
  "v_mfma_f32_16x16x32_bf16 v[36:39], v[92:95], v[228:231], v[36:39]\n"
  "v_mfma_f32_16x16x32_bf16 v[40:43], v[88:91], v[236:239], v[40:43]\n"
  "v_mfma_f32_16x16x32_bf16 v[44:47], v[92:95], v[236:239], v[44:47]\n"
  "v_mfma_f32_16x16x32_bf16 v[48:51], v[88:91], v[244:247], v[48:51]\n"
  "v_mfma_f32_16x16x32_bf16 v[52:55], v[92:95], v[244:247], v[52:55]\n"
  "v_mfma_f32_16x16x32_bf16 v[56:59], v[88:91], v[252:255], v[56:59]\n"
  "v_mfma_f32_16x16x32_bf16 v[60:63], v[92:95], v[252:255], v[60:63]\n"
  "v_cvt_pk_bf16_f32 v32, v32, v33\n"
  "v_cvt_pk_bf16_f32 v33, v34, v35\n"
  "v_cvt_pk_bf16_f32 v34, v36, v37\n"
  "v_cvt_pk_bf16_f32 v35, v38, v39\n"
  "ds_write_b128 v19, v[32:35] offset:0\n"
  "v_cvt_pk_bf16_f32 v40, v40, v41\n"
  "v_cvt_pk_bf16_f32 v41, v42, v43\n"
  "v_cvt_pk_bf16_f32 v42, v44, v45\n"
  "v_cvt_pk_bf16_f32 v43, v46, v47\n"
  "ds_write_b128 v19, v[40:43] offset:1024\n"
  "v_cvt_pk_bf16_f32 v48, v48, v49\n"
  "v_cvt_pk_bf16_f32 v49, v50, v51\n"
  "v_cvt_pk_bf16_f32 v50, v52, v53\n"
  "v_cvt_pk_bf16_f32 v51, v54, v55\n"
  "ds_write_b128 v19, v[48:51] offset:2048\n"
  "v_cvt_pk_bf16_f32 v56, v56, v57\n"
  "v_cvt_pk_bf16_f32 v57, v58, v59\n"
  "v_cvt_pk_bf16_f32 v58, v60, v61\n"
  "v_cvt_pk_bf16_f32 v59, v62, v63\n"
  "ds_write_b128 v19, v[56:59] offset:3072\n"
  "s_waitcnt vmcnt(0) lgkmcnt(0)\n"
  "s_cmp_ge_i32 s53, 0\n"
  "s_cbranch_scc1 C_JOIN_%=\n"
  "v_max_i32 v99, v99, v101\n"
  "v_max_i32_dpp v98, v98, v98 row_ror:1 row_mask:0xf bank_mask:0xf\n"
  "s_nop 0\n"
  "v_max_i32_dpp v99, v99, v99 row_ror:1 row_mask:0xf bank_mask:0xf\n"
  "v_max_i32_dpp v100, v100, v100 row_ror:1 row_mask:0xf bank_mask:0xf\n"
  "v_max_i32_dpp v98, v98, v98 row_ror:2 row_mask:0xf bank_mask:0xf\n"
  "v_max_i32_dpp v99, v99, v99 row_ror:2 row_mask:0xf bank_mask:0xf\n"
  "v_max_i32_dpp v100, v100, v100 row_ror:2 row_mask:0xf bank_mask:0xf\n"
  "v_max_i32_dpp v98, v98, v98 row_ror:4 row_mask:0xf bank_mask:0xf\n"
  "v_max_i32_dpp v99, v99, v99 row_ror:4 row_mask:0xf bank_mask:0xf\n"
  "v_max_i32_dpp v100, v100, v100 row_ror:4 row_mask:0xf bank_mask:0xf\n"
  "v_max_i32_dpp v98, v98, v98 row_ror:8 row_mask:0xf bank_mask:0xf\n"
  "v_max_i32_dpp v99, v99, v99 row_ror:8 row_mask:0xf bank_mask:0xf\n"
  "v_max_i32_dpp v100, v100, v100 row_ror:8 row_mask:0xf bank_mask:0xf\n"
  "v_readlane_b32 s68, v98, 0\n"
  "v_readlane_b32 s71, v98, 16\n"
  "s_max_i32 s68, s68, s71\n"
  "v_readlane_b32 s69, v98, 32\n"
  "v_readlane_b32 s71, v98, 48\n"
  "s_max_i32 s69, s69, s71\n"
  "v_readlane_b32 s71, v100, 0\n"
  "s_max_i32 s69, s69, s71\n"
  "v_readlane_b32 s71, v100, 16\n"
  "s_max_i32 s69, s69, s71\n"
  "v_readlane_b32 s71, v100, 32\n"
  "s_max_i32 s69, s69, s71\n"
  "v_readlane_b32 s70, v99, 0\n"
  "v_readlane_b32 s71, v99, 16\n"
  "s_max_i32 s70, s70, s71\n"
  "v_readlane_b32 s71, v99, 32\n"
  "s_max_i32 s70, s70, s71\n"
  "v_readlane_b32 s71, v99, 48\n"
  "s_max_i32 s70, s70, s71\n"
  "s_sub_i32 s71, s9, s68\n"
  "s_add_i32 s71, s71, 3\n"
  "s_ashr_i32 s71, s71, 2\n"
  "s_sub_i32 s9, s9, s71\n"
  "s_max_i32 s9, s9, s68\n"
  "s_sub_i32 s71, s10, s69\n"
  "s_add_i32 s71, s71, 3\n"
  "s_ashr_i32 s71, s71, 2\n"
  "s_sub_i32 s10, s10, s71\n"
  "s_max_i32 s10, s10, s69\n"
  "s_bfe_u32 s71, s53, 524304\n"
  "s_add_i32 s70, s70, s71\n"
  "s_sub_i32 s70, s70, 99\n"
  "s_ashr_i32 s70, s70, 1\n"
  "s_min_i32 s70, s70, 4\n"
  "s_max_i32 s70, s70, -1\n"
  "s_add_i32 s8, s8, s70\n"
  "s_max_i32 s8, s8, 0\n"
  "s_min_i32 s8, s8, 60\n"
  "s_bfe_u32 s71, s53, 524296\n"
  "s_add_i32 s71, s71, s8\n"
  "s_add_i32 s71, s71, s10\n"
  "s_cmp_gt_i32 s10, 0\n"
  "s_cselect_b32 s49, s71, s49\n"
  "s_and_b32 s71, s53, 255\n"
  "s_add_i32 s71, s71, s9\n"
  "s_cmp_gt_i32 s9, 0\n"
  "s_cselect_b32 s71, s71, -1\n"
  "v_mov_b32 v32, s71\n"
  "s_add_u32 s60, s56, 4\n"
  "s_bfe_u32 s68, s60, 131073\n"
  "s_lshl_b32 s68, s68, 18\n"
  "s_xor_b32 s60, s60, s47\n"
  "s_and_b32 s60, s60, 1\n"
  "s_lshl_b32 s60, s60, 13\n"
  "s_add_u32 s68, s68, s60\n"
  "buffer_store_dword v32, v16, s[16:19], s68 offen offset:4 sc1\n"
  "s_branch C_JOIN_%=\n"
  "C_LAST_%=:\n"
  "s_nop 7\n"
  "s_add_u32 s72, s58, 131072\n"
  "buffer_store_dword v23, v11, s[16:19], s58 offen sc1\n"
  "buffer_store_dword v23, v11, s[16:19], s72 offen sc1\n"
  "s_branch C_SKIP2_%=\n"
  "C_OTHER_%=:\n"
  "s_nop 7\n"
  "s_add_u32 s60, s56, 2\n"
  "s_bfe_u32 s57, s60, 131073\n"
  "s_lshl_b32 s57, s57, 18\n"
  "s_xor_b32 s60, s60, s47\n"
  "s_and_b32 s60, s60, 1\n"
  "s_lshl_b32 s60, s60, 13\n"
  "s_add_u32 s57, s57, s60\n"
  "s_xor_b32 s75, s96, 40960\n"
  "v_add_u32 v17, s75, v0\n"
  "s_sub_u32 s97, 237568, s97\n"
  "s_mul_i32 s60, s44, 5120\n"
  "s_add_u32 s60, s60, s97\n"
  "v_add_u32 v19, s60, v0\n"
  "s_add_u32 s59, s59, s55\n"
  "s_cmp_eq_u32 s44, 1\n"
  "s_cselect_b32 s98, 0, s44\n"
  "s_and_b32 s68, s56, 7\n"
  "s_mul_i32 s68, s68, 81920\n"
  "s_mul_i32 s60, s98, 20480\n"
  "s_add_u32 s68, s68, s60\n"
  "s_add_u32 s69, s68, 5120\n"
  "s_add_u32 s70, s68, 10240\n"
  "s_add_u32 s71, s68, 15360\n"
  "s_mul_i32 s74, s99, 40960\n"
  "s_mul_i32 s63, s56, 40960\n"
  "s_add_u32 s58, s99, 4\n"
  "s_lshl_b32 s58, s58, 7\n"
  "s_add_u32 s58, s58, 1835008\n"
  "s_mov_b64 s[66:67], 0\n"
  "v_mov_b32 v28, s3\n"
  "v_mov_b32 v29, s2\n"
  "s_cmp_ge_u32 s56, 62\n"
  "s_cbranch_scc0 C_DOU_%=\n"
  "C_WT4_%=:\n"
  "s_memrealtime s[64:65]\n"
  "s_waitcnt lgkmcnt(0)\n"
  "s_sub_i32 s60, s64, s59\n"
  "s_cmp_lt_i32 s60, 0\n"
  "s_cbranch_scc1 C_WT4_%=\n"
  "buffer_load_dword v22, v14, s[32:35], s74 offen sc1\n"
  "s_mov_b64 exec, 1\n"
  "buffer_store_dword v28, v29, s[16:19], 0 offen offset:4 sc1\n"
  "s_mov_b64 exec, -1\n"
  "buffer_load_dword v15, off, s[16:19], s58 sc1\n"
  "buffer_load_dwordx4 v[112:115], v8, s[16:19], s68 offen offset:0 sc1\n"
  "buffer_load_dwordx4 v[116:119], v8, s[16:19], s68 offen offset:1280 sc1\n"
  "buffer_load_dwordx4 v[120:123], v8, s[16:19], s68 offen offset:2560 sc1\n"
  "buffer_load_dwordx4 v[124:127], v8, s[16:19], s68 offen offset:3840 sc1\n"
  "buffer_load_dwordx4 v[128:131], v9, s[16:19], s68 offen sc1\n"
  "buffer_load_dwordx4 v[132:135], v8, s[16:19], s69 offen offset:0 sc1\n"
  "buffer_load_dwordx4 v[136:139], v8, s[16:19], s69 offen offset:1280 sc1\n"
  "buffer_load_dwordx4 v[140:143], v8, s[16:19], s69 offen offset:2560 sc1\n"
  "buffer_load_dwordx4 v[144:147], v8, s[16:19], s69 offen offset:3840 sc1\n"
  "buffer_load_dwordx4 v[148:151], v9, s[16:19], s69 offen sc1\n"
  "buffer_load_dwordx4 v[152:155], v8, s[16:19], s70 offen offset:0 sc1\n"
  "buffer_load_dwordx4 v[156:159], v8, s[16:19], s70 offen offset:1280 sc1\n"
  "buffer_load_dwordx4 v[160:163], v8, s[16:19], s70 offen offset:2560 sc1\n"
  "buffer_load_dwordx4 v[164:167], v8, s[16:19], s70 offen offset:3840 sc1\n"
  "buffer_load_dwordx4 v[168:171], v9, s[16:19], s70 offen sc1\n"
  "buffer_load_dwordx4 v[172:175], v8, s[16:19], s71 offen offset:0 sc1\n"
  "buffer_load_dwordx4 v[176:179], v8, s[16:19], s71 offen offset:1280 sc1\n"
  "buffer_load_dwordx4 v[180:183], v8, s[16:19], s71 offen offset:2560 sc1\n"
  "buffer_load_dwordx4 v[184:187], v8, s[16:19], s71 offen offset:3840 sc1\n"
  "buffer_load_dwordx4 v[188:191], v9, s[16:19], s71 offen sc1\n"
  "buffer_load_dwordx4 v[96:99], v13, s[16:19], s68 offen offset:0 sc1\n"
  "buffer_load_dwordx4 v[100:103], v13, s[16:19], s68 offen offset:1024 sc1\n"
  "buffer_load_dwordx4 v[104:107], v13, s[16:19], s68 offen offset:2048 sc1\n"
  "buffer_load_dwordx4 v[108:111], v13, s[16:19], s68 offen offset:3072 sc1\n"
  "s_branch C_NOU_%=\n"
  "C_DOU_%=:\n"
  "s_nop 7\n"
  "ds_read_b128 v[64:67], v17 offset:0\n"
  "ds_read_b128 v[68:71], v17 offset:10240\n"
  "ds_read_b128 v[72:75], v17 offset:20480\n"
  "ds_read_b128 v[76:79], v17 offset:30720\n"
  "s_waitcnt lgkmcnt(3)\n"
  "v_mfma_f32_16x16x32_bf16 v[32:35], v[64:67], a[0:3], 0\n"
  "s_waitcnt lgkmcnt(2)\n"
  "v_mfma_f32_16x16x32_bf16 v[36:39], v[68:71], a[0:3], 0\n"
  "ds_read_b128 v[80:83], v17 offset:1024\n"
  "v_mfma_f32_16x16x32_bf16 v[40:43], v[64:67], a[8:11], 0\n"
  "v_mfma_f32_16x16x32_bf16 v[44:47], v[68:71], a[8:11], 0\n"
  "ds_read_b128 v[84:87], v17 offset:11264\n"
  "v_mfma_f32_16x16x32_bf16 v[48:51], v[64:67], a[16:19], 0\n"
  "v_mfma_f32_16x16x32_bf16 v[52:55], v[68:71], a[16:19], 0\n"
  "ds_read_b128 v[88:91], v17 offset:21504\n"
  "v_mfma_f32_16x16x32_bf16 v[56:59], v[64:67], a[24:27], 0\n"
  "v_mfma_f32_16x16x32_bf16 v[60:63], v[68:71], a[24:27], 0\n"
  "ds_read_b128 v[92:95], v17 offset:31744\n"
  "s_waitcnt lgkmcnt(5)\n"
  "v_mfma_f32_16x16x32_bf16 v[32:35], v[72:75], a[4:7], v[32:35]\n"
  "s_waitcnt lgkmcnt(4)\n"
  "v_mfma_f32_16x16x32_bf16 v[36:39], v[76:79], a[4:7], v[36:39]\n"
  "v_mfma_f32_16x16x32_bf16 v[40:43], v[72:75], a[12:15], v[40:43]\n"
  "v_mfma_f32_16x16x32_bf16 v[44:47], v[76:79], a[12:15], v[44:47]\n"
  "v_mfma_f32_16x16x32_bf16 v[48:51], v[72:75], a[20:23], v[48:51]\n"
  "v_mfma_f32_16x16x32_bf16 v[52:55], v[76:79], a[20:23], v[52:55]\n"
  "v_mfma_f32_16x16x32_bf16 v[56:59], v[72:75], a[28:31], v[56:59]\n"
  "v_mfma_f32_16x16x32_bf16 v[60:63], v[76:79], a[28:31], v[60:63]\n"
  "s_waitcnt lgkmcnt(3)\n"
  "v_mfma_f32_16x16x32_bf16 v[32:35], v[80:83], a[32:35], v[32:35]\n"
  "s_waitcnt lgkmcnt(2)\n"
  "v_mfma_f32_16x16x32_bf16 v[36:39], v[84:87], a[32:35], v[36:39]\n"
  "ds_read_b128 v[64:67], v17 offset:2048\n"
  "v_mfma_f32_16x16x32_bf16 v[40:43], v[80:83], a[40:43], v[40:43]\n"
  "v_mfma_f32_16x16x32_bf16 v[44:47], v[84:87], a[40:43], v[44:47]\n"
  "ds_read_b128 v[68:71], v17 offset:12288\n"
  "v_mfma_f32_16x16x32_bf16 v[48:51], v[80:83], a[48:51], v[48:51]\n"
  "v_mfma_f32_16x16x32_bf16 v[52:55], v[84:87], a[48:51], v[52:55]\n"
  "ds_read_b128 v[72:75], v17 offset:22528\n"
  "v_mfma_f32_16x16x32_bf16 v[56:59], v[80:83], a[56:59], v[56:59]\n"
  "v_mfma_f32_16x16x32_bf16 v[60:63], v[84:87], a[56:59], v[60:63]\n"
  "ds_read_b128 v[76:79], v17 offset:32768\n"
  "s_waitcnt lgkmcnt(5)\n"
  "v_mfma_f32_16x16x32_bf16 v[32:35], v[88:91], a[36:39], v[32:35]\n"
  "s_waitcnt lgkmcnt(4)\n"
  "v_mfma_f32_16x16x32_bf16 v[36:39], v[92:95], a[36:39], v[36:39]\n"
  "v_mfma_f32_16x16x32_bf16 v[40:43], v[88:91], a[44:47], v[40:43]\n"
  "v_mfma_f32_16x16x32_bf16 v[44:47], v[92:95], a[44:47], v[44:47]\n"
  "v_mfma_f32_16x16x32_bf16 v[48:51], v[88:91], a[52:55], v[48:51]\n"
  "v_mfma_f32_16x16x32_bf16 v[52:55], v[92:95], a[52:55], v[52:55]\n"
  "v_mfma_f32_16x16x32_bf16 v[56:59], v[88:91], a[60:63], v[56:59]\n"
  "v_mfma_f32_16x16x32_bf16 v[60:63], v[92:95], a[60:63], v[60:63]\n"
  "s_waitcnt lgkmcnt(3)\n"
  "v_mfma_f32_16x16x32_bf16 v[32:35], v[64:67], a[64:67], v[32:35]\n"
  "s_waitcnt lgkmcnt(2)\n"
  "v_mfma_f32_16x16x32_bf16 v[36:39], v[68:71], a[64:67], v[36:39]\n"
  "ds_read_b128 v[80:83], v17 offset:3072\n"
  "v_mfma_f32_16x16x32_bf16 v[40:43], v[64:67], a[72:75], v[40:43]\n"
  "v_mfma_f32_16x16x32_bf16 v[44:47], v[68:71], a[72:75], v[44:47]\n"
  "ds_read_b128 v[84:87], v17 offset:13312\n"
  "v_mfma_f32_16x16x32_bf16 v[48:51], v[64:67], a[80:83], v[48:51]\n"
  "v_mfma_f32_16x16x32_bf16 v[52:55], v[68:71], a[80:83], v[52:55]\n"
  "ds_read_b128 v[88:91], v17 offset:23552\n"
  "v_mfma_f32_16x16x32_bf16 v[56:59], v[64:67], a[88:91], v[56:59]\n"
  "v_mfma_f32_16x16x32_bf16 v[60:63], v[68:71], a[88:91], v[60:63]\n"
  "ds_read_b128 v[92:95], v17 offset:33792\n"
  "s_waitcnt lgkmcnt(5)\n"
  "v_mfma_f32_16x16x32_bf16 v[32:35], v[72:75], a[68:71], v[32:35]\n"
  "s_waitcnt lgkmcnt(4)\n"
  "v_mfma_f32_16x16x32_bf16 v[36:39], v[76:79], a[68:71], v[36:39]\n"
  "v_mfma_f32_16x16x32_bf16 v[40:43], v[72:75], a[76:79], v[40:43]\n"
  "v_mfma_f32_16x16x32_bf16 v[44:47], v[76:79], a[76:79], v[44:47]\n"
  "v_mfma_f32_16x16x32_bf16 v[48:51], v[72:75], a[84:87], v[48:51]\n"
  "v_mfma_f32_16x16x32_bf16 v[52:55], v[76:79], a[84:87], v[52:55]\n"
  "v_mfma_f32_16x16x32_bf16 v[56:59], v[72:75], a[92:95], v[56:59]\n"
  "v_mfma_f32_16x16x32_bf16 v[60:63], v[76:79], a[92:95], v[60:63]\n"
  "s_waitcnt lgkmcnt(3)\n"
  "v_mfma_f32_16x16x32_bf16 v[32:35], v[80:83], a[96:99], v[32:35]\n"
  "s_waitcnt lgkmcnt(2)\n"
  "v_mfma_f32_16x16x32_bf16 v[36:39], v[84:87], a[96:99], v[36:39]\n"
  "ds_read_b128 v[64:67], v17 offset:4096\n"
  "v_mfma_f32_16x16x32_bf16 v[40:43], v[80:83], a[104:107], v[40:43]\n"
  "v_mfma_f32_16x16x32_bf16 v[44:47], v[84:87], a[104:107], v[44:47]\n"
  "ds_read_b128 v[68:71], v17 offset:14336\n"
  "v_mfma_f32_16x16x32_bf16 v[48:51], v[80:83], a[112:115], v[48:51]\n"
  "v_mfma_f32_16x16x32_bf16 v[52:55], v[84:87], a[112:115], v[52:55]\n"
  "ds_read_b128 v[72:75], v17 offset:24576\n"
  "v_mfma_f32_16x16x32_bf16 v[56:59], v[80:83], a[120:123], v[56:59]\n"
  "v_mfma_f32_16x16x32_bf16 v[60:63], v[84:87], a[120:123], v[60:63]\n"
  "ds_read_b128 v[76:79], v17 offset:34816\n"
  "s_waitcnt lgkmcnt(5)\n"
  "v_mfma_f32_16x16x32_bf16 v[32:35], v[88:91], a[100:103], v[32:35]\n"
  "s_waitcnt lgkmcnt(4)\n"
  "v_mfma_f32_16x16x32_bf16 v[36:39], v[92:95], a[100:103], v[36:39]\n"
  "v_mfma_f32_16x16x32_bf16 v[40:43], v[88:91], a[108:111], v[40:43]\n"
  "v_mfma_f32_16x16x32_bf16 v[44:47], v[92:95], a[108:111], v[44:47]\n"
  "v_mfma_f32_16x16x32_bf16 v[48:51], v[88:91], a[116:119], v[48:51]\n"
  "v_mfma_f32_16x16x32_bf16 v[52:55], v[92:95], a[116:119], v[52:55]\n"
  "v_mfma_f32_16x16x32_bf16 v[56:59], v[88:91], a[124:127], v[56:59]\n"
  "v_mfma_f32_16x16x32_bf16 v[60:63], v[92:95], a[124:127], v[60:63]\n"
  "s_waitcnt lgkmcnt(3)\n"
  "v_mfma_f32_16x16x32_bf16 v[32:35], v[64:67], a[128:131], v[32:35]\n"
  "s_waitcnt lgkmcnt(2)\n"
  "v_mfma_f32_16x16x32_bf16 v[36:39], v[68:71], a[128:131], v[36:39]\n"
  "ds_read_b128 v[80:83], v17 offset:5120\n"
  "v_mfma_f32_16x16x32_bf16 v[40:43], v[64:67], a[136:139], v[40:43]\n"
  "v_mfma_f32_16x16x32_bf16 v[44:47], v[68:71], a[136:139], v[44:47]\n"
  "ds_read_b128 v[84:87], v17 offset:15360\n"
  "v_mfma_f32_16x16x32_bf16 v[48:51], v[64:67], a[144:147], v[48:51]\n"
  "v_mfma_f32_16x16x32_bf16 v[52:55], v[68:71], a[144:147], v[52:55]\n"
  "ds_read_b128 v[88:91], v17 offset:25600\n"
  "v_mfma_f32_16x16x32_bf16 v[56:59], v[64:67], a[152:155], v[56:59]\n"
  "v_mfma_f32_16x16x32_bf16 v[60:63], v[68:71], a[152:155], v[60:63]\n"
  "ds_read_b128 v[92:95], v17 offset:35840\n"
  "s_waitcnt lgkmcnt(5)\n"
  "v_mfma_f32_16x16x32_bf16 v[32:35], v[72:75], a[132:135], v[32:35]\n"
  "s_waitcnt lgkmcnt(4)\n"
  "v_mfma_f32_16x16x32_bf16 v[36:39], v[76:79], a[132:135], v[36:39]\n"
  "v_mfma_f32_16x16x32_bf16 v[40:43], v[72:75], a[140:143], v[40:43]\n"
  "v_mfma_f32_16x16x32_bf16 v[44:47], v[76:79], a[140:143], v[44:47]\n"
  "v_mfma_f32_16x16x32_bf16 v[48:51], v[72:75], a[148:151], v[48:51]\n"
  "v_mfma_f32_16x16x32_bf16 v[52:55], v[76:79], a[148:151], v[52:55]\n"
  "v_mfma_f32_16x16x32_bf16 v[56:59], v[72:75], a[156:159], v[56:59]\n"
  "v_mfma_f32_16x16x32_bf16 v[60:63], v[76:79], a[156:159], v[60:63]\n"
  "s_waitcnt lgkmcnt(0)\n"
  "C_WT3_%=:\n"
  "s_memrealtime s[64:65]\n"
  "s_waitcnt lgkmcnt(0)\n"
  "s_sub_i32 s60, s64, s59\n"
  "s_cmp_lt_i32 s60, 0\n"
  "s_cbranch_scc1 C_WT3_%=\n"
  "v_mfma_f32_16x16x32_bf16 v[32:35], v[80:83], a[160:163], v[32:35]\n"
  "buffer_load_dword v22, v14, s[32:35], s74 offen sc1\n"
  "v_mfma_f32_16x16x32_bf16 v[36:39], v[84:87], a[160:163], v[36:39]\n"
  "ds_read_b128 v[64:67], v17 offset:6144\n"
  "v_mfma_f32_16x16x32_bf16 v[40:43], v[80:83], a[168:171], v[40:43]\n"
  "v_mfma_f32_16x16x32_bf16 v[44:47], v[84:87], a[168:171], v[44:47]\n"
  "ds_read_b128 v[68:71], v17 offset:16384\n"
  "s_mov_b64 exec, 1\n"
  "buffer_store_dword v28, v29, s[16:19], 0 offen offset:4 sc1\n"
  "s_mov_b64 exec, -1\n"
  "buffer_load_dword v15, off, s[16:19], s58 sc1\n"
  "v_mfma_f32_16x16x32_bf16 v[48:51], v[80:83], a[176:179], v[48:51]\n"
  "v_mfma_f32_16x16x32_bf16 v[52:55], v[84:87], a[176:179], v[52:55]\n"
  "ds_read_b128 v[72:75], v17 offset:26624\n"
  "v_mfma_f32_16x16x32_bf16 v[56:59], v[80:83], a[184:187], v[56:59]\n"
  "buffer_load_dwordx4 v[112:115], v8, s[16:19], s68 offen offset:0 sc1\n"
  "v_mfma_f32_16x16x32_bf16 v[60:63], v[84:87], a[184:187], v[60:63]\n"
  "ds_read_b128 v[76:79], v17 offset:36864\n"
  "v_mfma_f32_16x16x32_bf16 v[32:35], v[88:91], a[164:167], v[32:35]\n"
  "v_mfma_f32_16x16x32_bf16 v[36:39], v[92:95], a[164:167], v[36:39]\n"
  "buffer_load_dwordx4 v[116:119], v8, s[16:19], s68 offen offset:1280 sc1\n"
  "v_mfma_f32_16x16x32_bf16 v[40:43], v[88:91], a[172:175], v[40:43]\n"
  "v_mfma_f32_16x16x32_bf16 v[44:47], v[92:95], a[172:175], v[44:47]\n"
  "v_mfma_f32_16x16x32_bf16 v[48:51], v[88:91], a[180:183], v[48:51]\n"
  "buffer_load_dwordx4 v[120:123], v8, s[16:19], s68 offen offset:2560 sc1\n"
  "v_mfma_f32_16x16x32_bf16 v[52:55], v[92:95], a[180:183], v[52:55]\n"
  "v_mfma_f32_16x16x32_bf16 v[56:59], v[88:91], a[188:191], v[56:59]\n"
  "v_mfma_f32_16x16x32_bf16 v[60:63], v[92:95], a[188:191], v[60:63]\n"
  "buffer_load_dwordx4 v[124:127], v8, s[16:19], s68 offen offset:3840 sc1\n"
  "s_waitcnt lgkmcnt(3)\n"
  "v_mfma_f32_16x16x32_bf16 v[32:35], v[64:67], a[192:195], v[32:35]\n"
  "s_waitcnt lgkmcnt(2)\n"
  "v_mfma_f32_16x16x32_bf16 v[36:39], v[68:71], a[192:195], v[36:39]\n"
  "ds_read_b128 v[80:83], v17 offset:7168\n"
  "v_mfma_f32_16x16x32_bf16 v[40:43], v[64:67], a[200:203], v[40:43]\n"
  "buffer_load_dwordx4 v[128:131], v9, s[16:19], s68 offen sc1\n"
  "v_mfma_f32_16x16x32_bf16 v[44:47], v[68:71], a[200:203], v[44:47]\n"
  "ds_read_b128 v[84:87], v17 offset:17408\n"
  "v_mfma_f32_16x16x32_bf16 v[48:51], v[64:67], a[208:211], v[48:51]\n"
  "v_mfma_f32_16x16x32_bf16 v[52:55], v[68:71], a[208:211], v[52:55]\n"
  "ds_read_b128 v[88:91], v17 offset:27648\n"
  "buffer_load_dwordx4 v[132:135], v8, s[16:19], s69 offen offset:0 sc1\n"
  "v_mfma_f32_16x16x32_bf16 v[56:59], v[64:67], a[216:219], v[56:59]\n"
  "v_mfma_f32_16x16x32_bf16 v[60:63], v[68:71], a[216:219], v[60:63]\n"
  "ds_read_b128 v[92:95], v17 offset:37888\n"
  "s_waitcnt lgkmcnt(5)\n"
  "v_mfma_f32_16x16x32_bf16 v[32:35], v[72:75], a[196:199], v[32:35]\n"
  "buffer_load_dwordx4 v[136:139], v8, s[16:19], s69 offen offset:1280 sc1\n"
  "s_waitcnt lgkmcnt(4)\n"
  "v_mfma_f32_16x16x32_bf16 v[36:39], v[76:79], a[196:199], v[36:39]\n"
  "v_mfma_f32_16x16x32_bf16 v[40:43], v[72:75], a[204:207], v[40:43]\n"
  "v_mfma_f32_16x16x32_bf16 v[44:47], v[76:79], a[204:207], v[44:47]\n"
  "buffer_load_dwordx4 v[140:143], v8, s[16:19], s69 offen offset:2560 sc1\n"
  "v_mfma_f32_16x16x32_bf16 v[48:51], v[72:75], a[212:215], v[48:51]\n"
  "v_mfma_f32_16x16x32_bf16 v[52:55], v[76:79], a[212:215], v[52:55]\n"
  "v_mfma_f32_16x16x32_bf16 v[56:59], v[72:75], a[220:223], v[56:59]\n"
  "buffer_load_dwordx4 v[144:147], v8, s[16:19], s69 offen offset:3840 sc1\n"
  "v_mfma_f32_16x16x32_bf16 v[60:63], v[76:79], a[220:223], v[60:63]\n"
  "s_waitcnt lgkmcnt(3)\n"
  "v_mfma_f32_16x16x32_bf16 v[32:35], v[80:83], a[224:227], v[32:35]\n"
  "s_waitcnt lgkmcnt(2)\n"
  "v_mfma_f32_16x16x32_bf16 v[36:39], v[84:87], a[224:227], v[36:39]\n"
  "ds_read_b128 v[64:67], v17 offset:8192\n"
  "buffer_load_dwordx4 v[148:151], v9, s[16:19], s69 offen sc1\n"
  "v_mfma_f32_16x16x32_bf16 v[40:43], v[80:83], a[232:235], v[40:43]\n"
  "v_mfma_f32_16x16x32_bf16 v[44:47], v[84:87], a[232:235], v[44:47]\n"
  "ds_read_b128 v[68:71], v17 offset:18432\n"
  "v_mfma_f32_16x16x32_bf16 v[48:51], v[80:83], a[240:243], v[48:51]\n"
  "buffer_load_dwordx4 v[152:155], v8, s[16:19], s70 offen offset:0 sc1\n"
  "v_mfma_f32_16x16x32_bf16 v[52:55], v[84:87], a[240:243], v[52:55]\n"
  "ds_read_b128 v[72:75], v17 offset:28672\n"
  "v_mfma_f32_16x16x32_bf16 v[56:59], v[80:83], a[248:251], v[56:59]\n"
  "v_mfma_f32_16x16x32_bf16 v[60:63], v[84:87], a[248:251], v[60:63]\n"
  "ds_read_b128 v[76:79], v17 offset:38912\n"
  "buffer_load_dwordx4 v[156:159], v8, s[16:19], s70 offen offset:1280 sc1\n"
  "s_waitcnt lgkmcnt(5)\n"
  "v_mfma_f32_16x16x32_bf16 v[32:35], v[88:91], a[228:231], v[32:35]\n"
  "s_waitcnt lgkmcnt(4)\n"
  "v_mfma_f32_16x16x32_bf16 v[36:39], v[92:95], a[228:231], v[36:39]\n"
  "v_mfma_f32_16x16x32_bf16 v[40:43], v[88:91], a[236:239], v[40:43]\n"
  "buffer_load_dwordx4 v[160:163], v8, s[16:19], s70 offen offset:2560 sc1\n"
  "v_mfma_f32_16x16x32_bf16 v[44:47], v[92:95], a[236:239], v[44:47]\n"
  "v_mfma_f32_16x16x32_bf16 v[48:51], v[88:91], a[244:247], v[48:51]\n"
  "v_mfma_f32_16x16x32_bf16 v[52:55], v[92:95], a[244:247], v[52:55]\n"
  "buffer_load_dwordx4 v[164:167], v8, s[16:19], s70 offen offset:3840 sc1\n"
  "v_mfma_f32_16x16x32_bf16 v[56:59], v[88:91], a[252:255], v[56:59]\n"
  "v_mfma_f32_16x16x32_bf16 v[60:63], v[92:95], a[252:255], v[60:63]\n"
  "s_waitcnt lgkmcnt(3)\n"
  "v_mfma_f32_16x16x32_bf16 v[32:35], v[64:67], v[192:195], v[32:35]\n"
  "buffer_load_dwordx4 v[168:171], v9, s[16:19], s70 offen sc1\n"
  "s_waitcnt lgkmcnt(2)\n"
  "v_mfma_f32_16x16x32_bf16 v[36:39], v[68:71], v[192:195], v[36:39]\n"
  "ds_read_b128 v[80:83], v17 offset:9216\n"
  "v_mfma_f32_16x16x32_bf16 v[40:43], v[64:67], v[200:203], v[40:43]\n"
  "v_mfma_f32_16x16x32_bf16 v[44:47], v[68:71], v[200:203], v[44:47]\n"
  "ds_read_b128 v[84:87], v17 offset:19456\n"
  "buffer_load_dwordx4 v[172:175], v8, s[16:19], s71 offen offset:0 sc1\n"
  "v_mfma_f32_16x16x32_bf16 v[48:51], v[64:67], v[208:211], v[48:51]\n"
  "v_mfma_f32_16x16x32_bf16 v[52:55], v[68:71], v[208:211], v[52:55]\n"
  "ds_read_b128 v[88:91], v17 offset:29696\n"
  "v_mfma_f32_16x16x32_bf16 v[56:59], v[64:67], v[216:219], v[56:59]\n"
  "buffer_load_dwordx4 v[176:179], v8, s[16:19], s71 offen offset:1280 sc1\n"
  "v_mfma_f32_16x16x32_bf16 v[60:63], v[68:71], v[216:219], v[60:63]\n"
  "ds_read_b128 v[92:95], v17 offset:39936\n"
  "s_waitcnt lgkmcnt(5)\n"
  "v_mfma_f32_16x16x32_bf16 v[32:35], v[72:75], v[196:199], v[32:35]\n"
  "s_waitcnt lgkmcnt(4)\n"
  "v_mfma_f32_16x16x32_bf16 v[36:39], v[76:79], v[196:199], v[36:39]\n"
  "buffer_load_dwordx4 v[180:183], v8, s[16:19], s71 offen offset:2560 sc1\n"
  "v_mfma_f32_16x16x32_bf16 v[40:43], v[72:75], v[204:207], v[40:43]\n"
  "v_mfma_f32_16x16x32_bf16 v[44:47], v[76:79], v[204:207], v[44:47]\n"
  "v_mfma_f32_16x16x32_bf16 v[48:51], v[72:75], v[212:215], v[48:51]\n"
  "buffer_load_dwordx4 v[184:187], v8, s[16:19], s71 offen offset:3840 sc1\n"
  "v_mfma_f32_16x16x32_bf16 v[52:55], v[76:79], v[212:215], v[52:55]\n"
  "v_mfma_f32_16x16x32_bf16 v[56:59], v[72:75], v[220:223], v[56:59]\n"
  "v_mfma_f32_16x16x32_bf16 v[60:63], v[76:79], v[220:223], v[60:63]\n"
  "buffer_load_dwordx4 v[188:191], v9, s[16:19], s71 offen sc1\n"
  "s_waitcnt lgkmcnt(3)\n"
  "v_mfma_f32_16x16x32_bf16 v[32:35], v[80:83], v[224:227], v[32:35]\n"
  "s_waitcnt lgkmcnt(2)\n"
  "v_mfma_f32_16x16x32_bf16 v[36:39], v[84:87], v[224:227], v[36:39]\n"
  "v_mfma_f32_16x16x32_bf16 v[40:43], v[80:83], v[232:235], v[40:43]\n"
  "buffer_load_dwordx4 v[96:99], v13, s[16:19], s68 offen offset:0 sc1\n"
  "v_mfma_f32_16x16x32_bf16 v[44:47], v[84:87], v[232:235], v[44:47]\n"
  "v_mfma_f32_16x16x32_bf16 v[48:51], v[80:83], v[240:243], v[48:51]\n"
  "v_mfma_f32_16x16x32_bf16 v[52:55], v[84:87], v[240:243], v[52:55]\n"
  "buffer_load_dwordx4 v[100:103], v13, s[16:19], s68 offen offset:1024 sc1\n"
  "v_mfma_f32_16x16x32_bf16 v[56:59], v[80:83], v[248:251], v[56:59]\n"
  "v_mfma_f32_16x16x32_bf16 v[60:63], v[84:87], v[248:251], v[60:63]\n"
  "s_waitcnt lgkmcnt(1)\n"
  "v_mfma_f32_16x16x32_bf16 v[32:35], v[88:91], v[228:231], v[32:35]\n"
  "buffer_load_dwordx4 v[104:107], v13, s[16:19], s68 offen offset:2048 sc1\n"
  "s_waitcnt lgkmcnt(0)\n"
  "v_mfma_f32_16x16x32_bf16 v[36:39], v[92:95], v[228:231], v[36:39]\n"
  "v_mfma_f32_16x16x32_bf16 v[40:43], v[88:91], v[236:239], v[40:43]\n"
  "v_mfma_f32_16x16x32_bf16 v[44:47], v[92:95], v[236:239], v[44:47]\n"
  "buffer_load_dwordx4 v[108:111], v13, s[16:19], s68 offen offset:3072 sc1\n"
  "v_mfma_f32_16x16x32_bf16 v[48:51], v[88:91], v[244:247], v[48:51]\n"
  "v_mfma_f32_16x16x32_bf16 v[52:55], v[92:95], v[244:247], v[52:55]\n"
  "v_mfma_f32_16x16x32_bf16 v[56:59], v[88:91], v[252:255], v[56:59]\n"
  "v_mfma_f32_16x16x32_bf16 v[60:63], v[92:95], v[252:255], v[60:63]\n"
  "v_cvt_pk_bf16_f32 v32, v32, v33\n"
  "v_cvt_pk_bf16_f32 v33, v34, v35\n"
  "v_cvt_pk_bf16_f32 v34, v36, v37\n"
  "v_cvt_pk_bf16_f32 v35, v38, v39\n"
  "ds_write_b128 v19, v[32:35] offset:0\n"
  "v_cvt_pk_bf16_f32 v40, v40, v41\n"
  "v_cvt_pk_bf16_f32 v41, v42, v43\n"
  "v_cvt_pk_bf16_f32 v42, v44, v45\n"
  "v_cvt_pk_bf16_f32 v43, v46, v47\n"
  "ds_write_b128 v19, v[40:43] offset:1024\n"
  "v_cvt_pk_bf16_f32 v48, v48, v49\n"
  "v_cvt_pk_bf16_f32 v49, v50, v51\n"
  "v_cvt_pk_bf16_f32 v50, v52, v53\n"
  "v_cvt_pk_bf16_f32 v51, v54, v55\n"
  "ds_write_b128 v19, v[48:51] offset:2048\n"
  "v_cvt_pk_bf16_f32 v56, v56, v57\n"
  "v_cvt_pk_bf16_f32 v57, v58, v59\n"
  "v_cvt_pk_bf16_f32 v58, v60, v61\n"
  "v_cvt_pk_bf16_f32 v59, v62, v63\n"
  "ds_write_b128 v19, v[56:59] offset:3072\n"
  "C_NOU_%=:\n"
  "s_mov_b32 s62, 0\n"
  "s_waitcnt vmcnt(24)\n"
  "v_readfirstlane_b32 s60, v15\n"
  "s_cmp_lg_u32 s60, -1\n"
  "s_cbranch_scc1 C_F4K_%=\n"
  "s_cmp_ge_u32 s56, 60\n"
  "s_cbranch_scc1 C_F4K_%=\n"
  "s_waitcnt vmcnt(0) lgkmcnt(0)\n"
  "C_F4P_%=:\n"
  "buffer_load_dword v15, off, s[16:19], s58 sc1\n"
  "s_waitcnt vmcnt(0)\n"
  "v_readfirstlane_b32 s60, v15\n"
  "s_cmp_lg_u32 s60, -1\n"
  "s_cbranch_scc1 C_F4K_%=\n"
  "s_sleep 4\n"
  "s_add_u32 s62, s62, 1\n"
  "s_cmp_gt_u32 s62, 300000\n"
  "s_cbranch_scc1 C_FAIL_%=\n"
  "s_and_b32 s60, s62, 255\n"
  "s_cmp_lg_u32 s60, 0\n"
  "s_cbranch_scc1 C_F4P_%=\n"
  "v_mov_b32 v15, 0\n"
  "buffer_load_dword v15, v15, s[40:43], 0 offen sc1\n"
  "s_waitcnt vmcnt(0)\n"
  "v_readfirstlane_b32 s60, v15\n"
  "s_cmp_lg_u32 s60, 0\n"
  "s_cbranch_scc1 C_EXIT_%=\n"
  "s_branch C_F4P_%=\n"
  "C_F4K_%=:\n"
  "s_sub_u32 s98, s44, 1\n"
  "s_add_u32 s60, s99, 4\n"
  "s_mul_i32 s72, s60, 40960\n"
  "s_mul_i32 s73, s98, 13312\n"
  "s_add_u32 s72, s72, s73\n"
  "s_add_u32 s73, s73, s96\n"
  "s_mov_b32 m0, s73\n"
  "s_nop 0\n"
  "buffer_load_dwordx4 v0, s[24:27], s72 offen offset:0 sc1 lds\n"
  "buffer_load_dwordx4 v0, s[24:27], s72 offen offset:1024 sc1 lds\n"
  "buffer_load_dwordx4 v0, s[24:27], s72 offen offset:2048 sc1 lds\n"
  "buffer_load_dwordx4 v0, s[24:27], s72 offen offset:3072 sc1 lds\n"
  "s_add_u32 s72, s72, 4096\n"
  "s_add_u32 s73, s73, 4096\n"
  "s_mov_b32 m0, s73\n"
  "s_nop 0\n"
  "buffer_load_dwordx4 v0, s[24:27], s72 offen offset:0 sc1 lds\n"
  "buffer_load_dwordx4 v0, s[24:27], s72 offen offset:1024 sc1 lds\n"
  "buffer_load_dwordx4 v0, s[24:27], s72 offen offset:2048 sc1 lds\n"
  "buffer_load_dwordx4 v0, s[24:27], s72 offen offset:3072 sc1 lds\n"
  "s_add_u32 s72, s72, 4096\n"
  "s_add_u32 s73, s73, 4096\n"
  "s_mov_b32 m0, s73\n"
  "s_nop 0\n"
  "buffer_load_dwordx4 v0, s[24:27], s72 offen offset:0 sc1 lds\n"
  "buffer_load_dwordx4 v0, s[24:27], s72 offen offset:1024 sc1 lds\n"
  "buffer_load_dwordx4 v0, s[24:27], s72 offen offset:2048 sc1 lds\n"
  "buffer_load_dwordx4 v0, s[24:27], s72 offen offset:3072 sc1 lds\n"
  "s_add_u32 s72, s72, 4096\n"
  "s_add_u32 s73, s73, 4096\n"
  "s_mov_b32 m0, s73\n"
  "s_nop 0\n"
  "buffer_load_dwordx4 v0, s[24:27], s72 offen offset:0 sc1 lds\n"
  "buffer_load_dwordx4 v0, s[24:27], s72 offen offset:1024 sc1 lds\n"
  "buffer_load_dwordx2 v[30:31], v10, s[16:19], s57 offen sc1\n"
  "s_cmp_eq_u32 s44, 1\n"
  "s_cbranch_scc1 C_W1_%=\n"
  "ds_read_b128 v[32:35], v18 offset:0\n"
  "ds_read_b128 v[36:39], v18 offset:1024\n"
  "ds_read_b128 v[40:43], v18 offset:2048\n"
  "ds_read_b128 v[44:47], v18 offset:3072\n"
  "ds_read_b128 v[48:51], v18 offset:4096\n"
  "ds_read_b128 v[52:55], v18 offset:5120\n"
  "ds_read_b128 v[56:59], v18 offset:6144\n"
  "ds_read_b128 v[60:63], v18 offset:7168\n"
  "ds_read_b128 v[64:67], v18 offset:8192\n"
  "ds_read_b128 v[68:71], v18 offset:9216\n"
  "s_waitcnt lgkmcnt(10)\n"
  "ds_read_b128 v[72:75], v18 offset:10240\n"
  "ds_read_b128 v[76:79], v18 offset:11264\n"
  "ds_read_b128 v[80:83], v18 offset:12288\n"
  "ds_read_b128 v[84:87], v18 offset:13312\n"
  "s_waitcnt lgkmcnt(10)\n"
  "ds_read_b128 v[88:91], v18 offset:14336\n"
  "ds_read_b128 v[92:95], v18 offset:15360\n"
  "s_waitcnt vmcnt(18)\n"
  "ds_read_b128 v[96:99], v18 offset:16384\n"
  "s_waitcnt vmcnt(17)\n"
  "ds_read_b128 v[100:103], v18 offset:17408\n"
  "s_waitcnt vmcnt(16) lgkmcnt(10)\n"
  "ds_read_b128 v[104:107], v18 offset:18432\n"
  "s_waitcnt vmcnt(15)\n"
  "ds_read_b128 v[108:111], v18 offset:19456\n"
  "s_waitcnt lgkmcnt(0)\n"
  "C_X2_%=:\n"
  "v_mfma_f32_16x16x32_bf16 v[24:27], v[32:35], v[112:115], 0\n"
  "v_mfma_f32_16x16x32_bf16 v[24:27], v[36:39], v[116:119], v[24:27]\n"
  "v_mfma_f32_16x16x32_bf16 v[24:27], v[40:43], v[120:123], v[24:27]\n"
  "v_mfma_f32_16x16x32_bf16 v[24:27], v[44:47], v[124:127], v[24:27]\n"
  "v_mfma_f32_16x16x32_bf16 v[24:27], v[48:51], v[128:131], v[24:27]\n"
  "v_mfma_f32_16x16x32_bf16 v[24:27], v[52:55], v[132:135], v[24:27]\n"
  "v_mfma_f32_16x16x32_bf16 v[24:27], v[56:59], v[136:139], v[24:27]\n"
  "v_mfma_f32_16x16x32_bf16 v[24:27], v[60:63], v[140:143], v[24:27]\n"
  "v_mfma_f32_16x16x32_bf16 v[24:27], v[64:67], v[144:147], v[24:27]\n"
  "v_mfma_f32_16x16x32_bf16 v[24:27], v[68:71], v[148:151], v[24:27]\n"
  "v_mfma_f32_16x16x32_bf16 v[24:27], v[72:75], v[152:155], v[24:27]\n"
  "v_mfma_f32_16x16x32_bf16 v[24:27], v[76:79], v[156:159], v[24:27]\n"
  "v_mfma_f32_16x16x32_bf16 v[24:27], v[80:83], v[160:163], v[24:27]\n"
  "v_mfma_f32_16x16x32_bf16 v[24:27], v[84:87], v[164:167], v[24:27]\n"
  "v_mfma_f32_16x16x32_bf16 v[24:27], v[88:91], v[168:171], v[24:27]\n"
  "v_mfma_f32_16x16x32_bf16 v[24:27], v[92:95], v[172:175], v[24:27]\n"
  "v_mfma_f32_16x16x32_bf16 v[24:27], v[96:99], v[176:179], v[24:27]\n"
  "v_mfma_f32_16x16x32_bf16 v[24:27], v[100:103], v[180:183], v[24:27]\n"
  "v_mfma_f32_16x16x32_bf16 v[24:27], v[104:107], v[184:187], v[24:27]\n"
  "v_mfma_f32_16x16x32_bf16 v[24:27], v[108:111], v[188:191], v[24:27]\n"
  "s_nop 7\n"
  "v_add_f32_dpp v24, v26, v24 row_ror:8 row_mask:0xf bank_mask:0xf\n"
  "v_add_f32_dpp v25, v27, v25 row_ror:8 row_mask:0xf bank_mask:0xf\n"
  "v_cmp_u_f32 vcc, v24, v24\n"
  "v_lshlrev_b32 v28, 16, v22\n"
  "v_and_b32 v29, 0xffff0000, v22\n"
  "s_andn2_b64 vcc, vcc, s[66:67]\n"
  "v_add_f32 v24, v24, v28\n"
  "v_add_f32 v25, v25, v29\n"
  "s_cbranch_vccnz C_XREDO_%=\n"
  "v_cvt_pk_bf16_f32 v24, v24, v25\n"
  "buffer_store_dword v24, v12, s[36:39], s63 offen\n"
  "v_min_u32 v25, s78, v24\n"
  "buffer_store_dword v25, v14, s[20:23], s63 offen sc1\n"
  "s_waitcnt vmcnt(2)\n"
  "s_branch C_JOIN_%=\n"
  "C_W1_%=:\n"
  "ds_read_b64 v[32:33], v18 offset:0\n"
  "ds_read_b64 v[34:35], v18 offset:1024\n"
  "ds_read_b64 v[36:37], v18 offset:2048\n"
  "ds_read_b64 v[38:39], v18 offset:3072\n"
  "ds_read_b128 v[64:67], v18 offset:4096\n"
  "ds_read_b64 v[40:41], v18 offset:5120\n"
  "ds_read_b64 v[42:43], v18 offset:6144\n"
  "ds_read_b64 v[44:45], v18 offset:7168\n"
  "ds_read_b64 v[46:47], v18 offset:8192\n"
  "ds_read_b128 v[68:71], v18 offset:9216\n"
  "s_waitcnt lgkmcnt(10)\n"
  "ds_read_b64 v[48:49], v18 offset:10240\n"
  "ds_read_b64 v[50:51], v18 offset:11264\n"
  "ds_read_b64 v[52:53], v18 offset:12288\n"
  "ds_read_b64 v[54:55], v18 offset:13312\n"
  "s_waitcnt lgkmcnt(10)\n"
  "ds_read_b128 v[72:75], v18 offset:14336\n"
  "ds_read_b64 v[56:57], v18 offset:15360\n"
  "ds_read_b64 v[58:59], v18 offset:16384\n"
  "ds_read_b64 v[60:61], v18 offset:17408\n"
  "s_waitcnt lgkmcnt(10)\n"
  "ds_read_b64 v[62:63], v18 offset:18432\n"
  "ds_read_b128 v[76:79], v18 offset:19456\n"
  "s_waitcnt lgkmcnt(0)\n"
  "C_X1_%=:\n"
  "s_waitcnt vmcnt(38)\n"
  "v_mfma_f32_16x16x16_bf16 v[80:83], v[32:33], v[112:113], 0\n"
  "v_mfma_f32_16x16x16_bf16 v[84:87], v[32:33], v[114:115], 0\n"
  "s_waitcnt vmcnt(37)\n"
  "v_mfma_f32_16x16x16_bf16 v[80:83], v[34:35], v[116:117], v[80:83]\n"
  "v_mfma_f32_16x16x16_bf16 v[84:87], v[34:35], v[118:119], v[84:87]\n"
  "s_waitcnt vmcnt(36)\n"
  "v_mfma_f32_16x16x16_bf16 v[80:83], v[36:37], v[120:121], v[80:83]\n"
  "v_mfma_f32_16x16x16_bf16 v[84:87], v[36:37], v[122:123], v[84:87]\n"
  "s_waitcnt vmcnt(35)\n"
  "v_mfma_f32_16x16x16_bf16 v[80:83], v[38:39], v[124:125], v[80:83]\n"
  "v_mfma_f32_16x16x16_bf16 v[84:87], v[38:39], v[126:127], v[84:87]\n"
  "s_waitcnt vmcnt(33)\n"
  "v_mfma_f32_16x16x16_bf16 v[80:83], v[40:41], v[132:133], v[80:83]\n"
  "v_mfma_f32_16x16x16_bf16 v[84:87], v[40:41], v[134:135], v[84:87]\n"
  "s_waitcnt vmcnt(32)\n"
  "v_mfma_f32_16x16x16_bf16 v[80:83], v[42:43], v[136:137], v[80:83]\n"
  "v_mfma_f32_16x16x16_bf16 v[84:87], v[42:43], v[138:139], v[84:87]\n"
  "s_waitcnt vmcnt(31)\n"
  "v_mfma_f32_16x16x16_bf16 v[80:83], v[44:45], v[140:141], v[80:83]\n"
  "v_mfma_f32_16x16x16_bf16 v[84:87], v[44:45], v[142:143], v[84:87]\n"
  "s_waitcnt vmcnt(30)\n"
  "v_mfma_f32_16x16x16_bf16 v[80:83], v[46:47], v[144:145], v[80:83]\n"
  "v_mfma_f32_16x16x16_bf16 v[84:87], v[46:47], v[146:147], v[84:87]\n"
  "s_waitcnt vmcnt(28)\n"
  "v_mfma_f32_16x16x16_bf16 v[80:83], v[48:49], v[152:153], v[80:83]\n"
  "v_mfma_f32_16x16x16_bf16 v[84:87], v[48:49], v[154:155], v[84:87]\n"
  "s_waitcnt vmcnt(27)\n"
  "v_mfma_f32_16x16x16_bf16 v[80:83], v[50:51], v[156:157], v[80:83]\n"
  "v_mfma_f32_16x16x16_bf16 v[84:87], v[50:51], v[158:159], v[84:87]\n"
  "s_waitcnt vmcnt(26)\n"
  "v_mfma_f32_16x16x16_bf16 v[80:83], v[52:53], v[160:161], v[80:83]\n"
  "v_mfma_f32_16x16x16_bf16 v[84:87], v[52:53], v[162:163], v[84:87]\n"
  "s_waitcnt vmcnt(25)\n"
  "v_mfma_f32_16x16x16_bf16 v[80:83], v[54:55], v[164:165], v[80:83]\n"
  "v_mfma_f32_16x16x16_bf16 v[84:87], v[54:55], v[166:167], v[84:87]\n"
  "s_waitcnt vmcnt(23)\n"
  "v_mfma_f32_16x16x16_bf16 v[80:83], v[56:57], v[172:173], v[80:83]\n"
  "v_mfma_f32_16x16x16_bf16 v[84:87], v[56:57], v[174:175], v[84:87]\n"
  "s_waitcnt vmcnt(22)\n"
  "v_mfma_f32_16x16x16_bf16 v[80:83], v[58:59], v[176:177], v[80:83]\n"
  "v_mfma_f32_16x16x16_bf16 v[84:87], v[58:59], v[178:179], v[84:87]\n"
  "s_waitcnt vmcnt(21)\n"
  "v_mfma_f32_16x16x16_bf16 v[80:83], v[60:61], v[180:181], v[80:83]\n"
  "v_mfma_f32_16x16x16_bf16 v[84:87], v[60:61], v[182:183], v[84:87]\n"
  "s_waitcnt vmcnt(20)\n"
  "v_mfma_f32_16x16x16_bf16 v[80:83], v[62:63], v[184:185], v[80:83]\n"
  "v_mfma_f32_16x16x16_bf16 v[84:87], v[62:63], v[186:187], v[84:87]\n"
  "v_mfma_f32_16x16x32_bf16 v[88:91], v[64:67], v[128:131], 0\n"
  "s_waitcnt vmcnt(18)\n"
  "v_mfma_f32_16x16x32_bf16 v[92:95], v[64:67], v[96:99], 0\n"
  "v_mfma_f32_16x16x32_bf16 v[88:91], v[68:71], v[148:151], v[88:91]\n"
  "s_waitcnt vmcnt(17)\n"
  "v_mfma_f32_16x16x32_bf16 v[92:95], v[68:71], v[100:103], v[92:95]\n"
  "v_mfma_f32_16x16x32_bf16 v[88:91], v[72:75], v[168:171], v[88:91]\n"
  "s_waitcnt vmcnt(16)\n"
  "v_mfma_f32_16x16x32_bf16 v[92:95], v[72:75], v[104:107], v[92:95]\n"
  "v_mfma_f32_16x16x32_bf16 v[88:91], v[76:79], v[188:191], v[88:91]\n"
  "s_waitcnt vmcnt(15)\n"
  "v_mfma_f32_16x16x32_bf16 v[92:95], v[76:79], v[108:111], v[92:95]\n"
  "s_nop 5\n"
  "v_pk_add_f32 v[80:81], v[80:81], v[88:89]\n"
  "v_pk_add_f32 v[82:83], v[82:83], v[90:91]\n"
  "v_pk_add_f32 v[84:85], v[84:85], v[92:93]\n"
  "v_pk_add_f32 v[86:87], v[86:87], v[94:95]\n"
  "v_add_f32_dpp v24, v82, v80 row_ror:8 row_mask:0xf bank_mask:0x3\n"
  "v_add_f32_dpp v25, v83, v81 row_ror:8 row_mask:0xf bank_mask:0x3\n"
  "v_add_f32_dpp v24, v84, v86 row_ror:8 row_mask:0xf bank_mask:0xc\n"
  "v_add_f32_dpp v25, v85, v87 row_ror:8 row_mask:0xf bank_mask:0xc\n"
  "v_cmp_u_f32 vcc, v24, v24\n"
  "v_lshlrev_b32 v28, 16, v22\n"
  "v_and_b32 v29, 0xffff0000, v22\n"
  "s_andn2_b64 vcc, vcc, s[66:67]\n"
  "v_add_f32 v24, v24, v28\n"
  "v_add_f32 v25, v25, v29\n"
  "s_cbranch_vccnz C_X1REDO_%=\n"
  "v_cvt_pk_bf16_f32 v24, v24, v25\n"
  "buffer_store_dword v24, v12, s[36:39], s63 offen\n"
  "v_min_u32 v25, s78, v24\n"
  "buffer_store_dword v25, v14, s[20:23], s63 offen sc1\n"
  "s_waitcnt vmcnt(2)\n"
  "C_JOIN_%=:\n"
  "s_mov_b32 s96, s75\n"
  "s_add_u32 s56, s56, 2\n"
  "s_add_u32 s99, s99, 2\n"
  "s_cmp_lt_u32 s56, 64\n"
  "s_cbranch_scc1 C_LOOP_%=\n"
  "s_waitcnt vmcnt(0)\n"
  "s_mov_b32 s38, 2621440\n"
  "s_mov_b32 s22, 0\n"
  "s_add_u32 s46, s46, 64\n"
  "s_cmp_lt_u32 s46, 128\n"
  "s_cbranch_scc1 C_LAYER_%=\n"
  "s_branch C_EXIT_%=\n"
  "C_MBAD_%=:\n"
  "s_nop 7\n"
  "s_add_u32 s62, s62, 1\n"
  "s_cmp_gt_u32 s62, 300000\n"
  "s_cbranch_scc1 C_FAIL_%=\n"
  "s_and_b32 s60, s62, 255\n"
  "s_cmp_lg_u32 s60, 0\n"
  "s_cbranch_scc1 C_MP_%=\n"
  "v_mov_b32 v15, 0\n"
  "buffer_load_dword v15, v15, s[40:43], 0 offen sc1\n"
  "s_waitcnt vmcnt(0)\n"
  "v_readfirstlane_b32 s60, v15\n"
  "s_cmp_lg_u32 s60, 0\n"
  "s_cbranch_scc1 C_EXIT_%=\n"
  "s_branch C_MP_%=\n"
  "C_REDO_%=:\n"
  "s_nop 7\n"
  "s_waitcnt vmcnt(0) lgkmcnt(0)\n"
  "s_mov_b32 s62, 0\n"
  "C_REDOP_%=:\n"
  "s_nop 7\n"
  "buffer_load_dwordx4 v[112:115], v6, s[16:19], s68 offen offset:0 sc1\n"
  "buffer_load_dwordx4 v[116:119], v6, s[16:19], s68 offen offset:1280 sc1\n"
  "buffer_load_dwordx4 v[120:123], v6, s[16:19], s68 offen offset:2560 sc1\n"
  "buffer_load_dwordx4 v[124:127], v6, s[16:19], s68 offen offset:3840 sc1\n"
  "buffer_load_dwordx4 v[128:131], v7, s[16:19], s68 offen sc1\n"
  "buffer_load_dwordx4 v[132:135], v6, s[16:19], s69 offen offset:0 sc1\n"
  "buffer_load_dwordx4 v[136:139], v6, s[16:19], s69 offen offset:1280 sc1\n"
  "buffer_load_dwordx4 v[140:143], v6, s[16:19], s69 offen offset:2560 sc1\n"
  "buffer_load_dwordx4 v[144:147], v6, s[16:19], s69 offen offset:3840 sc1\n"
  "buffer_load_dwordx4 v[148:151], v7, s[16:19], s69 offen sc1\n"
  "buffer_load_dwordx4 v[152:155], v6, s[16:19], s70 offen offset:0 sc1\n"
  "buffer_load_dwordx4 v[156:159], v6, s[16:19], s70 offen offset:1280 sc1\n"
  "buffer_load_dwordx4 v[160:163], v6, s[16:19], s70 offen offset:2560 sc1\n"
  "buffer_load_dwordx4 v[164:167], v6, s[16:19], s70 offen offset:3840 sc1\n"
  "buffer_load_dwordx4 v[168:171], v7, s[16:19], s70 offen sc1\n"
  "buffer_load_dwordx4 v[172:175], v6, s[16:19], s71 offen offset:0 sc1\n"
  "buffer_load_dwordx4 v[176:179], v6, s[16:19], s71 offen offset:1280 sc1\n"
  "buffer_load_dwordx4 v[180:183], v6, s[16:19], s71 offen offset:2560 sc1\n"
  "buffer_load_dwordx4 v[184:187], v6, s[16:19], s71 offen offset:3840 sc1\n"
  "buffer_load_dwordx4 v[188:191], v7, s[16:19], s71 offen sc1\n"
  "s_waitcnt vmcnt(19)\n"
  "v_max3_u32 v15, v112, v113, v114\n"
  "s_waitcnt vmcnt(18)\n"
  "v_max3_u32 v15, v115, v116, v15\n"
  "v_max3_u32 v15, v117, v118, v15\n"
  "s_waitcnt vmcnt(17)\n"
  "v_max3_u32 v15, v119, v120, v15\n"
  "v_max3_u32 v15, v121, v122, v15\n"
  "s_waitcnt vmcnt(16)\n"
  "v_max3_u32 v15, v123, v124, v15\n"
  "v_max3_u32 v15, v125, v126, v15\n"
  "s_waitcnt vmcnt(15)\n"
  "v_max3_u32 v15, v127, v128, v15\n"
  "v_max3_u32 v15, v129, v130, v15\n"
  "s_waitcnt vmcnt(14)\n"
  "v_max3_u32 v15, v131, v132, v15\n"
  "v_max3_u32 v15, v133, v134, v15\n"
  "s_waitcnt vmcnt(13)\n"
  "v_max3_u32 v15, v135, v136, v15\n"
  "v_max3_u32 v15, v137, v138, v15\n"
  "s_waitcnt vmcnt(12)\n"
  "v_max3_u32 v15, v139, v140, v15\n"
  "v_max3_u32 v15, v141, v142, v15\n"
  "s_waitcnt vmcnt(11)\n"
  "v_max3_u32 v15, v143, v144, v15\n"
  "v_max3_u32 v15, v145, v146, v15\n"
  "s_waitcnt vmcnt(10)\n"
  "v_max3_u32 v15, v147, v148, v15\n"
  "v_max3_u32 v15, v149, v150, v15\n"
  "s_waitcnt vmcnt(9)\n"
  "v_max3_u32 v15, v151, v152, v15\n"
  "v_max3_u32 v15, v153, v154, v15\n"
  "s_waitcnt vmcnt(8)\n"
  "v_max3_u32 v15, v155, v156, v15\n"
  "v_max3_u32 v15, v157, v158, v15\n"
  "s_waitcnt vmcnt(7)\n"
  "v_max3_u32 v15, v159, v160, v15\n"
  "v_max3_u32 v15, v161, v162, v15\n"
  "s_waitcnt vmcnt(6)\n"
  "v_max3_u32 v15, v163, v164, v15\n"
  "v_max3_u32 v15, v165, v166, v15\n"
  "s_waitcnt vmcnt(5)\n"
  "v_max3_u32 v15, v167, v168, v15\n"
  "v_max3_u32 v15, v169, v170, v15\n"
  "s_waitcnt vmcnt(4)\n"
  "v_max3_u32 v15, v171, v172, v15\n"
  "v_max3_u32 v15, v173, v174, v15\n"
  "s_waitcnt vmcnt(3)\n"
  "v_max3_u32 v15, v175, v176, v15\n"
  "v_max3_u32 v15, v177, v178, v15\n"
  "s_waitcnt vmcnt(2)\n"
  "v_max3_u32 v15, v179, v180, v15\n"
  "v_max3_u32 v15, v181, v182, v15\n"
  "s_waitcnt vmcnt(1)\n"
  "v_max3_u32 v15, v183, v184, v15\n"
  "v_max3_u32 v15, v185, v186, v15\n"
  "s_waitcnt vmcnt(0)\n"
  "v_max3_u32 v15, v187, v188, v15\n"
  "v_max3_u32 v15, v189, v190, v15\n"
  "v_max_u32 v15, v191, v15\n"
  "s_waitcnt vmcnt(0) lgkmcnt(0)\n"
  "v_cmp_eq_u32 vcc, -1, v15\n"
  "s_nop 1\n"
  "s_mov_b64 s[66:67], -1\n"
  "s_cbranch_vccz C_P2_%=\n"
  "s_add_u32 s62, s62, 1\n"
  "s_cmp_gt_u32 s62, 300000\n"
  "s_cbranch_scc1 C_FAIL_%=\n"
  "s_and_b32 s60, s62, 255\n"
  "s_cmp_lg_u32 s60, 0\n"
  "s_cbranch_scc1 C_REDOP_%=\n"
  "v_mov_b32 v15, 0\n"
  "buffer_load_dword v15, v15, s[40:43], 0 offen sc1\n"
  "s_waitcnt vmcnt(0)\n"
  "v_readfirstlane_b32 s60, v15\n"
  "s_cmp_lg_u32 s60, 0\n"
  "s_cbranch_scc1 C_EXIT_%=\n"
  "s_branch C_REDOP_%=\n"
  "C_XREDO_%=:\n"
  "s_nop 7\n"
  "s_waitcnt vmcnt(0) lgkmcnt(0)\n"
  "s_mov_b32 s62, 0\n"
  "C_XREDOP_%=:\n"
  "s_nop 7\n"
  "buffer_load_dwordx4 v[112:115], v8, s[16:19], s68 offen offset:0 sc1\n"
  "buffer_load_dwordx4 v[116:119], v8, s[16:19], s68 offen offset:1280 sc1\n"
  "buffer_load_dwordx4 v[120:123], v8, s[16:19], s68 offen offset:2560 sc1\n"
  "buffer_load_dwordx4 v[124:127], v8, s[16:19], s68 offen offset:3840 sc1\n"
  "buffer_load_dwordx4 v[128:131], v9, s[16:19], s68 offen sc1\n"
  "buffer_load_dwordx4 v[132:135], v8, s[16:19], s69 offen offset:0 sc1\n"
  "buffer_load_dwordx4 v[136:139], v8, s[16:19], s69 offen offset:1280 sc1\n"
  "buffer_load_dwordx4 v[140:143], v8, s[16:19], s69 offen offset:2560 sc1\n"
  "buffer_load_dwordx4 v[144:147], v8, s[16:19], s69 offen offset:3840 sc1\n"
  "buffer_load_dwordx4 v[148:151], v9, s[16:19], s69 offen sc1\n"
  "buffer_load_dwordx4 v[152:155], v8, s[16:19], s70 offen offset:0 sc1\n"
  "buffer_load_dwordx4 v[156:159], v8, s[16:19], s70 offen offset:1280 sc1\n"
  "buffer_load_dwordx4 v[160:163], v8, s[16:19], s70 offen offset:2560 sc1\n"
  "buffer_load_dwordx4 v[164:167], v8, s[16:19], s70 offen offset:3840 sc1\n"
  "buffer_load_dwordx4 v[168:171], v9, s[16:19], s70 offen sc1\n"
  "buffer_load_dwordx4 v[172:175], v8, s[16:19], s71 offen offset:0 sc1\n"
  "buffer_load_dwordx4 v[176:179], v8, s[16:19], s71 offen offset:1280 sc1\n"
  "buffer_load_dwordx4 v[180:183], v8, s[16:19], s71 offen offset:2560 sc1\n"
  "buffer_load_dwordx4 v[184:187], v8, s[16:19], s71 offen offset:3840 sc1\n"
  "buffer_load_dwordx4 v[188:191], v9, s[16:19], s71 offen sc1\n"
  "s_waitcnt vmcnt(19)\n"
  "v_max3_u32 v15, v112, v113, v114\n"
  "s_waitcnt vmcnt(18)\n"
  "v_max3_u32 v15, v115, v116, v15\n"
  "v_max3_u32 v15, v117, v118, v15\n"
  "s_waitcnt vmcnt(17)\n"
  "v_max3_u32 v15, v119, v120, v15\n"
  "v_max3_u32 v15, v121, v122, v15\n"
  "s_waitcnt vmcnt(16)\n"
  "v_max3_u32 v15, v123, v124, v15\n"
  "v_max3_u32 v15, v125, v126, v15\n"
  "s_waitcnt vmcnt(15)\n"
  "v_max3_u32 v15, v127, v128, v15\n"
  "v_max3_u32 v15, v129, v130, v15\n"
  "s_waitcnt vmcnt(14)\n"
  "v_max3_u32 v15, v131, v132, v15\n"
  "v_max3_u32 v15, v133, v134, v15\n"
  "s_waitcnt vmcnt(13)\n"
  "v_max3_u32 v15, v135, v136, v15\n"
  "v_max3_u32 v15, v137, v138, v15\n"
  "s_waitcnt vmcnt(12)\n"
  "v_max3_u32 v15, v139, v140, v15\n"
  "v_max3_u32 v15, v141, v142, v15\n"
  "s_waitcnt vmcnt(11)\n"
  "v_max3_u32 v15, v143, v144, v15\n"
  "v_max3_u32 v15, v145, v146, v15\n"
  "s_waitcnt vmcnt(10)\n"
  "v_max3_u32 v15, v147, v148, v15\n"
  "v_max3_u32 v15, v149, v150, v15\n"
  "s_waitcnt vmcnt(9)\n"
  "v_max3_u32 v15, v151, v152, v15\n"
  "v_max3_u32 v15, v153, v154, v15\n"
  "s_waitcnt vmcnt(8)\n"
  "v_max3_u32 v15, v155, v156, v15\n"
  "v_max3_u32 v15, v157, v158, v15\n"
  "s_waitcnt vmcnt(7)\n"
  "v_max3_u32 v15, v159, v160, v15\n"
  "v_max3_u32 v15, v161, v162, v15\n"
  "s_waitcnt vmcnt(6)\n"
  "v_max3_u32 v15, v163, v164, v15\n"
  "v_max3_u32 v15, v165, v166, v15\n"
  "s_waitcnt vmcnt(5)\n"
  "v_max3_u32 v15, v167, v168, v15\n"
  "v_max3_u32 v15, v169, v170, v15\n"
  "s_waitcnt vmcnt(4)\n"
  "v_max3_u32 v15, v171, v172, v15\n"
  "v_max3_u32 v15, v173, v174, v15\n"
  "s_waitcnt vmcnt(3)\n"
  "v_max3_u32 v15, v175, v176, v15\n"
  "v_max3_u32 v15, v177, v178, v15\n"
  "s_waitcnt vmcnt(2)\n"
  "v_max3_u32 v15, v179, v180, v15\n"
  "v_max3_u32 v15, v181, v182, v15\n"
  "s_waitcnt vmcnt(1)\n"
  "v_max3_u32 v15, v183, v184, v15\n"
  "v_max3_u32 v15, v185, v186, v15\n"
  "s_waitcnt vmcnt(0)\n"
  "v_max3_u32 v15, v187, v188, v15\n"
  "v_max3_u32 v15, v189, v190, v15\n"
  "v_max_u32 v15, v191, v15\n"
  "s_waitcnt vmcnt(0) lgkmcnt(0)\n"
  "v_cmp_eq_u32 vcc, -1, v15\n"
  "s_nop 1\n"
  "s_mov_b64 s[66:67], -1\n"
  "s_cbranch_vccz C_X2_%=\n"
  "s_add_u32 s62, s62, 1\n"
  "s_cmp_gt_u32 s62, 300000\n"
  "s_cbranch_scc1 C_FAIL_%=\n"
  "s_and_b32 s60, s62, 255\n"
  "s_cmp_lg_u32 s60, 0\n"
  "s_cbranch_scc1 C_XREDOP_%=\n"
  "v_mov_b32 v15, 0\n"
  "buffer_load_dword v15, v15, s[40:43], 0 offen sc1\n"
  "s_waitcnt vmcnt(0)\n"
  "v_readfirstlane_b32 s60, v15\n"
  "s_cmp_lg_u32 s60, 0\n"
  "s_cbranch_scc1 C_EXIT_%=\n"
  "s_branch C_XREDOP_%=\n"
  "C_X1REDO_%=:\n"
  "s_nop 7\n"
  "s_waitcnt vmcnt(0) lgkmcnt(0)\n"
  "s_mov_b32 s62, 0\n"
  "C_X1REDOP_%=:\n"
  "s_nop 7\n"
  "buffer_load_dwordx4 v[112:115], v8, s[16:19], s68 offen offset:0 sc1\n"
  "buffer_load_dwordx4 v[116:119], v8, s[16:19], s68 offen offset:1280 sc1\n"
  "buffer_load_dwordx4 v[120:123], v8, s[16:19], s68 offen offset:2560 sc1\n"
  "buffer_load_dwordx4 v[124:127], v8, s[16:19], s68 offen offset:3840 sc1\n"
  "buffer_load_dwordx4 v[128:131], v9, s[16:19], s68 offen sc1\n"
  "buffer_load_dwordx4 v[132:135], v8, s[16:19], s69 offen offset:0 sc1\n"
  "buffer_load_dwordx4 v[136:139], v8, s[16:19], s69 offen offset:1280 sc1\n"
  "buffer_load_dwordx4 v[140:143], v8, s[16:19], s69 offen offset:2560 sc1\n"
  "buffer_load_dwordx4 v[144:147], v8, s[16:19], s69 offen offset:3840 sc1\n"
  "buffer_load_dwordx4 v[148:151], v9, s[16:19], s69 offen sc1\n"
  "buffer_load_dwordx4 v[152:155], v8, s[16:19], s70 offen offset:0 sc1\n"
  "buffer_load_dwordx4 v[156:159], v8, s[16:19], s70 offen offset:1280 sc1\n"
  "buffer_load_dwordx4 v[160:163], v8, s[16:19], s70 offen offset:2560 sc1\n"
  "buffer_load_dwordx4 v[164:167], v8, s[16:19], s70 offen offset:3840 sc1\n"
  "buffer_load_dwordx4 v[168:171], v9, s[16:19], s70 offen sc1\n"
  "buffer_load_dwordx4 v[172:175], v8, s[16:19], s71 offen offset:0 sc1\n"
  "buffer_load_dwordx4 v[176:179], v8, s[16:19], s71 offen offset:1280 sc1\n"
  "buffer_load_dwordx4 v[180:183], v8, s[16:19], s71 offen offset:2560 sc1\n"
  "buffer_load_dwordx4 v[184:187], v8, s[16:19], s71 offen offset:3840 sc1\n"
  "buffer_load_dwordx4 v[188:191], v9, s[16:19], s71 offen sc1\n"
  "buffer_load_dwordx4 v[96:99], v13, s[16:19], s68 offen offset:0 sc1\n"
  "buffer_load_dwordx4 v[100:103], v13, s[16:19], s68 offen offset:1024 sc1\n"
  "buffer_load_dwordx4 v[104:107], v13, s[16:19], s68 offen offset:2048 sc1\n"
  "buffer_load_dwordx4 v[108:111], v13, s[16:19], s68 offen offset:3072 sc1\n"
  "s_waitcnt vmcnt(3)\n"
  "v_max3_u32 v15, v96, v97, v98\n"
  "s_waitcnt vmcnt(2)\n"
  "v_max3_u32 v15, v99, v100, v15\n"
  "v_max3_u32 v15, v101, v102, v15\n"
  "s_waitcnt vmcnt(1)\n"
  "v_max3_u32 v15, v103, v104, v15\n"
  "v_max3_u32 v15, v105, v106, v15\n"
  "s_waitcnt vmcnt(0)\n"
  "v_max3_u32 v15, v107, v108, v15\n"
  "v_max3_u32 v15, v109, v110, v15\n"
  "v_max3_u32 v15, v111, v112, v15\n"
  "v_max3_u32 v15, v113, v114, v15\n"
  "v_max3_u32 v15, v115, v116, v15\n"
  "v_max3_u32 v15, v117, v118, v15\n"
  "v_max3_u32 v15, v119, v120, v15\n"
  "v_max3_u32 v15, v121, v122, v15\n"
  "v_max3_u32 v15, v123, v124, v15\n"
  "v_max3_u32 v15, v125, v126, v15\n"
  "v_max3_u32 v15, v127, v128, v15\n"
  "v_max3_u32 v15, v129, v130, v15\n"
  "v_max3_u32 v15, v131, v132, v15\n"
  "v_max3_u32 v15, v133, v134, v15\n"
  "v_max3_u32 v15, v135, v136, v15\n"
  "v_max3_u32 v15, v137, v138, v15\n"
  "v_max3_u32 v15, v139, v140, v15\n"
  "v_max3_u32 v15, v141, v142, v15\n"
  "v_max3_u32 v15, v143, v144, v15\n"
  "v_max3_u32 v15, v145, v146, v15\n"
  "v_max3_u32 v15, v147, v148, v15\n"
  "v_max3_u32 v15, v149, v150, v15\n"
  "v_max3_u32 v15, v151, v152, v15\n"
  "v_max3_u32 v15, v153, v154, v15\n"
  "v_max3_u32 v15, v155, v156, v15\n"
  "v_max3_u32 v15, v157, v158, v15\n"
  "v_max3_u32 v15, v159, v160, v15\n"
  "v_max3_u32 v15, v161, v162, v15\n"
  "v_max3_u32 v15, v163, v164, v15\n"
  "v_max3_u32 v15, v165, v166, v15\n"
  "v_max3_u32 v15, v167, v168, v15\n"
  "v_max3_u32 v15, v169, v170, v15\n"
  "v_max3_u32 v15, v171, v172, v15\n"
  "v_max3_u32 v15, v173, v174, v15\n"
  "v_max3_u32 v15, v175, v176, v15\n"
  "v_max3_u32 v15, v177, v178, v15\n"
  "v_max3_u32 v15, v179, v180, v15\n"
  "v_max3_u32 v15, v181, v182, v15\n"
  "v_max3_u32 v15, v183, v184, v15\n"
  "v_max3_u32 v15, v185, v186, v15\n"
  "v_max3_u32 v15, v187, v188, v15\n"
  "v_max3_u32 v15, v189, v190, v15\n"
  "v_max_u32 v15, v191, v15\n"
  "s_waitcnt vmcnt(0) lgkmcnt(0)\n"
  "v_cmp_eq_u32 vcc, -1, v15\n"
  "s_nop 1\n"
  "s_mov_b64 s[66:67], -1\n"
  "s_cbranch_vccz C_X1_%=\n"
  "s_add_u32 s62, s62, 1\n"
  "s_cmp_gt_u32 s62, 300000\n"
  "s_cbranch_scc1 C_FAIL_%=\n"
  "s_and_b32 s60, s62, 255\n"
  "s_cmp_lg_u32 s60, 0\n"
  "s_cbranch_scc1 C_X1REDOP_%=\n"
  "v_mov_b32 v15, 0\n"
  "buffer_load_dword v15, v15, s[40:43], 0 offen sc1\n"
  "s_waitcnt vmcnt(0)\n"
  "v_readfirstlane_b32 s60, v15\n"
  "s_cmp_lg_u32 s60, 0\n"
  "s_cbranch_scc1 C_EXIT_%=\n"
  "s_branch C_X1REDOP_%=\n"
  "C_FAIL_%=:\n"
  "s_nop 7\n"
  "v_mov_b32 v32, 0\n"
  "v_mov_b32 v33, 1\n"
  "buffer_store_dword v33, v32, s[40:43], 0 offen sc1\n"
  "C_EXIT_%=:\n"
  "s_nop 7\n"
  "s_waitcnt vmcnt(0)\n"
  "s_endpgm\n"
      :: "{s[16:31]}"(sa), "{s[32:47]}"(sb), "{s[48:55]}"(sc), "{s79}"(dbgoff), "{s2}"(sto), "{v[0:15]}"(va), "{v[16:23]}"(vb) : "v255", "a255", "s99", "vcc", "memory");
}

static int envi(const char*, int d) { return d; }
extern "C" void kernel_launch(void* const* d_in, const int* in_sizes, int n_in, void* d_out, int out_size, void* d_ws, size_t ws_size, hipStream_t stream) {
  Args a;
  a.x0 = (const u16*)d_in[0]; a.wqkv = (const u16*)d_in[1]; a.wo = (const u16*)d_in[2]; a.mem0 = (const u16*)d_in[4];
  a.Wq = (const u16*)d_in[5]; a.Wk = (const u16*)d_in[6]; a.Wv = (const u16*)d_in[7]; a.Wo = (const u16*)d_in[8];
  a.out = (u16*)d_out;
  a.ws = (u8*)(((uintptr_t)d_ws + (2u << 20) - 1) & ~(uintptr_t)((2u << 20) - 1));
  a.marg = envi("R_MP", 8) | (envi("R_MM", 20) << 8) | (envi("R_G", 4) << 16); a.pad = 0;
  a.dp = envi("R_DP", 135); a.dm = envi("R_DM", 72); a.skew = envi("R_SKEW", 20); a.delta = envi("R_DELTA", 20); a.lat = envi("R_LAT", 50); a.pubw = envi("R_PUBW", 50);
  (void)hipFuncSetAttribute((const void*)k_pre, hipFuncAttributeMaxDynamicSharedMemorySize, 81920);
  (void)hipFuncSetAttribute((const void*)k_main, hipFuncAttributeMaxDynamicSharedMemorySize, 163840);
  k_pre<<<512, 256, 81920, stream>>>(a);
  k_main<<<256, 256, 163840, stream>>>(a);
}
```

```cpp
#include <hip/hip_runtime.h>
#include <hip/hip_bf16.h>
#include <stdint.h>

typedef __attribute__((ext_vector_type(8))) __bf16 bf16x8;
typedef __attribute__((ext_vector_type(4))) __bf16 bf16x4;
typedef __attribute__((ext_vector_type(2))) __bf16 bf16x2;
typedef __attribute__((ext_vector_type(4))) short s16x4;
typedef __attribute__((ext_vector_type(4))) float f32x4;
typedef __attribute__((ext_vector_type(4))) unsigned u32x4;
typedef __attribute__((ext_vector_type(2))) unsigned u32x2;
typedef __attribute__((ext_vector_type(16))) unsigned u32x16;
typedef __attribute__((ext_vector_type(8))) unsigned u32x8;
typedef unsigned short u16;
typedef unsigned char u8;
#define GAS __attribute__((address_space(1)))
#define LAS __attribute__((address_space(3)))
#define CAT4(a, b) __builtin_shufflevector(a, b, 0,1,2,3,4,5,6,7)
#define MFMA32(a, b, c) __builtin_amdgcn_mfma_f32_16x16x32_bf16(a, b, c, 0, 0, 0)
#define Z4 ((f32x4){0.f, 0.f, 0.f, 0.f})
#define ARM4 ((u32x4){0xFFFFFFFFu, 0xFFFFFFFFu, 0xFFFFFFFFu, 0xFFFFFFFFu})
#define DEV __device__ __forceinline__
#define SB() __builtin_amdgcn_sched_barrier(0)

constexpr unsigned SZ_X = 2048 * 640 * 2, SEGB = 40960;
constexpr unsigned SZ_RING = 2u << 20, PXB = 1u << 20, FLB = 7u << 18, RING_FILL = FLB + 400 * 128;
constexpr size_t OFF_O = SZ_RING;
constexpr size_t OFF_XR = OFF_O + 2 * SZ_X;
constexpr size_t OFF_X2 = OFF_XR + 2 * SZ_X;
constexpr size_t OFF_KVP = OFF_X2 + SZ_X;
constexpr size_t OFF_XNF = OFF_KVP + (1u << 20);
constexpr size_t OFF_PQ = OFF_XNF + 2 * SZ_X + 4 * SEGB;
constexpr size_t OFF_XN0 = OFF_PQ + (4u << 20);
constexpr size_t OFF_WQF = OFF_XN0 + SZ_X;
constexpr size_t OFF_CF = OFF_WQF + SZ_X;
constexpr size_t OFF_CTL = OFF_CF + (size_t)32 * 640 * 640 * 2;

#define DBGOUT(o)
#define TL(i)
#define TP(i)
struct Args { const u16* x0; const u16* wqkv; const u16* wo; const u16* mem0; const u16* Wq; const u16* Wk; const u16* Wv; const u16* Wo; u16* out; u8* ws; int dp, dm, skew, delta, lat, pubw, marg, pad; };

DEV float rbf(float f) { return (float)(__bf16)f; }
DEV bf16x4 cvt4(f32x4 v) { bf16x4 r; r[0] = (__bf16)v[0]; r[1] = (__bf16)v[1]; r[2] = (__bf16)v[2]; r[3] = (__bf16)v[3]; return r; }
DEV int pi16(int p) { return 8 * ((p >> 1) & 1) + 2 * (p >> 2) + (p & 1); }
DEV int drow(int u, int gb, int j) { return 16 * (2 * u + (gb >> 1)) + pi16(8 * (gb & 1) + j); }
DEV bf16x8 tr8(const u8* p0, const u8* p1) {
  s16x4 a = __builtin_amdgcn_ds_read_tr16_b64_v4i16((LAS s16x4*)p0), b = __builtin_amdgcn_ds_read_tr16_b64_v4i16((LAS s16x4*)p1);
  return __builtin_bit_cast(bf16x8, CAT4(a, b));
}
DEV unsigned mx4(u32x4 v) { return max(max(v.x, v.y), max(v.z, v.w)); }
DEV u32x4 canon4(u32x4 v) { return __builtin_elementwise_min(v, (u32x4){0xFFFFFFFEu, 0xFFFFFFFEu, 0xFFFFFFFEu, 0xFFFFFFFEu}); }
DEV float lo16(unsigned v) { return __uint_as_float(v << 16); }
DEV float hi16(unsigned v) { return __uint_as_float(v & 0xFFFF0000u); }
DEV unsigned pk2(float a, float b) { bf16x2 p; p[0] = (__bf16)a; p[1] = (__bf16)b; return __builtin_bit_cast(unsigned, p); }
DEV unsigned pkc(float a, float b) { return min(pk2(a, b), 0xFFFFFFFEu); }
DEV __amdgpu_buffer_rsrc_t rsrc(const void* p, unsigned bytes) { return __builtin_amdgcn_make_buffer_rsrc((void*)p, 0, (int)bytes, 0x00020000); }
DEV bool spin_fail(unsigned& spins, unsigned* ctl) {
  if (++spins > 300000u) { __hip_atomic_store((GAS unsigned*)ctl, 1u, __ATOMIC_RELAXED, __HIP_MEMORY_SCOPE_AGENT); return true; }
  return (spins & 63u) == 0u && __builtin_amdgcn_readfirstlane(__hip_atomic_load((GAS unsigned*)ctl, __ATOMIC_RELAXED, __HIP_MEMORY_SCOPE_AGENT));
}

#define OPQ(x) asm volatile("" : "+v"(x))
struct LinS { float sN[3]; f32x4 sT[2]; };
DEV void lin_weights(const u8* wqf, u8* smem, int tid) {
  u32x4 v[20];
#pragma unroll
  for (int i = 0; i < 20; ++i) v[i] = *(const u32x4*)(wqf + i * 4096 + tid * 16);
#pragma unroll
  for (int i = 0; i < 20; ++i) *(u32x4*)(smem + i * 4096 + tid * 16) = v[i];
}
#define WF(i, t) (*(const bf16x8*)(wl + ((i) * 4 + (t)) * 1024))
DEV void lin_sums(const u8* wl, LinS& w) {
  bf16x8 one;
#pragma unroll
  for (int j = 0; j < 8; ++j) one[j] = (__bf16)1.f;
  f32x4 n0 = Z4, n1 = Z4, n2 = Z4, t0 = Z4, t1 = Z4;
#pragma unroll 4
  for (int i = 0; i < 20; ++i) { n0 = MFMA32(one, WF(i, 0), n0); n1 = MFMA32(one, WF(i, 1), n1); n2 = MFMA32(one, WF(i, 2), n2); t0 = MFMA32(WF(i, 3), one, t0); t1 = MFMA32(WF(i, 2), one, t1); }
  w.sN[0] = n0[0]; w.sN[1] = n1[0]; w.sN[2] = n2[0]; w.sT[0] = t0; w.sT[1] = t1;
}
template <int AUX>
DEV void lin_load(__amdgpu_buffer_rsrc_t rx, int tile, u32x4 (&x)[20], int lane) {
#pragma unroll
  for (int i = 0; i < 20; ++i) { x[i] = __builtin_amdgcn_raw_buffer_load_b128(rx, lane * 16 + (i & 3) * 1024, tile * 20480 + (i >> 2) * 4096, AUX); SB(); }
}
DEV float phi_r(float t) {
  float e = __builtin_amdgcn_exp2f(fminf(t * 1.4426950408889634f, 0.f)), p = fmaxf(t + 1e-6f, 1e-6f);
  OPQ(p);
  return p + e;
}
template <bool RAW>
DEV bool lin_tile(u32x4 (&x)[20], __amdgpu_buffer_rsrc_t rx, int tile, const u8* wl, const LinS& w, f32x4 (&kv)[4], __amdgpu_buffer_rsrc_t rq, int h, int lane, bool check, unsigned pfo, unsigned (&dbg)[3]) {
  const int g = lane >> 4, n16 = lane & 15;
  f32x4 n0 = Z4, n1 = Z4, n2 = Z4, t0 = Z4, t1 = Z4, gr = Z4;
  asm volatile("" ::: "memory");
  bf16x8 wf[3][4];
#pragma unroll
  for (int t = 0; t < 8; ++t) wf[t >> 2][t & 3] = WF(t >> 2, t & 3);
  SB();
#pragma unroll
  for (int i = 0; i < 20; ++i) {
    const bf16x8 xf = __builtin_bit_cast(bf16x8, x[i]);
    const int c = i % 3, n = (i + 2) % 3, j = i < 18 ? i + 2 : i;
    n0 = MFMA32(xf, wf[c][0], n0); if (i < 18) wf[n][0] = WF(j, 0); SB();
    n1 = MFMA32(xf, wf[c][1], n1); if (i < 18) wf[n][1] = WF(j, 1); SB();
    n2 = MFMA32(xf, wf[c][2], n2); if (i < 18) wf[n][2] = WF(j, 2); SB();
    t1 = MFMA32(wf[c][2], xf, t1); if (i < 18) wf[n][3] = WF(j, 3); SB();
    if (RAW) gr = MFMA32(xf, xf, gr);
    t0 = MFMA32(wf[c][3], xf, t0); SB();
    x[i] = __builtin_amdgcn_raw_buffer_load_b128(rx, pfo + (i & 3) * 1024, (tile + 4) * 20480 + (i >> 2) * 4096, RAW ? 16 : 0);
    SB();
  }
  if (RAW) {
    const float dg = (n16 & 2) ? ((n16 & 1) ? gr[3] : gr[2]) : ((n16 & 1) ? gr[1] : gr[0]);
    float s1 = __shfl(t1[0], 48 + n16), s2 = __shfl(dg, (n16 >> 2) * 16 + n16);
    if (check && __any(s1 != s1)) return false;
    OPQ(s1);
    float mu = s1 * (1.f / 640.f);
    OPQ(mu);
    float rs = __builtin_amdgcn_rsqf(fmaxf(s2 * (1.f / 640.f) - mu * mu, 0.f) + 1e-5f), mr = mu * rs;
    OPQ(rs); OPQ(mr);
    float mi[4], ri[4];
#pragma unroll
    for (int i = 0; i < 4; ++i) { mi[i] = __shfl(mr, 4 * g + i); ri[i] = __shfl(rs, 4 * g + i); }
#pragma unroll
    for (int i = 0; i < 4; ++i) {
      float a0 = mr * w.sT[0][i]; OPQ(a0); float a1 = mr * w.sT[1][i]; OPQ(a1);
      float b0 = mi[i] * w.sN[0]; OPQ(b0); float b1 = mi[i] * w.sN[1]; OPQ(b1); float b2 = mi[i] * w.sN[2]; OPQ(b2);
      a0 = __builtin_fmaf(t0[i], rs, -a0); a1 = __builtin_fmaf(t1[i], rs, -a1);
      b0 = __builtin_fmaf(n0[i], ri[i], -b0); b1 = __builtin_fmaf(n1[i], ri[i], -b1); b2 = __builtin_fmaf(n2[i], ri[i], -b2);
      OPQ(a0); OPQ(a1); OPQ(b0); OPQ(b1); OPQ(b2);
      t0[i] = a0; t1[i] = a1; n0[i] = b0; n1[i] = b1; n2[i] = b2;
    }
  }
  const float cst = n16 == 12 ? 1.f : 0.f;
#pragma unroll
  for (int i = 0; i < 4; ++i) {
    n0[i] = phi_r(n0[i]);
    const float pm = phi_r(n2[i]), pq = phi_r(t1[i]);
    n2[i] = n16 < 4 ? pm : (n16 < 8 ? n2[i] : cst);
    t0[i] = phi_r(t0[i]);
    t1[i] = g == 2 ? pq : 0.f;
  }
  const bf16x4 z = cvt4(Z4);
  const bf16x8 K = CAT4(cvt4(n0), z), V = CAT4(cvt4(n1), z), M = CAT4(cvt4(n2), z);
  kv[0] = MFMA32(K, V, kv[0]); kv[1] = MFMA32(K, M, kv[1]); kv[2] = MFMA32(M, V, kv[2]); kv[3] = MFMA32(M, M, kv[3]);
  __builtin_amdgcn_raw_buffer_store_b128(__builtin_bit_cast(u32x4, CAT4(cvt4(t0), cvt4(t1))), rq, lane * 16, (tile * 32 + h) * 1024, 16);
  return true;
}
DEV void lin_first(const Args& a, u8* smem) {
  const int tid = threadIdx.x, wave = __builtin_amdgcn_readfirstlane(tid >> 6), lane = tid & 63;
  const int j2 = (blockIdx.x >> 3) - 8, h = (blockIdx.x & 7) * 4 + (j2 & 3), rg = j2 >> 2;
  const int t0 = rg < 2 ? rg * 24 : 8 + rg * 20, t1 = t0 + (rg < 2 ? 24 : 20);
  const __amdgpu_buffer_rsrc_t rx = rsrc(a.ws + OFF_XN0, SZ_X), rq = rsrc(a.ws + OFF_PQ, 4u << 20);
  u32x4 x[20];
  lin_load<0>(rx, t0 + wave, x, lane);
  lin_weights(a.ws + OFF_WQF + (size_t)h * 81920, smem, tid);
  __syncthreads();
  f32x4 kv[4] = {Z4, Z4, Z4, Z4};
  LinS w;
  unsigned dbg[3] = {0, 0, 0};
#pragma unroll 1
  for (int tile = t0 + wave; tile < t1; tile += 4) lin_tile<false>(x, rx, tile, smem + lane * 16, w, kv, rq, h, lane, false, tile + 4 < t1 ? lane * 16 : 0x80000000u, dbg);
  DBGOUT(8);
  u8* KS = smem + 81920;
#pragma unroll
  for (int t = 0; t < 4; ++t) *(f32x4*)(KS + wave * 4096 + t * 1024 + lane * 16) = kv[t];
  asm volatile("s_waitcnt vmcnt(0)" ::: "memory");
  __syncthreads();
  const f32x4 s = (*(const f32x4*)(KS + tid * 16) + *(const f32x4*)(KS + 4096 + tid * 16)) + (*(const f32x4*)(KS + 8192 + tid * 16) + *(const f32x4*)(KS + 12288 + tid * 16));
  __builtin_amdgcn_raw_buffer_store_b128(canon4(__builtin_bit_cast(u32x4, s)), rsrc(a.ws + OFF_KVP, 1u << 20), tid * 16, (h * 6 + rg) * 4096, 16);
  __syncthreads();
}

static_assert(RING_FILL <= 461 * 4096 && 461 * 4096 <= SZ_RING - 16384, "ring fill");
__global__ void __launch_bounds__(256, 2) k_pre(Args a) {
  extern __shared__ __attribute__((aligned(16))) u8 smem[];
  const int tid = threadIdx.x, wave = __builtin_amdgcn_readfirstlane(tid >> 6), lane = tid & 63, g = lane >> 4, n16 = lane & 15;
  const int b = blockIdx.x & 255;
  TP(0);
  if (blockIdx.x >= 256) {
    if (b == 0 && tid == 0) *(unsigned*)(a.ws + OFF_CTL) = 0u;
    if (b < 128) {
      const int row = b * 16 + wave * 4 + g;
      u32x4 x[5];
#pragma unroll
      for (int i = 0; i < 5; ++i) x[i] = *(const u32x4*)(a.x0 + (size_t)row * 640 + (i * 16 + n16) * 8);
      float s = 0.f;
#pragma unroll
      for (int i = 0; i < 5; ++i)
#pragma unroll
        for (int d = 0; d < 4; ++d) { const unsigned xd = x[i][d]; s += lo16(xd) + hi16(xd); }
      s += __shfl_xor(s, 1); s += __shfl_xor(s, 2); s += __shfl_xor(s, 4); s += __shfl_xor(s, 8);
      float mu = s * (1.f / 640.f), q = 0.f;
      OPQ(mu);
#pragma unroll
      for (int i = 0; i < 5; ++i)
#pragma unroll
        for (int d = 0; d < 4; ++d) { const unsigned xd = x[i][d]; const float d0 = lo16(xd) - mu, d1 = hi16(xd) - mu; q += d0 * d0 + d1 * d1; }
      q += __shfl_xor(q, 1); q += __shfl_xor(q, 2); q += __shfl_xor(q, 4); q += __shfl_xor(q, 8);
      float rs = __builtin_amdgcn_rsqf(q * (1.f / 640.f) + 1e-5f);
      OPQ(rs);
#pragma unroll
      for (int i = 0; i < 5; ++i) {
        u32x4 y;
#pragma unroll
        for (int d = 0; d < 4; ++d) { const unsigned xd = x[i][d]; y[d] = pk2((lo16(xd) - mu) * rs, (hi16(xd) - mu) * rs); }
        *(u32x4*)(smem + (i * 16 + n16) * 272 + (wave * 4 + g) * 16) = y;
      }
      __syncthreads();
#pragma unroll
      for (int i = 0; i < 5; ++i) *(u32x4*)(a.ws + OFF_XN0 + (size_t)b * 20480 + i * 4096 + tid * 16) = *(const u32x4*)(smem + (i * 16 + (tid >> 4)) * 272 + (tid & 15) * 16);
    } else {
      const int h = (b - 128) >> 2, kq = b & 3, pc = tid & 15, tile = pc >> 2, p = pc & 3;
      const int col = 20 * h + (tile == 0 ? 640 + 4 * p : tile == 1 ? 1280 + 4 * p : tile == 3 ? 4 * p : (p == 0 ? 656 : p == 1 ? 1296 : 16));
      u32x2 v[10];
#pragma unroll
      for (int i = 0; i < 10; ++i) v[i] = *(const u32x2*)(a.wqkv + (size_t)(kq * 160 + (tid >> 4) + 16 * i) * 1920 + col);
#pragma unroll
      for (int i = 0; i < 10; ++i) *(u32x2*)(smem + ((tid >> 4) + 16 * i) * 144 + pc * 8) = pc == 11 ? (u32x2){0x3F80u, 0u} : v[i];
      __syncthreads();
      const u8* p0 = smem + (8 * g + (n16 >> 2)) * 144 + wave * 32 + (lane & 3) * 8;
#pragma unroll
      for (int i = 0; i < 5; ++i) *(bf16x8*)(a.ws + OFF_WQF + (size_t)h * 81920 + (kq * 5 + i) * 4096 + tid * 16) = tr8(p0 + i * 32 * 144, p0 + (i * 32 + 4) * 144);
    }
    TP(4);
#pragma unroll
    for (int i = 0; i < 4; ++i) {
      const unsigned n = i * 256 + b;
      const size_t p = n < 461 ? (size_t)n * 4096 : n < 541 ? OFF_O + (n - 461) * 4096 : n < 701 ? OFF_XR + (n - 541) * 4096 : OFF_KVP + (n - 701) * 4096;
      if (n < 893) *(u32x4*)(a.ws + p + tid * 16) = ARM4;
    }
    TP(5);
    return;
  }
  const int pr = (b & 7) * 2 + ((b >> 3) & 1), ch = (b >> 4) & 7, hf2 = b >> 7, e = wave >> 1, h = pr * 2 + e, u = wave & 1;
  u32x4 vo[15];
  u32x4 va[12];
  const __amdgpu_buffer_rsrc_t rv = rsrc(a.Wv + (size_t)320 * hf2 * 6144 + pr * 384, 320 * 12288);
  unsigned ga[3], la[3];
#pragma unroll
  for (int j = 0; j < 3; ++j) { const unsigned n = tid + 256 * j; ga[j] = (n / 48) * 12288 + (n % 48) * 16; la[j] = (n / 48) * 784 + (n % 48) * 16; }
#pragma unroll
  for (int i = 0; i < 15; ++i) { const unsigned n = tid + 256 * (i % 5); vo[i] = __builtin_amdgcn_raw_buffer_load_b128(rsrc(a.Wo + (size_t)pr * 384 * 640 + ch * 80, 384 * 1280), (n / 10) * 1280 + (n % 10) * 16, (i / 5) * 128 * 1280, 0); }
#pragma unroll
  for (int j = 0; j < 12; ++j) va[j] = __builtin_amdgcn_raw_buffer_load_b128(rv, ga[j % 3], (j / 3) * 16 * 12288, 0);
  TP(1);
  TP(2);
#pragma unroll
  for (int i = 0; i < 15; ++i) { const unsigned n = tid + 256 * (i % 5); *(u32x4*)(smem + (n / 10) * 176 + (n % 10) * 16 + (i / 5) * 128 * 176) = vo[i]; }
  __syncthreads();
  const u8* bp = smem + (e * 192 + 8 * g + (n16 >> 2)) * 176 + (lane & 3) * 8;
  const __amdgpu_buffer_rsrc_t rcf = rsrc(a.ws + OFF_CF, 32 * 640 * 640 * 2);
  const unsigned cfo = (((((ch * 10 + (n16 >> 3)) * 4 + (h >> 3)) * 10 + hf2 * 5) * 4 + ((h & 7) >> 1)) * 128 + u * 64 + g * 16 + ((n16 & 7) >> 1) * 4 + e * 2 + (n16 & 1)) * 16;
  bf16x8 bfr[5][6];
#pragma unroll
  for (int ct = 0; ct < 5; ++ct)
#pragma unroll
    for (int ks = 0; ks < 6; ++ks) bfr[ct][ks] = tr8(bp + ks * 32 * 176 + ct * 32, bp + (ks * 32 + 4) * 176 + ct * 32);
  TP(3);
  const u8* ap0 = smem + drow(u, n16 >> 2, n16 & 3) * 784 + e * 384 + g * 16;
  const u8* ap1 = smem + drow(u, n16 >> 2, 4 + (n16 & 3)) * 784 + e * 384 + g * 16;
#pragma unroll
  for (int i = 0; i < 5; ++i) {
    __syncthreads();
#pragma unroll
    for (int j = 0; j < 12; ++j) *(u32x4*)(smem + la[j % 3] + (j / 3) * 16 * 784) = va[j];
    if (i < 4) {
#pragma unroll
      for (int j = 0; j < 12; ++j) va[j] = __builtin_amdgcn_raw_buffer_load_b128(rv, ga[j % 3], (64 * (i + 1) + (j / 3) * 16) * 12288, 0);
    }
    __syncthreads();
    bf16x8 af[6]; bf16x4 lo[5];
#pragma unroll
    for (int ks = 0; ks < 6; ++ks) af[ks] = *(const bf16x8*)(ap0 + ks * 64);
    {
      f32x4 c[5] = {Z4, Z4, Z4, Z4, Z4};
#pragma unroll
      for (int ks = 0; ks < 6; ++ks) {
#pragma unroll
        for (int ct = 0; ct < 5; ++ct) c[ct] = MFMA32(af[ks], bfr[ct][ks], c[ct]);
        af[ks] = *(const bf16x8*)(ap1 + ks * 64);
        SB();
      }
#pragma unroll
      for (int ct = 0; ct < 5; ++ct) lo[ct] = cvt4(c[ct]);
    }
    f32x4 c[5] = {Z4, Z4, Z4, Z4, Z4};
#pragma unroll
    for (int ks = 0; ks < 6; ++ks)
#pragma unroll
      for (int ct = 0; ct < 5; ++ct) c[ct] = MFMA32(af[ks], bfr[ct][ks], c[ct]);
#pragma unroll
    for (int ct = 0; ct < 5; ++ct) __builtin_amdgcn_raw_buffer_store_b128(__builtin_bit_cast(u32x4, CAT4(lo[ct], cvt4(c[ct]))), rcf, cfo, (ct * 2 * 20480 + i * 512) * 16, 0);
    if (i == 0) TP(4);
  }
  TP(5);
}

template <int N, int STRIDE>
DEV bool poll_frags(__amdgpu_buffer_rsrc_t r, unsigned vo, unsigned so, u32x4 (&v)[N], unsigned* ctl) {
  for (unsigned spins = 0;;) {
    unsigned m = 0;
#pragma unroll
    for (int i = 0; i < N; ++i) v[i] = __builtin_amdgcn_raw_buffer_load_b128(r, vo + ((i * STRIDE) & 4095), so + ((i * STRIDE) & ~4095), 16);
#pragma unroll
    for (int i = 0; i < N; ++i) m = max(m, mx4(v[i]));
    if (__all(m != 0xFFFFFFFFu)) return true;
    __builtin_amdgcn_s_sleep(N == 1 ? 4 : 1);
    if (spin_fail(spins, ctl)) return false;
  }
}
template <int layer>
DEV bool lin_out(const Args& a, u8* smem, const int id, const f32x4 ks, const bf16x8 pq0, const bf16x8 pq1) {
  int tid = threadIdx.x;
  OPQ(tid);
  const int wave = __builtin_amdgcn_readfirstlane(tid >> 6), lane = tid & 63;
  unsigned* ctl = (unsigned*)(a.ws + OFF_CTL);
  u8* KS = smem + 81920;
  const __amdgpu_buffer_rsrc_t rq = rsrc(a.ws + OFF_PQ, 4u << 20), rX2 = rsrc(a.ws + OFF_X2, SZ_X);
  auto ldq = [&](int rd) { return __builtin_bit_cast(bf16x8, __builtin_amdgcn_raw_buffer_load_b128(rq, lane * 16, ((rd * 4 + wave) * 32 + id) * 1024, 16)); };
  const int g = lane >> 4, n16 = lane & 15;
  const __amdgpu_buffer_rsrc_t rO = rsrc(a.ws + OFF_O, 2 * SZ_X), rR = rsrc(a.ws + OFF_XR, 2 * SZ_X), rN = rsrc(a.ws + OFF_XNF, 2 * SZ_X), rx0 = rsrc(a.x0, SZ_X);
  const unsigned f0 = 20 * id + 4 * g, f4 = 20 * id + 16;
  const unsigned oo0 = (f0 >> 3) * 256 + n16 * 16 + ((f0 >> 2) & 1) * 8, oo4 = g == 0 ? (f4 >> 3) * 256 + n16 * 16 + ((f4 >> 2) & 1) * 8 : 0x80000000u;
  const u8* ob = smem + 102400 + (8 * g + (n16 >> 2)) * 80 + (lane & 3) * 16;
    *(f32x4*)(KS + 16384 + tid * 16) = ks;
    __syncthreads();
    bf16x8 E0, E1, EZ;
    {
      const u8* S = KS + 16384;
      const int c1 = 4 + (n16 & 3);
      f32x4 a0 = *(const f32x4*)(S + lane * 16), a1 = *(const f32x4*)(S + 2048 + n16 * 16);
      f32x4 b0 = *(const f32x4*)(S + 1024 + (g * 16 + c1) * 16), b1 = *(const f32x4*)(S + 3072 + c1 * 16);
      f32x4 z0 = *(const f32x4*)(S + 1024 + (g * 16 + 12) * 16), z1 = *(const f32x4*)(S + 3072 + 12 * 16);
      if (g != 2) { a1 = Z4; b1 = Z4; z1 = Z4; }
      E0 = CAT4(cvt4(a0), cvt4(a1)); E1 = CAT4(cvt4(b0), cvt4(b1)); EZ = CAT4(cvt4(z0), cvt4(z1));
    }
    TL(1 + layer * 8);
    auto attn_out = [&](bf16x8 pq, int rd) {
      const f32x4 n0 = MFMA32(E0, pq, Z4), n1 = MFMA32(E1, pq, Z4), dn = MFMA32(EZ, pq, Z4);
      const float r = __builtin_amdgcn_rcpf(dn[0] + 1e-6f);
      const unsigned so = (layer * 128 + rd * 4 + wave) * 20480;
      __builtin_amdgcn_raw_buffer_store_b64((u32x2){pkc(n0[0] * r, n0[1] * r), pkc(n0[2] * r, n0[3] * r)}, rO, oo0, so, 16);
      __builtin_amdgcn_raw_buffer_store_b64((u32x2){pkc(n1[0] * r, n1[1] * r), pkc(n1[2] * r, n1[3] * r)}, rO, oo4, so, 16);
      if (layer * 32 + rd < 60) {
        __builtin_amdgcn_raw_buffer_store_b64((u32x2){0xFFFFFFFFu, 0xFFFFFFFFu}, rO, oo0, so + 16 * 20480, 16);
        __builtin_amdgcn_raw_buffer_store_b64((u32x2){0xFFFFFFFFu, 0xFFFFFFFFu}, rO, oo4, so + 16 * 20480, 16);
      }
    };
    attn_out(pq0, 0); attn_out(pq1, 1);
    SB();
    bf16x8 wo[20][2];
#pragma unroll
    for (int k = 0; k < 20; ++k) { wo[k][0] = tr8(ob + k * 2560, ob + k * 2560 + 320); wo[k][1] = tr8(ob + k * 2560 + 8, ob + k * 2560 + 328); }
#pragma unroll 1
    for (int rd = 0; rd < 32; ++rd) {
      const bf16x8 pqn = ldq(rd < 30 ? rd + 2 : 31);
      if (id == 24 && rd == 0 && layer == 0) {
        unsigned best[2] = {1000000u, 1000000u};
        asm volatile("s_waitcnt vmcnt(0)" ::: "memory");
        for (int rep = 0; rep < 4; ++rep)
          for (int cc = 0; cc < 2; ++cc) {
            unsigned* p = (unsigned*)(a.ws + SZ_RING - 16384 + cc * 8192 + wave * 2048 + rep * 256);
            unsigned v, z = 0;
            const unsigned c0 = (unsigned)__builtin_amdgcn_s_memtime();
            if (lane == 0) asm volatile("global_atomic_or %0, %1, %2, off sc0\n s_waitcnt vmcnt(0)" : "=&v"(v) : "v"(p), "v"(z) : "memory");
            const unsigned c1 = (unsigned)__builtin_amdgcn_s_memtime();
            best[cc] = min(best[cc], c1 - c0);
          }
        if (lane == 0) *(u32x2*)(KS + wave * 8) = (u32x2){best[0], best[1]};
        __syncthreads();
        if (tid == 0) {
          const u32x4 p0 = *(const u32x4*)KS, p1 = *(const u32x4*)(KS + 16);
          const unsigned b0 = min(min(p0.x, p0.z), min(p1.x, p1.z)), b1 = min(min(p0.y, p0.w), min(p1.y, p1.w));
          __hip_atomic_store((GAS unsigned*)(a.ws + FLB + 130 * 128), b1 < b0 ? 1u : 0u, __ATOMIC_RELAXED, __HIP_MEMORY_SCOPE_AGENT);
        }
      }
      if (rd == 0) TL(2 + layer * 8);
      if (rd == 1) TL(3 + layer * 8);
      if (rd == 8) TL(4 + layer * 8);
      if (id >= 20 && rd < 30) attn_out(pqn, rd + 2);
      if (id < 20) {
        const unsigned tile = rd * 4 + wave, so = (layer * 128 + tile) * 20480;
        const u32x4 res = layer == 0 ? __builtin_amdgcn_raw_buffer_load_b128(rx0, n16 * 1280 + id * 64 + g * 16, tile * 20480, 0)
                                     : __builtin_amdgcn_raw_buffer_load_b128(rX2, id * 1024 + lane * 16, tile * 20480, 16);
        u32x4 o[20];
        if (!poll_frags<20, 1024>(rO, lane * 16, so, o, ctl)) return false;
        f32x4 c0 = Z4, c1 = Z4;
#pragma unroll
        for (int k = 0; k < 20; ++k) {
          c0 = MFMA32(wo[k][0], __builtin_bit_cast(bf16x8, o[k]), c0); c1 = MFMA32(wo[k][1], __builtin_bit_cast(bf16x8, o[k]), c1);
        }
        const u32x4 y = {pk2(lo16(res.x) + c0[0], hi16(res.x) + c0[1]), pk2(lo16(res.y) + c0[2], hi16(res.y) + c0[3]), pk2(lo16(res.z) + c1[0], hi16(res.z) + c1[1]), pk2(lo16(res.w) + c1[2], hi16(res.w) + c1[3])};
        __builtin_amdgcn_raw_buffer_store_b128(canon4(y), rR, id * 1024 + lane * 16, so, 16);
        if (layer * 32 + rd < 56) __builtin_amdgcn_raw_buffer_store_b128(ARM4, rR, id * 1024 + lane * 16, so + 32 * 20480, 16);
      } else if ((rd % 6) == ((id - 20) >> 1)) {
        const unsigned sg = layer * 64 + rd * 2 + (id & 1), tok = (wave & 1) * 8 + (lane & 7), part = lane >> 3;
        if (layer == 0) {
#pragma unroll
          for (int i = 0; i < 10; ++i) __builtin_amdgcn_raw_buffer_store_b128(ARM4, rX2, tid * 16, sg * SEGB + i * 4096, 16);
        }
        u32x4 x[10];
        if (!poll_frags<10, 256>(rR, part * 2560 + tok * 16, sg * SEGB + (wave >> 1) * 20480, x, ctl)) return false;
        float s = 0.f;
#pragma unroll
        for (int i = 0; i < 10; ++i)
#pragma unroll
          for (int d = 0; d < 4; ++d) { const unsigned xd = x[i][d]; s += lo16(xd) + hi16(xd); }
        s += __shfl_xor(s, 8); s += __shfl_xor(s, 16); s += __shfl_xor(s, 32);
        float mu = s * (1.f / 640.f), q = 0.f;
        OPQ(mu);
#pragma unroll
        for (int i = 0; i < 10; ++i)
#pragma unroll
          for (int d = 0; d < 4; ++d) { const unsigned xd = x[i][d]; const float d0 = lo16(xd) - mu, d1 = hi16(xd) - mu; q += d0 * d0 + d1 * d1; }
        q += __shfl_xor(q, 8); q += __shfl_xor(q, 16); q += __shfl_xor(q, 32);
        float rs = __builtin_amdgcn_rsqf(q * (1.f / 640.f) + 1e-5f);
        OPQ(rs);
#pragma unroll
        for (int i = 0; i < 5; ++i) {
          unsigned y[8];
#pragma unroll
          for (int d = 0; d < 8; ++d) { const unsigned xd = x[2 * i + (d >> 2)][d & 3]; y[d] = pk2((lo16(xd) - mu) * rs, (hi16(xd) - mu) * rs); }
          const unsigned gr = part * 5 + i, t = gr >> 2, u = (gr >> 1) & 1;
          const unsigned vo = ((2 * u + (wave >> 1)) * 10 + t) * 1024 + (gr & 1) * 512 + tok * 16;
          __builtin_amdgcn_raw_buffer_store_b128((u32x4){y[0], y[4], y[1], y[5]}, rN, vo, sg * SEGB, 16);
          __builtin_amdgcn_raw_buffer_store_b128((u32x4){y[2], y[6], y[3], y[7]}, rN, vo + 256, sg * SEGB, 16);
        }
        asm volatile("s_waitcnt vmcnt(0)" ::: "memory");
        __syncthreads();
        if (tid == 0) __hip_atomic_store((GAS unsigned*)(a.ws + FLB + sg * 128), 1u, __ATOMIC_RELAXED, __HIP_MEMORY_SCOPE_AGENT);
      }
      if (id < 20 && rd < 30) attn_out(pqn, rd + 2);
    }
    TL(5 + layer * 8);
  return true;
}
DEV bool lin_second(const Args& a, u8* smem, const int id, f32x4& ks, bf16x8& pq0, bf16x8& pq1) {
  int tid = threadIdx.x;
  OPQ(tid);
  const int wave = __builtin_amdgcn_readfirstlane(tid >> 6), lane = tid & 63;
  unsigned* ctl = (unsigned*)(a.ws + OFF_CTL);
  u8* KS = smem + 81920;
  const __amdgpu_buffer_rsrc_t rq = rsrc(a.ws + OFF_PQ, 4u << 20), rX2 = rsrc(a.ws + OFF_X2, SZ_X);
  auto ldq = [&](int rd) { return __builtin_bit_cast(bf16x8, __builtin_amdgcn_raw_buffer_load_b128(rq, lane * 16, ((rd * 4 + wave) * 32 + id) * 1024, 16)); };
    lin_weights(a.ws + OFF_WQF + (size_t)id * 81920, smem, tid);
    __syncthreads();
    LinS w; lin_sums(smem + lane * 16, w);
    f32x4 kv[4] = {Z4, Z4, Z4, Z4};
    u32x4 x[20];
    unsigned dbg[3] = {0, 0, 0};
    bool have = false;
#pragma unroll 1
    for (int tile = wave; tile < 128;) {
      const unsigned probe = __builtin_amdgcn_raw_buffer_load_b32(rX2, tile < 122 ? id * 512 + lane * 4 : 0x80000000u, (tile + 6) * 20480, 16);
      if (!have) {
        u32x4 one[1];
        if (!poll_frags<1, 0>(rX2, (id & 15) * 1024 + lane * 16, tile * 20480, one, ctl)) return false;
        if (tile >= 124) TL(6);
        if (!poll_frags<20, 1024>(rX2, lane * 16, tile * 20480, x, ctl)) return false;
      }
      const bool ahead = tile < 122 && __all(probe != 0xFFFFFFFFu);
      if (lin_tile<true>(x, rX2, tile, smem + lane * 16, w, kv, rq, id, lane, have, ahead ? lane * 16 : 0x80000000u, dbg)) { have = ahead; tile += 4; }
      else have = false;
    }
    DBGOUT(12);
    pq0 = ldq(0); pq1 = ldq(1);
#pragma unroll
    for (int t = 0; t < 4; ++t) *(f32x4*)(KS + wave * 4096 + t * 1024 + lane * 16) = kv[t];
    TL(7);
    asm volatile("s_waitcnt vmcnt(0)" ::: "memory");
    __syncthreads();
    ks = (*(const f32x4*)(KS + tid * 16) + *(const f32x4*)(KS + 4096 + tid * 16)) + (*(const f32x4*)(KS + 8192 + tid * 16) + *(const f32x4*)(KS + 12288 + tid * 16));
  return true;
}
DEV void lin_role(const Args& a, u8* smem, const int id) {
  int tid = threadIdx.x;
  OPQ(tid);
  const int wave = __builtin_amdgcn_readfirstlane(tid >> 6), lane = tid & 63;
  unsigned* ctl = (unsigned*)(a.ws + OFF_CTL);
  u8* KS = smem + 81920;
  const __amdgpu_buffer_rsrc_t rq = rsrc(a.ws + OFF_PQ, 4u << 20), rX2 = rsrc(a.ws + OFF_X2, SZ_X);
  auto ldq = [&](int rd) { return __builtin_bit_cast(bf16x8, __builtin_amdgcn_raw_buffer_load_b128(rq, lane * 16, ((rd * 4 + wave) * 32 + id) * 1024, 16)); };
  TL(0);
  f32x4 ks;
  {
    u32x4 p[6];
    if (!poll_frags<6, 4096>(rsrc(a.ws + OFF_KVP, 1u << 20), tid * 16, id * 6 * 4096, p, ctl)) return;
#define F4(x) __builtin_bit_cast(f32x4, x)
    ks = ((F4(p[0]) + F4(p[1])) + (F4(p[2]) + F4(p[3]))) + (F4(p[4]) + F4(p[5]));
  }
  bf16x8 pq0 = ldq(0), pq1 = ldq(1);
  if (!lin_out<0>(a, smem, id, ks, pq0, pq1)) return;
  if (!lin_second(a, smem, id, ks, pq0, pq1)) return;
  lin_out<1>(a, smem, id, ks, pq0, pq1);
}

#define SRD(vec, i, ptr, bytes) do { unsigned long long p_ = (unsigned long long)(ptr); vec[i] = (unsigned)p_; vec[i + 1] = (unsigned)(p_ >> 32) & 0xFFFFu; vec[i + 2] = (unsigned)(bytes); vec[i + 3] = 0x00020000u; } while (0)
__global__ void __launch_bounds__(256, 1) k_main(Args a) {
  extern __shared__ __attribute__((aligned(16))) u8 smem[];
  const unsigned b = blockIdx.x, wave = __builtin_amdgcn_readfirstlane(threadIdx.x >> 6), lane = threadIdx.x & 63;
  const unsigned par = (b & 7) >> 2, sub = b & 3, jj = b >> 3;
  if (b >= 224 && b < 244) {
    const unsigned tid = threadIdx.x;
    u32x4 v[10];
#pragma unroll
    for (int i = 0; i < 10; ++i) v[i] = *(const u32x4*)(a.wo + (size_t)((tid >> 2) + 64 * i) * 640 + (b - 224) * 32 + (tid & 3) * 8);
#pragma unroll
    for (int i = 0; i < 10; ++i) *(u32x4*)(smem + 102400 + ((tid >> 2) + 64 * i) * 80 + (tid & 3) * 16) = v[i];
  }
  if (jj >= 8) lin_first(a, smem);
  if (jj >= 28) { lin_role(a, smem, b - 224); return; }
  const unsigned g = lane >> 4, n16 = lane & 15, L16 = lane * 16, OOB = 0x80000000u;
  const bool isH = jj < 8;
  const unsigned h = sub * 8 + jj, c = (jj - 8) * 4 + sub;
  u32x16 sa, sb; u32x8 sc;
  SRD(sa, 0, a.ws, SZ_RING); SRD(sa, 4, a.ws + OFF_X2, SZ_X); SRD(sa, 8, a.ws + OFF_XNF, 2 * SZ_X + 4 * SEGB);
  const unsigned wcol = (h * 192 + wave * 48) * 2;
  if (isH) { SRD(sa, 12, (const u8*)a.Wq + wcol, 640 * 12288 - wcol); SRD(sb, 0, (const u8*)a.Wk + wcol, 640 * 12288 - wcol); }
  else { SRD(sa, 12, a.ws + OFF_CF + (size_t)(c * 4 + wave) * 81920, 81920); SRD(sb, 0, a.ws + OFF_XR, 2 * SZ_X); }
  SRD(sb, 4, a.out, SZ_X); SRD(sb, 8, a.ws + OFF_CTL, 2u << 20);
  sb[12] = wave; sb[13] = par; sb[14] = 0; sb[15] = 0;
  sc[0] = isH ? (wave < 2 ? OOB : 0u) : (unsigned)a.dp; sc[1] = isH ? PXB + (wave == 1 ? 20480 - 512 + h * 128 : wave * 20480 + h * 640) : (unsigned)a.dm;
  sc[2] = isH ? sub * 65536 + 3 * 16384 + 7168 : (unsigned)a.skew; sc[3] = isH ? (wave >> 1) * 20480 + (wave & 1) * 5120 : (unsigned)a.delta; sc[4] = a.lat; sc[5] = isH ? PXB + (wave == 1 ? 8 : wave * 20480) + h * 640 : (unsigned)a.marg | (c == 0 ? OOB : 0u); sc[6] = jj;
  sc[7] = isH ? (h >> 3) * 16384 + (h & 7) * 640 : (unsigned)a.pubw;
  const unsigned sto = (isH ? wave != 3 : wave > 1) ? OOB : FLB + (136 + par * 128 + (isH ? h : 32 + c)) * 128;
  const unsigned dbgoff = 256 + (b * 4 + wave) * 32;
  const unsigned mo = sub * 65536 + wave * 16384 + 5120 + L16, mo2 = sub * 65536 + (wave & 1) * 16384 + 7168 + (wave >> 1) * 512 + lane * 8;
  const unsigned m2w = (8 + (wave & 1)) * 1024 + (wave >> 1) * 512 + lane * 8, idx = (lane & 8) ? 0xEEEEu : 0x4444u, sch = sub * 65536 + 3 * 16384 + 7168;
  u32x16 va; u32x8 vb, vc = {0, 0, 0, 0, 0, 0, 0, 0};
  if (isH) {
    const unsigned r8 = lane >> 3, q8 = lane & 7;
    va[0] = L16; va[1] = L16 + (wave ^ 1) * 1024; va[2] = L16 + (wave ^ 2) * 1024; va[3] = L16 + (wave ^ 3) * 1024; va[4] = L16 + wave * 1024;
    va[5] = 65536 + wave * 24576 + L16; va[6] = mo; va[7] = mo2; va[8] = 10240 + m2w; va[9] = idx;
    const unsigned sw = n16 * 128 + (((2 * g) ^ n16) & 7) * 16, r7 = n16 & 7, a8 = n16 >> 3, rd = 4 * (wave & 1) + g, cp = (2 * (wave >> 1) + a8) * 65536;
    va[10] = 30720 + wave * 1024 + r7 * 128 + ((2 * g + a8) ^ r7) * 16; va[11] = (q8 & 1) ? OOB : 512 + r8 * 16 + q8 * 2;
    va[12] = g >= 2 ? 34816 + wave * 256 + r7 * 32 + (g - 2) * 16 + a8 * 8 : 35840 + lane * 8;
    va[13] = 30720 + a8 * 2048 + rd * 128 + (q8 ^ rd) * 16; va[14] = 34816 + a8 * 512 + rd * 32 + q8 * 4;
    va[15] = cp + (q8 >> 1) * 128 + rd * 16 + (q8 & 1) * 8;
    vb[0] = (q8 & 1) ? OOB : cp + 512 + rd * 16 + q8 * 2; vb[1] = 40960 + wave * 4096 + sw;
    vb[2] = 57344 + wave * 1024 + (g >> 1) * 512 + n16 * 32 + (g & 1) * 16; vb[3] = (q8 & 1) * 16 + (q8 >> 1) * 4 - (wave * 8 + r8);
#pragma unroll
    for (int j = 0; j < 3; ++j) { const unsigned cc = 64 * j + lane; vc[j] = (cc / 6) * 12288 + (cc % 6) * 16; }
#pragma unroll
    for (int j = 0; j < 2; ++j) { const unsigned ta = drow(0, g, 4 * j + (n16 >> 2)) * 96 + (lane & 3) * 8; vc[3 + j] = wave * 16384 + ta; vc[5 + j] = 65536 + wave * 24576 + ta; }
#pragma unroll
    for (int j = 0; j < 4; ++j) {
      const unsigned base = (j < 2 ? 0 : 8) + 4 * (g & 1) + 2 * (j & 1);
      vb[4 + j] = (n16 == base ? 0x3F80u : 0u) | (n16 == base + 1 ? 0x3F800000u : 0u);
    }
  } else {
    const unsigned e1 = (lane >> 3) & 1, r7 = lane & 7, t = c >> 3;
    va[0] = L16; va[1] = 81920 + wave * 1024 + L16; va[2] = mo; va[3] = mo2; va[4] = 81920 + m2w; va[5] = idx;
    va[6] = sub * 65536 + e1 * 640 + g * 128 + r7 * 16; va[7] = sub * 65536 + (2 * g + e1) * 640 + 512 + r7 * 16;
    va[8] = PXB + e1 * 640 + g * 128 + r7 * 16; va[9] = PXB + (2 * g + e1) * 640 + 512 + r7 * 16;
    va[10] = sch;
    va[11] = (t & 3) * 16384 + 5120 + (t >> 2) * 1024 + (c & 7) * 128 + r7 * 16 + g * 4 + e1 * 65536;
    const unsigned tok = wave == 1 ? e1 * 8 + r7 : wave * 8 + r7;
    const bool off = e1 && wave != 1;
    va[12] = off ? OOB : tok * 1280 + c * 16 + g * 4;
    va[13] = wave == 1 ? PXB + 20480 + (2 * g + e1) * 128 + r7 * 16 : OOB;
    va[14] = off ? OOB : (tok >> 4) * 20480 + c * 256 + (tok & 15) * 16 + g * 4; va[15] = 0;
    vb[0] = (c == 0 && wave == 0 && lane < 4) ? lane * 65536 + 3 * 16384 + 7168 : OOB;
    vb[1] = vb[2] = vb[3] = vb[4] = vb[5] = vb[6] = 0;
    vb[7] = min(*(const unsigned*)(a.mem0 + r7 * 640 + c * 8 + g * 2), 0xFFFFFFFEu);
  }
  asm volatile(
  "s_waitcnt vmcnt(0) lgkmcnt(0)\n s_nop 7\n s_cmp_ge_u32 s54, 8\n s_cbranch_scc1 C_ENTRY_%=\n"
  "s_mov_b32 s76, 0x3dd53b95\n"
  "s_mov_b32 s77, 0xceabfb8f\n"
  "s_mov_b32 s78, 0xfffffffe\n"
  "s_mov_b32 s46, 0\n"
  "v_cmp_ge_i32 s[8:9], 0, v19\n"
  "v_cmp_ge_i32 s[10:11], -1, v19\n"
  "v_cmp_ge_i32 s[12:13], -2, v19\n"
  "v_cmp_ge_i32 s[14:15], -3, v19\n"
  "s_lshl_b32 s69, s44, 14\n"
  "s_mul_i32 s70, s44, 24576\n"
  "s_add_u32 s70, s70, 65536\n"
  "s_add_u32 s68, s44, 0\n"
  "s_mul_i32 s68, s68, 786432\n"
  "s_add_u32 s71, s68, 393216\n"
  "s_add_u32 m0, s69, 0\n"
  "s_nop 0\n"
  "buffer_load_dwordx4 v24, s[28:31], s68 offen lds\n"
  "s_add_u32 m0, s69, 1024\n"
  "s_nop 0\n"
  "buffer_load_dwordx4 v25, s[28:31], s68 offen lds\n"
  "s_add_u32 m0, s69, 2048\n"
  "s_nop 0\n"
  "buffer_load_dwordx4 v26, s[28:31], s68 offen lds\n"
  "s_add_u32 m0, s69, 3072\n"
  "s_nop 0\n"
  "buffer_load_dwordx4 v24, s[28:31], s71 offen lds\n"
  "s_add_u32 m0, s69, 4096\n"
  "s_nop 0\n"
  "buffer_load_dwordx4 v25, s[28:31], s71 offen lds\n"
  "s_add_u32 m0, s69, 5120\n"
  "s_nop 0\n"
  "buffer_load_dwordx4 v26, s[28:31], s71 offen lds\n"
  "s_add_u32 s68, s44, 0\n"
  "s_mul_i32 s68, s68, 786432\n"
  "s_add_u32 s71, s68, 393216\n"
  "s_add_u32 m0, s69, 6144\n"
  "s_nop 0\n"
  "buffer_load_dwordx4 v24, s[32:35], s68 offen lds\n"
  "s_add_u32 m0, s69, 7168\n"
  "s_nop 0\n"
  "buffer_load_dwordx4 v25, s[32:35], s68 offen lds\n"
  "s_add_u32 m0, s69, 8192\n"
  "s_nop 0\n"
  "buffer_load_dwordx4 v26, s[32:35], s68 offen lds\n"
  "s_add_u32 m0, s69, 9216\n"
  "s_nop 0\n"
  "buffer_load_dwordx4 v24, s[32:35], s71 offen lds\n"
  "s_add_u32 m0, s69, 10240\n"
  "s_nop 0\n"
  "buffer_load_dwordx4 v25, s[32:35], s71 offen lds\n"
  "s_add_u32 m0, s69, 11264\n"
  "s_nop 0\n"
  "buffer_load_dwordx4 v26, s[32:35], s71 offen lds\n"
  "s_add_u32 s68, s44, 4\n"
  "s_mul_i32 s68, s68, 786432\n"
  "s_add_u32 s71, s68, 393216\n"
  "s_add_u32 m0, s70, 0\n"
  "s_nop 0\n"
  "buffer_load_dwordx4 v24, s[28:31], s68 offen lds\n"
  "s_add_u32 m0, s70, 1024\n"
  "s_nop 0\n"
  "buffer_load_dwordx4 v25, s[28:31], s68 offen lds\n"
  "s_add_u32 m0, s70, 2048\n"
  "s_nop 0\n"
  "buffer_load_dwordx4 v26, s[28:31], s68 offen lds\n"
  "s_add_u32 m0, s70, 3072\n"
  "s_nop 0\n"
  "buffer_load_dwordx4 v24, s[28:31], s71 offen lds\n"
  "s_add_u32 m0, s70, 4096\n"
  "s_nop 0\n"
  "buffer_load_dwordx4 v25, s[28:31], s71 offen lds\n"
  "s_add_u32 m0, s70, 5120\n"
  "s_nop 0\n"
  "buffer_load_dwordx4 v26, s[28:31], s71 offen lds\n"
  "s_add_u32 s68, s44, 4\n"
  "s_mul_i32 s68, s68, 786432\n"
  "s_add_u32 s71, s68, 393216\n"
  "s_add_u32 m0, s70, 6144\n"
  "s_nop 0\n"
  "buffer_load_dwordx4 v24, s[32:35], s68 offen lds\n"
  "s_add_u32 m0, s70, 7168\n"
  "s_nop 0\n"
  "buffer_load_dwordx4 v25, s[32:35], s68 offen lds\n"
  "s_add_u32 m0, s70, 8192\n"
  "s_nop 0\n"
  "buffer_load_dwordx4 v26, s[32:35], s68 offen lds\n"
  "s_add_u32 m0, s70, 9216\n"
  "s_nop 0\n"
  "buffer_load_dwordx4 v24, s[32:35], s71 offen lds\n"
  "s_add_u32 m0, s70, 10240\n"
  "s_nop 0\n"
  "buffer_load_dwordx4 v25, s[32:35], s71 offen lds\n"
  "s_add_u32 m0, s70, 11264\n"
  "s_nop 0\n"
  "buffer_load_dwordx4 v26, s[32:35], s71 offen lds\n"
  "s_xor_b32 s68, s44, 1\n"
  "s_mul_i32 s68, s68, 786432\n"
  "s_add_u32 s71, s68, 393216\n"
  "s_add_u32 m0, s70, 12288\n"
  "s_nop 0\n"
  "buffer_load_dwordx4 v24, s[28:31], s68 offen lds\n"
  "s_add_u32 m0, s70, 13312\n"
  "s_nop 0\n"
  "buffer_load_dwordx4 v25, s[28:31], s68 offen lds\n"
  "s_add_u32 m0, s70, 14336\n"
  "s_nop 0\n"
  "buffer_load_dwordx4 v26, s[28:31], s68 offen lds\n"
  "s_add_u32 m0, s70, 15360\n"
  "s_nop 0\n"
  "buffer_load_dwordx4 v24, s[28:31], s71 offen lds\n"
  "s_add_u32 m0, s70, 16384\n"
  "s_nop 0\n"
  "buffer_load_dwordx4 v25, s[28:31], s71 offen lds\n"
  "s_add_u32 m0, s70, 17408\n"
  "s_nop 0\n"
  "buffer_load_dwordx4 v26, s[28:31], s71 offen lds\n"
  "s_xor_b32 s68, s44, 1\n"
  "s_mul_i32 s68, s68, 786432\n"
  "s_add_u32 s71, s68, 393216\n"
  "s_add_u32 m0, s70, 18432\n"
  "s_nop 0\n"
  "buffer_load_dwordx4 v24, s[32:35], s68 offen lds\n"
  "s_add_u32 m0, s70, 19456\n"
  "s_nop 0\n"
  "buffer_load_dwordx4 v25, s[32:35], s68 offen lds\n"
  "s_add_u32 m0, s70, 20480\n"
  "s_nop 0\n"
  "buffer_load_dwordx4 v26, s[32:35], s68 offen lds\n"
  "s_add_u32 m0, s70, 21504\n"
  "s_nop 0\n"
  "buffer_load_dwordx4 v24, s[32:35], s71 offen lds\n"
  "s_add_u32 m0, s70, 22528\n"
  "s_nop 0\n"
  "buffer_load_dwordx4 v25, s[32:35], s71 offen lds\n"
  "s_add_u32 m0, s70, 23552\n"
  "s_nop 0\n"
  "buffer_load_dwordx4 v26, s[32:35], s71 offen lds\n"
  "s_waitcnt vmcnt(30)\n"
  "ds_read_b64_tr_b16 a[0:1], v27 offset:0\n"
  "ds_read_b64_tr_b16 a[2:3], v28 offset:0\n"
  "ds_read_b64_tr_b16 a[4:5], v27 offset:3072\n"
  "ds_read_b64_tr_b16 a[6:7], v28 offset:3072\n"
  "ds_read_b64_tr_b16 a[8:9], v27 offset:32\n"
  "ds_read_b64_tr_b16 a[10:11], v28 offset:32\n"
  "ds_read_b64_tr_b16 a[12:13], v27 offset:3104\n"
  "ds_read_b64_tr_b16 a[14:15], v28 offset:3104\n"
  "ds_read_b64_tr_b16 a[16:17], v27 offset:64\n"
  "ds_read_b64_tr_b16 a[18:19], v28 offset:64\n"
  "ds_read_b64_tr_b16 a[20:21], v27 offset:3136\n"
  "ds_read_b64_tr_b16 a[22:23], v28 offset:3136\n"
  "s_waitcnt lgkmcnt(0)\n"
  "s_xor_b32 s68, s44, 2\n"
  "s_mul_i32 s68, s68, 786432\n"
  "s_add_u32 s71, s68, 393216\n"
  "s_add_u32 m0, s69, 0\n"
  "s_nop 0\n"
  "buffer_load_dwordx4 v24, s[28:31], s68 offen lds\n"
  "s_add_u32 m0, s69, 1024\n"
  "s_nop 0\n"
  "buffer_load_dwordx4 v25, s[28:31], s68 offen lds\n"
  "s_add_u32 m0, s69, 2048\n"
  "s_nop 0\n"
  "buffer_load_dwordx4 v26, s[28:31], s68 offen lds\n"
  "s_add_u32 m0, s69, 3072\n"
  "s_nop 0\n"
  "buffer_load_dwordx4 v24, s[28:31], s71 offen lds\n"
  "s_add_u32 m0, s69, 4096\n"
  "s_nop 0\n"
  "buffer_load_dwordx4 v25, s[28:31], s71 offen lds\n"
  "s_add_u32 m0, s69, 5120\n"
  "s_nop 0\n"
  "buffer_load_dwordx4 v26, s[28:31], s71 offen lds\n"
  "s_waitcnt vmcnt(30)\n"
  "ds_read_b64_tr_b16 a[24:25], v27 offset:6144\n"
  "ds_read_b64_tr_b16 a[26:27], v28 offset:6144\n"
  "ds_read_b64_tr_b16 a[28:29], v27 offset:9216\n"
  "ds_read_b64_tr_b16 a[30:31], v28 offset:9216\n"
  "ds_read_b64_tr_b16 a[32:33], v27 offset:6176\n"
  "ds_read_b64_tr_b16 a[34:35], v28 offset:6176\n"
  "ds_read_b64_tr_b16 a[36:37], v27 offset:9248\n"
  "ds_read_b64_tr_b16 a[38:39], v28 offset:9248\n"
  "ds_read_b64_tr_b16 v[32:33], v27 offset:6208\n"
  "ds_read_b64_tr_b16 v[34:35], v28 offset:6208\n"
  "ds_read_b64_tr_b16 v[36:37], v27 offset:9280\n"
  "ds_read_b64_tr_b16 v[38:39], v28 offset:9280\n"
  "s_waitcnt lgkmcnt(0)\n"
  "s_xor_b32 s68, s44, 2\n"
  "s_mul_i32 s68, s68, 786432\n"
  "s_add_u32 s71, s68, 393216\n"
  "s_add_u32 m0, s69, 6144\n"
  "s_nop 0\n"
  "buffer_load_dwordx4 v24, s[32:35], s68 offen lds\n"
  "s_add_u32 m0, s69, 7168\n"
  "s_nop 0\n"
  "buffer_load_dwordx4 v25, s[32:35], s68 offen lds\n"
  "s_add_u32 m0, s69, 8192\n"
  "s_nop 0\n"
  "buffer_load_dwordx4 v26, s[32:35], s68 offen lds\n"
  "s_add_u32 m0, s69, 9216\n"
  "s_nop 0\n"
  "buffer_load_dwordx4 v24, s[32:35], s71 offen lds\n"
  "s_add_u32 m0, s69, 10240\n"
  "s_nop 0\n"
  "buffer_load_dwordx4 v25, s[32:35], s71 offen lds\n"
  "s_add_u32 m0, s69, 11264\n"
  "s_nop 0\n"
  "buffer_load_dwordx4 v26, s[32:35], s71 offen lds\n"
  "s_waitcnt vmcnt(30)\n"
  "ds_read_b64_tr_b16 a[40:41], v29 offset:0\n"
  "ds_read_b64_tr_b16 a[42:43], v30 offset:0\n"
  "ds_read_b64_tr_b16 a[44:45], v29 offset:3072\n"
  "ds_read_b64_tr_b16 a[46:47], v30 offset:3072\n"
  "ds_read_b64_tr_b16 a[48:49], v29 offset:32\n"
  "ds_read_b64_tr_b16 a[50:51], v30 offset:32\n"
  "ds_read_b64_tr_b16 a[52:53], v29 offset:3104\n"
  "ds_read_b64_tr_b16 a[54:55], v30 offset:3104\n"
  "ds_read_b64_tr_b16 a[56:57], v29 offset:64\n"
  "ds_read_b64_tr_b16 a[58:59], v30 offset:64\n"
  "ds_read_b64_tr_b16 a[60:61], v29 offset:3136\n"
  "ds_read_b64_tr_b16 a[62:63], v30 offset:3136\n"
  "s_waitcnt lgkmcnt(0)\n"
  "s_xor_b32 s68, s44, 3\n"
  "s_mul_i32 s68, s68, 786432\n"
  "s_add_u32 s71, s68, 393216\n"
  "s_add_u32 m0, s70, 0\n"
  "s_nop 0\n"
  "buffer_load_dwordx4 v24, s[28:31], s68 offen lds\n"
  "s_add_u32 m0, s70, 1024\n"
  "s_nop 0\n"
  "buffer_load_dwordx4 v25, s[28:31], s68 offen lds\n"
  "s_add_u32 m0, s70, 2048\n"
  "s_nop 0\n"
  "buffer_load_dwordx4 v26, s[28:31], s68 offen lds\n"
  "s_add_u32 m0, s70, 3072\n"
  "s_nop 0\n"
  "buffer_load_dwordx4 v24, s[28:31], s71 offen lds\n"
  "s_add_u32 m0, s70, 4096\n"
  "s_nop 0\n"
  "buffer_load_dwordx4 v25, s[28:31], s71 offen lds\n"
  "s_add_u32 m0, s70, 5120\n"
  "s_nop 0\n"
  "buffer_load_dwordx4 v26, s[28:31], s71 offen lds\n"
  "s_waitcnt vmcnt(30)\n"
  "ds_read_b64_tr_b16 a[64:65], v29 offset:6144\n"
  "ds_read_b64_tr_b16 a[66:67], v30 offset:6144\n"
  "ds_read_b64_tr_b16 a[68:69], v29 offset:9216\n"
  "ds_read_b64_tr_b16 a[70:71], v30 offset:9216\n"
  "ds_read_b64_tr_b16 a[72:73], v29 offset:6176\n"
  "ds_read_b64_tr_b16 a[74:75], v30 offset:6176\n"
  "ds_read_b64_tr_b16 a[76:77], v29 offset:9248\n"
  "ds_read_b64_tr_b16 a[78:79], v30 offset:9248\n"
  "ds_read_b64_tr_b16 v[40:41], v29 offset:6208\n"
  "ds_read_b64_tr_b16 v[42:43], v30 offset:6208\n"
  "ds_read_b64_tr_b16 v[44:45], v29 offset:9280\n"
  "ds_read_b64_tr_b16 v[46:47], v30 offset:9280\n"
  "s_waitcnt lgkmcnt(0)\n"
  "s_xor_b32 s68, s44, 3\n"
  "s_mul_i32 s68, s68, 786432\n"
  "s_add_u32 s71, s68, 393216\n"
  "s_add_u32 m0, s70, 6144\n"
  "s_nop 0\n"
  "buffer_load_dwordx4 v24, s[32:35], s68 offen lds\n"
  "s_add_u32 m0, s70, 7168\n"
  "s_nop 0\n"
  "buffer_load_dwordx4 v25, s[32:35], s68 offen lds\n"
  "s_add_u32 m0, s70, 8192\n"
  "s_nop 0\n"
  "buffer_load_dwordx4 v26, s[32:35], s68 offen lds\n"
  "s_add_u32 m0, s70, 9216\n"
  "s_nop 0\n"
  "buffer_load_dwordx4 v24, s[32:35], s71 offen lds\n"
  "s_add_u32 m0, s70, 10240\n"
  "s_nop 0\n"
  "buffer_load_dwordx4 v25, s[32:35], s71 offen lds\n"
  "s_add_u32 m0, s70, 11264\n"
  "s_nop 0\n"
  "buffer_load_dwordx4 v26, s[32:35], s71 offen lds\n"
  "s_waitcnt vmcnt(30)\n"
  "ds_read_b64_tr_b16 a[80:81], v29 offset:12288\n"
  "ds_read_b64_tr_b16 a[82:83], v30 offset:12288\n"
  "ds_read_b64_tr_b16 a[84:85], v29 offset:15360\n"
  "ds_read_b64_tr_b16 a[86:87], v30 offset:15360\n"
  "ds_read_b64_tr_b16 a[88:89], v29 offset:12320\n"
  "ds_read_b64_tr_b16 a[90:91], v30 offset:12320\n"
  "ds_read_b64_tr_b16 a[92:93], v29 offset:15392\n"
  "ds_read_b64_tr_b16 a[94:95], v30 offset:15392\n"
  "ds_read_b64_tr_b16 a[96:97], v29 offset:12352\n"
  "ds_read_b64_tr_b16 a[98:99], v30 offset:12352\n"
  "ds_read_b64_tr_b16 a[100:101], v29 offset:15424\n"
  "ds_read_b64_tr_b16 a[102:103], v30 offset:15424\n"
  "s_waitcnt lgkmcnt(0)\n"
  "s_xor_b32 s68, s44, 1\n"
  "s_add_u32 s68, s68, 4\n"
  "s_mul_i32 s68, s68, 786432\n"
  "s_add_u32 s71, s68, 393216\n"
  "s_add_u32 m0, s70, 12288\n"
  "s_nop 0\n"
  "buffer_load_dwordx4 v24, s[28:31], s68 offen lds\n"
  "s_add_u32 m0, s70, 13312\n"
  "s_nop 0\n"
  "buffer_load_dwordx4 v25, s[28:31], s68 offen lds\n"
  "s_add_u32 m0, s70, 14336\n"
  "s_nop 0\n"
  "buffer_load_dwordx4 v26, s[28:31], s68 offen lds\n"
  "s_add_u32 m0, s70, 15360\n"
  "s_nop 0\n"
  "buffer_load_dwordx4 v24, s[28:31], s71 offen lds\n"
  "s_add_u32 m0, s70, 16384\n"
  "s_nop 0\n"
  "buffer_load_dwordx4 v25, s[28:31], s71 offen lds\n"
  "s_add_u32 m0, s70, 17408\n"
  "s_nop 0\n"
  "buffer_load_dwordx4 v26, s[28:31], s71 offen lds\n"
  "s_waitcnt vmcnt(30)\n"
  "ds_read_b64_tr_b16 a[104:105], v29 offset:18432\n"
  "ds_read_b64_tr_b16 a[106:107], v30 offset:18432\n"
  "ds_read_b64_tr_b16 a[108:109], v29 offset:21504\n"
  "ds_read_b64_tr_b16 a[110:111], v30 offset:21504\n"
  "ds_read_b64_tr_b16 v[48:49], v29 offset:18464\n"
  "ds_read_b64_tr_b16 v[50:51], v30 offset:18464\n"
  "ds_read_b64_tr_b16 v[52:53], v29 offset:21536\n"
  "ds_read_b64_tr_b16 v[54:55], v30 offset:21536\n"
  "ds_read_b64_tr_b16 v[56:57], v29 offset:18496\n"
  "ds_read_b64_tr_b16 v[58:59], v30 offset:18496\n"
  "ds_read_b64_tr_b16 v[60:61], v29 offset:21568\n"
  "ds_read_b64_tr_b16 v[62:63], v30 offset:21568\n"
  "s_waitcnt lgkmcnt(0)\n"
  "s_xor_b32 s68, s44, 1\n"
  "s_add_u32 s68, s68, 4\n"
  "s_mul_i32 s68, s68, 786432\n"
  "s_add_u32 s71, s68, 393216\n"
  "s_add_u32 m0, s70, 18432\n"
  "s_nop 0\n"
  "buffer_load_dwordx4 v24, s[32:35], s68 offen lds\n"
  "s_add_u32 m0, s70, 19456\n"
  "s_nop 0\n"
  "buffer_load_dwordx4 v25, s[32:35], s68 offen lds\n"
  "s_add_u32 m0, s70, 20480\n"
  "s_nop 0\n"
  "buffer_load_dwordx4 v26, s[32:35], s68 offen lds\n"
  "s_add_u32 m0, s70, 21504\n"
  "s_nop 0\n"
  "buffer_load_dwordx4 v24, s[32:35], s71 offen lds\n"
  "s_add_u32 m0, s70, 22528\n"
  "s_nop 0\n"
  "buffer_load_dwordx4 v25, s[32:35], s71 offen lds\n"
  "s_add_u32 m0, s70, 23552\n"
  "s_nop 0\n"
  "buffer_load_dwordx4 v26, s[32:35], s71 offen lds\n"
  "s_waitcnt vmcnt(30)\n"
  "ds_read_b64_tr_b16 a[112:113], v27 offset:0\n"
  "ds_read_b64_tr_b16 a[114:115], v28 offset:0\n"
  "ds_read_b64_tr_b16 a[116:117], v27 offset:3072\n"
  "ds_read_b64_tr_b16 a[118:119], v28 offset:3072\n"
  "ds_read_b64_tr_b16 a[120:121], v27 offset:32\n"
  "ds_read_b64_tr_b16 a[122:123], v28 offset:32\n"
  "ds_read_b64_tr_b16 a[124:125], v27 offset:3104\n"
  "ds_read_b64_tr_b16 a[126:127], v28 offset:3104\n"
  "ds_read_b64_tr_b16 a[128:129], v27 offset:64\n"
  "ds_read_b64_tr_b16 a[130:131], v28 offset:64\n"
  "ds_read_b64_tr_b16 a[132:133], v27 offset:3136\n"
  "ds_read_b64_tr_b16 a[134:135], v28 offset:3136\n"
  "s_waitcnt lgkmcnt(0)\n"
  "s_xor_b32 s68, s44, 2\n"
  "s_add_u32 s68, s68, 4\n"
  "s_mul_i32 s68, s68, 786432\n"
  "s_add_u32 s71, s68, 393216\n"
  "s_add_u32 m0, s69, 0\n"
  "s_nop 0\n"
  "buffer_load_dwordx4 v24, s[28:31], s68 offen lds\n"
  "s_add_u32 m0, s69, 1024\n"
  "s_nop 0\n"
  "buffer_load_dwordx4 v25, s[28:31], s68 offen lds\n"
  "s_add_u32 m0, s69, 2048\n"
  "s_nop 0\n"
  "buffer_load_dwordx4 v26, s[28:31], s68 offen lds\n"
  "s_add_u32 m0, s69, 3072\n"
  "s_nop 0\n"
  "buffer_load_dwordx4 v24, s[28:31], s71 offen lds\n"
  "s_add_u32 m0, s69, 4096\n"
  "s_nop 0\n"
  "buffer_load_dwordx4 v25, s[28:31], s71 offen lds\n"
  "s_add_u32 m0, s69, 5120\n"
  "s_nop 0\n"
  "buffer_load_dwordx4 v26, s[28:31], s71 offen lds\n"
  "s_waitcnt vmcnt(30)\n"
  "ds_read_b64_tr_b16 a[136:137], v27 offset:6144\n"
  "ds_read_b64_tr_b16 a[138:139], v28 offset:6144\n"
  "ds_read_b64_tr_b16 a[140:141], v27 offset:9216\n"
  "ds_read_b64_tr_b16 a[142:143], v28 offset:9216\n"
  "ds_read_b64_tr_b16 a[144:145], v27 offset:6176\n"
  "ds_read_b64_tr_b16 a[146:147], v28 offset:6176\n"
  "ds_read_b64_tr_b16 a[148:149], v27 offset:9248\n"
  "ds_read_b64_tr_b16 a[150:151], v28 offset:9248\n"
  "ds_read_b64_tr_b16 v[64:65], v27 offset:6208\n"
  "ds_read_b64_tr_b16 v[66:67], v28 offset:6208\n"
  "ds_read_b64_tr_b16 v[68:69], v27 offset:9280\n"
  "ds_read_b64_tr_b16 v[70:71], v28 offset:9280\n"
  "s_waitcnt lgkmcnt(0)\n"
  "s_xor_b32 s68, s44, 2\n"
  "s_add_u32 s68, s68, 4\n"
  "s_mul_i32 s68, s68, 786432\n"
  "s_add_u32 s71, s68, 393216\n"
  "s_add_u32 m0, s69, 6144\n"
  "s_nop 0\n"
  "buffer_load_dwordx4 v24, s[32:35], s68 offen lds\n"
  "s_add_u32 m0, s69, 7168\n"
  "s_nop 0\n"
  "buffer_load_dwordx4 v25, s[32:35], s68 offen lds\n"
  "s_add_u32 m0, s69, 8192\n"
  "s_nop 0\n"
  "buffer_load_dwordx4 v26, s[32:35], s68 offen lds\n"
  "s_add_u32 m0, s69, 9216\n"
  "s_nop 0\n"
  "buffer_load_dwordx4 v24, s[32:35], s71 offen lds\n"
  "s_add_u32 m0, s69, 10240\n"
  "s_nop 0\n"
  "buffer_load_dwordx4 v25, s[32:35], s71 offen lds\n"
  "s_add_u32 m0, s69, 11264\n"
  "s_nop 0\n"
  "buffer_load_dwordx4 v26, s[32:35], s71 offen lds\n"
  "s_waitcnt vmcnt(30)\n"
  "ds_read_b64_tr_b16 a[152:153], v29 offset:0\n"
  "ds_read_b64_tr_b16 a[154:155], v30 offset:0\n"
  "ds_read_b64_tr_b16 a[156:157], v29 offset:3072\n"
  "ds_read_b64_tr_b16 a[158:159], v30 offset:3072\n"
  "ds_read_b64_tr_b16 a[160:161], v29 offset:32\n"
  "ds_read_b64_tr_b16 a[162:163], v30 offset:32\n"
  "ds_read_b64_tr_b16 a[164:165], v29 offset:3104\n"
  "ds_read_b64_tr_b16 a[166:167], v30 offset:3104\n"
  "ds_read_b64_tr_b16 a[168:169], v29 offset:64\n"
  "ds_read_b64_tr_b16 a[170:171], v30 offset:64\n"
  "ds_read_b64_tr_b16 a[172:173], v29 offset:3136\n"
  "ds_read_b64_tr_b16 a[174:175], v30 offset:3136\n"
  "s_waitcnt lgkmcnt(0)\n"
  "s_xor_b32 s68, s44, 3\n"
  "s_add_u32 s68, s68, 4\n"
  "s_mul_i32 s68, s68, 786432\n"
  "s_add_u32 s71, s68, 393216\n"
  "s_add_u32 m0, s70, 0\n"
  "s_nop 0\n"
  "buffer_load_dwordx4 v24, s[28:31], s68 offen lds\n"
  "s_add_u32 m0, s70, 1024\n"
  "s_nop 0\n"
  "buffer_load_dwordx4 v25, s[28:31], s68 offen lds\n"
  "s_add_u32 m0, s70, 2048\n"
  "s_nop 0\n"
  "buffer_load_dwordx4 v26, s[28:31], s68 offen lds\n"
  "s_add_u32 m0, s70, 3072\n"
  "s_nop 0\n"
  "buffer_load_dwordx4 v24, s[28:31], s71 offen lds\n"
  "s_add_u32 m0, s70, 4096\n"
  "s_nop 0\n"
  "buffer_load_dwordx4 v25, s[28:31], s71 offen lds\n"
  "s_add_u32 m0, s70, 5120\n"
  "s_nop 0\n"
  "buffer_load_dwordx4 v26, s[28:31], s71 offen lds\n"
  "s_waitcnt vmcnt(30)\n"
  "ds_read_b64_tr_b16 a[176:177], v29 offset:6144\n"
  "ds_read_b64_tr_b16 a[178:179], v30 offset:6144\n"
  "ds_read_b64_tr_b16 a[180:181], v29 offset:9216\n"
  "ds_read_b64_tr_b16 a[182:183], v30 offset:9216\n"
  "ds_read_b64_tr_b16 v[72:73], v29 offset:6176\n"
  "ds_read_b64_tr_b16 v[74:75], v30 offset:6176\n"
  "ds_read_b64_tr_b16 v[76:77], v29 offset:9248\n"
  "ds_read_b64_tr_b16 v[78:79], v30 offset:9248\n"
  "ds_read_b64_tr_b16 v[80:81], v29 offset:6208\n"
  "ds_read_b64_tr_b16 v[82:83], v30 offset:6208\n"
  "ds_read_b64_tr_b16 v[84:85], v29 offset:9280\n"
  "ds_read_b64_tr_b16 v[86:87], v30 offset:9280\n"
  "s_waitcnt lgkmcnt(0)\n"
  "s_xor_b32 s68, s44, 3\n"
  "s_add_u32 s68, s68, 4\n"
  "s_mul_i32 s68, s68, 786432\n"
  "s_add_u32 s71, s68, 393216\n"
  "s_add_u32 m0, s70, 6144\n"
  "s_nop 0\n"
  "buffer_load_dwordx4 v24, s[32:35], s68 offen lds\n"
  "s_add_u32 m0, s70, 7168\n"
  "s_nop 0\n"
  "buffer_load_dwordx4 v25, s[32:35], s68 offen lds\n"
  "s_add_u32 m0, s70, 8192\n"
  "s_nop 0\n"
  "buffer_load_dwordx4 v26, s[32:35], s68 offen lds\n"
  "s_add_u32 m0, s70, 9216\n"
  "s_nop 0\n"
  "buffer_load_dwordx4 v24, s[32:35], s71 offen lds\n"
  "s_add_u32 m0, s70, 10240\n"
  "s_nop 0\n"
  "buffer_load_dwordx4 v25, s[32:35], s71 offen lds\n"
  "s_add_u32 m0, s70, 11264\n"
  "s_nop 0\n"
  "buffer_load_dwordx4 v26, s[32:35], s71 offen lds\n"
  "s_waitcnt vmcnt(30)\n"
  "ds_read_b64_tr_b16 a[184:185], v29 offset:12288\n"
  "ds_read_b64_tr_b16 a[186:187], v30 offset:12288\n"
  "ds_read_b64_tr_b16 a[188:189], v29 offset:15360\n"
  "ds_read_b64_tr_b16 a[190:191], v30 offset:15360\n"
  "ds_read_b64_tr_b16 a[192:193], v29 offset:12320\n"
  "ds_read_b64_tr_b16 a[194:195], v30 offset:12320\n"
  "ds_read_b64_tr_b16 a[196:197], v29 offset:15392\n"
  "ds_read_b64_tr_b16 a[198:199], v30 offset:15392\n"
  "ds_read_b64_tr_b16 a[200:201], v29 offset:12352\n"
  "ds_read_b64_tr_b16 a[202:203], v30 offset:12352\n"
  "ds_read_b64_tr_b16 a[204:205], v29 offset:15424\n"
  "ds_read_b64_tr_b16 a[206:207], v30 offset:15424\n"
  "s_waitcnt lgkmcnt(0)\n"
  "s_mov_b32 s68, 8\n"
  "s_mul_i32 s68, s68, 786432\n"
  "s_add_u32 s71, s68, 393216\n"
  "s_add_u32 m0, s70, 12288\n"
  "s_nop 0\n"
  "buffer_load_dwordx4 v24, s[28:31], s68 offen lds\n"
  "s_add_u32 m0, s70, 13312\n"
  "s_nop 0\n"
  "buffer_load_dwordx4 v25, s[28:31], s68 offen lds\n"
  "s_add_u32 m0, s70, 14336\n"
  "s_nop 0\n"
  "buffer_load_dwordx4 v26, s[28:31], s68 offen lds\n"
  "s_add_u32 m0, s70, 15360\n"
  "s_nop 0\n"
  "buffer_load_dwordx4 v24, s[28:31], s71 offen lds\n"
  "s_add_u32 m0, s70, 16384\n"
  "s_nop 0\n"
  "buffer_load_dwordx4 v25, s[28:31], s71 offen lds\n"
  "s_add_u32 m0, s70, 17408\n"
  "s_nop 0\n"
  "buffer_load_dwordx4 v26, s[28:31], s71 offen lds\n"
  "s_waitcnt vmcnt(30)\n"
  "ds_read_b64_tr_b16 v[88:89], v29 offset:18432\n"
  "ds_read_b64_tr_b16 v[90:91], v30 offset:18432\n"
  "ds_read_b64_tr_b16 v[92:93], v29 offset:21504\n"
  "ds_read_b64_tr_b16 v[94:95], v30 offset:21504\n"
  "ds_read_b64_tr_b16 a[208:209], v29 offset:18464\n"
  "ds_read_b64_tr_b16 a[210:211], v30 offset:18464\n"
  "ds_read_b64_tr_b16 a[212:213], v29 offset:21536\n"
  "ds_read_b64_tr_b16 a[214:215], v30 offset:21536\n"
  "ds_read_b64_tr_b16 a[216:217], v29 offset:18496\n"
  "ds_read_b64_tr_b16 a[218:219], v30 offset:18496\n"
  "ds_read_b64_tr_b16 a[220:221], v29 offset:21568\n"
  "ds_read_b64_tr_b16 a[222:223], v30 offset:21568\n"
  "s_waitcnt lgkmcnt(0)\n"
  "s_mov_b32 s68, 8\n"
  "s_mul_i32 s68, s68, 786432\n"
  "s_add_u32 s71, s68, 393216\n"
  "s_add_u32 m0, s70, 18432\n"
  "s_nop 0\n"
  "buffer_load_dwordx4 v24, s[32:35], s68 offen lds\n"
  "s_add_u32 m0, s70, 19456\n"
  "s_nop 0\n"
  "buffer_load_dwordx4 v25, s[32:35], s68 offen lds\n"
  "s_add_u32 m0, s70, 20480\n"
  "s_nop 0\n"
  "buffer_load_dwordx4 v26, s[32:35], s68 offen lds\n"
  "s_add_u32 m0, s70, 21504\n"
  "s_nop 0\n"
  "buffer_load_dwordx4 v24, s[32:35], s71 offen lds\n"
  "s_add_u32 m0, s70, 22528\n"
  "s_nop 0\n"
  "buffer_load_dwordx4 v25, s[32:35], s71 offen lds\n"
  "s_add_u32 m0, s70, 23552\n"
  "s_nop 0\n"
  "buffer_load_dwordx4 v26, s[32:35], s71 offen lds\n"
  "s_waitcnt vmcnt(30)\n"
  "ds_read_b64_tr_b16 a[224:225], v27 offset:0\n"
  "ds_read_b64_tr_b16 a[226:227], v28 offset:0\n"
  "ds_read_b64_tr_b16 a[228:229], v27 offset:3072\n"
  "ds_read_b64_tr_b16 a[230:231], v28 offset:3072\n"
  "ds_read_b64_tr_b16 a[232:233], v27 offset:32\n"
  "ds_read_b64_tr_b16 a[234:235], v28 offset:32\n"
  "ds_read_b64_tr_b16 a[236:237], v27 offset:3104\n"
  "ds_read_b64_tr_b16 a[238:239], v28 offset:3104\n"
  "ds_read_b64_tr_b16 a[240:241], v27 offset:64\n"
  "ds_read_b64_tr_b16 a[242:243], v28 offset:64\n"
  "ds_read_b64_tr_b16 a[244:245], v27 offset:3136\n"
  "ds_read_b64_tr_b16 a[246:247], v28 offset:3136\n"
  "s_waitcnt lgkmcnt(0)\n"
  "s_mov_b32 s68, 9\n"
  "s_mul_i32 s68, s68, 786432\n"
  "s_add_u32 s71, s68, 393216\n"
  "s_add_u32 m0, s69, 0\n"
  "s_nop 0\n"
  "buffer_load_dwordx4 v24, s[28:31], s68 offen lds\n"
  "s_add_u32 m0, s69, 1024\n"
  "s_nop 0\n"
  "buffer_load_dwordx4 v25, s[28:31], s68 offen lds\n"
  "s_add_u32 m0, s69, 2048\n"
  "s_nop 0\n"
  "buffer_load_dwordx4 v26, s[28:31], s68 offen lds\n"
  "s_add_u32 m0, s69, 3072\n"
  "s_nop 0\n"
  "buffer_load_dwordx4 v24, s[28:31], s71 offen lds\n"
  "s_add_u32 m0, s69, 4096\n"
  "s_nop 0\n"
  "buffer_load_dwordx4 v25, s[28:31], s71 offen lds\n"
  "s_add_u32 m0, s69, 5120\n"
  "s_nop 0\n"
  "buffer_load_dwordx4 v26, s[28:31], s71 offen lds\n"
  "s_waitcnt vmcnt(30)\n"
  "ds_read_b64_tr_b16 a[248:249], v27 offset:6144\n"
  "ds_read_b64_tr_b16 a[250:251], v28 offset:6144\n"
  "ds_read_b64_tr_b16 a[252:253], v27 offset:9216\n"
  "ds_read_b64_tr_b16 a[254:255], v28 offset:9216\n"
  "ds_read_b64_tr_b16 v[96:97], v27 offset:6176\n"
  "ds_read_b64_tr_b16 v[98:99], v28 offset:6176\n"
  "ds_read_b64_tr_b16 v[100:101], v27 offset:9248\n"
  "ds_read_b64_tr_b16 v[102:103], v28 offset:9248\n"
  "ds_read_b64_tr_b16 v[128:129], v27 offset:6208\n"
  "ds_read_b64_tr_b16 v[130:131], v28 offset:6208\n"
  "ds_read_b64_tr_b16 v[132:133], v27 offset:9280\n"
  "ds_read_b64_tr_b16 v[134:135], v28 offset:9280\n"
  "s_waitcnt lgkmcnt(0)\n"
  "s_mov_b32 s68, 9\n"
  "s_mul_i32 s68, s68, 786432\n"
  "s_add_u32 s71, s68, 393216\n"
  "s_add_u32 m0, s69, 6144\n"
  "s_nop 0\n"
  "buffer_load_dwordx4 v24, s[32:35], s68 offen lds\n"
  "s_add_u32 m0, s69, 7168\n"
  "s_nop 0\n"
  "buffer_load_dwordx4 v25, s[32:35], s68 offen lds\n"
  "s_add_u32 m0, s69, 8192\n"
  "s_nop 0\n"
  "buffer_load_dwordx4 v26, s[32:35], s68 offen lds\n"
  "s_add_u32 m0, s69, 9216\n"
  "s_nop 0\n"
  "buffer_load_dwordx4 v24, s[32:35], s71 offen lds\n"
  "s_add_u32 m0, s69, 10240\n"
  "s_nop 0\n"
  "buffer_load_dwordx4 v25, s[32:35], s71 offen lds\n"
  "s_add_u32 m0, s69, 11264\n"
  "s_nop 0\n"
  "buffer_load_dwordx4 v26, s[32:35], s71 offen lds\n"
  "s_waitcnt vmcnt(30)\n"
  "ds_read_b64_tr_b16 v[136:137], v29 offset:0\n"
  "ds_read_b64_tr_b16 v[138:139], v30 offset:0\n"
  "ds_read_b64_tr_b16 v[140:141], v29 offset:3072\n"
  "ds_read_b64_tr_b16 v[142:143], v30 offset:3072\n"
  "ds_read_b64_tr_b16 v[144:145], v29 offset:32\n"
  "ds_read_b64_tr_b16 v[146:147], v30 offset:32\n"
  "ds_read_b64_tr_b16 v[148:149], v29 offset:3104\n"
  "ds_read_b64_tr_b16 v[150:151], v30 offset:3104\n"
  "ds_read_b64_tr_b16 v[152:153], v29 offset:64\n"
  "ds_read_b64_tr_b16 v[154:155], v30 offset:64\n"
  "ds_read_b64_tr_b16 v[156:157], v29 offset:3136\n"
  "ds_read_b64_tr_b16 v[158:159], v30 offset:3136\n"
  "s_waitcnt lgkmcnt(0)\n"
  "s_waitcnt vmcnt(24)\n"
  "ds_read_b64_tr_b16 v[104:105], v29 offset:6144\n"
  "ds_read_b64_tr_b16 v[106:107], v30 offset:6144\n"
  "ds_read_b64_tr_b16 v[108:109], v29 offset:9216\n"
  "ds_read_b64_tr_b16 v[110:111], v30 offset:9216\n"
  "ds_read_b64_tr_b16 v[160:161], v29 offset:6176\n"
  "ds_read_b64_tr_b16 v[162:163], v30 offset:6176\n"
  "ds_read_b64_tr_b16 v[164:165], v29 offset:9248\n"
  "ds_read_b64_tr_b16 v[166:167], v30 offset:9248\n"
  "ds_read_b64_tr_b16 v[168:169], v29 offset:6208\n"
  "ds_read_b64_tr_b16 v[170:171], v30 offset:6208\n"
  "ds_read_b64_tr_b16 v[172:173], v29 offset:9280\n"
  "ds_read_b64_tr_b16 v[174:175], v30 offset:9280\n"
  "s_waitcnt lgkmcnt(0)\n"
  "s_waitcnt vmcnt(18)\n"
  "ds_read_b64_tr_b16 v[176:177], v29 offset:12288\n"
  "ds_read_b64_tr_b16 v[178:179], v30 offset:12288\n"
  "ds_read_b64_tr_b16 v[180:181], v29 offset:15360\n"
  "ds_read_b64_tr_b16 v[182:183], v30 offset:15360\n"
  "ds_read_b64_tr_b16 v[184:185], v29 offset:12320\n"
  "ds_read_b64_tr_b16 v[186:187], v30 offset:12320\n"
  "ds_read_b64_tr_b16 v[188:189], v29 offset:15392\n"
  "ds_read_b64_tr_b16 v[190:191], v30 offset:15392\n"
  "ds_read_b64_tr_b16 v[192:193], v29 offset:12352\n"
  "ds_read_b64_tr_b16 v[194:195], v30 offset:12352\n"
  "ds_read_b64_tr_b16 v[196:197], v29 offset:15424\n"
  "ds_read_b64_tr_b16 v[198:199], v30 offset:15424\n"
  "s_waitcnt lgkmcnt(0)\n"
  "s_waitcnt vmcnt(12)\n"
  "ds_read_b64_tr_b16 v[200:201], v29 offset:18432\n"
  "ds_read_b64_tr_b16 v[202:203], v30 offset:18432\n"
  "ds_read_b64_tr_b16 v[204:205], v29 offset:21504\n"
  "ds_read_b64_tr_b16 v[206:207], v30 offset:21504\n"
  "ds_read_b64_tr_b16 v[112:113], v29 offset:18464\n"
  "ds_read_b64_tr_b16 v[114:115], v30 offset:18464\n"
  "ds_read_b64_tr_b16 v[116:117], v29 offset:21536\n"
  "ds_read_b64_tr_b16 v[118:119], v30 offset:21536\n"
  "ds_read_b64_tr_b16 v[208:209], v29 offset:18496\n"
  "ds_read_b64_tr_b16 v[210:211], v30 offset:18496\n"
  "ds_read_b64_tr_b16 v[212:213], v29 offset:21568\n"
  "ds_read_b64_tr_b16 v[214:215], v30 offset:21568\n"
  "s_waitcnt lgkmcnt(0)\n"
  "s_waitcnt vmcnt(6)\n"
  "ds_read_b64_tr_b16 v[216:217], v27 offset:0\n"
  "ds_read_b64_tr_b16 v[218:219], v28 offset:0\n"
  "ds_read_b64_tr_b16 v[220:221], v27 offset:3072\n"
  "ds_read_b64_tr_b16 v[222:223], v28 offset:3072\n"
  "ds_read_b64_tr_b16 v[224:225], v27 offset:32\n"
  "ds_read_b64_tr_b16 v[226:227], v28 offset:32\n"
  "ds_read_b64_tr_b16 v[228:229], v27 offset:3104\n"
  "ds_read_b64_tr_b16 v[230:231], v28 offset:3104\n"
  "ds_read_b64_tr_b16 v[232:233], v27 offset:64\n"
  "ds_read_b64_tr_b16 v[234:235], v28 offset:64\n"
  "ds_read_b64_tr_b16 v[236:237], v27 offset:3136\n"
  "ds_read_b64_tr_b16 v[238:239], v28 offset:3136\n"
  "s_waitcnt lgkmcnt(0)\n"
  "s_waitcnt vmcnt(0)\n"
  "ds_read_b64_tr_b16 v[240:241], v27 offset:6144\n"
  "ds_read_b64_tr_b16 v[242:243], v28 offset:6144\n"
  "ds_read_b64_tr_b16 v[244:245], v27 offset:9216\n"
  "ds_read_b64_tr_b16 v[246:247], v28 offset:9216\n"
  "ds_read_b64_tr_b16 v[248:249], v27 offset:6176\n"
  "ds_read_b64_tr_b16 v[250:251], v28 offset:6176\n"
  "ds_read_b64_tr_b16 v[252:253], v27 offset:9248\n"
  "ds_read_b64_tr_b16 v[254:255], v28 offset:9248\n"
  "ds_read_b64_tr_b16 v[120:121], v27 offset:6208\n"
  "ds_read_b64_tr_b16 v[122:123], v28 offset:6208\n"
  "ds_read_b64_tr_b16 v[124:125], v27 offset:9280\n"
  "ds_read_b64_tr_b16 v[126:127], v28 offset:9280\n"
  "s_waitcnt lgkmcnt(0)\n"
  "ds_write_b128 v5, v[32:35] offset:0\n"
  "ds_write_b128 v5, v[36:39] offset:1024\n"
  "ds_write_b128 v5, v[40:43] offset:2048\n"
  "ds_write_b128 v5, v[44:47] offset:3072\n"
  "ds_write_b128 v5, v[48:51] offset:4096\n"
  "ds_write_b128 v5, v[52:55] offset:5120\n"
  "ds_write_b128 v5, v[56:59] offset:6144\n"
  "ds_write_b128 v5, v[60:63] offset:7168\n"
  "ds_write_b128 v5, v[64:67] offset:8192\n"
  "ds_write_b128 v5, v[68:71] offset:9216\n"
  "ds_write_b128 v5, v[72:75] offset:10240\n"
  "ds_write_b128 v5, v[76:79] offset:11264\n"
  "ds_write_b128 v5, v[80:83] offset:12288\n"
  "ds_write_b128 v5, v[84:87] offset:13312\n"
  "s_waitcnt lgkmcnt(10)\n"
  "ds_write_b128 v5, v[88:91] offset:14336\n"
  "ds_write_b128 v5, v[92:95] offset:15360\n"
  "ds_write_b128 v5, v[96:99] offset:16384\n"
  "ds_write_b128 v5, v[100:103] offset:17408\n"
  "s_waitcnt lgkmcnt(10)\n"
  "ds_write_b128 v5, v[104:107] offset:18432\n"
  "ds_write_b128 v5, v[108:111] offset:19456\n"
  "ds_write_b128 v5, v[112:115] offset:20480\n"
  "ds_write_b128 v5, v[116:119] offset:21504\n"
  "s_waitcnt lgkmcnt(10)\n"
  "ds_write_b128 v5, v[120:123] offset:22528\n"
  "ds_write_b128 v5, v[124:127] offset:23552\n"
  "s_mov_b32 s62, 0\n"
  "s_mov_b32 s74, 1851648\n"
  "H_CALP_%=:\n"
  "s_nop 7\n"
  "buffer_load_dword v19, off, s[16:19], s74 sc1\n"
  "s_waitcnt vmcnt(0)\n"
  "v_readfirstlane_b32 s47, v19\n"
  "s_cmp_lg_u32 s47, -1\n"
  "s_cbranch_scc1 H_CALK_%=\n"
  "s_sleep 4\n"
  "s_add_u32 s62, s62, 1\n"
  "s_cmp_gt_u32 s62, 300000\n"
  "s_cbranch_scc1 H_FAIL_%=\n"
  "s_and_b32 s60, s62, 255\n"
  "s_cmp_lg_u32 s60, 0\n"
  "s_cbranch_scc1 H_CALP_%=\n"
  "v_mov_b32 v19, 0\n"
  "buffer_load_dword v19, v19, s[40:43], 0 offen sc1\n"
  "s_waitcnt vmcnt(0)\n"
  "v_readfirstlane_b32 s60, v19\n"
  "s_cmp_lg_u32 s60, 0\n"
  "s_cbranch_scc1 H_EXIT_%=\n"
  "s_branch H_CALP_%=\n"
  "H_CALK_%=:\n"
  "s_nop 7\n"
  "s_waitcnt lgkmcnt(0)\n"
  "s_memtime s[64:65]\n"
  "s_memrealtime s[66:67]\n"
  "s_waitcnt lgkmcnt(0)\n"
  "s_mov_b32 s96, s64\n"
  "s_mov_b32 s97, s66\n"
  "s_mov_b32 s98, 352\n"
  "H_LAYER_%=:\n"
  "s_nop 7\n"
  "s_mov_b32 s56, s45\n"
  "s_add_u32 s99, s46, s45\n"
  "s_add_u32 s74, s99, 0\n"
  "s_lshl_b32 s74, s74, 7\n"
  "s_add_u32 s74, s74, 1835008\n"
  "s_mov_b32 s62, 0\n"
  "s_cmp_ge_u32 s56, 64\n"
  "s_cbranch_scc1 H_F0K_%=\n"
  "s_waitcnt vmcnt(0) lgkmcnt(0)\n"
  "H_F0P_%=:\n"
  "buffer_load_dword v19, off, s[16:19], s74 sc1\n"
  "s_waitcnt vmcnt(0)\n"
  "v_readfirstlane_b32 s60, v19\n"
  "s_cmp_lg_u32 s60, -1\n"
  "s_cbranch_scc1 H_F0K_%=\n"
  "s_sleep 4\n"
  "s_add_u32 s62, s62, 1\n"
  "s_cmp_gt_u32 s62, 300000\n"
  "s_cbranch_scc1 H_FAIL_%=\n"
  "s_and_b32 s60, s62, 255\n"
  "s_cmp_lg_u32 s60, 0\n"
  "s_cbranch_scc1 H_F0P_%=\n"
  "v_mov_b32 v19, 0\n"
  "buffer_load_dword v19, v19, s[40:43], 0 offen sc1\n"
  "s_waitcnt vmcnt(0)\n"
  "v_readfirstlane_b32 s60, v19\n"
  "s_cmp_lg_u32 s60, 0\n"
  "s_cbranch_scc1 H_EXIT_%=\n"
  "s_branch H_F0P_%=\n"
  "H_F0K_%=:\n"
  "s_barrier\n"
  "s_add_u32 s72, s99, 0\n"
  "s_mul_i32 s72, s72, 40960\n"
  "s_add_u32 s73, s51, 0\n"
  "s_add_u32 s72, s72, s73\n"
  "s_mov_b32 m0, s73\n"
  "s_nop 0\n"
  "buffer_load_dwordx4 v0, s[24:27], s72 offen offset:0 sc1 lds\n"
  "buffer_load_dwordx4 v0, s[24:27], s72 offen offset:1024 sc1 lds\n"
  "buffer_load_dwordx4 v0, s[24:27], s72 offen offset:2048 sc1 lds\n"
  "buffer_load_dwordx4 v0, s[24:27], s72 offen offset:3072 sc1 lds\n"
  "s_add_u32 s72, s72, 4096\n"
  "s_add_u32 m0, s73, 4096\n"
  "s_nop 0\n"
  "buffer_load_dwordx4 v0, s[24:27], s72 offen offset:0 sc1 lds\n"
  "s_waitcnt vmcnt(0)\n"
  "s_barrier\n"
  "ds_read_b128 v[40:43], v4 offset:0\n"
  "ds_read_b128 v[44:47], v4 offset:20480\n"
  "ds_read_b128 v[80:83], v5 offset:0\n"
  "ds_read_b128 v[84:87], v5 offset:1024\n"
  "s_waitcnt lgkmcnt(3)\n"
  "v_mfma_f32_16x16x32_bf16 v[56:59], a[0:3], v[40:43], 0\n"
  "ds_read_b128 v[48:51], v4 offset:4096\n"
  "s_add_u32 s72, s99, 0\n"
  "v_mfma_f32_16x16x32_bf16 v[60:63], a[8:11], v[40:43], 0\n"
  "ds_read_b128 v[52:55], v4 offset:24576\n"
  "s_mul_i32 s72, s72, 40960\n"
  "v_mfma_f32_16x16x32_bf16 v[64:67], a[16:19], v[40:43], 0\n"
  "ds_read_b128 v[88:91], v5 offset:2048\n"
  "s_add_u32 s73, s51, 10240\n"
  "v_mfma_f32_16x16x32_bf16 v[68:71], a[24:27], v[40:43], 0\n"
  "ds_read_b128 v[92:95], v5 offset:3072\n"
  "s_add_u32 s72, s72, s73\n"
  "v_mfma_f32_16x16x32_bf16 v[72:75], a[32:35], v[40:43], 0\n"
  "s_mov_b32 m0, s73\n"
  "s_nop 0\n"
  "buffer_load_dwordx4 v0, s[24:27], s72 offen offset:0 sc1 lds\n"
  "s_waitcnt lgkmcnt(5)\n"
  "v_mfma_f32_16x16x32_bf16 v[76:79], v[80:83], v[40:43], 0\n"
  "buffer_load_dwordx4 v0, s[24:27], s72 offen offset:1024 sc1 lds\n"
  "v_mfma_f32_16x16x32_bf16 v[56:59], a[4:7], v[44:47], v[56:59]\n"
  "buffer_load_dwordx4 v0, s[24:27], s72 offen offset:2048 sc1 lds\n"
  "v_mfma_f32_16x16x32_bf16 v[60:63], a[12:15], v[44:47], v[60:63]\n"
  "buffer_load_dwordx4 v0, s[24:27], s72 offen offset:3072 sc1 lds\n"
  "v_mfma_f32_16x16x32_bf16 v[64:67], a[20:23], v[44:47], v[64:67]\n"
  "s_add_u32 s72, s72, 4096\n"
  "s_add_u32 m0, s73, 4096\n"
  "v_mfma_f32_16x16x32_bf16 v[68:71], a[28:31], v[44:47], v[68:71]\n"
  "buffer_load_dwordx4 v0, s[24:27], s72 offen offset:0 sc1 lds\n"
  "v_mfma_f32_16x16x32_bf16 v[72:75], a[36:39], v[44:47], v[72:75]\n"
  "s_add_u32 s70, s99, 2\n"
  "s_lshl_b32 s70, s70, 7\n"
  "s_waitcnt lgkmcnt(4)\n"
  "v_mfma_f32_16x16x32_bf16 v[76:79], v[84:87], v[44:47], v[76:79]\n"
  "s_add_u32 s70, s70, 1835008\n"
  "buffer_load_dword v19, off, s[16:19], s70 sc1\n"
  "s_waitcnt lgkmcnt(3)\n"
  "v_mfma_f32_16x16x32_bf16 v[56:59], a[40:43], v[48:51], v[56:59]\n"
  "ds_read_b128 v[40:43], v1 offset:0\n"
  "v_mfma_f32_16x16x32_bf16 v[60:63], a[48:51], v[48:51], v[60:63]\n"
  "ds_read_b128 v[44:47], v1 offset:20480\n"
  "v_mfma_f32_16x16x32_bf16 v[64:67], a[56:59], v[48:51], v[64:67]\n"
  "ds_read_b128 v[96:99], v5 offset:4096\n"
  "v_mfma_f32_16x16x32_bf16 v[68:71], a[64:67], v[48:51], v[68:71]\n"
  "ds_read_b128 v[100:103], v5 offset:5120\n"
  "v_mfma_f32_16x16x32_bf16 v[72:75], a[72:75], v[48:51], v[72:75]\n"
  "ds_read_b128 v[80:83], v5 offset:6144\n"
  "s_waitcnt lgkmcnt(6)\n"
  "v_mfma_f32_16x16x32_bf16 v[76:79], v[88:91], v[48:51], v[76:79]\n"
  "ds_read_b128 v[84:87], v5 offset:7168\n"
  "v_mfma_f32_16x16x32_bf16 v[56:59], a[44:47], v[52:55], v[56:59]\n"
  "v_mfma_f32_16x16x32_bf16 v[60:63], a[52:55], v[52:55], v[60:63]\n"
  "v_mfma_f32_16x16x32_bf16 v[64:67], a[60:63], v[52:55], v[64:67]\n"
  "v_mfma_f32_16x16x32_bf16 v[68:71], a[68:71], v[52:55], v[68:71]\n"
  "v_mfma_f32_16x16x32_bf16 v[72:75], a[76:79], v[52:55], v[72:75]\n"
  "s_waitcnt lgkmcnt(6)\n"
  "v_mfma_f32_16x16x32_bf16 v[76:79], v[92:95], v[52:55], v[76:79]\n"
  "s_waitcnt lgkmcnt(5)\n"
  "v_mfma_f32_16x16x32_bf16 v[56:59], a[80:83], v[40:43], v[56:59]\n"
  "ds_read_b128 v[48:51], v2 offset:0\n"
  "v_mfma_f32_16x16x32_bf16 v[60:63], a[88:91], v[40:43], v[60:63]\n"
  "ds_read_b128 v[52:55], v2 offset:20480\n"
  "v_mfma_f32_16x16x32_bf16 v[64:67], a[96:99], v[40:43], v[64:67]\n"
  "ds_read_b128 v[88:91], v5 offset:8192\n"
  "v_mfma_f32_16x16x32_bf16 v[68:71], a[104:107], v[40:43], v[68:71]\n"
  "ds_read_b128 v[92:95], v5 offset:9216\n"
  "s_waitcnt lgkmcnt(7)\n"
  "v_mfma_f32_16x16x32_bf16 v[72:75], v[96:99], v[40:43], v[72:75]\n"
  "s_waitcnt lgkmcnt(5)\n"
  "v_mfma_f32_16x16x32_bf16 v[76:79], v[80:83], v[40:43], v[76:79]\n"
  "v_mfma_f32_16x16x32_bf16 v[56:59], a[84:87], v[44:47], v[56:59]\n"
  "v_mfma_f32_16x16x32_bf16 v[60:63], a[92:95], v[44:47], v[60:63]\n"
  "v_mfma_f32_16x16x32_bf16 v[64:67], a[100:103], v[44:47], v[64:67]\n"
  "v_mfma_f32_16x16x32_bf16 v[68:71], a[108:111], v[44:47], v[68:71]\n"
  "v_mfma_f32_16x16x32_bf16 v[72:75], v[100:103], v[44:47], v[72:75]\n"
  "s_waitcnt lgkmcnt(4)\n"
  "v_mfma_f32_16x16x32_bf16 v[76:79], v[84:87], v[44:47], v[76:79]\n"
  "s_waitcnt lgkmcnt(3)\n"
  "v_mfma_f32_16x16x32_bf16 v[56:59], a[112:115], v[48:51], v[56:59]\n"
  "ds_read_b128 v[40:43], v3 offset:0\n"
  "v_mfma_f32_16x16x32_bf16 v[60:63], a[120:123], v[48:51], v[60:63]\n"
  "ds_read_b128 v[44:47], v3 offset:20480\n"
  "v_mfma_f32_16x16x32_bf16 v[64:67], a[128:131], v[48:51], v[64:67]\n"
  "ds_read_b128 v[96:99], v5 offset:10240\n"
  "v_mfma_f32_16x16x32_bf16 v[68:71], a[136:139], v[48:51], v[68:71]\n"
  "ds_read_b128 v[100:103], v5 offset:11264\n"
  "v_mfma_f32_16x16x32_bf16 v[72:75], a[144:147], v[48:51], v[72:75]\n"
  "ds_read_b128 v[80:83], v5 offset:12288\n"
  "s_waitcnt lgkmcnt(6)\n"
  "v_mfma_f32_16x16x32_bf16 v[76:79], v[88:91], v[48:51], v[76:79]\n"
  "ds_read_b128 v[84:87], v5 offset:13312\n"
  "v_mfma_f32_16x16x32_bf16 v[56:59], a[116:119], v[52:55], v[56:59]\n"
  "v_mfma_f32_16x16x32_bf16 v[60:63], a[124:127], v[52:55], v[60:63]\n"
  "v_mfma_f32_16x16x32_bf16 v[64:67], a[132:135], v[52:55], v[64:67]\n"
  "v_mfma_f32_16x16x32_bf16 v[68:71], a[140:143], v[52:55], v[68:71]\n"
  "v_mfma_f32_16x16x32_bf16 v[72:75], a[148:151], v[52:55], v[72:75]\n"
  "s_waitcnt lgkmcnt(6)\n"
  "v_mfma_f32_16x16x32_bf16 v[76:79], v[92:95], v[52:55], v[76:79]\n"
  "s_waitcnt lgkmcnt(5)\n"
  "v_mfma_f32_16x16x32_bf16 v[56:59], a[152:155], v[40:43], v[56:59]\n"
  "ds_read_b128 v[48:51], v1 offset:4096\n"
  "v_mfma_f32_16x16x32_bf16 v[60:63], a[160:163], v[40:43], v[60:63]\n"
  "ds_read_b128 v[52:55], v1 offset:24576\n"
  "v_mfma_f32_16x16x32_bf16 v[64:67], a[168:171], v[40:43], v[64:67]\n"
  "ds_read_b128 v[88:91], v5 offset:14336\n"
  "v_mfma_f32_16x16x32_bf16 v[68:71], a[176:179], v[40:43], v[68:71]\n"
  "ds_read_b128 v[92:95], v5 offset:15360\n"
  "s_waitcnt lgkmcnt(7)\n"
  "v_mfma_f32_16x16x32_bf16 v[72:75], v[96:99], v[40:43], v[72:75]\n"
  "s_waitcnt lgkmcnt(5)\n"
  "v_mfma_f32_16x16x32_bf16 v[76:79], v[80:83], v[40:43], v[76:79]\n"
  "v_mfma_f32_16x16x32_bf16 v[56:59], a[156:159], v[44:47], v[56:59]\n"
  "v_mfma_f32_16x16x32_bf16 v[60:63], a[164:167], v[44:47], v[60:63]\n"
  "v_mfma_f32_16x16x32_bf16 v[64:67], a[172:175], v[44:47], v[64:67]\n"
  "v_mfma_f32_16x16x32_bf16 v[68:71], a[180:183], v[44:47], v[68:71]\n"
  "v_mfma_f32_16x16x32_bf16 v[72:75], v[100:103], v[44:47], v[72:75]\n"
  "s_waitcnt lgkmcnt(4)\n"
  "v_mfma_f32_16x16x32_bf16 v[76:79], v[84:87], v[44:47], v[76:79]\n"
  "s_waitcnt lgkmcnt(3)\n"
  "v_mfma_f32_16x16x32_bf16 v[56:59], a[184:187], v[48:51], v[56:59]\n"
  "ds_read_b128 v[40:43], v2 offset:4096\n"
  "v_mfma_f32_16x16x32_bf16 v[60:63], a[192:195], v[48:51], v[60:63]\n"
  "ds_read_b128 v[44:47], v2 offset:24576\n"
  "v_mfma_f32_16x16x32_bf16 v[64:67], a[200:203], v[48:51], v[64:67]\n"
  "ds_read_b128 v[96:99], v5 offset:16384\n"
  "s_waitcnt lgkmcnt(4)\n"
  "v_mfma_f32_16x16x32_bf16 v[68:71], v[88:91], v[48:51], v[68:71]\n"
  "ds_read_b128 v[100:103], v5 offset:17408\n"
  "v_mfma_f32_16x16x32_bf16 v[72:75], a[208:211], v[48:51], v[72:75]\n"
  "v_mfma_f32_16x16x32_bf16 v[76:79], a[216:219], v[48:51], v[76:79]\n"
  "v_mfma_f32_16x16x32_bf16 v[56:59], a[188:191], v[52:55], v[56:59]\n"
  "v_mfma_f32_16x16x32_bf16 v[60:63], a[196:199], v[52:55], v[60:63]\n"
  "v_mfma_f32_16x16x32_bf16 v[64:67], a[204:207], v[52:55], v[64:67]\n"
  "s_waitcnt lgkmcnt(4)\n"
  "v_mfma_f32_16x16x32_bf16 v[68:71], v[92:95], v[52:55], v[68:71]\n"
  "v_mfma_f32_16x16x32_bf16 v[72:75], a[212:215], v[52:55], v[72:75]\n"
  "v_mfma_f32_16x16x32_bf16 v[76:79], a[220:223], v[52:55], v[76:79]\n"
  "s_waitcnt lgkmcnt(3)\n"
  "v_mfma_f32_16x16x32_bf16 v[56:59], a[224:227], v[40:43], v[56:59]\n"
  "ds_read_b128 v[48:51], v3 offset:4096\n"
  "v_mfma_f32_16x16x32_bf16 v[60:63], a[232:235], v[40:43], v[60:63]\n"
  "ds_read_b128 v[52:55], v3 offset:24576\n"
  "v_mfma_f32_16x16x32_bf16 v[64:67], a[240:243], v[40:43], v[64:67]\n"
  "ds_read_b128 v[80:83], v5 offset:18432\n"
  "v_mfma_f32_16x16x32_bf16 v[68:71], a[248:251], v[40:43], v[68:71]\n"
  "ds_read_b128 v[84:87], v5 offset:19456\n"
  "s_waitcnt lgkmcnt(5)\n"
  "v_mfma_f32_16x16x32_bf16 v[72:75], v[96:99], v[40:43], v[72:75]\n"
  "v_mfma_f32_16x16x32_bf16 v[76:79], v[128:131], v[40:43], v[76:79]\n"
  "v_mfma_f32_16x16x32_bf16 v[56:59], a[228:231], v[44:47], v[56:59]\n"
  "v_mfma_f32_16x16x32_bf16 v[60:63], a[236:239], v[44:47], v[60:63]\n"
  "v_mfma_f32_16x16x32_bf16 v[64:67], a[244:247], v[44:47], v[64:67]\n"
  "v_mfma_f32_16x16x32_bf16 v[68:71], a[252:255], v[44:47], v[68:71]\n"
  "s_waitcnt lgkmcnt(4)\n"
  "v_mfma_f32_16x16x32_bf16 v[72:75], v[100:103], v[44:47], v[72:75]\n"
  "v_mfma_f32_16x16x32_bf16 v[76:79], v[132:135], v[44:47], v[76:79]\n"
  "s_waitcnt lgkmcnt(3)\n"
  "v_mfma_f32_16x16x32_bf16 v[56:59], v[136:139], v[48:51], v[56:59]\n"
  "ds_read_b128 v[40:43], v0 offset:8192\n"
  "v_mfma_f32_16x16x32_bf16 v[60:63], v[144:147], v[48:51], v[60:63]\n"
  "ds_read_b128 v[44:47], v0 offset:28672\n"
  "v_mfma_f32_16x16x32_bf16 v[64:67], v[152:155], v[48:51], v[64:67]\n"
  "ds_read_b128 v[88:91], v5 offset:20480\n"
  "s_waitcnt lgkmcnt(4)\n"
  "v_mfma_f32_16x16x32_bf16 v[68:71], v[80:83], v[48:51], v[68:71]\n"
  "ds_read_b128 v[92:95], v5 offset:21504\n"
  "v_mfma_f32_16x16x32_bf16 v[72:75], v[160:163], v[48:51], v[72:75]\n"
  "v_mfma_f32_16x16x32_bf16 v[76:79], v[168:171], v[48:51], v[76:79]\n"
  "v_mfma_f32_16x16x32_bf16 v[56:59], v[140:143], v[52:55], v[56:59]\n"
  "v_mfma_f32_16x16x32_bf16 v[60:63], v[148:151], v[52:55], v[60:63]\n"
  "v_mfma_f32_16x16x32_bf16 v[64:67], v[156:159], v[52:55], v[64:67]\n"
  "s_waitcnt lgkmcnt(4)\n"
  "v_mfma_f32_16x16x32_bf16 v[68:71], v[84:87], v[52:55], v[68:71]\n"
  "v_mfma_f32_16x16x32_bf16 v[72:75], v[164:167], v[52:55], v[72:75]\n"
  "v_mfma_f32_16x16x32_bf16 v[76:79], v[172:175], v[52:55], v[76:79]\n"
  "s_waitcnt vmcnt(0) lgkmcnt(0)\n"
  "H_LOOP_%=:\n"
  "v_mfma_f32_16x16x32_bf16 v[56:59], v[176:179], v[40:43], v[56:59]\n"
  "ds_read_b128 v[48:51], v0 offset:9216\n"
  "s_add_u32 s60, s56, 0\n"
  "v_mfma_f32_16x16x32_bf16 v[60:63], v[184:187], v[40:43], v[60:63]\n"
  "ds_read_b128 v[52:55], v0 offset:29696\n"
  "s_bfe_u32 s57, s60, 131073\n"
  "v_mfma_f32_16x16x32_bf16 v[64:67], v[192:195], v[40:43], v[64:67]\n"
  "ds_read_b128 v[96:99], v5 offset:22528\n"
  "s_lshl_b32 s57, s57, 18\n"
  "v_mfma_f32_16x16x32_bf16 v[68:71], v[200:203], v[40:43], v[68:71]\n"
  "ds_read_b128 v[100:103], v5 offset:23552\n"
  "s_xor_b32 s60, s60, s47\n"
  "v_mfma_f32_16x16x32_bf16 v[72:75], v[88:91], v[40:43], v[72:75]\n"
  "s_and_b32 s60, s60, 1\n"
  "s_lshl_b32 s60, s60, 13\n"
  "v_mfma_f32_16x16x32_bf16 v[76:79], v[208:211], v[40:43], v[76:79]\n"
  "s_add_u32 s57, s57, s60\n"
  "s_add_u32 s63, s57, s55\n"
  "v_mfma_f32_16x16x32_bf16 v[56:59], v[180:183], v[44:47], v[56:59]\n"
  "s_add_u32 s75, s57, s50\n"
  "v_mfma_f32_16x16x32_bf16 v[60:63], v[188:191], v[44:47], v[60:63]\n"
  "v_mfma_f32_16x16x32_bf16 v[64:67], v[196:199], v[44:47], v[64:67]\n"
  "v_mfma_f32_16x16x32_bf16 v[68:71], v[204:207], v[44:47], v[68:71]\n"
  "v_mfma_f32_16x16x32_bf16 v[72:75], v[92:95], v[44:47], v[72:75]\n"
  "v_mfma_f32_16x16x32_bf16 v[76:79], v[212:215], v[44:47], v[76:79]\n"
  "s_waitcnt lgkmcnt(3)\n"
  "v_mfma_f32_16x16x32_bf16 v[56:59], v[216:219], v[48:51], v[56:59]\n"
  "v_mfma_f32_16x16x32_bf16 v[60:63], v[224:227], v[48:51], v[60:63]\n"
  "v_mfma_f32_16x16x32_bf16 v[64:67], v[232:235], v[48:51], v[64:67]\n"
  "v_mfma_f32_16x16x32_bf16 v[68:71], v[240:243], v[48:51], v[68:71]\n"
  "v_mfma_f32_16x16x32_bf16 v[72:75], v[248:251], v[48:51], v[72:75]\n"
  "s_waitcnt vmcnt(5)\n"
  "s_waitcnt lgkmcnt(0)\n"
  "s_barrier\n"
  "s_mov_b32 s62, 0\n"
  "v_readfirstlane_b32 s60, v19\n"
  "s_cmp_lg_u32 s60, -1\n"
  "s_cbranch_scc1 H_F2K_%=\n"
  "s_cmp_ge_u32 s56, 62\n"
  "s_cbranch_scc1 H_F2K_%=\n"
  "s_waitcnt vmcnt(0) lgkmcnt(0)\n"
  "H_F2P_%=:\n"
  "buffer_load_dword v19, off, s[16:19], s70 sc1\n"
  "s_waitcnt vmcnt(0)\n"
  "v_readfirstlane_b32 s60, v19\n"
  "s_cmp_lg_u32 s60, -1\n"
  "s_cbranch_scc1 H_F2K_%=\n"
  "s_sleep 4\n"
  "s_add_u32 s62, s62, 1\n"
  "s_cmp_gt_u32 s62, 300000\n"
  "s_cbranch_scc1 H_FAIL_%=\n"
  "s_and_b32 s60, s62, 255\n"
  "s_cmp_lg_u32 s60, 0\n"
  "s_cbranch_scc1 H_F2P_%=\n"
  "v_mov_b32 v19, 0\n"
  "buffer_load_dword v19, v19, s[40:43], 0 offen sc1\n"
  "s_waitcnt vmcnt(0)\n"
  "v_readfirstlane_b32 s60, v19\n"
  "s_cmp_lg_u32 s60, 0\n"
  "s_cbranch_scc1 H_EXIT_%=\n"
  "s_branch H_F2P_%=\n"
  "H_F2K_%=:\n"
  "v_mfma_f32_16x16x32_bf16 v[76:79], v[96:99], v[48:51], v[76:79]\n"
  "ds_read_b128 v[40:43], v4 offset:10240\n"
  "v_mfma_f32_16x16x32_bf16 v[56:59], v[220:223], v[52:55], v[56:59]\n"
  "ds_read_b128 v[44:47], v4 offset:30720\n"
  "v_mfma_f32_16x16x32_bf16 v[60:63], v[228:231], v[52:55], v[60:63]\n"
  "ds_read_b128 v[80:83], v5 offset:0\n"
  "v_mfma_f32_16x16x32_bf16 v[64:67], v[236:239], v[52:55], v[64:67]\n"
  "ds_read_b128 v[84:87], v5 offset:1024\n"
  "v_mfma_f32_16x16x32_bf16 v[68:71], v[244:247], v[52:55], v[68:71]\n"
  "v_mfma_f32_16x16x32_bf16 v[72:75], v[252:255], v[52:55], v[72:75]\n"
  "v_mfma_f32_16x16x32_bf16 v[76:79], v[100:103], v[52:55], v[76:79]\n"
  "s_waitcnt lgkmcnt(3)\n"
  "v_mfma_f32_16x16x32_bf16 v[104:107], a[0:3], v[40:43], 0\n"
  "ds_read_b128 v[48:51], v4 offset:14336\n"
  "s_add_u32 s72, s99, 2\n"
  "v_mfma_f32_16x16x32_bf16 v[108:111], a[8:11], v[40:43], 0\n"
  "ds_read_b128 v[52:55], v4 offset:34816\n"
  "s_mul_i32 s72, s72, 40960\n"
  "v_mfma_f32_16x16x32_bf16 v[112:115], a[16:19], v[40:43], 0\n"
  "ds_read_b128 v[88:91], v5 offset:2048\n"
  "s_add_u32 s73, s51, 0\n"
  "v_mfma_f32_16x16x32_bf16 v[116:119], a[24:27], v[40:43], 0\n"
  "ds_read_b128 v[92:95], v5 offset:3072\n"
  "s_add_u32 s72, s72, s73\n"
  "v_mfma_f32_16x16x32_bf16 v[120:123], a[32:35], v[40:43], 0\n"
  "s_mov_b32 m0, s73\n"
  "s_nop 0\n"
  "buffer_load_dwordx4 v0, s[24:27], s72 offen offset:0 sc1 lds\n"
  "s_waitcnt lgkmcnt(5)\n"
  "v_mfma_f32_16x16x32_bf16 v[124:127], v[80:83], v[40:43], 0\n"
  "v_mfma_f32_16x16x32_bf16 v[104:107], a[4:7], v[44:47], v[104:107]\n"
  "v_mfma_f32_16x16x32_bf16 v[108:111], a[12:15], v[44:47], v[108:111]\n"
  "v_mfma_f32_16x16x32_bf16 v[112:115], a[20:23], v[44:47], v[112:115]\n"
  "buffer_load_dwordx4 v0, s[24:27], s72 offen offset:1024 sc1 lds\n"
  "v_mfma_f32_16x16x32_bf16 v[116:119], a[28:31], v[44:47], v[116:119]\n"
  "v_mfma_f32_16x16x32_bf16 v[120:123], a[36:39], v[44:47], v[120:123]\n"
  "s_waitcnt lgkmcnt(4)\n"
  "v_mfma_f32_16x16x32_bf16 v[124:127], v[84:87], v[44:47], v[124:127]\n"
  "s_waitcnt lgkmcnt(3)\n"
  "v_mfma_f32_16x16x32_bf16 v[104:107], a[40:43], v[48:51], v[104:107]\n"
  "ds_read_b128 v[40:43], v1 offset:10240\n"
  "buffer_load_dwordx4 v0, s[24:27], s72 offen offset:2048 sc1 lds\n"
  "v_mfma_f32_16x16x32_bf16 v[108:111], a[48:51], v[48:51], v[108:111]\n"
  "ds_read_b128 v[44:47], v1 offset:30720\n"
  "v_mfma_f32_16x16x32_bf16 v[112:115], a[56:59], v[48:51], v[112:115]\n"
  "ds_read_b128 v[96:99], v5 offset:4096\n"
  "v_mfma_f32_16x16x32_bf16 v[116:119], a[64:67], v[48:51], v[116:119]\n"
  "ds_read_b128 v[100:103], v5 offset:5120\n"
  "v_mfma_f32_16x16x32_bf16 v[120:123], a[72:75], v[48:51], v[120:123]\n"
  "ds_read_b128 v[80:83], v5 offset:6144\n"
  "s_waitcnt lgkmcnt(6)\n"
  "v_mfma_f32_16x16x32_bf16 v[124:127], v[88:91], v[48:51], v[124:127]\n"
  "ds_read_b128 v[84:87], v5 offset:7168\n"
  "v_mfma_f32_16x16x32_bf16 v[104:107], a[44:47], v[52:55], v[104:107]\n"
  "buffer_load_dwordx4 v0, s[24:27], s72 offen offset:3072 sc1 lds\n"
  "v_mfma_f32_16x16x32_bf16 v[108:111], a[52:55], v[52:55], v[108:111]\n"
  "v_mfma_f32_16x16x32_bf16 v[112:115], a[60:63], v[52:55], v[112:115]\n"
  "v_mfma_f32_16x16x32_bf16 v[116:119], a[68:71], v[52:55], v[116:119]\n"
  "v_mfma_f32_16x16x32_bf16 v[120:123], a[76:79], v[52:55], v[120:123]\n"
  "s_add_u32 s72, s72, 4096\n"
  "s_add_u32 m0, s73, 4096\n"
  "s_waitcnt lgkmcnt(6)\n"
  "v_mfma_f32_16x16x32_bf16 v[124:127], v[92:95], v[52:55], v[124:127]\n"
  "buffer_load_dwordx4 v0, s[24:27], s72 offen offset:0 sc1 lds\n"
  "s_waitcnt lgkmcnt(5)\n"
  "v_mfma_f32_16x16x32_bf16 v[104:107], a[80:83], v[40:43], v[104:107]\n"
  "ds_read_b128 v[48:51], v2 offset:10240\n"
  "v_mfma_f32_16x16x32_bf16 v[108:111], a[88:91], v[40:43], v[108:111]\n"
  "ds_read_b128 v[52:55], v2 offset:30720\n"
  "v_mfma_f32_16x16x32_bf16 v[112:115], a[96:99], v[40:43], v[112:115]\n"
  "ds_read_b128 v[88:91], v5 offset:8192\n"
  "v_mfma_f32_16x16x32_bf16 v[116:119], a[104:107], v[40:43], v[116:119]\n"
  "ds_read_b128 v[92:95], v5 offset:9216\n"
  "s_waitcnt lgkmcnt(7)\n"
  "v_mfma_f32_16x16x32_bf16 v[120:123], v[96:99], v[40:43], v[120:123]\n"
  "s_waitcnt lgkmcnt(5)\n"
  "v_mfma_f32_16x16x32_bf16 v[124:127], v[80:83], v[40:43], v[124:127]\n"
  "v_cvt_pk_bf16_f32 v24, v68, v69\n"
  "v_cvt_pk_bf16_f32 v25, v70, v71\n"
  "v_mfma_f32_16x16x32_bf16 v[104:107], a[84:87], v[44:47], v[104:107]\n"
  "v_cvt_pk_bf16_f32 v26, v72, v73\n"
  "v_cvt_pk_bf16_f32 v27, v74, v75\n"
  "v_mfma_f32_16x16x32_bf16 v[108:111], a[92:95], v[44:47], v[108:111]\n"
  "v_cvt_pk_bf16_f32 v28, v56, v57\n"
  "v_cvt_pk_bf16_f32 v29, v58, v59\n"
  "v_mfma_f32_16x16x32_bf16 v[112:115], a[100:103], v[44:47], v[112:115]\n"
  "v_cvt_pk_bf16_f32 v30, v60, v61\n"
  "v_cvt_pk_bf16_f32 v31, v62, v63\n"
  "v_mfma_f32_16x16x32_bf16 v[116:119], a[108:111], v[44:47], v[116:119]\n"
  "v_cvt_pk_bf16_f32 v34, v64, v65\n"
  "v_cvt_pk_bf16_f32 v35, v66, v67\n"
  "v_mfma_f32_16x16x32_bf16 v[120:123], v[100:103], v[44:47], v[120:123]\n"
  "v_cvt_pk_bf16_f32 v32, v76, v77\n"
  "v_cvt_pk_bf16_f32 v33, v78, v79\n"
  "s_waitcnt lgkmcnt(4)\n"
  "v_mfma_f32_16x16x32_bf16 v[124:127], v[84:87], v[44:47], v[124:127]\n"
  "v_mov_b64 v[56:57], 0\n"
  "s_waitcnt lgkmcnt(3)\n"
  "v_mfma_f32_16x16x32_bf16 v[104:107], a[112:115], v[48:51], v[104:107]\n"
  "ds_read_b128 v[40:43], v3 offset:10240\n"
  "v_mov_b64 v[58:59], 0\n"
  "v_mfma_f32_16x16x32_bf16 v[108:111], a[120:123], v[48:51], v[108:111]\n"
  "ds_read_b128 v[44:47], v3 offset:30720\n"
  "v_mov_b64 v[60:61], 0\n"
  "v_mfma_f32_16x16x32_bf16 v[112:115], a[128:131], v[48:51], v[112:115]\n"
  "ds_read_b128 v[96:99], v5 offset:10240\n"
  "v_mov_b64 v[62:63], 0\n"
  "v_mfma_f32_16x16x32_bf16 v[116:119], a[136:139], v[48:51], v[116:119]\n"
  "ds_read_b128 v[100:103], v5 offset:11264\n"
  "v_mov_b64 v[64:65], 0\n"
  "v_mfma_f32_16x16x32_bf16 v[120:123], a[144:147], v[48:51], v[120:123]\n"
  "ds_read_b128 v[80:83], v5 offset:12288\n"
  "v_mov_b64 v[66:67], 0\n"
  "s_waitcnt lgkmcnt(6)\n"
  "v_mfma_f32_16x16x32_bf16 v[124:127], v[88:91], v[48:51], v[124:127]\n"
  "ds_read_b128 v[84:87], v5 offset:13312\n"
  "v_mov_b64 v[68:69], 0\n"
  "v_mfma_f32_16x16x32_bf16 v[104:107], a[116:119], v[52:55], v[104:107]\n"
  "v_mov_b64 v[70:71], 0\n"
  "v_mfma_f32_16x16x32_bf16 v[108:111], a[124:127], v[52:55], v[108:111]\n"
  "v_mov_b64 v[72:73], 0\n"
  "v_mfma_f32_16x16x32_bf16 v[112:115], a[132:135], v[52:55], v[112:115]\n"
  "v_mov_b64 v[74:75], 0\n"
  "v_mfma_f32_16x16x32_bf16 v[116:119], a[140:143], v[52:55], v[116:119]\n"
  "v_mov_b64 v[76:77], 0\n"
  "v_mfma_f32_16x16x32_bf16 v[120:123], a[148:151], v[52:55], v[120:123]\n"
  "v_mov_b64 v[78:79], 0\n"
  "s_waitcnt lgkmcnt(6)\n"
  "v_mfma_f32_16x16x32_bf16 v[124:127], v[92:95], v[52:55], v[124:127]\n"
  "s_waitcnt lgkmcnt(5)\n"
  "v_mfma_f32_16x16x32_bf16 v[104:107], a[152:155], v[40:43], v[104:107]\n"
  "ds_read_b128 v[48:51], v1 offset:14336\n"
  "v_mfma_f32_16x16x32_bf16 v[108:111], a[160:163], v[40:43], v[108:111]\n"
  "ds_read_b128 v[52:55], v1 offset:34816\n"
  "v_mfma_f32_16x16x32_bf16 v[112:115], a[168:171], v[40:43], v[112:115]\n"
  "ds_read_b128 v[88:91], v5 offset:14336\n"
  "v_mfma_f32_16x16x32_bf16 v[116:119], a[176:179], v[40:43], v[116:119]\n"
  "ds_read_b128 v[92:95], v5 offset:15360\n"
  "s_waitcnt lgkmcnt(7)\n"
  "v_mfma_f32_16x16x32_bf16 v[120:123], v[96:99], v[40:43], v[120:123]\n"
  "s_waitcnt lgkmcnt(5)\n"
  "v_mfma_f32_16x16x32_bf16 v[124:127], v[80:83], v[40:43], v[124:127]\n"
  "v_mfma_f32_16x16x32_bf16 v[104:107], a[156:159], v[44:47], v[104:107]\n"
  "v_mfma_f32_16x16x32_bf16 v[108:111], a[164:167], v[44:47], v[108:111]\n"
  "v_mfma_f32_16x16x32_bf16 v[112:115], a[172:175], v[44:47], v[112:115]\n"
  "v_mfma_f32_16x16x32_bf16 v[116:119], a[180:183], v[44:47], v[116:119]\n"
  "v_mfma_f32_16x16x32_bf16 v[120:123], v[100:103], v[44:47], v[120:123]\n"
  "s_waitcnt lgkmcnt(4)\n"
  "v_mfma_f32_16x16x32_bf16 v[124:127], v[84:87], v[44:47], v[124:127]\n"
  "s_waitcnt lgkmcnt(3)\n"
  "v_mfma_f32_16x16x32_bf16 v[104:107], a[184:187], v[48:51], v[104:107]\n"
  "ds_read_b128 v[40:43], v2 offset:14336\n"
  "v_mfma_f32_16x16x32_bf16 v[108:111], a[192:195], v[48:51], v[108:111]\n"
  "ds_read_b128 v[44:47], v2 offset:34816\n"
  "v_mfma_f32_16x16x32_bf16 v[112:115], a[200:203], v[48:51], v[112:115]\n"
  "ds_read_b128 v[96:99], v5 offset:16384\n"
  "s_waitcnt lgkmcnt(4)\n"
  "v_mfma_f32_16x16x32_bf16 v[116:119], v[88:91], v[48:51], v[116:119]\n"
  "ds_read_b128 v[100:103], v5 offset:17408\n"
  "v_mfma_f32_16x16x32_bf16 v[120:123], a[208:211], v[48:51], v[120:123]\n"
  "v_mfma_f32_16x16x32_bf16 v[124:127], a[216:219], v[48:51], v[124:127]\n"
  "v_mfma_f32_16x16x32_bf16 v[104:107], a[188:191], v[52:55], v[104:107]\n"
  "v_mfma_f32_16x16x32_bf16 v[108:111], a[196:199], v[52:55], v[108:111]\n"
  "v_mfma_f32_16x16x32_bf16 v[112:115], a[204:207], v[52:55], v[112:115]\n"
  "s_waitcnt lgkmcnt(4)\n"
  "v_mfma_f32_16x16x32_bf16 v[116:119], v[92:95], v[52:55], v[116:119]\n"
  "v_mfma_f32_16x16x32_bf16 v[120:123], a[212:215], v[52:55], v[120:123]\n"
  "v_mfma_f32_16x16x32_bf16 v[124:127], a[220:223], v[52:55], v[124:127]\n"
  "s_waitcnt lgkmcnt(3)\n"
  "v_mfma_f32_16x16x32_bf16 v[104:107], a[224:227], v[40:43], v[104:107]\n"
  "ds_read_b128 v[48:51], v3 offset:14336\n"
  "v_mfma_f32_16x16x32_bf16 v[108:111], a[232:235], v[40:43], v[108:111]\n"
  "ds_read_b128 v[52:55], v3 offset:34816\n"
  "v_mfma_f32_16x16x32_bf16 v[112:115], a[240:243], v[40:43], v[112:115]\n"
  "ds_read_b128 v[80:83], v5 offset:18432\n"
  "v_mfma_f32_16x16x32_bf16 v[116:119], a[248:251], v[40:43], v[116:119]\n"
  "ds_read_b128 v[84:87], v5 offset:19456\n"
  "s_waitcnt lgkmcnt(5)\n"
  "v_mfma_f32_16x16x32_bf16 v[120:123], v[96:99], v[40:43], v[120:123]\n"
  "v_mfma_f32_16x16x32_bf16 v[124:127], v[128:131], v[40:43], v[124:127]\n"
  "v_mfma_f32_16x16x32_bf16 v[104:107], a[228:231], v[44:47], v[104:107]\n"
  "v_mfma_f32_16x16x32_bf16 v[108:111], a[236:239], v[44:47], v[108:111]\n"
  "v_mfma_f32_16x16x32_bf16 v[112:115], a[244:247], v[44:47], v[112:115]\n"
  "v_mfma_f32_16x16x32_bf16 v[116:119], a[252:255], v[44:47], v[116:119]\n"
  "s_waitcnt lgkmcnt(4)\n"
  "v_mfma_f32_16x16x32_bf16 v[120:123], v[100:103], v[44:47], v[120:123]\n"
  "v_mfma_f32_16x16x32_bf16 v[124:127], v[132:135], v[44:47], v[124:127]\n"
  "s_waitcnt lgkmcnt(3)\n"
  "v_mfma_f32_16x16x32_bf16 v[104:107], v[136:139], v[48:51], v[104:107]\n"
  "ds_read_b128 v[40:43], v0 offset:18432\n"
  "buffer_load_dword v19, off, s[16:19], s75 sc1\n"
  "v_mfma_f32_16x16x32_bf16 v[108:111], v[144:147], v[48:51], v[108:111]\n"
  "ds_read_b128 v[44:47], v0 offset:38912\n"
  "s_mov_b64 s[66:67], 0\n"
  "v_mfma_f32_16x16x32_bf16 v[112:115], v[152:155], v[48:51], v[112:115]\n"
  "ds_read_b128 v[88:91], v5 offset:20480\n"
  "s_mov_b32 s52, 0\n"
  "s_waitcnt lgkmcnt(4)\n"
  "v_mfma_f32_16x16x32_bf16 v[116:119], v[80:83], v[48:51], v[116:119]\n"
  "ds_read_b128 v[92:95], v5 offset:21504\n"
  "v_mfma_f32_16x16x32_bf16 v[120:123], v[160:163], v[48:51], v[120:123]\n"
  "v_mfma_f32_16x16x32_bf16 v[124:127], v[168:171], v[48:51], v[124:127]\n"
  "v_mfma_f32_16x16x32_bf16 v[104:107], v[140:143], v[52:55], v[104:107]\n"
  "v_mfma_f32_16x16x32_bf16 v[108:111], v[148:151], v[52:55], v[108:111]\n"
  "v_mfma_f32_16x16x32_bf16 v[112:115], v[156:159], v[52:55], v[112:115]\n"
  "s_waitcnt lgkmcnt(4)\n"
  "v_mfma_f32_16x16x32_bf16 v[116:119], v[84:87], v[52:55], v[116:119]\n"
  "v_mfma_f32_16x16x32_bf16 v[120:123], v[164:167], v[52:55], v[120:123]\n"
  "v_mfma_f32_16x16x32_bf16 v[124:127], v[172:175], v[52:55], v[124:127]\n"
  "s_waitcnt lgkmcnt(3)\n"
  "v_mfma_f32_16x16x32_bf16 v[104:107], v[176:179], v[40:43], v[104:107]\n"
  "ds_read_b128 v[48:51], v0 offset:19456\n"
  "v_mfma_f32_16x16x32_bf16 v[108:111], v[184:187], v[40:43], v[108:111]\n"
  "ds_read_b128 v[52:55], v0 offset:39936\n"
  "v_mfma_f32_16x16x32_bf16 v[112:115], v[192:195], v[40:43], v[112:115]\n"
  "ds_read_b128 v[96:99], v5 offset:22528\n"
  "v_mfma_f32_16x16x32_bf16 v[116:119], v[200:203], v[40:43], v[116:119]\n"
  "ds_read_b128 v[100:103], v5 offset:23552\n"
  "s_waitcnt lgkmcnt(5)\n"
  "v_mfma_f32_16x16x32_bf16 v[120:123], v[88:91], v[40:43], v[120:123]\n"
  "v_mfma_f32_16x16x32_bf16 v[124:127], v[208:211], v[40:43], v[124:127]\n"
  "v_mfma_f32_16x16x32_bf16 v[104:107], v[180:183], v[44:47], v[104:107]\n"
  "v_mfma_f32_16x16x32_bf16 v[108:111], v[188:191], v[44:47], v[108:111]\n"
  "v_mfma_f32_16x16x32_bf16 v[112:115], v[196:199], v[44:47], v[112:115]\n"
  "v_mfma_f32_16x16x32_bf16 v[116:119], v[204:207], v[44:47], v[116:119]\n"
  "s_waitcnt lgkmcnt(4)\n"
  "v_mfma_f32_16x16x32_bf16 v[120:123], v[92:95], v[44:47], v[120:123]\n"
  "v_mfma_f32_16x16x32_bf16 v[124:127], v[212:215], v[44:47], v[124:127]\n"
  "s_waitcnt lgkmcnt(3)\n"
  "v_mfma_f32_16x16x32_bf16 v[116:119], v[240:243], v[48:51], v[116:119]\n"
  "s_waitcnt lgkmcnt(2)\n"
  "v_mfma_f32_16x16x32_bf16 v[116:119], v[244:247], v[52:55], v[116:119]\n"
  "v_mfma_f32_16x16x32_bf16 v[120:123], v[248:251], v[48:51], v[120:123]\n"
  "v_mfma_f32_16x16x32_bf16 v[120:123], v[252:255], v[52:55], v[120:123]\n"
  "v_mfma_f32_16x16x32_bf16 v[104:107], v[216:219], v[48:51], v[104:107]\n"
  "v_mfma_f32_16x16x32_bf16 v[104:107], v[220:223], v[52:55], v[104:107]\n"
  "v_mfma_f32_16x16x32_bf16 v[108:111], v[224:227], v[48:51], v[108:111]\n"
  "v_mov_b32 v0, v0\n"
  "v_mov_b32 v0, v0\n"
  "v_mov_b32 v0, v0\n"
  "v_cvt_pk_bf16_f32 v36, v116, v117\n"
  "v_cvt_pk_bf16_f32 v37, v118, v119\n"
  "v_mfma_f32_16x16x32_bf16 v[108:111], v[228:231], v[52:55], v[108:111]\n"
  "v_cvt_pk_bf16_f32 v38, v120, v121\n"
  "v_cvt_pk_bf16_f32 v39, v122, v123\n"
  "v_mfma_f32_16x16x32_bf16 v[112:115], v[232:235], v[48:51], v[112:115]\n"
  "v_cvt_pk_bf16_f32 v40, v104, v105\n"
  "v_cvt_pk_bf16_f32 v41, v106, v107\n"
  "v_mfma_f32_16x16x32_bf16 v[112:115], v[236:239], v[52:55], v[112:115]\n"
  "s_nop 1\n"
  "v_cvt_pk_bf16_f32 v42, v108, v109\n"
  "v_cvt_pk_bf16_f32 v43, v110, v111\n"
  "s_waitcnt lgkmcnt(1)\n"
  "v_mfma_f32_16x16x32_bf16 v[124:127], v[96:99], v[48:51], v[124:127]\n"
  "s_nop 1\n"
  "v_cvt_pk_bf16_f32 v46, v112, v113\n"
  "v_cvt_pk_bf16_f32 v47, v114, v115\n"
  "s_waitcnt lgkmcnt(0)\n"
  "v_mfma_f32_16x16x32_bf16 v[124:127], v[100:103], v[52:55], v[124:127]\n"
  "v_mov_b32 v0, v0\n"
  "v_mov_b32 v0, v0\n"
  "v_mov_b32 v0, v0\n"
  "v_mov_b32 v0, v0\n"
  "v_mov_b32 v0, v0\n"
  "v_mov_b32 v0, v0\n"
  "v_mov_b32 v0, v0\n"
  "v_mov_b32 v0, v0\n"
  "v_cvt_pk_bf16_f32 v44, v124, v125\n"
  "v_cvt_pk_bf16_f32 v45, v126, v127\n"
  "s_mov_b32 s62, 0\n"
  "s_waitcnt vmcnt(0)\n"
  "v_readfirstlane_b32 s59, v19\n"
  "s_cmp_lg_u32 s59, -1\n"
  "s_cbranch_scc1 H_SCHOK_%=\n"
  "H_SCHP_%=:\n"
  "s_nop 7\n"
  "buffer_load_dword v19, off, s[16:19], s75 sc1\n"
  "s_waitcnt vmcnt(0)\n"
  "v_readfirstlane_b32 s59, v19\n"
  "s_cmp_lg_u32 s59, -1\n"
  "s_cbranch_scc1 H_SCHOK_%=\n"
  "s_sleep 2\n"
  "s_add_u32 s62, s62, 1\n"
  "s_cmp_gt_u32 s62, 300000\n"
  "s_cbranch_scc1 H_FAIL_%=\n"
  "s_and_b32 s60, s62, 255\n"
  "s_cmp_lg_u32 s60, 0\n"
  "s_cbranch_scc1 H_SCHP_%=\n"
  "v_mov_b32 v19, 0\n"
  "buffer_load_dword v19, v19, s[40:43], 0 offen sc1\n"
  "s_waitcnt vmcnt(0)\n"
  "v_readfirstlane_b32 s60, v19\n"
  "s_cmp_lg_u32 s60, 0\n"
  "s_cbranch_scc1 H_EXIT_%=\n"
  "s_branch H_SCHP_%=\n"
  "H_SCHOK_%=:\n"
  "s_nop 7\n"
  "s_memrealtime s[64:65]\n"
  "s_waitcnt lgkmcnt(0)\n"
  "s_sub_i32 s60, s59, s64\n"
  "s_cmp_gt_i32 s60, 2000\n"
  "s_cselect_b32 s59, s64, s59\n"
  "s_cmp_lt_i32 s60, -100000\n"
  "s_cselect_b32 s59, s64, s59\n"
  "s_sub_i32 s60, s64, s59\n"
  "s_cmp_gt_i32 s60, 0\n"
  "s_cselect_b32 s59, s64, s59\n"
  "s_add_i32 s60, s60, 100\n"
  "s_max_i32 s60, s60, 0\n"
  "s_min_i32 s3, s60, 200\n"
  "s_cmp_lt_u32 s56, 4\n"
  "s_cselect_b32 s3, 0, s3\n"
  "v_xor_b32 v19, 16, v17\n"
  "s_barrier\n"
  "s_sub_u32 s0, s59, 19\n"
  "H_WT_%=:\n"
  "s_memrealtime s[64:65]\n"
  "s_waitcnt lgkmcnt(0)\n"
  "s_sub_i32 s60, s64, s0\n"
  "s_cmp_lt_i32 s60, 0\n"
  "s_cbranch_scc1 H_WT_%=\n"
  "s_cmp_gt_i32 s60, 10\n"
  "s_cbranch_scc1 H_WTC_%=\n"
  "s_memtime s[20:21]\n"
  "s_memrealtime s[22:23]\n"
  "s_nop 2\n"
  "s_memrealtime s[28:29]\n"
  "s_nop 2\n"
  "s_memrealtime s[30:31]\n"
  "s_nop 2\n"
  "s_memrealtime s[32:33]\n"
  "s_nop 2\n"
  "s_memrealtime s[34:35]\n"
  "s_nop 2\n"
  "s_memrealtime s[36:37]\n"
  "s_nop 2\n"
  "s_memrealtime s[38:39]\n"
  "s_mov_b32 s61, 0\n"
  "s_waitcnt lgkmcnt(0)\n"
  "s_cmp_eq_u32 s28, s22\n"
  "s_addc_u32 s61, s61, 0\n"
  "s_cmp_eq_u32 s30, s22\n"
  "s_addc_u32 s61, s61, 0\n"
  "s_cmp_eq_u32 s32, s22\n"
  "s_addc_u32 s61, s61, 0\n"
  "s_cmp_eq_u32 s34, s22\n"
  "s_addc_u32 s61, s61, 0\n"
  "s_cmp_eq_u32 s36, s22\n"
  "s_addc_u32 s61, s61, 0\n"
  "s_cmp_eq_u32 s38, s22\n"
  "s_addc_u32 s61, s61, 0\n"
  "s_sub_i32 s60, s59, s22\n"
  "s_sub_i32 s60, s60, 4\n"
  "s_mul_i32 s60, s60, s98\n"
  "s_ashr_i32 s60, s60, 4\n"
  "s_lshl4_add_u32 s60, s61, s60\n"
  "s_max_i32 s60, s60, 256\n"
  "s_min_i32 s60, s60, 508\n"
  "s_add_u32 s0, s20, s60\n"
  "s_and_b32 s60, s60, -4\n"
  "s_getpc_b64 s[64:65]\n"
  "s_sub_u32 s60, 532, s60\n"
  "s_add_u32 s64, s64, s60\n"
  "s_addc_u32 s65, s65, 0\n"
  "s_setpc_b64 s[64:65]\n"
  "s_nop 0\n"
  "s_nop 0\n"
  "s_nop 0\n"
  "s_nop 0\n"
  "s_nop 0\n"
  "s_nop 0\n"
  "s_nop 0\n"
  "s_nop 0\n"
  "s_nop 0\n"
  "s_nop 0\n"
  "s_nop 0\n"
  "s_nop 0\n"
  "s_nop 0\n"
  "s_nop 0\n"
  "s_nop 0\n"
  "s_nop 0\n"
  "s_nop 0\n"
  "s_nop 0\n"
  "s_nop 0\n"
  "s_nop 0\n"
  "s_nop 0\n"
  "s_nop 0\n"
  "s_nop 0\n"
  "s_nop 0\n"
  "s_nop 0\n"
  "s_nop 0\n"
  "s_nop 0\n"
  "s_nop 0\n"
  "s_nop 0\n"
  "s_nop 0\n"
  "s_nop 0\n"
  "s_nop 0\n"
  "s_nop 0\n"
  "s_nop 0\n"
  "s_nop 0\n"
  "s_nop 0\n"
  "s_nop 0\n"
  "s_nop 0\n"
  "s_nop 0\n"
  "s_nop 0\n"
  "s_nop 0\n"
  "s_nop 0\n"
  "s_nop 0\n"
  "s_nop 0\n"
  "s_nop 0\n"
  "s_nop 0\n"
  "s_nop 0\n"
  "s_nop 0\n"
  "s_nop 0\n"
  "s_nop 0\n"
  "s_nop 0\n"
  "s_nop 0\n"
  "s_nop 0\n"
  "s_nop 0\n"
  "s_nop 0\n"
  "s_nop 0\n"
  "s_nop 0\n"
  "s_nop 0\n"
  "s_nop 0\n"
  "s_nop 0\n"
  "s_nop 0\n"
  "s_nop 0\n"
  "s_nop 0\n"
  "s_nop 0\n"
  "H_WTK_%=:\n"
  "buffer_load_dwordx4 v[104:107], v6, s[16:19], s57 offen sc1\n"
  "v_mfma_f32_16x16x32_bf16 v[112:115], v[24:27], v[28:31], 0\n"
  "s_nop 1\n"
  "v_mfma_f32_16x16x32_bf16 v[116:119], v[24:27], v[40:43], 0\n"
  "s_nop 1\n"
  "v_mfma_f32_16x16x32_bf16 v[120:123], v[36:39], v[40:43], 0\n"
  "s_nop 1\n"
  "buffer_load_dwordx4 v[108:111], v6, s[16:19], s57 offen offset:1024 sc1\n"
  "v_mfma_f32_16x16x16_bf16 v[112:115], v[32:33], v[34:35], v[112:115]\n"
  "s_nop 1\n"
  "v_mfma_f32_16x16x16_bf16 v[116:119], v[32:33], v[46:47], v[116:119]\n"
  "s_nop 1\n"
  "v_mfma_f32_16x16x16_bf16 v[120:123], v[44:45], v[46:47], v[120:123]\n"
  "s_nop 1\n"
  "buffer_load_dwordx2 v[54:55], v7, s[16:19], s57 offen sc1\n"
  "ds_read_b128 v[80:83], v5 offset:0\n"
  "ds_read_b128 v[84:87], v5 offset:1024\n"
  "ds_read_b128 v[88:91], v5 offset:2048\n"
  "ds_read_b128 v[92:95], v5 offset:3072\n"
  "ds_read_b128 v[96:99], v5 offset:4096\n"
  "ds_read_b128 v[100:103], v5 offset:5120\n"
  "ds_write_b128 v17, v[112:115]\n"
  "ds_write_b128 v17, v[116:119] offset:2048\n"
  "ds_write_b128 v19, v[120:123] offset:2048\n"
  "s_waitcnt lgkmcnt(0)\n"
  "H_D0_%=:\n"
  "s_waitcnt vmcnt(2)\n"
  "v_smfmac_f32_16x16x64_bf16 v[56:59], v[104:107], a[0:7], v9\n"
  "ds_write_b128 v4, v[104:107] offset:10240\n"
  "v_smfmac_f32_16x16x64_bf16 v[60:63], v[104:107], a[8:15], v9\n"
  "v_cmp_eq_u32 vcc, -1, v104\n"
  "v_smfmac_f32_16x16x64_bf16 v[64:67], v[104:107], a[16:23], v9\n"
  "v_smfmac_f32_16x16x64_bf16 v[68:71], v[104:107], a[24:31], v9\n"
  "v_smfmac_f32_16x16x64_bf16 v[72:75], v[104:107], a[32:39], v9\n"
  "s_cbranch_vccnz H_FIX0_%=\n"
  "v_smfmac_f32_16x16x64_bf16 v[76:79], v[104:107], v[80:87], v9\n"
  "s_waitcnt lgkmcnt(0)\n"
  "s_barrier\n"
  "ds_read_b128 v[112:115], v1 offset:10240\n"
  "s_waitcnt vmcnt(1)\n"
  "v_smfmac_f32_16x16x64_bf16 v[56:59], v[108:111], a[40:47], v9\n"
  "ds_read_b128 v[116:119], v2 offset:10240\n"
  "v_smfmac_f32_16x16x64_bf16 v[60:63], v[108:111], a[48:55], v9\n"
  "ds_write_b128 v4, v[108:111] offset:14336\n"
  "v_smfmac_f32_16x16x64_bf16 v[64:67], v[108:111], a[56:63], v9\n"
  "v_smfmac_f32_16x16x64_bf16 v[68:71], v[108:111], a[64:71], v9\n"
  "s_waitcnt vmcnt(0)\n"
  "ds_write_b64 v8, v[54:55]\n"
  "v_smfmac_f32_16x16x64_bf16 v[72:75], v[108:111], a[72:79], v9\n"
  "ds_read_b128 v[80:83], v5 offset:6144\n"
  "v_smfmac_f32_16x16x64_bf16 v[76:79], v[108:111], v[88:95], v9\n"
  "ds_read_b128 v[84:87], v5 offset:7168\n"
  "s_waitcnt lgkmcnt(5)\n"
  "v_smfmac_f32_16x16x64_bf16 v[56:59], v[112:115], a[80:87], v9\n"
  "ds_read_b128 v[120:123], v3 offset:10240\n"
  "v_smfmac_f32_16x16x64_bf16 v[60:63], v[112:115], a[88:95], v9\n"
  "ds_read_b128 v[88:91], v5 offset:8192\n"
  "v_smfmac_f32_16x16x64_bf16 v[64:67], v[112:115], a[96:103], v9\n"
  "ds_read_b128 v[92:95], v5 offset:9216\n"
  "v_smfmac_f32_16x16x64_bf16 v[68:71], v[112:115], a[104:111], v9\n"
  "s_waitcnt lgkmcnt(5)\n"
  "s_barrier\n"
  "v_smfmac_f32_16x16x64_bf16 v[72:75], v[112:115], v[96:103], v9\n"
  "ds_read_b128 v[124:127], v1 offset:14336\n"
  "s_waitcnt lgkmcnt(4)\n"
  "v_smfmac_f32_16x16x64_bf16 v[76:79], v[112:115], v[80:87], v9\n"
  "ds_read_b128 v[104:107], v2 offset:14336\n"
  "v_smfmac_f32_16x16x64_bf16 v[56:59], v[116:119], a[112:119], v9\n"
  "ds_read_b128 v[96:99], v5 offset:10240\n"
  "v_smfmac_f32_16x16x64_bf16 v[60:63], v[116:119], a[120:127], v9\n"
  "ds_read_b128 v[100:103], v5 offset:11264\n"
  "v_smfmac_f32_16x16x64_bf16 v[64:67], v[116:119], a[128:135], v9\n"
  "ds_read_b128 v[80:83], v5 offset:12288\n"
  "v_smfmac_f32_16x16x64_bf16 v[68:71], v[116:119], a[136:143], v9\n"
  "ds_read_b128 v[84:87], v5 offset:13312\n"
  "v_smfmac_f32_16x16x64_bf16 v[72:75], v[116:119], a[144:151], v9\n"
  "ds_read_b128 v[108:111], v3 offset:14336\n"
  "s_waitcnt lgkmcnt(7)\n"
  "v_smfmac_f32_16x16x64_bf16 v[76:79], v[116:119], v[88:95], v9\n"
  "ds_read_b128 v[112:115], v0 offset:18432\n"
  "v_smfmac_f32_16x16x64_bf16 v[56:59], v[120:123], a[152:159], v9\n"
  "ds_read_b128 v[88:91], v5 offset:14336\n"
  "v_smfmac_f32_16x16x64_bf16 v[60:63], v[120:123], a[160:167], v9\n"
  "ds_read_b128 v[92:95], v5 offset:15360\n"
  "v_smfmac_f32_16x16x64_bf16 v[64:67], v[120:123], a[168:175], v9\n"
  "ds_read_b128 v[116:119], v0 offset:19456\n"
  "v_smfmac_f32_16x16x64_bf16 v[68:71], v[120:123], a[176:183], v9\n"
  "s_waitcnt lgkmcnt(7)\n"
  "v_smfmac_f32_16x16x64_bf16 v[72:75], v[120:123], v[96:103], v9\n"
  "s_waitcnt lgkmcnt(5)\n"
  "v_smfmac_f32_16x16x64_bf16 v[76:79], v[120:123], v[80:87], v9\n"
  "v_smfmac_f32_16x16x64_bf16 v[56:59], v[124:127], a[184:191], v9\n"
  "ds_read_b128 v[96:99], v5 offset:18432\n"
  "s_waitcnt lgkmcnt(2)\n"
  "v_smfmac_f32_16x16x64_bf16 v[68:71], v[124:127], v[88:95], v9\n"
  "ds_read_b128 v[100:103], v5 offset:19456\n"
  "v_smfmac_f32_16x16x64_bf16 v[56:59], v[104:107], a[224:231], v9\n"
  "ds_read_b128 v[80:83], v5 offset:16384\n"
  "v_smfmac_f32_16x16x64_bf16 v[68:71], v[104:107], a[248:255], v9\n"
  "ds_read_b128 v[84:87], v5 offset:17408\n"
  "v_smfmac_f32_16x16x64_bf16 v[56:59], v[108:111], v[136:143], v9\n"
  "ds_read_b128 v[88:91], v5 offset:20480\n"
  "s_waitcnt lgkmcnt(3)\n"
  "v_smfmac_f32_16x16x64_bf16 v[68:71], v[108:111], v[96:103], v9\n"
  "ds_read_b128 v[92:95], v5 offset:21504\n"
  "v_smfmac_f32_16x16x64_bf16 v[56:59], v[112:115], v[176:183], v9\n"
  "ds_read_b128 v[96:99], v5 offset:22528\n"
  "v_smfmac_f32_16x16x64_bf16 v[68:71], v[112:115], v[200:207], v9\n"
  "ds_read_b128 v[100:103], v5 offset:23552\n"
  "v_smfmac_f32_16x16x64_bf16 v[56:59], v[116:119], v[216:223], v9\n"
  "v_smfmac_f32_16x16x64_bf16 v[68:71], v[116:119], v[240:247], v9\n"
  "v_smfmac_f32_16x16x64_bf16 v[60:63], v[124:127], a[192:199], v9\n"
  "s_nop 1\n"
  "v_smfmac_f32_16x16x64_bf16 v[72:75], v[124:127], a[208:215], v9\n"
  "s_nop 1\n"
  "v_smfmac_f32_16x16x64_bf16 v[60:63], v[104:107], a[232:239], v9\n"
  "s_nop 1\n"
  "s_waitcnt lgkmcnt(4)\n"
  "v_smfmac_f32_16x16x64_bf16 v[72:75], v[104:107], v[80:87], v9\n"
  "v_cvt_pk_bf16_f32 v56, v56, v57\n"
  "v_cvt_pk_bf16_f32 v57, v58, v59\n"
  "v_smfmac_f32_16x16x64_bf16 v[60:63], v[108:111], v[144:151], v9\n"
  "v_cvt_pk_bf16_f32 v58, v68, v69\n"
  "v_cvt_pk_bf16_f32 v59, v70, v71\n"
  "v_smfmac_f32_16x16x64_bf16 v[72:75], v[108:111], v[160:167], v9\n"
  "s_nop 1\n"
  "v_mfma_f32_16x16x32_bf16 v[68:71], v[56:59], v[20:23], 0\n"
  "v_smfmac_f32_16x16x64_bf16 v[60:63], v[112:115], v[184:191], v9\n"
  "s_nop 1\n"
  "s_waitcnt lgkmcnt(2)\n"
  "v_smfmac_f32_16x16x64_bf16 v[72:75], v[112:115], v[88:95], v9\n"
  "s_nop 1\n"
  "v_smfmac_f32_16x16x64_bf16 v[60:63], v[116:119], v[224:231], v9\n"
  "s_nop 1\n"
  "v_smfmac_f32_16x16x64_bf16 v[72:75], v[116:119], v[248:255], v9\n"
  "v_cvt_pk_bf16_f32 v48, v68, v69\n"
  "v_cvt_pk_bf16_f32 v49, v70, v71\n"
  "v_smfmac_f32_16x16x64_bf16 v[64:67], v[124:127], a[200:207], v9\n"
  "v_cmp_u_f32 vcc, v68, v68\n"
  "s_nop 0\n"
  "v_smfmac_f32_16x16x64_bf16 v[76:79], v[124:127], a[216:223], v9\n"
  "s_andn2_b64 vcc, vcc, s[66:67]\n"
  "s_cbranch_vccnz H_REDO_%=\n"
  "v_smfmac_f32_16x16x64_bf16 v[64:67], v[104:107], a[240:247], v9\n"
  "v_cvt_pk_bf16_f32 v60, v60, v61\n"
  "v_cvt_pk_bf16_f32 v61, v62, v63\n"
  "v_smfmac_f32_16x16x64_bf16 v[76:79], v[104:107], v[128:135], v9\n"
  "v_cvt_pk_bf16_f32 v62, v72, v73\n"
  "v_cvt_pk_bf16_f32 v63, v74, v75\n"
  "v_smfmac_f32_16x16x64_bf16 v[64:67], v[108:111], v[152:159], v9\n"
  "s_nop 1\n"
  "v_mfma_f32_16x16x32_bf16 v[72:75], v[60:63], v[20:23], 0\n"
  "v_smfmac_f32_16x16x64_bf16 v[76:79], v[108:111], v[168:175], v9\n"
  "s_nop 1\n"
  "v_smfmac_f32_16x16x64_bf16 v[64:67], v[112:115], v[192:199], v9\n"
  "s_nop 1\n"
  "v_smfmac_f32_16x16x64_bf16 v[76:79], v[112:115], v[208:215], v9\n"
  "s_nop 1\n"
  "v_smfmac_f32_16x16x64_bf16 v[64:67], v[116:119], v[232:239], v9\n"
  "v_cvt_pk_bf16_f32 v50, v72, v73\n"
  "v_cvt_pk_bf16_f32 v51, v74, v75\n"
  "s_waitcnt lgkmcnt(0)\n"
  "v_smfmac_f32_16x16x64_bf16 v[76:79], v[116:119], v[96:103], v9\n"
  "v_mfma_f32_16x16x32_bf16 v[120:123], v[24:27], v[48:51], 0\n"
  "s_nop 1\n"
  "v_mfma_f32_16x16x32_bf16 v[56:59], v[36:39], v[48:51], 0\n"
  "s_nop 1\n"
  "v_cvt_pk_bf16_f32 v64, v64, v65\n"
  "v_cvt_pk_bf16_f32 v65, v66, v67\n"
  "v_cvt_pk_bf16_f32 v66, v76, v77\n"
  "v_cvt_pk_bf16_f32 v67, v78, v79\n"
  "s_nop 1\n"
  "v_mfma_f32_16x16x32_bf16 v[76:79], v[64:67], v[20:23], 0\n"
  "v_mov_b32 v0, v0\n"
  "v_mov_b32 v0, v0\n"
  "v_mfma_f32_16x16x32_bf16 v[68:71], v[48:51], v[48:51], 0\n"
  "v_mov_b32 v0, v0\n"
  "v_mov_b32 v0, v0\n"
  "v_mov_b32 v0, v0\n"
  "v_mov_b32 v0, v0\n"
  "v_mov_b32 v0, v0\n"
  "v_cvt_pk_bf16_f32 v52, v76, v77\n"
  "v_cvt_pk_bf16_f32 v53, v78, v79\n"
  "s_nop 1\n"
  "v_mfma_f32_16x16x16_bf16 v[56:59], v[44:45], v[52:53], v[56:59]\n"
  "s_nop 1\n"
  "v_mfma_f32_16x16x16_bf16 v[120:123], v[32:33], v[52:53], v[120:123]\n"
  "s_nop 1\n"
  "v_mfma_f32_16x16x16_bf16 v[68:71], v[52:53], v[52:53], v[68:71]\n"
  "v_mov_b32 v0, v0\n"
  "v_mov_b32 v0, v0\n"
  "v_mov_b32 v0, v0\n"
  "v_mov_b32 v0, v0\n"
  "v_mov_b32 v0, v0\n"
  "v_mov_b32_dpp v120, v56 row_ror:8 row_mask:0xf bank_mask:0xc\n"
  "v_mov_b32_dpp v121, v57 row_ror:8 row_mask:0xf bank_mask:0xc\n"
  "v_mov_b32_dpp v122, v58 row_ror:8 row_mask:0xf bank_mask:0xc\n"
  "v_mov_b32_dpp v123, v59 row_ror:8 row_mask:0xf bank_mask:0xc\n"
  "ds_write_b128 v10, v[120:123]\n"
  "v_mov_b32_dpp v68, v70 row_ror:8 row_mask:0xf bank_mask:0xc\n"
  "v_mov_b32_dpp v69, v71 row_ror:8 row_mask:0xf bank_mask:0xc\n"
  "ds_write_b64 v12, v[68:69]\n"
  "v_mfma_f32_16x16x32_bf16 v[104:107], v[48:51], v[28:31], 0\n"
  "v_mfma_f32_16x16x32_bf16 v[108:111], v[48:51], v[40:43], 0\n"
  "v_lshrrev_b32 v124, 3, v0\n"
  "v_lshrrev_b32 v126, 2, v4\n"
  "s_waitcnt lgkmcnt(0)\n"
  "s_barrier\n"
  "ds_read_b128 v[56:59], v13\n"
  "ds_read_b128 v[60:63], v13 offset:1024\n"
  "v_and_b32 v124, 112, v124\n"
  "ds_read2st64_b32 v[64:65], v14 offset0:0 offset1:1\n"
  "v_mfma_f32_16x16x16_bf16 v[104:107], v[52:53], v[34:35], v[104:107]\n"
  "v_mfma_f32_16x16x16_bf16 v[108:111], v[52:53], v[46:47], v[108:111]\n"
  "v_add_u32 v126, 57344, v126\n"
  "s_waitcnt lgkmcnt(1)\n"
  "v_pk_add_f32 v[56:57], v[56:57], v[60:61]\n"
  "v_pk_add_f32 v[58:59], v[58:59], v[62:63]\n"
  "s_waitcnt lgkmcnt(0)\n"
  "v_add_f32 v64, v64, v65\n"
  "v_add_f32_dpp v56, v56, v56 row_ror:8 row_mask:0xf bank_mask:0xf\n"
  "v_add_f32_dpp v57, v57, v57 row_ror:8 row_mask:0xf bank_mask:0xf\n"
  "v_add_f32_dpp v58, v58, v58 row_ror:8 row_mask:0xf bank_mask:0xf\n"
  "v_add_f32_dpp v59, v59, v59 row_ror:8 row_mask:0xf bank_mask:0xf\n"
  "v_add_f32_dpp v64, v64, v64 row_ror:8 row_mask:0xf bank_mask:0xf\n"
  "v_max3_f32 v68, v56, v57, v64\n"
  "v_max3_f32 v68, v58, v59, v68\n"
  "v_mul_f32 v70, s76, v56\n"
  "v_mul_f32 v71, s76, v57\n"
  "v_max_f32_dpp v68, v68, v68 quad_perm:[1,0,3,2] row_mask:0xf bank_mask:0xf\n"
  "v_mul_f32 v72, s76, v58\n"
  "v_mul_f32 v73, s76, v59\n"
  "v_max_f32_dpp v68, v68, v68 quad_perm:[2,3,0,1] row_mask:0xf bank_mask:0xf\n"
  "v_mul_f32 v74, s76, v64\n"
  "v_xor_b32 v125, v124, v4\n"
  "v_max_f32_dpp v68, v68, v68 row_half_mirror row_mask:0xf bank_mask:0xf\n"
  "v_fma_f32 v70, -v68, s76, v70\n"
  "v_fma_f32 v71, -v68, s76, v71\n"
  "v_fma_f32 v72, -v68, s76, v72\n"
  "v_fma_f32 v73, -v68, s76, v73\n"
  "v_fma_f32 v74, -v68, s76, v74\n"
  "v_exp_f32 v74, v74\n"
  "v_exp_f32 v70, v70\n"
  "v_exp_f32 v71, v71\n"
  "v_exp_f32 v72, v72\n"
  "v_exp_f32 v73, v73\n"
  "v_add_f32 v66, v74, v70\n"
  "v_add_f32 v67, v71, v72\n"
  "v_add_f32 v66, v66, v73\n"
  "v_add_f32 v68, v66, v67\n"
  "v_mov_b32_dpp v75, v74 quad_perm:[1,0,3,2] row_mask:0xf bank_mask:0xf\n"
  "ds_read_b128 v[40:43], v4 offset:0\n"
  "v_add_f32_dpp v68, v68, v68 quad_perm:[1,0,3,2] row_mask:0xf bank_mask:0xf\n"
  "ds_read_b128 v[44:47], v4 offset:20480\n"
  "ds_read_b128 v[80:83], v5 offset:0\n"
  "v_add_f32_dpp v68, v68, v68 quad_perm:[2,3,0,1] row_mask:0xf bank_mask:0xf\n"
  "ds_read_b128 v[84:87], v5 offset:1024\n"
  "v_add_u32 v125, 40960, v125\n"
  "v_add_f32_dpp v68, v68, v68 row_half_mirror row_mask:0xf bank_mask:0xf\n"
  "v_rcp_f32 v68, v68\n"
  "v_permlane32_swap_b32 v104, v108\n"
  "v_pk_mul_f32 v[70:71], v[70:71], v[68:69] op_sel_hi:[1,0]\n"
  "v_pk_mul_f32 v[72:73], v[72:73], v[68:69] op_sel_hi:[1,0]\n"
  "v_pk_mul_f32 v[74:75], v[74:75], v[68:69] op_sel_hi:[1,0]\n"
  "v_cvt_pk_bf16_f32 v66, |v70|, |v71|\n"
  "v_cvt_pk_bf16_f32 v67, |v72|, |v73|\n"
  "v_cvt_pk_bf16_f32 v68, |v74|, |v75|\n"
  "buffer_store_dwordx2 v[66:67], v15, s[16:19], s63 offen sc1\n"
  "buffer_store_dword v68, v16, s[16:19], s63 offen sc1\n"
  "s_memtime s[64:65]\n"
  "s_memrealtime s[20:21]\n"
  "s_waitcnt lgkmcnt(0)\n"
  "v_mfma_f32_16x16x32_bf16 v[56:59], a[0:3], v[40:43], 0\n"
  "ds_read_b128 v[48:51], v4 offset:4096\n"
  "s_and_b32 s71, s56, 7\n"
  "v_mfma_f32_16x16x32_bf16 v[60:63], a[8:11], v[40:43], 0\n"
  "ds_read_b128 v[52:55], v4 offset:24576\n"
  "s_xor_b32 s75, s71, 4\n"
  "v_mfma_f32_16x16x32_bf16 v[64:67], a[16:19], v[40:43], 0\n"
  "ds_read_b128 v[88:91], v5 offset:2048\n"
  "s_mul_i32 s71, s71, 81920\n"
  "v_mfma_f32_16x16x32_bf16 v[68:71], a[24:27], v[40:43], 0\n"
  "ds_read_b128 v[92:95], v5 offset:3072\n"
  "s_mul_i32 s75, s75, 81920\n"
  "v_mfma_f32_16x16x32_bf16 v[72:75], a[32:35], v[40:43], 0\n"
  "s_add_u32 s68, s71, s49\n"
  "s_add_u32 s71, s71, s53\n"
  "v_mfma_f32_16x16x32_bf16 v[76:79], v[80:83], v[40:43], 0\n"
  "s_add_u32 s74, s75, s49\n"
  "s_add_u32 s75, s75, s53\n"
  "v_mfma_f32_16x16x32_bf16 v[56:59], a[4:7], v[44:47], v[56:59]\n"
  "s_xor_b32 s63, s63, 524288\n"
  "s_or_b32 s66, s66, s52\n"
  "v_mfma_f32_16x16x32_bf16 v[60:63], a[12:15], v[44:47], v[60:63]\n"
  "s_cmp_lt_u32 s56, 2\n"
  "s_cselect_b64 s[66:67], -1, s[66:67]\n"
  "v_mfma_f32_16x16x32_bf16 v[64:67], a[20:23], v[44:47], v[64:67]\n"
  "s_cmp_eq_u64 s[66:67], 0\n"
  "s_cselect_b32 s69, s2, 0x80000000\n"
  "v_mfma_f32_16x16x32_bf16 v[68:71], a[28:31], v[44:47], v[68:71]\n"
  "v_permlane32_swap_b32 v105, v109\n"
  "v_mfma_f32_16x16x32_bf16 v[72:75], a[36:39], v[44:47], v[72:75]\n"
  "v_permlane32_swap_b32 v106, v110\n"
  "v_mfma_f32_16x16x32_bf16 v[76:79], v[84:87], v[44:47], v[76:79]\n"
  "v_permlane32_swap_b32 v107, v111\n"
  "s_waitcnt lgkmcnt(3)\n"
  "v_mfma_f32_16x16x32_bf16 v[56:59], a[40:43], v[48:51], v[56:59]\n"
  "ds_read_b128 v[40:43], v1 offset:0\n"
  "ds_write_b128 v18, v[108:111]\n"
  "v_mfma_f32_16x16x32_bf16 v[60:63], a[48:51], v[48:51], v[60:63]\n"
  "ds_read_b128 v[44:47], v1 offset:20480\n"
  "v_mfma_f32_16x16x32_bf16 v[64:67], a[56:59], v[48:51], v[64:67]\n"
  "ds_read_b128 v[96:99], v5 offset:4096\n"
  "v_mfma_f32_16x16x32_bf16 v[68:71], a[64:67], v[48:51], v[68:71]\n"
  "ds_read_b128 v[100:103], v5 offset:5120\n"
  "v_mfma_f32_16x16x32_bf16 v[72:75], a[72:75], v[48:51], v[72:75]\n"
  "ds_read_b128 v[80:83], v5 offset:6144\n"
  "s_waitcnt lgkmcnt(7)\n"
  "v_mfma_f32_16x16x32_bf16 v[76:79], v[88:91], v[48:51], v[76:79]\n"
  "ds_read_b128 v[84:87], v5 offset:7168\n"
  "v_mfma_f32_16x16x32_bf16 v[56:59], a[44:47], v[52:55], v[56:59]\n"
  "s_waitcnt lgkmcnt(0)\n"
  "s_barrier\n"
  "v_mfma_f32_16x16x32_bf16 v[60:63], a[52:55], v[52:55], v[60:63]\n"
  "ds_read_b128 v[112:115], v125 offset:8192\n"
  "ds_read_b128 v[116:119], v125 offset:12288\n"
  "v_mfma_f32_16x16x32_bf16 v[64:67], a[60:63], v[52:55], v[64:67]\n"
  "ds_read_b128 v[108:111], v125 offset:4096\n"
  "ds_read_b128 v[104:107], v125\n"
  "v_mfma_f32_16x16x32_bf16 v[68:71], a[68:71], v[52:55], v[68:71]\n"
  "ds_read2st64_b32 v[120:121], v126 offset0:0 offset1:4\n"
  "ds_read2st64_b32 v[122:123], v126 offset0:8 offset1:12\n"
  "v_mfma_f32_16x16x32_bf16 v[72:75], a[76:79], v[52:55], v[72:75]\n"
  "v_mfma_f32_16x16x32_bf16 v[76:79], v[92:95], v[52:55], v[76:79]\n"
  "v_mfma_f32_16x16x32_bf16 v[56:59], a[80:83], v[40:43], v[56:59]\n"
  "ds_read_b128 v[48:51], v2 offset:0\n"
  "v_mfma_f32_16x16x32_bf16 v[60:63], a[88:91], v[40:43], v[60:63]\n"
  "ds_read_b128 v[52:55], v2 offset:20480\n"
  "v_and_b32 v127, 96, v0\n"
  "v_mfma_f32_16x16x32_bf16 v[64:67], a[96:99], v[40:43], v[64:67]\n"
  "ds_read_b128 v[88:91], v5 offset:8192\n"
  "v_lshl_add_u32 v124, v127, 2, v124\n"
  "v_mfma_f32_16x16x32_bf16 v[68:71], a[104:107], v[40:43], v[68:71]\n"
  "ds_read_b128 v[92:95], v5 offset:9216\n"
  "v_bfe_u32 v127, v0, 4, 1\n"
  "v_mfma_f32_16x16x32_bf16 v[72:75], v[96:99], v[40:43], v[72:75]\n"
  "v_mfma_f32_16x16x32_bf16 v[76:79], v[80:83], v[40:43], v[76:79]\n"
  "s_waitcnt lgkmcnt(8)\n"
  "v_add_f32 v112, v112, v116\n"
  "v_add_f32 v113, v113, v117\n"
  "v_add_f32 v114, v114, v118\n"
  "v_mfma_f32_16x16x32_bf16 v[56:59], a[84:87], v[44:47], v[56:59]\n"
  "v_add_f32 v115, v115, v119\n"
  "s_waitcnt lgkmcnt(6)\n"
  "v_add_f32 v104, v104, v108\n"
  "v_add_f32 v105, v105, v109\n"
  "v_mfma_f32_16x16x32_bf16 v[60:63], a[92:95], v[44:47], v[60:63]\n"
  "v_add_f32 v106, v106, v110\n"
  "v_add_f32 v107, v107, v111\n"
  "s_waitcnt lgkmcnt(4)\n"
  "v_add_f32 v120, v120, v122\n"
  "v_mfma_f32_16x16x32_bf16 v[64:67], a[100:103], v[44:47], v[64:67]\n"
  "v_add_f32 v121, v121, v123\n"
  "v_add_f32 v104, v104, v112\n"
  "v_add_f32 v105, v105, v113\n"
  "v_mfma_f32_16x16x32_bf16 v[68:71], a[108:111], v[44:47], v[68:71]\n"
  "v_add_f32 v106, v106, v114\n"
  "v_add_f32 v107, v107, v115\n"
  "v_add_f32 v120, v120, v121\n"
  "v_mfma_f32_16x16x32_bf16 v[72:75], v[100:103], v[44:47], v[72:75]\n"
  "v_mul_f32 v112, s76, v104\n"
  "v_mul_f32 v113, s76, v105\n"
  "v_mul_f32 v114, s76, v106\n"
  "v_mfma_f32_16x16x32_bf16 v[76:79], v[84:87], v[44:47], v[76:79]\n"
  "v_mul_f32 v115, s76, v107\n"
  "v_mul_f32 v116, s76, v120\n"
  "v_mov_b32 v108, s77\n"
  "s_waitcnt lgkmcnt(3)\n"
  "v_mfma_f32_16x16x32_bf16 v[56:59], a[112:115], v[48:51], v[56:59]\n"
  "ds_read_b128 v[40:43], v3 offset:0\n"
  "v_cndmask_b32 v112, v108, v112, s[8:9]\n"
  "v_cndmask_b32 v113, v108, v113, s[10:11]\n"
  "v_mfma_f32_16x16x32_bf16 v[60:63], a[120:123], v[48:51], v[60:63]\n"
  "ds_read_b128 v[44:47], v3 offset:20480\n"
  "v_cndmask_b32 v114, v108, v114, s[12:13]\n"
  "v_cndmask_b32 v115, v108, v115, s[14:15]\n"
  "v_mfma_f32_16x16x32_bf16 v[64:67], a[128:131], v[48:51], v[64:67]\n"
  "ds_read_b128 v[96:99], v5 offset:10240\n"
  "v_max3_f32 v110, v112, v113, v116\n"
  "v_max3_f32 v110, v114, v115, v110\n"
  "v_mfma_f32_16x16x32_bf16 v[68:71], a[136:139], v[48:51], v[68:71]\n"
  "ds_read_b128 v[100:103], v5 offset:11264\n"
  "v_lshl_add_u32 v124, v127, 3, v124\n"
  "v_max_f32_dpp v110, v110, v110 quad_perm:[1,0,3,2] row_mask:0xf bank_mask:0xf\n"
  "v_mfma_f32_16x16x32_bf16 v[72:75], a[144:147], v[48:51], v[72:75]\n"
  "ds_read_b128 v[80:83], v5 offset:12288\n"
  "v_and_or_b32 v124, v11, s48, v124\n"
  "v_max_f32_dpp v110, v110, v110 quad_perm:[2,3,0,1] row_mask:0xf bank_mask:0xf\n"
  "s_waitcnt lgkmcnt(6)\n"
  "v_mfma_f32_16x16x32_bf16 v[76:79], v[88:91], v[48:51], v[76:79]\n"
  "ds_read_b128 v[84:87], v5 offset:13312\n"
  "v_mov_b64 v[122:123], -1\n"
  "v_mfma_f32_16x16x32_bf16 v[56:59], a[116:119], v[52:55], v[56:59]\n"
  "v_max_f32_dpp v110, v110, v110 row_half_mirror row_mask:0xf bank_mask:0xf\n"
  "v_sub_f32 v112, v112, v110\n"
  "v_sub_f32 v113, v113, v110\n"
  "v_mfma_f32_16x16x32_bf16 v[60:63], a[124:127], v[52:55], v[60:63]\n"
  "v_sub_f32 v114, v114, v110\n"
  "v_sub_f32 v115, v115, v110\n"
  "v_sub_f32 v116, v116, v110\n"
  "v_mfma_f32_16x16x32_bf16 v[64:67], a[132:135], v[52:55], v[64:67]\n"
  "v_exp_f32 v116, v116\n"
  "v_exp_f32 v112, v112\n"
  "v_mfma_f32_16x16x32_bf16 v[68:71], a[140:143], v[52:55], v[68:71]\n"
  "v_exp_f32 v113, v113\n"
  "v_exp_f32 v114, v114\n"
  "v_mfma_f32_16x16x32_bf16 v[72:75], a[148:151], v[52:55], v[72:75]\n"
  "v_exp_f32 v115, v115\n"
  "v_add_f32 v108, v116, v112\n"
  "s_waitcnt lgkmcnt(6)\n"
  "v_mfma_f32_16x16x32_bf16 v[76:79], v[92:95], v[52:55], v[76:79]\n"
  "v_add_f32 v109, v113, v114\n"
  "v_add_f32 v108, v108, v115\n"
  "v_add_f32 v110, v108, v109\n"
  "s_waitcnt lgkmcnt(5)\n"
  "v_mfma_f32_16x16x32_bf16 v[56:59], a[152:155], v[40:43], v[56:59]\n"
  "ds_read_b128 v[48:51], v1 offset:4096\n"
  "v_mov_b32_dpp v117, v116 quad_perm:[1,0,3,2] row_mask:0xf bank_mask:0xf\n"
  "v_add_f32_dpp v110, v110, v110 quad_perm:[1,0,3,2] row_mask:0xf bank_mask:0xf\n"
  "v_mfma_f32_16x16x32_bf16 v[60:63], a[160:163], v[40:43], v[60:63]\n"
  "ds_read_b128 v[52:55], v1 offset:24576\n"
  "s_sub_u32 s60, s0, s96\n"
  "v_add_f32_dpp v110, v110, v110 quad_perm:[2,3,0,1] row_mask:0xf bank_mask:0xf\n"
  "v_mfma_f32_16x16x32_bf16 v[64:67], a[168:171], v[40:43], v[64:67]\n"
  "ds_read_b128 v[88:91], v5 offset:14336\n"
  "s_sub_u32 s61, s59, s97\n"
  "v_add_f32_dpp v110, v110, v110 row_half_mirror row_mask:0xf bank_mask:0xf\n"
  "v_mfma_f32_16x16x32_bf16 v[68:71], a[176:179], v[40:43], v[68:71]\n"
  "ds_read_b128 v[92:95], v5 offset:15360\n"
  "v_rcp_f32 v110, v110\n"
  "s_waitcnt lgkmcnt(7)\n"
  "v_mfma_f32_16x16x32_bf16 v[72:75], v[96:99], v[40:43], v[72:75]\n"
  "v_mul_f32 v112, v112, v110\n"
  "v_mul_f32 v113, v113, v110\n"
  "v_mul_f32 v114, v114, v110\n"
  "s_waitcnt lgkmcnt(5)\n"
  "v_mfma_f32_16x16x32_bf16 v[76:79], v[80:83], v[40:43], v[76:79]\n"
  "v_mul_f32 v115, v115, v110\n"
  "v_mul_f32 v116, v116, v110\n"
  "v_mul_f32 v117, v117, v110\n"
  "v_mfma_f32_16x16x32_bf16 v[56:59], a[156:159], v[44:47], v[56:59]\n"
  "v_cvt_pk_bf16_f32 v108, |v112|, |v113|\n"
  "v_cvt_pk_bf16_f32 v109, |v114|, |v115|\n"
  "v_cvt_pk_bf16_f32 v110, |v116|, |v117|\n"
  "v_mfma_f32_16x16x32_bf16 v[60:63], a[164:167], v[44:47], v[60:63]\n"
  "buffer_store_dwordx2 v[108:109], v124, s[16:19], s71 offen sc1\n"
  "buffer_store_dword v110, v11, s[16:19], s68 offen sc1\n"
  "v_mfma_f32_16x16x32_bf16 v[64:67], a[172:175], v[44:47], v[64:67]\n"
  "s_add_u32 s72, s99, 2\n"
  "v_mfma_f32_16x16x32_bf16 v[68:71], a[180:183], v[44:47], v[68:71]\n"
  "s_mul_i32 s72, s72, 40960\n"
  "s_add_u32 s73, s51, 10240\n"
  "v_mfma_f32_16x16x32_bf16 v[72:75], v[100:103], v[44:47], v[72:75]\n"
  "s_add_u32 s72, s72, s73\n"
  "s_mov_b32 m0, s73\n"
  "s_waitcnt lgkmcnt(4)\n"
  "v_mfma_f32_16x16x32_bf16 v[76:79], v[84:87], v[44:47], v[76:79]\n"
  "buffer_load_dwordx4 v0, s[24:27], s72 offen offset:0 sc1 lds\n"
  "s_waitcnt lgkmcnt(3)\n"
  "v_mfma_f32_16x16x32_bf16 v[56:59], a[184:187], v[48:51], v[56:59]\n"
  "ds_read_b128 v[40:43], v2 offset:4096\n"
  "buffer_load_dwordx4 v0, s[24:27], s72 offen offset:1024 sc1 lds\n"
  "v_mfma_f32_16x16x32_bf16 v[60:63], a[192:195], v[48:51], v[60:63]\n"
  "ds_read_b128 v[44:47], v2 offset:24576\n"
  "buffer_load_dwordx4 v0, s[24:27], s72 offen offset:2048 sc1 lds\n"
  "v_mfma_f32_16x16x32_bf16 v[64:67], a[200:203], v[48:51], v[64:67]\n"
  "ds_read_b128 v[96:99], v5 offset:16384\n"
  "buffer_load_dwordx4 v0, s[24:27], s72 offen offset:3072 sc1 lds\n"
  "s_waitcnt lgkmcnt(4)\n"
  "v_mfma_f32_16x16x32_bf16 v[68:71], v[88:91], v[48:51], v[68:71]\n"
  "ds_read_b128 v[100:103], v5 offset:17408\n"
  "s_add_u32 s72, s72, 4096\n"
  "v_mfma_f32_16x16x32_bf16 v[72:75], a[208:211], v[48:51], v[72:75]\n"
  "s_add_u32 m0, s73, 4096\n"
  "s_nop 0\n"
  "buffer_load_dwordx4 v0, s[24:27], s72 offen offset:0 sc1 lds\n"
  "v_mfma_f32_16x16x32_bf16 v[76:79], a[216:219], v[48:51], v[76:79]\n"
  "s_add_u32 s70, s99, 4\n"
  "s_lshl_b32 s70, s70, 7\n"
  "v_mfma_f32_16x16x32_bf16 v[56:59], a[188:191], v[52:55], v[56:59]\n"
  "s_add_u32 s70, s70, 1835008\n"
  "buffer_load_dword v19, off, s[16:19], s70 sc1\n"
  "v_mfma_f32_16x16x32_bf16 v[60:63], a[196:199], v[52:55], v[60:63]\n"
  "v_cvt_f32_u32 v118, s61\n"
  "v_cvt_f32_u32 v119, s60\n"
  "v_mfma_f32_16x16x32_bf16 v[64:67], a[204:207], v[52:55], v[64:67]\n"
  "v_rcp_f32 v118, v118\n"
  "s_waitcnt lgkmcnt(4)\n"
  "v_mfma_f32_16x16x32_bf16 v[68:71], v[92:95], v[52:55], v[68:71]\n"
  "s_mov_b32 s96, s0\n"
  "s_mov_b32 s97, s59\n"
  "v_mfma_f32_16x16x32_bf16 v[72:75], a[212:215], v[52:55], v[72:75]\n"
  "v_mul_f32 v118, v118, v119\n"
  "v_min_f32 v118, 0x41c80000, v118\n"
  "v_mfma_f32_16x16x32_bf16 v[76:79], a[220:223], v[52:55], v[76:79]\n"
  "v_max_f32 v118, 0x41600000, v118\n"
  "v_mul_f32 v119, 0x41800000, v118\n"
  "s_waitcnt lgkmcnt(3)\n"
  "v_mfma_f32_16x16x32_bf16 v[56:59], a[224:227], v[40:43], v[56:59]\n"
  "ds_read_b128 v[48:51], v3 offset:4096\n"
  "v_rcp_f32 v118, v118\n"
  "v_mfma_f32_16x16x32_bf16 v[60:63], a[232:235], v[40:43], v[60:63]\n"
  "ds_read_b128 v[52:55], v3 offset:24576\n"
  "v_cvt_u32_f32 v119, v119\n"
  "v_mfma_f32_16x16x32_bf16 v[64:67], a[240:243], v[40:43], v[64:67]\n"
  "ds_read_b128 v[80:83], v5 offset:18432\n"
  "s_sub_i32 s60, s64, s0\n"
  "v_mfma_f32_16x16x32_bf16 v[68:71], a[248:251], v[40:43], v[68:71]\n"
  "ds_read_b128 v[84:87], v5 offset:19456\n"
  "s_lshl_b32 s60, s60, 2\n"
  "s_waitcnt lgkmcnt(5)\n"
  "v_mfma_f32_16x16x32_bf16 v[72:75], v[96:99], v[40:43], v[72:75]\n"
  "v_readfirstlane_b32 s98, v119\n"
  "v_cvt_f32_i32 v119, s60\n"
  "v_mfma_f32_16x16x32_bf16 v[76:79], v[128:131], v[40:43], v[76:79]\n"
  "s_sub_i32 s60, s20, s59\n"
  "v_mul_f32 v118, v118, v119\n"
  "v_mfma_f32_16x16x32_bf16 v[56:59], a[228:231], v[44:47], v[56:59]\n"
  "s_lshl_b32 s60, s60, 2\n"
  "v_cvt_i32_f32 v118, v118\n"
  "v_mfma_f32_16x16x32_bf16 v[60:63], a[236:239], v[44:47], v[60:63]\n"
  "s_add_u32 s61, s60, 16\n"
  "v_max_i32 v118, s60, v118\n"
  "v_mfma_f32_16x16x32_bf16 v[64:67], a[244:247], v[44:47], v[64:67]\n"
  "v_min_i32 v118, s61, v118\n"
  "v_mov_b32 v119, s3\n"
  "v_mfma_f32_16x16x32_bf16 v[68:71], a[252:255], v[44:47], v[68:71]\n"
  "v_mov_b32 v127, s69\n"
  "s_mov_b64 exec, 1\n"
  "buffer_store_dwordx2 v[118:119], v127, s[16:19], 0 offen sc1\n"
  "s_mov_b64 exec, -1\n"
  "s_waitcnt lgkmcnt(4)\n"
  "v_mfma_f32_16x16x32_bf16 v[72:75], v[100:103], v[44:47], v[72:75]\n"
  "buffer_store_dwordx2 v[122:123], v124, s[16:19], s75 offen sc1\n"
  "v_mfma_f32_16x16x32_bf16 v[76:79], v[132:135], v[44:47], v[76:79]\n"
  "buffer_store_dword v122, v11, s[16:19], s74 offen sc1\n"
  "s_waitcnt lgkmcnt(3)\n"
  "v_mfma_f32_16x16x32_bf16 v[56:59], v[136:139], v[48:51], v[56:59]\n"
  "ds_read_b128 v[40:43], v0 offset:8192\n"
  "buffer_store_dwordx2 v[122:123], v15, s[16:19], s63 offen sc1\n"
  "v_mfma_f32_16x16x32_bf16 v[60:63], v[144:147], v[48:51], v[60:63]\n"
  "ds_read_b128 v[44:47], v0 offset:28672\n"
  "buffer_store_dword v122, v16, s[16:19], s63 offen sc1\n"
  "v_mfma_f32_16x16x32_bf16 v[64:67], v[152:155], v[48:51], v[64:67]\n"
  "ds_read_b128 v[88:91], v5 offset:20480\n"
  "s_waitcnt lgkmcnt(4)\n"
  "v_mfma_f32_16x16x32_bf16 v[68:71], v[80:83], v[48:51], v[68:71]\n"
  "ds_read_b128 v[92:95], v5 offset:21504\n"
  "v_mfma_f32_16x16x32_bf16 v[72:75], v[160:163], v[48:51], v[72:75]\n"
  "v_mfma_f32_16x16x32_bf16 v[76:79], v[168:171], v[48:51], v[76:79]\n"
  "v_mfma_f32_16x16x32_bf16 v[56:59], v[140:143], v[52:55], v[56:59]\n"
  "v_mfma_f32_16x16x32_bf16 v[60:63], v[148:151], v[52:55], v[60:63]\n"
  "v_mfma_f32_16x16x32_bf16 v[64:67], v[156:159], v[52:55], v[64:67]\n"
  "s_waitcnt lgkmcnt(4)\n"
  "v_mfma_f32_16x16x32_bf16 v[68:71], v[84:87], v[52:55], v[68:71]\n"
  "v_mfma_f32_16x16x32_bf16 v[72:75], v[164:167], v[52:55], v[72:75]\n"
  "v_mfma_f32_16x16x32_bf16 v[76:79], v[172:175], v[52:55], v[76:79]\n"
  "s_waitcnt lgkmcnt(0)\n"
  "s_add_u32 s56, s56, 2\n"
  "s_add_u32 s99, s99, 2\n"
  "s_cmp_lt_u32 s56, 64\n"
  "s_cbranch_scc1 H_LOOP_%=\n"
  "s_waitcnt vmcnt(0)\n"
  "s_add_u32 s46, s46, 64\n"
  "s_cmp_lt_u32 s46, 128\n"
  "s_cbranch_scc1 H_LAYER_%=\n"
  "s_branch H_EXIT_%=\n"
  "H_WTC_%=:\n"
  "H_WTD_%=:\n"
  "s_memrealtime s[64:65]\n"
  "s_waitcnt lgkmcnt(0)\n"
  "s_sub_i32 s60, s64, s59\n"
  "s_cmp_lt_i32 s60, 0\n"
  "s_cbranch_scc1 H_WTD_%=\n"
  "s_mov_b32 s59, s64\n"
  "s_memtime s[64:65]\n"
  "s_waitcnt lgkmcnt(0)\n"
  "s_mov_b32 s0, s64\n"
  "s_branch H_WTK_%=\n"
  "H_REDO_%=:\n"
  "s_nop 7\n"
  "s_waitcnt vmcnt(0) lgkmcnt(0)\n"
  "s_barrier\n"
  "s_mov_b32 s62, 0\n"
  "s_mov_b64 s[66:67], -1\n"
  "v_mov_b64 v[56:57], 0\n"
  "v_mov_b64 v[58:59], 0\n"
  "v_mov_b64 v[60:61], 0\n"
  "v_mov_b64 v[62:63], 0\n"
  "v_mov_b64 v[64:65], 0\n"
  "v_mov_b64 v[66:67], 0\n"
  "v_mov_b64 v[68:69], 0\n"
  "v_mov_b64 v[70:71], 0\n"
  "v_mov_b64 v[72:73], 0\n"
  "v_mov_b64 v[74:75], 0\n"
  "v_mov_b64 v[76:77], 0\n"
  "v_mov_b64 v[78:79], 0\n"
  "ds_read_b128 v[80:83], v5 offset:0\n"
  "ds_read_b128 v[84:87], v5 offset:1024\n"
  "ds_read_b128 v[88:91], v5 offset:2048\n"
  "ds_read_b128 v[92:95], v5 offset:3072\n"
  "ds_read_b128 v[96:99], v5 offset:4096\n"
  "ds_read_b128 v[100:103], v5 offset:5120\n"
  "s_waitcnt lgkmcnt(0)\n"
  "H_RP_%=:\n"
  "buffer_load_dwordx4 v[104:107], v6, s[16:19], s57 offen sc1\n"
  "buffer_load_dwordx4 v[108:111], v6, s[16:19], s57 offen offset:1024 sc1\n"
  "buffer_load_dwordx2 v[54:55], v7, s[16:19], s57 offen sc1\n"
  "s_waitcnt vmcnt(2)\n"
  "v_max3_u32 v19, v104, v105, v106\n"
  "s_waitcnt vmcnt(1)\n"
  "v_max3_u32 v19, v107, v108, v19\n"
  "v_max3_u32 v19, v109, v110, v19\n"
  "s_waitcnt vmcnt(0)\n"
  "v_max3_u32 v19, v111, v54, v19\n"
  "v_max_u32 v19, v55, v19\n"
  "v_cmp_eq_u32 vcc, -1, v19\n"
  "s_nop 1\n"
  "s_cbranch_vccz H_D0_%=\n"
  "s_add_u32 s62, s62, 1\n"
  "s_cmp_gt_u32 s62, 300000\n"
  "s_cbranch_scc1 H_FAIL_%=\n"
  "s_and_b32 s60, s62, 255\n"
  "s_cmp_lg_u32 s60, 0\n"
  "s_cbranch_scc1 H_RP_%=\n"
  "v_mov_b32 v19, 0\n"
  "buffer_load_dword v19, v19, s[40:43], 0 offen sc1\n"
  "s_waitcnt vmcnt(0)\n"
  "v_readfirstlane_b32 s60, v19\n"
  "s_cmp_lg_u32 s60, 0\n"
  "s_cbranch_scc1 H_EXIT_%=\n"
  "s_branch H_RP_%=\n"
  "H_FIX0_%=:\n"
  "s_nop 7\n"
  "s_waitcnt vmcnt(0) lgkmcnt(0)\n"
  "s_mov_b32 s62, 0\n"
  "s_mov_b32 s52, 1\n"
  "v_mov_b64 v[56:57], 0\n"
  "v_mov_b64 v[58:59], 0\n"
  "v_mov_b64 v[60:61], 0\n"
  "v_mov_b64 v[62:63], 0\n"
  "v_mov_b64 v[64:65], 0\n"
  "v_mov_b64 v[66:67], 0\n"
  "v_mov_b64 v[68:69], 0\n"
  "v_mov_b64 v[70:71], 0\n"
  "v_mov_b64 v[72:73], 0\n"
  "v_mov_b64 v[74:75], 0\n"
  "v_mov_b64 v[76:77], 0\n"
  "v_mov_b64 v[78:79], 0\n"
  "s_branch H_RP_%=\n"
  "H_FAIL_%=:\n"
  "s_nop 7\n"
  "v_mov_b32 v56, 0\n"
  "v_mov_b32 v57, 1\n"
  "buffer_store_dword v57, v56, s[40:43], 0 offen sc1\n"
  "H_EXIT_%=:\n"
  "s_nop 7\n"
  "s_waitcnt vmcnt(0)\n"
  "s_endpgm\n"
  "C_ENTRY_%=:\n"
  "s_mov_b32 s76, 0x3dd53b95\n"
  "s_mov_b32 s77, 0xceabfb8f\n"
  "s_mov_b32 s78, 0xfffffffe\n"
  "s_mov_b32 s46, 0\n"
  "s_mov_b32 s68, 0\n"
  "buffer_load_dwordx4 a[0:3], v0, s[28:31], s68 offen offset:0\n"
  "buffer_load_dwordx4 a[4:7], v0, s[28:31], s68 offen offset:1024\n"
  "s_mov_b32 s68, 2048\n"
  "buffer_load_dwordx4 a[8:11], v0, s[28:31], s68 offen offset:0\n"
  "buffer_load_dwordx4 a[12:15], v0, s[28:31], s68 offen offset:1024\n"
  "s_mov_b32 s68, 4096\n"
  "buffer_load_dwordx4 a[16:19], v0, s[28:31], s68 offen offset:0\n"
  "buffer_load_dwordx4 a[20:23], v0, s[28:31], s68 offen offset:1024\n"
  "s_mov_b32 s68, 6144\n"
  "buffer_load_dwordx4 a[24:27], v0, s[28:31], s68 offen offset:0\n"
  "buffer_load_dwordx4 a[28:31], v0, s[28:31], s68 offen offset:1024\n"
  "s_mov_b32 s68, 8192\n"
  "buffer_load_dwordx4 a[32:35], v0, s[28:31], s68 offen offset:0\n"
  "buffer_load_dwordx4 a[36:39], v0, s[28:31], s68 offen offset:1024\n"
  "s_mov_b32 s68, 10240\n"
  "buffer_load_dwordx4 a[40:43], v0, s[28:31], s68 offen offset:0\n"
  "buffer_load_dwordx4 a[44:47], v0, s[28:31], s68 offen offset:1024\n"
  "s_mov_b32 s68, 12288\n"
  "buffer_load_dwordx4 a[48:51], v0, s[28:31], s68 offen offset:0\n"
  "buffer_load_dwordx4 a[52:55], v0, s[28:31], s68 offen offset:1024\n"
  "s_mov_b32 s68, 14336\n"
  "buffer_load_dwordx4 a[56:59], v0, s[28:31], s68 offen offset:0\n"
  "buffer_load_dwordx4 a[60:63], v0, s[28:31], s68 offen offset:1024\n"
  "s_mov_b32 s68, 16384\n"
  "buffer_load_dwordx4 a[64:67], v0, s[28:31], s68 offen offset:0\n"
  "buffer_load_dwordx4 a[68:71], v0, s[28:31], s68 offen offset:1024\n"
  "s_mov_b32 s68, 18432\n"
  "buffer_load_dwordx4 a[72:75], v0, s[28:31], s68 offen offset:0\n"
  "buffer_load_dwordx4 a[76:79], v0, s[28:31], s68 offen offset:1024\n"
  "s_mov_b32 s68, 20480\n"
  "buffer_load_dwordx4 a[80:83], v0, s[28:31], s68 offen offset:0\n"
  "buffer_load_dwordx4 a[84:87], v0, s[28:31], s68 offen offset:1024\n"
  "s_mov_b32 s68, 22528\n"
  "buffer_load_dwordx4 a[88:91], v0, s[28:31], s68 offen offset:0\n"
  "buffer_load_dwordx4 a[92:95], v0, s[28:31], s68 offen offset:1024\n"
  "s_mov_b32 s68, 24576\n"
  "buffer_load_dwordx4 a[96:99], v0, s[28:31], s68 offen offset:0\n"
  "buffer_load_dwordx4 a[100:103], v0, s[28:31], s68 offen offset:1024\n"
  "s_mov_b32 s68, 26624\n"
  "buffer_load_dwordx4 a[104:107], v0, s[28:31], s68 offen offset:0\n"
  "buffer_load_dwordx4 a[108:111], v0, s[28:31], s68 offen offset:1024\n"
  "s_mov_b32 s68, 28672\n"
  "buffer_load_dwordx4 a[112:115], v0, s[28:31], s68 offen offset:0\n"
  "buffer_load_dwordx4 a[116:119], v0, s[28:31], s68 offen offset:1024\n"
  "s_mov_b32 s68, 30720\n"
  "buffer_load_dwordx4 a[120:123], v0, s[28:31], s68 offen offset:0\n"
  "buffer_load_dwordx4 a[124:127], v0, s[28:31], s68 offen offset:1024\n"
  "s_mov_b32 s68, 32768\n"
  "buffer_load_dwordx4 a[128:131], v0, s[28:31], s68 offen offset:0\n"
  "buffer_load_dwordx4 a[132:135], v0, s[28:31], s68 offen offset:1024\n"
  "s_mov_b32 s68, 34816\n"
  "buffer_load_dwordx4 a[136:139], v0, s[28:31], s68 offen offset:0\n"
  "buffer_load_dwordx4 a[140:143], v0, s[28:31], s68 offen offset:1024\n"
  "s_mov_b32 s68, 36864\n"
  "buffer_load_dwordx4 a[144:147], v0, s[28:31], s68 offen offset:0\n"
  "buffer_load_dwordx4 a[148:151], v0, s[28:31], s68 offen offset:1024\n"
  "s_mov_b32 s68, 38912\n"
  "buffer_load_dwordx4 a[152:155], v0, s[28:31], s68 offen offset:0\n"
  "buffer_load_dwordx4 a[156:159], v0, s[28:31], s68 offen offset:1024\n"
  "s_mov_b32 s68, 40960\n"
  "buffer_load_dwordx4 a[160:163], v0, s[28:31], s68 offen offset:0\n"
  "buffer_load_dwordx4 a[164:167], v0, s[28:31], s68 offen offset:1024\n"
  "s_mov_b32 s68, 43008\n"
  "buffer_load_dwordx4 a[168:171], v0, s[28:31], s68 offen offset:0\n"
  "buffer_load_dwordx4 a[172:175], v0, s[28:31], s68 offen offset:1024\n"
  "s_mov_b32 s68, 45056\n"
  "buffer_load_dwordx4 a[176:179], v0, s[28:31], s68 offen offset:0\n"
  "buffer_load_dwordx4 a[180:183], v0, s[28:31], s68 offen offset:1024\n"
  "s_mov_b32 s68, 47104\n"
  "buffer_load_dwordx4 a[184:187], v0, s[28:31], s68 offen offset:0\n"
  "buffer_load_dwordx4 a[188:191], v0, s[28:31], s68 offen offset:1024\n"
  "s_mov_b32 s68, 49152\n"
  "buffer_load_dwordx4 a[192:195], v0, s[28:31], s68 offen offset:0\n"
  "buffer_load_dwordx4 a[196:199], v0, s[28:31], s68 offen offset:1024\n"
  "s_mov_b32 s68, 51200\n"
  "buffer_load_dwordx4 a[200:203], v0, s[28:31], s68 offen offset:0\n"
  "buffer_load_dwordx4 a[204:207], v0, s[28:31], s68 offen offset:1024\n"
  "s_mov_b32 s68, 53248\n"
  "buffer_load_dwordx4 a[208:211], v0, s[28:31], s68 offen offset:0\n"
  "buffer_load_dwordx4 a[212:215], v0, s[28:31], s68 offen offset:1024\n"
  "s_mov_b32 s68, 55296\n"
  "buffer_load_dwordx4 a[216:219], v0, s[28:31], s68 offen offset:0\n"
  "buffer_load_dwordx4 a[220:223], v0, s[28:31], s68 offen offset:1024\n"
  "s_mov_b32 s68, 57344\n"
  "buffer_load_dwordx4 a[224:227], v0, s[28:31], s68 offen offset:0\n"
  "buffer_load_dwordx4 a[228:231], v0, s[28:31], s68 offen offset:1024\n"
  "s_mov_b32 s68, 59392\n"
  "buffer_load_dwordx4 a[232:235], v0, s[28:31], s68 offen offset:0\n"
  "buffer_load_dwordx4 a[236:239], v0, s[28:31], s68 offen offset:1024\n"
  "s_mov_b32 s68, 61440\n"
  "s_waitcnt vmcnt(50)\n"
  "buffer_load_dwordx4 a[240:243], v0, s[28:31], s68 offen offset:0\n"
  "buffer_load_dwordx4 a[244:247], v0, s[28:31], s68 offen offset:1024\n"
  "s_mov_b32 s68, 63488\n"
  "buffer_load_dwordx4 a[248:251], v0, s[28:31], s68 offen offset:0\n"
  "buffer_load_dwordx4 a[252:255], v0, s[28:31], s68 offen offset:1024\n"
  "s_mov_b32 s68, 65536\n"
  "buffer_load_dwordx4 v[192:195], v0, s[28:31], s68 offen offset:0\n"
  "buffer_load_dwordx4 v[196:199], v0, s[28:31], s68 offen offset:1024\n"
  "s_mov_b32 s68, 67584\n"
  "buffer_load_dwordx4 v[200:203], v0, s[28:31], s68 offen offset:0\n"
  "buffer_load_dwordx4 v[204:207], v0, s[28:31], s68 offen offset:1024\n"
  "s_mov_b32 s68, 69632\n"
  "buffer_load_dwordx4 v[208:211], v0, s[28:31], s68 offen offset:0\n"
  "buffer_load_dwordx4 v[212:215], v0, s[28:31], s68 offen offset:1024\n"
  "s_mov_b32 s68, 71680\n"
  "s_waitcnt vmcnt(50)\n"
  "buffer_load_dwordx4 v[216:219], v0, s[28:31], s68 offen offset:0\n"
  "buffer_load_dwordx4 v[220:223], v0, s[28:31], s68 offen offset:1024\n"
  "s_mov_b32 s68, 73728\n"
  "buffer_load_dwordx4 v[224:227], v0, s[28:31], s68 offen offset:0\n"
  "buffer_load_dwordx4 v[228:231], v0, s[28:31], s68 offen offset:1024\n"
  "s_mov_b32 s68, 75776\n"
  "buffer_load_dwordx4 v[232:235], v0, s[28:31], s68 offen offset:0\n"
  "buffer_load_dwordx4 v[236:239], v0, s[28:31], s68 offen offset:1024\n"
  "s_mov_b32 s68, 77824\n"
  "buffer_load_dwordx4 v[240:243], v0, s[28:31], s68 offen offset:0\n"
  "buffer_load_dwordx4 v[244:247], v0, s[28:31], s68 offen offset:1024\n"
  "s_mov_b32 s68, 79872\n"
  "buffer_load_dwordx4 v[248:251], v0, s[28:31], s68 offen offset:0\n"
  "buffer_load_dwordx4 v[252:255], v0, s[28:31], s68 offen offset:1024\n"
  "s_mov_b32 s38, 0\n"
  "v_add_u32 v20, 81920, v0\n"
  "s_waitcnt vmcnt(0)\n"
  "s_mov_b32 s62, 0\n"
  "s_mov_b32 s74, 1851648\n"
  "C_CALP_%=:\n"
  "s_nop 7\n"
  "buffer_load_dword v15, off, s[16:19], s74 sc1\n"
  "s_waitcnt vmcnt(0)\n"
  "v_readfirstlane_b32 s47, v15\n"
  "s_cmp_lg_u32 s47, -1\n"
  "s_cbranch_scc1 C_CALK_%=\n"
  "s_sleep 4\n"
  "s_add_u32 s62, s62, 1\n"
  "s_cmp_gt_u32 s62, 300000\n"
  "s_cbranch_scc1 C_FAIL_%=\n"
  "s_and_b32 s60, s62, 255\n"
  "s_cmp_lg_u32 s60, 0\n"
  "s_cbranch_scc1 C_CALP_%=\n"
  "v_mov_b32 v15, 0\n"
  "buffer_load_dword v15, v15, s[40:43], 0 offen sc1\n"
  "s_waitcnt vmcnt(0)\n"
  "v_readfirstlane_b32 s60, v15\n"
  "s_cmp_lg_u32 s60, 0\n"
  "s_cbranch_scc1 C_EXIT_%=\n"
  "s_branch C_CALP_%=\n"
  "C_CALK_%=:\n"
  "s_nop 7\n"
  "s_cmp_eq_u32 s45, 0\n"
  "s_cbranch_scc1 C_NOM0_%=\n"
  "s_mov_b32 s56, 0\n"
  "s_add_u32 s60, s56, 0\n"
  "s_bfe_u32 s58, s60, 131073\n"
  "s_lshl_b32 s58, s58, 18\n"
  "s_xor_b32 s60, s60, s47\n"
  "s_and_b32 s60, s60, 1\n"
  "s_lshl_b32 s60, s60, 13\n"
  "s_add_u32 s58, s58, s60\n"
  "v_mov_b32 v15, 0\n"
  "buffer_store_dword v15, v16, s[16:19], s58 offen sc1\n"
  "s_cmp_lg_u32 s44, 0\n"
  "s_cbranch_scc1 C_NOM0_%=\n"
  "s_add_u32 s72, s58, 131072\n"
  "buffer_store_dword v23, v11, s[16:19], s58 offen sc1\n"
  "buffer_store_dword v23, v11, s[16:19], s72 offen sc1\n"
  "C_NOM0_%=:\n"
  "s_nop 7\n"
  "s_waitcnt lgkmcnt(0)\n"
  "s_memtime s[64:65]\n"
  "s_memrealtime s[66:67]\n"
  "s_waitcnt lgkmcnt(0)\n"
  "s_mov_b32 s4, s64\n"
  "s_mov_b32 s5, s66\n"
  "s_mov_b32 s6, 352\n"
  "s_mov_b32 s8, 0\n"
  "s_mov_b32 s9, 0\n"
  "s_mov_b32 s10, 0\n"
  "C_LAYER_%=:\n"
  "s_nop 7\n"
  "s_mov_b32 s56, s45\n"
  "s_add_u32 s99, s46, s45\n"
  "s_add_u32 s74, s99, 0\n"
  "s_lshl_b32 s74, s74, 7\n"
  "s_add_u32 s74, s74, 1835008\n"
  "s_mov_b32 s62, 0\n"
  "s_cmp_ge_u32 s56, 64\n"
  "s_cbranch_scc1 C_F0K_%=\n"
  "s_waitcnt vmcnt(0) lgkmcnt(0)\n"
  "C_F0P_%=:\n"
  "buffer_load_dword v15, off, s[16:19], s74 sc1\n"
  "s_waitcnt vmcnt(0)\n"
  "v_readfirstlane_b32 s60, v15\n"
  "s_cmp_lg_u32 s60, -1\n"
  "s_cbranch_scc1 C_F0K_%=\n"
  "s_sleep 4\n"
  "s_add_u32 s62, s62, 1\n"
  "s_cmp_gt_u32 s62, 300000\n"
  "s_cbranch_scc1 C_FAIL_%=\n"
  "s_and_b32 s60, s62, 255\n"
  "s_cmp_lg_u32 s60, 0\n"
  "s_cbranch_scc1 C_F0P_%=\n"
  "v_mov_b32 v15, 0\n"
  "buffer_load_dword v15, v15, s[40:43], 0 offen sc1\n"
  "s_waitcnt vmcnt(0)\n"
  "v_readfirstlane_b32 s60, v15\n"
  "s_cmp_lg_u32 s60, 0\n"
  "s_cbranch_scc1 C_EXIT_%=\n"
  "s_branch C_F0P_%=\n"
  "C_F0K_%=:\n"
  "s_barrier\n"
  "s_mov_b32 s96, 0\n"
  "s_mov_b32 s97, 108544\n"
  "s_add_u32 s60, s99, 0\n"
  "s_mul_i32 s72, s60, 40960\n"
  "s_mul_i32 s73, s44, 10240\n"
  "s_add_u32 s72, s72, s73\n"
  "s_add_u32 s73, s73, s96\n"
  "s_mov_b32 m0, s73\n"
  "s_nop 0\n"
  "buffer_load_dwordx4 v0, s[24:27], s72 offen offset:0 sc1 lds\n"
  "buffer_load_dwordx4 v0, s[24:27], s72 offen offset:1024 sc1 lds\n"
  "buffer_load_dwordx4 v0, s[24:27], s72 offen offset:2048 sc1 lds\n"
  "buffer_load_dwordx4 v0, s[24:27], s72 offen offset:3072 sc1 lds\n"
  "s_add_u32 s72, s72, 4096\n"
  "s_add_u32 s73, s73, 4096\n"
  "s_mov_b32 m0, s73\n"
  "s_nop 0\n"
  "buffer_load_dwordx4 v0, s[24:27], s72 offen offset:0 sc1 lds\n"
  "buffer_load_dwordx4 v0, s[24:27], s72 offen offset:1024 sc1 lds\n"
  "buffer_load_dwordx4 v0, s[24:27], s72 offen offset:2048 sc1 lds\n"
  "buffer_load_dwordx4 v0, s[24:27], s72 offen offset:3072 sc1 lds\n"
  "s_add_u32 s72, s72, 4096\n"
  "s_add_u32 s73, s73, 4096\n"
  "s_mov_b32 m0, s73\n"
  "s_nop 0\n"
  "buffer_load_dwordx4 v0, s[24:27], s72 offen offset:0 sc1 lds\n"
  "buffer_load_dwordx4 v0, s[24:27], s72 offen offset:1024 sc1 lds\n"
  "s_waitcnt vmcnt(0)\n"
  "s_barrier\n"
  "v_mov_b32 v17, v0\n"
  "s_mul_i32 s60, s44, 5120\n"
  "s_add_u32 s60, s60, s97\n"
  "v_add_u32 v19, s60, v0\n"
  "ds_read_b128 v[64:67], v17 offset:0\n"
  "ds_read_b128 v[68:71], v17 offset:10240\n"
  "ds_read_b128 v[72:75], v17 offset:20480\n"
  "ds_read_b128 v[76:79], v17 offset:30720\n"
  "s_waitcnt lgkmcnt(3)\n"
  "v_mfma_f32_16x16x32_bf16 v[32:35], v[64:67], a[0:3], 0\n"
  "s_waitcnt lgkmcnt(2)\n"
  "v_mfma_f32_16x16x32_bf16 v[36:39], v[68:71], a[0:3], 0\n"
  "ds_read_b128 v[80:83], v17 offset:1024\n"
  "v_mfma_f32_16x16x32_bf16 v[40:43], v[64:67], a[8:11], 0\n"
  "v_mfma_f32_16x16x32_bf16 v[44:47], v[68:71], a[8:11], 0\n"
  "ds_read_b128 v[84:87], v17 offset:11264\n"
  "v_mfma_f32_16x16x32_bf16 v[48:51], v[64:67], a[16:19], 0\n"
  "v_mfma_f32_16x16x32_bf16 v[52:55], v[68:71], a[16:19], 0\n"
  "ds_read_b128 v[88:91], v17 offset:21504\n"
  "v_mfma_f32_16x16x32_bf16 v[56:59], v[64:67], a[24:27], 0\n"
  "v_mfma_f32_16x16x32_bf16 v[60:63], v[68:71], a[24:27], 0\n"
  "ds_read_b128 v[92:95], v17 offset:31744\n"
  "s_waitcnt lgkmcnt(5)\n"
  "v_mfma_f32_16x16x32_bf16 v[32:35], v[72:75], a[4:7], v[32:35]\n"
  "s_waitcnt lgkmcnt(4)\n"
  "v_mfma_f32_16x16x32_bf16 v[36:39], v[76:79], a[4:7], v[36:39]\n"
  "v_mfma_f32_16x16x32_bf16 v[40:43], v[72:75], a[12:15], v[40:43]\n"
  "v_mfma_f32_16x16x32_bf16 v[44:47], v[76:79], a[12:15], v[44:47]\n"
  "v_mfma_f32_16x16x32_bf16 v[48:51], v[72:75], a[20:23], v[48:51]\n"
  "v_mfma_f32_16x16x32_bf16 v[52:55], v[76:79], a[20:23], v[52:55]\n"
  "v_mfma_f32_16x16x32_bf16 v[56:59], v[72:75], a[28:31], v[56:59]\n"
  "v_mfma_f32_16x16x32_bf16 v[60:63], v[76:79], a[28:31], v[60:63]\n"
  "s_waitcnt lgkmcnt(3)\n"
  "v_mfma_f32_16x16x32_bf16 v[32:35], v[80:83], a[32:35], v[32:35]\n"
  "s_waitcnt lgkmcnt(2)\n"
  "v_mfma_f32_16x16x32_bf16 v[36:39], v[84:87], a[32:35], v[36:39]\n"
  "ds_read_b128 v[64:67], v17 offset:2048\n"
  "v_mfma_f32_16x16x32_bf16 v[40:43], v[80:83], a[40:43], v[40:43]\n"
  "v_mfma_f32_16x16x32_bf16 v[44:47], v[84:87], a[40:43], v[44:47]\n"
  "ds_read_b128 v[68:71], v17 offset:12288\n"
  "v_mfma_f32_16x16x32_bf16 v[48:51], v[80:83], a[48:51], v[48:51]\n"
  "v_mfma_f32_16x16x32_bf16 v[52:55], v[84:87], a[48:51], v[52:55]\n"
  "ds_read_b128 v[72:75], v17 offset:22528\n"
  "v_mfma_f32_16x16x32_bf16 v[56:59], v[80:83], a[56:59], v[56:59]\n"
  "v_mfma_f32_16x16x32_bf16 v[60:63], v[84:87], a[56:59], v[60:63]\n"
  "ds_read_b128 v[76:79], v17 offset:32768\n"
  "s_waitcnt lgkmcnt(5)\n"
  "v_mfma_f32_16x16x32_bf16 v[32:35], v[88:91], a[36:39], v[32:35]\n"
  "s_waitcnt lgkmcnt(4)\n"
  "v_mfma_f32_16x16x32_bf16 v[36:39], v[92:95], a[36:39], v[36:39]\n"
  "v_mfma_f32_16x16x32_bf16 v[40:43], v[88:91], a[44:47], v[40:43]\n"
  "v_mfma_f32_16x16x32_bf16 v[44:47], v[92:95], a[44:47], v[44:47]\n"
  "v_mfma_f32_16x16x32_bf16 v[48:51], v[88:91], a[52:55], v[48:51]\n"
  "v_mfma_f32_16x16x32_bf16 v[52:55], v[92:95], a[52:55], v[52:55]\n"
  "v_mfma_f32_16x16x32_bf16 v[56:59], v[88:91], a[60:63], v[56:59]\n"
  "v_mfma_f32_16x16x32_bf16 v[60:63], v[92:95], a[60:63], v[60:63]\n"
  "s_waitcnt lgkmcnt(3)\n"
  "v_mfma_f32_16x16x32_bf16 v[32:35], v[64:67], a[64:67], v[32:35]\n"
  "s_waitcnt lgkmcnt(2)\n"
  "v_mfma_f32_16x16x32_bf16 v[36:39], v[68:71], a[64:67], v[36:39]\n"
  "ds_read_b128 v[80:83], v17 offset:3072\n"
  "v_mfma_f32_16x16x32_bf16 v[40:43], v[64:67], a[72:75], v[40:43]\n"
  "v_mfma_f32_16x16x32_bf16 v[44:47], v[68:71], a[72:75], v[44:47]\n"
  "ds_read_b128 v[84:87], v17 offset:13312\n"
  "v_mfma_f32_16x16x32_bf16 v[48:51], v[64:67], a[80:83], v[48:51]\n"
  "v_mfma_f32_16x16x32_bf16 v[52:55], v[68:71], a[80:83], v[52:55]\n"
  "ds_read_b128 v[88:91], v17 offset:23552\n"
  "v_mfma_f32_16x16x32_bf16 v[56:59], v[64:67], a[88:91], v[56:59]\n"
  "v_mfma_f32_16x16x32_bf16 v[60:63], v[68:71], a[88:91], v[60:63]\n"
  "ds_read_b128 v[92:95], v17 offset:33792\n"
  "s_waitcnt lgkmcnt(5)\n"
  "v_mfma_f32_16x16x32_bf16 v[32:35], v[72:75], a[68:71], v[32:35]\n"
  "s_waitcnt lgkmcnt(4)\n"
  "v_mfma_f32_16x16x32_bf16 v[36:39], v[76:79], a[68:71], v[36:39]\n"
  "v_mfma_f32_16x16x32_bf16 v[40:43], v[72:75], a[76:79], v[40:43]\n"
  "v_mfma_f32_16x16x32_bf16 v[44:47], v[76:79], a[76:79], v[44:47]\n"
  "v_mfma_f32_16x16x32_bf16 v[48:51], v[72:75], a[84:87], v[48:51]\n"
  "v_mfma_f32_16x16x32_bf16 v[52:55], v[76:79], a[84:87], v[52:55]\n"
  "v_mfma_f32_16x16x32_bf16 v[56:59], v[72:75], a[92:95], v[56:59]\n"
  "v_mfma_f32_16x16x32_bf16 v[60:63], v[76:79], a[92:95], v[60:63]\n"
  "s_waitcnt lgkmcnt(3)\n"
  "v_mfma_f32_16x16x32_bf16 v[32:35], v[80:83], a[96:99], v[32:35]\n"
  "s_waitcnt lgkmcnt(2)\n"
  "v_mfma_f32_16x16x32_bf16 v[36:39], v[84:87], a[96:99], v[36:39]\n"
  "ds_read_b128 v[64:67], v17 offset:4096\n"
  "v_mfma_f32_16x16x32_bf16 v[40:43], v[80:83], a[104:107], v[40:43]\n"
  "v_mfma_f32_16x16x32_bf16 v[44:47], v[84:87], a[104:107], v[44:47]\n"
  "ds_read_b128 v[68:71], v17 offset:14336\n"
  "v_mfma_f32_16x16x32_bf16 v[48:51], v[80:83], a[112:115], v[48:51]\n"
  "v_mfma_f32_16x16x32_bf16 v[52:55], v[84:87], a[112:115], v[52:55]\n"
  "ds_read_b128 v[72:75], v17 offset:24576\n"
  "v_mfma_f32_16x16x32_bf16 v[56:59], v[80:83], a[120:123], v[56:59]\n"
  "v_mfma_f32_16x16x32_bf16 v[60:63], v[84:87], a[120:123], v[60:63]\n"
  "ds_read_b128 v[76:79], v17 offset:34816\n"
  "s_waitcnt lgkmcnt(5)\n"
  "v_mfma_f32_16x16x32_bf16 v[32:35], v[88:91], a[100:103], v[32:35]\n"
  "s_waitcnt lgkmcnt(4)\n"
  "v_mfma_f32_16x16x32_bf16 v[36:39], v[92:95], a[100:103], v[36:39]\n"
  "v_mfma_f32_16x16x32_bf16 v[40:43], v[88:91], a[108:111], v[40:43]\n"
  "v_mfma_f32_16x16x32_bf16 v[44:47], v[92:95], a[108:111], v[44:47]\n"
  "v_mfma_f32_16x16x32_bf16 v[48:51], v[88:91], a[116:119], v[48:51]\n"
  "v_mfma_f32_16x16x32_bf16 v[52:55], v[92:95], a[116:119], v[52:55]\n"
  "v_mfma_f32_16x16x32_bf16 v[56:59], v[88:91], a[124:127], v[56:59]\n"
  "v_mfma_f32_16x16x32_bf16 v[60:63], v[92:95], a[124:127], v[60:63]\n"
  "s_waitcnt lgkmcnt(3)\n"
  "v_mfma_f32_16x16x32_bf16 v[32:35], v[64:67], a[128:131], v[32:35]\n"
  "s_waitcnt lgkmcnt(2)\n"
  "v_mfma_f32_16x16x32_bf16 v[36:39], v[68:71], a[128:131], v[36:39]\n"
  "ds_read_b128 v[80:83], v17 offset:5120\n"
  "v_mfma_f32_16x16x32_bf16 v[40:43], v[64:67], a[136:139], v[40:43]\n"
  "v_mfma_f32_16x16x32_bf16 v[44:47], v[68:71], a[136:139], v[44:47]\n"
  "ds_read_b128 v[84:87], v17 offset:15360\n"
  "v_mfma_f32_16x16x32_bf16 v[48:51], v[64:67], a[144:147], v[48:51]\n"
  "v_mfma_f32_16x16x32_bf16 v[52:55], v[68:71], a[144:147], v[52:55]\n"
  "ds_read_b128 v[88:91], v17 offset:25600\n"
  "v_mfma_f32_16x16x32_bf16 v[56:59], v[64:67], a[152:155], v[56:59]\n"
  "v_mfma_f32_16x16x32_bf16 v[60:63], v[68:71], a[152:155], v[60:63]\n"
  "ds_read_b128 v[92:95], v17 offset:35840\n"
  "s_waitcnt lgkmcnt(5)\n"
  "v_mfma_f32_16x16x32_bf16 v[32:35], v[72:75], a[132:135], v[32:35]\n"
  "s_waitcnt lgkmcnt(4)\n"
  "v_mfma_f32_16x16x32_bf16 v[36:39], v[76:79], a[132:135], v[36:39]\n"
  "v_mfma_f32_16x16x32_bf16 v[40:43], v[72:75], a[140:143], v[40:43]\n"
  "v_mfma_f32_16x16x32_bf16 v[44:47], v[76:79], a[140:143], v[44:47]\n"
  "v_mfma_f32_16x16x32_bf16 v[48:51], v[72:75], a[148:151], v[48:51]\n"
  "v_mfma_f32_16x16x32_bf16 v[52:55], v[76:79], a[148:151], v[52:55]\n"
  "v_mfma_f32_16x16x32_bf16 v[56:59], v[72:75], a[156:159], v[56:59]\n"
  "v_mfma_f32_16x16x32_bf16 v[60:63], v[76:79], a[156:159], v[60:63]\n"
  "s_waitcnt lgkmcnt(3)\n"
  "v_mfma_f32_16x16x32_bf16 v[32:35], v[80:83], a[160:163], v[32:35]\n"
  "s_waitcnt lgkmcnt(2)\n"
  "v_mfma_f32_16x16x32_bf16 v[36:39], v[84:87], a[160:163], v[36:39]\n"
  "ds_read_b128 v[64:67], v17 offset:6144\n"
  "v_mfma_f32_16x16x32_bf16 v[40:43], v[80:83], a[168:171], v[40:43]\n"
  "v_mfma_f32_16x16x32_bf16 v[44:47], v[84:87], a[168:171], v[44:47]\n"
  "ds_read_b128 v[68:71], v17 offset:16384\n"
  "v_mfma_f32_16x16x32_bf16 v[48:51], v[80:83], a[176:179], v[48:51]\n"
  "v_mfma_f32_16x16x32_bf16 v[52:55], v[84:87], a[176:179], v[52:55]\n"
  "ds_read_b128 v[72:75], v17 offset:26624\n"
  "v_mfma_f32_16x16x32_bf16 v[56:59], v[80:83], a[184:187], v[56:59]\n"
  "v_mfma_f32_16x16x32_bf16 v[60:63], v[84:87], a[184:187], v[60:63]\n"
  "ds_read_b128 v[76:79], v17 offset:36864\n"
  "s_waitcnt lgkmcnt(5)\n"
  "v_mfma_f32_16x16x32_bf16 v[32:35], v[88:91], a[164:167], v[32:35]\n"
  "s_waitcnt lgkmcnt(4)\n"
  "v_mfma_f32_16x16x32_bf16 v[36:39], v[92:95], a[164:167], v[36:39]\n"
  "v_mfma_f32_16x16x32_bf16 v[40:43], v[88:91], a[172:175], v[40:43]\n"
  "v_mfma_f32_16x16x32_bf16 v[44:47], v[92:95], a[172:175], v[44:47]\n"
  "v_mfma_f32_16x16x32_bf16 v[48:51], v[88:91], a[180:183], v[48:51]\n"
  "v_mfma_f32_16x16x32_bf16 v[52:55], v[92:95], a[180:183], v[52:55]\n"
  "v_mfma_f32_16x16x32_bf16 v[56:59], v[88:91], a[188:191], v[56:59]\n"
  "v_mfma_f32_16x16x32_bf16 v[60:63], v[92:95], a[188:191], v[60:63]\n"
  "s_waitcnt lgkmcnt(3)\n"
  "v_mfma_f32_16x16x32_bf16 v[32:35], v[64:67], a[192:195], v[32:35]\n"
  "s_waitcnt lgkmcnt(2)\n"
  "v_mfma_f32_16x16x32_bf16 v[36:39], v[68:71], a[192:195], v[36:39]\n"
  "ds_read_b128 v[80:83], v17 offset:7168\n"
  "v_mfma_f32_16x16x32_bf16 v[40:43], v[64:67], a[200:203], v[40:43]\n"
  "v_mfma_f32_16x16x32_bf16 v[44:47], v[68:71], a[200:203], v[44:47]\n"
  "ds_read_b128 v[84:87], v17 offset:17408\n"
  "v_mfma_f32_16x16x32_bf16 v[48:51], v[64:67], a[208:211], v[48:51]\n"
  "v_mfma_f32_16x16x32_bf16 v[52:55], v[68:71], a[208:211], v[52:55]\n"
  "ds_read_b128 v[88:91], v17 offset:27648\n"
  "v_mfma_f32_16x16x32_bf16 v[56:59], v[64:67], a[216:219], v[56:59]\n"
  "v_mfma_f32_16x16x32_bf16 v[60:63], v[68:71], a[216:219], v[60:63]\n"
  "ds_read_b128 v[92:95], v17 offset:37888\n"
  "s_waitcnt lgkmcnt(5)\n"
  "v_mfma_f32_16x16x32_bf16 v[32:35], v[72:75], a[196:199], v[32:35]\n"
  "s_waitcnt lgkmcnt(4)\n"
  "v_mfma_f32_16x16x32_bf16 v[36:39], v[76:79], a[196:199], v[36:39]\n"
  "v_mfma_f32_16x16x32_bf16 v[40:43], v[72:75], a[204:207], v[40:43]\n"
  "v_mfma_f32_16x16x32_bf16 v[44:47], v[76:79], a[204:207], v[44:47]\n"
  "v_mfma_f32_16x16x32_bf16 v[48:51], v[72:75], a[212:215], v[48:51]\n"
  "v_mfma_f32_16x16x32_bf16 v[52:55], v[76:79], a[212:215], v[52:55]\n"
  "v_mfma_f32_16x16x32_bf16 v[56:59], v[72:75], a[220:223], v[56:59]\n"
  "v_mfma_f32_16x16x32_bf16 v[60:63], v[76:79], a[220:223], v[60:63]\n"
  "s_waitcnt lgkmcnt(3)\n"
  "v_mfma_f32_16x16x32_bf16 v[32:35], v[80:83], a[224:227], v[32:35]\n"
  "s_waitcnt lgkmcnt(2)\n"
  "v_mfma_f32_16x16x32_bf16 v[36:39], v[84:87], a[224:227], v[36:39]\n"
  "ds_read_b128 v[64:67], v17 offset:8192\n"
  "v_mfma_f32_16x16x32_bf16 v[40:43], v[80:83], a[232:235], v[40:43]\n"
  "v_mfma_f32_16x16x32_bf16 v[44:47], v[84:87], a[232:235], v[44:47]\n"
  "ds_read_b128 v[68:71], v17 offset:18432\n"
  "v_mfma_f32_16x16x32_bf16 v[48:51], v[80:83], a[240:243], v[48:51]\n"
  "v_mfma_f32_16x16x32_bf16 v[52:55], v[84:87], a[240:243], v[52:55]\n"
  "ds_read_b128 v[72:75], v17 offset:28672\n"
  "v_mfma_f32_16x16x32_bf16 v[56:59], v[80:83], a[248:251], v[56:59]\n"
  "v_mfma_f32_16x16x32_bf16 v[60:63], v[84:87], a[248:251], v[60:63]\n"
  "ds_read_b128 v[76:79], v17 offset:38912\n"
  "s_waitcnt lgkmcnt(5)\n"
  "v_mfma_f32_16x16x32_bf16 v[32:35], v[88:91], a[228:231], v[32:35]\n"
  "s_waitcnt lgkmcnt(4)\n"
  "v_mfma_f32_16x16x32_bf16 v[36:39], v[92:95], a[228:231], v[36:39]\n"
  "v_mfma_f32_16x16x32_bf16 v[40:43], v[88:91], a[236:239], v[40:43]\n"
  "v_mfma_f32_16x16x32_bf16 v[44:47], v[92:95], a[236:239], v[44:47]\n"
  "v_mfma_f32_16x16x32_bf16 v[48:51], v[88:91], a[244:247], v[48:51]\n"
  "v_mfma_f32_16x16x32_bf16 v[52:55], v[92:95], a[244:247], v[52:55]\n"
  "v_mfma_f32_16x16x32_bf16 v[56:59], v[88:91], a[252:255], v[56:59]\n"
  "v_mfma_f32_16x16x32_bf16 v[60:63], v[92:95], a[252:255], v[60:63]\n"
  "s_waitcnt lgkmcnt(3)\n"
  "v_mfma_f32_16x16x32_bf16 v[32:35], v[64:67], v[192:195], v[32:35]\n"
  "s_waitcnt lgkmcnt(2)\n"
  "v_mfma_f32_16x16x32_bf16 v[36:39], v[68:71], v[192:195], v[36:39]\n"
  "ds_read_b128 v[80:83], v17 offset:9216\n"
  "v_mfma_f32_16x16x32_bf16 v[40:43], v[64:67], v[200:203], v[40:43]\n"
  "v_mfma_f32_16x16x32_bf16 v[44:47], v[68:71], v[200:203], v[44:47]\n"
  "ds_read_b128 v[84:87], v17 offset:19456\n"
  "v_mfma_f32_16x16x32_bf16 v[48:51], v[64:67], v[208:211], v[48:51]\n"
  "v_mfma_f32_16x16x32_bf16 v[52:55], v[68:71], v[208:211], v[52:55]\n"
  "ds_read_b128 v[88:91], v17 offset:29696\n"
  "v_mfma_f32_16x16x32_bf16 v[56:59], v[64:67], v[216:219], v[56:59]\n"
  "v_mfma_f32_16x16x32_bf16 v[60:63], v[68:71], v[216:219], v[60:63]\n"
  "ds_read_b128 v[92:95], v17 offset:39936\n"
  "s_waitcnt lgkmcnt(5)\n"
  "v_mfma_f32_16x16x32_bf16 v[32:35], v[72:75], v[196:199], v[32:35]\n"
  "s_waitcnt lgkmcnt(4)\n"
  "v_mfma_f32_16x16x32_bf16 v[36:39], v[76:79], v[196:199], v[36:39]\n"
  "v_mfma_f32_16x16x32_bf16 v[40:43], v[72:75], v[204:207], v[40:43]\n"
  "v_mfma_f32_16x16x32_bf16 v[44:47], v[76:79], v[204:207], v[44:47]\n"
  "v_mfma_f32_16x16x32_bf16 v[48:51], v[72:75], v[212:215], v[48:51]\n"
  "v_mfma_f32_16x16x32_bf16 v[52:55], v[76:79], v[212:215], v[52:55]\n"
  "v_mfma_f32_16x16x32_bf16 v[56:59], v[72:75], v[220:223], v[56:59]\n"
  "v_mfma_f32_16x16x32_bf16 v[60:63], v[76:79], v[220:223], v[60:63]\n"
  "s_waitcnt lgkmcnt(3)\n"
  "v_mfma_f32_16x16x32_bf16 v[32:35], v[80:83], v[224:227], v[32:35]\n"
  "s_waitcnt lgkmcnt(2)\n"
  "v_mfma_f32_16x16x32_bf16 v[36:39], v[84:87], v[224:227], v[36:39]\n"
  "v_mfma_f32_16x16x32_bf16 v[40:43], v[80:83], v[232:235], v[40:43]\n"
  "v_mfma_f32_16x16x32_bf16 v[44:47], v[84:87], v[232:235], v[44:47]\n"
  "v_mfma_f32_16x16x32_bf16 v[48:51], v[80:83], v[240:243], v[48:51]\n"
  "v_mfma_f32_16x16x32_bf16 v[52:55], v[84:87], v[240:243], v[52:55]\n"
  "v_mfma_f32_16x16x32_bf16 v[56:59], v[80:83], v[248:251], v[56:59]\n"
  "v_mfma_f32_16x16x32_bf16 v[60:63], v[84:87], v[248:251], v[60:63]\n"
  "s_waitcnt lgkmcnt(1)\n"
  "v_mfma_f32_16x16x32_bf16 v[32:35], v[88:91], v[228:231], v[32:35]\n"
  "s_waitcnt lgkmcnt(0)\n"
  "v_mfma_f32_16x16x32_bf16 v[36:39], v[92:95], v[228:231], v[36:39]\n"
  "v_mfma_f32_16x16x32_bf16 v[40:43], v[88:91], v[236:239], v[40:43]\n"
  "v_mfma_f32_16x16x32_bf16 v[44:47], v[92:95], v[236:239], v[44:47]\n"
  "v_mfma_f32_16x16x32_bf16 v[48:51], v[88:91], v[244:247], v[48:51]\n"
  "v_mfma_f32_16x16x32_bf16 v[52:55], v[92:95], v[244:247], v[52:55]\n"
  "v_mfma_f32_16x16x32_bf16 v[56:59], v[88:91], v[252:255], v[56:59]\n"
  "v_mfma_f32_16x16x32_bf16 v[60:63], v[92:95], v[252:255], v[60:63]\n"
  "v_cvt_pk_bf16_f32 v32, v32, v33\n"
  "v_cvt_pk_bf16_f32 v33, v34, v35\n"
  "v_cvt_pk_bf16_f32 v34, v36, v37\n"
  "v_cvt_pk_bf16_f32 v35, v38, v39\n"
  "ds_write_b128 v19, v[32:35] offset:0\n"
  "v_cvt_pk_bf16_f32 v40, v40, v41\n"
  "v_cvt_pk_bf16_f32 v41, v42, v43\n"
  "v_cvt_pk_bf16_f32 v42, v44, v45\n"
  "v_cvt_pk_bf16_f32 v43, v46, v47\n"
  "ds_write_b128 v19, v[40:43] offset:1024\n"
  "v_cvt_pk_bf16_f32 v48, v48, v49\n"
  "v_cvt_pk_bf16_f32 v49, v50, v51\n"
  "v_cvt_pk_bf16_f32 v50, v52, v53\n"
  "v_cvt_pk_bf16_f32 v51, v54, v55\n"
  "ds_write_b128 v19, v[48:51] offset:2048\n"
  "v_cvt_pk_bf16_f32 v56, v56, v57\n"
  "v_cvt_pk_bf16_f32 v57, v58, v59\n"
  "v_cvt_pk_bf16_f32 v58, v60, v61\n"
  "v_cvt_pk_bf16_f32 v59, v62, v63\n"
  "ds_write_b128 v19, v[56:59] offset:3072\n"
  "s_add_u32 s74, s99, 2\n"
  "s_lshl_b32 s74, s74, 7\n"
  "s_add_u32 s74, s74, 1835008\n"
  "s_mov_b32 s62, 0\n"
  "s_cmp_ge_u32 s56, 62\n"
  "s_cbranch_scc1 C_F2K_%=\n"
  "s_waitcnt vmcnt(0) lgkmcnt(0)\n"
  "C_F2P_%=:\n"
  "buffer_load_dword v15, off, s[16:19], s74 sc1\n"
  "s_waitcnt vmcnt(0)\n"
  "v_readfirstlane_b32 s60, v15\n"
  "s_cmp_lg_u32 s60, -1\n"
  "s_cbranch_scc1 C_F2K_%=\n"
  "s_sleep 4\n"
  "s_add_u32 s62, s62, 1\n"
  "s_cmp_gt_u32 s62, 300000\n"
  "s_cbranch_scc1 C_FAIL_%=\n"
  "s_and_b32 s60, s62, 255\n"
  "s_cmp_lg_u32 s60, 0\n"
  "s_cbranch_scc1 C_F2P_%=\n"
  "v_mov_b32 v15, 0\n"
  "buffer_load_dword v15, v15, s[40:43], 0 offen sc1\n"
  "s_waitcnt vmcnt(0)\n"
  "v_readfirstlane_b32 s60, v15\n"
  "s_cmp_lg_u32 s60, 0\n"
  "s_cbranch_scc1 C_EXIT_%=\n"
  "s_branch C_F2P_%=\n"
  "C_F2K_%=:\n"
  "s_mov_b32 s75, 40960\n"
  "s_add_u32 s60, s99, 2\n"
  "s_mul_i32 s72, s60, 40960\n"
  "s_mul_i32 s73, s44, 10240\n"
  "s_add_u32 s72, s72, s73\n"
  "s_add_u32 s73, s73, s75\n"
  "s_mov_b32 m0, s73\n"
  "s_nop 0\n"
  "buffer_load_dwordx4 v0, s[24:27], s72 offen offset:0 sc1 lds\n"
  "buffer_load_dwordx4 v0, s[24:27], s72 offen offset:1024 sc1 lds\n"
  "buffer_load_dwordx4 v0, s[24:27], s72 offen offset:2048 sc1 lds\n"
  "buffer_load_dwordx4 v0, s[24:27], s72 offen offset:3072 sc1 lds\n"
  "s_add_u32 s72, s72, 4096\n"
  "s_add_u32 s73, s73, 4096\n"
  "s_mov_b32 m0, s73\n"
  "s_nop 0\n"
  "buffer_load_dwordx4 v0, s[24:27], s72 offen offset:0 sc1 lds\n"
  "buffer_load_dwordx4 v0, s[24:27], s72 offen offset:1024 sc1 lds\n"
  "buffer_load_dwordx4 v0, s[24:27], s72 offen offset:2048 sc1 lds\n"
  "buffer_load_dwordx4 v0, s[24:27], s72 offen offset:3072 sc1 lds\n"
  "s_add_u32 s72, s72, 4096\n"
  "s_add_u32 s73, s73, 4096\n"
  "s_mov_b32 m0, s73\n"
  "s_nop 0\n"
  "buffer_load_dwordx4 v0, s[24:27], s72 offen offset:0 sc1 lds\n"
  "buffer_load_dwordx4 v0, s[24:27], s72 offen offset:1024 sc1 lds\n"
  "s_add_u32 s60, s56, 0\n"
  "s_bfe_u32 s57, s60, 131073\n"
  "s_lshl_b32 s57, s57, 18\n"
  "s_xor_b32 s60, s60, s47\n"
  "s_and_b32 s60, s60, 1\n"
  "s_lshl_b32 s60, s60, 13\n"
  "s_add_u32 s57, s57, s60\n"
  "v_mov_b32 v30, -1\n"
  "v_mov_b32 v31, -1\n"
  "C_LOOP_%=:\n"
  "s_nop 7\n"
  "s_add_u32 s60, s56, 1\n"
  "s_bfe_u32 s58, s60, 131073\n"
  "s_lshl_b32 s58, s58, 18\n"
  "s_xor_b32 s60, s60, s47\n"
  "s_and_b32 s60, s60, 1\n"
  "s_lshl_b32 s60, s60, 13\n"
  "s_add_u32 s58, s58, s60\n"
  "v_readfirstlane_b32 s61, v31\n"
  "s_cmp_lg_u32 s61, -1\n"
  "s_cselect_b32 s48, s61, s48\n"
  "s_mov_b32 s62, 0\n"
  "v_readfirstlane_b32 s59, v30\n"
  "s_cmp_lg_u32 s59, -1\n"
  "s_cbranch_scc1 C_SCHOK_%=\n"
  "C_SCHP_%=:\n"
  "s_nop 7\n"
  "buffer_load_dword v30, v10, s[16:19], s57 offen sc1\n"
  "s_waitcnt vmcnt(0)\n"
  "v_readfirstlane_b32 s59, v30\n"
  "s_cmp_lg_u32 s59, -1\n"
  "s_cbranch_scc1 C_SCHOK_%=\n"
  "s_sleep 2\n"
  "s_add_u32 s62, s62, 1\n"
  "s_cmp_gt_u32 s62, 300000\n"
  "s_cbranch_scc1 C_FAIL_%=\n"
  "s_and_b32 s60, s62, 255\n"
  "s_cmp_lg_u32 s60, 0\n"
  "s_cbranch_scc1 C_SCHP_%=\n"
  "v_mov_b32 v30, 0\n"
  "buffer_load_dword v30, v30, s[40:43], 0 offen sc1\n"
  "s_waitcnt vmcnt(0)\n"
  "v_readfirstlane_b32 s60, v30\n"
  "s_cmp_lg_u32 s60, 0\n"
  "s_cbranch_scc1 C_EXIT_%=\n"
  "s_branch C_SCHP_%=\n"
  "C_SCHOK_%=:\n"
  "s_nop 7\n"
  "s_waitcnt lgkmcnt(0)\n"
  "s_memrealtime s[64:65]\n"
  "s_waitcnt lgkmcnt(0)\n"
  "s_sub_i32 s60, s59, s64\n"
  "s_cmp_gt_i32 s60, 2000\n"
  "s_cselect_b32 s59, s64, s59\n"
  "s_cmp_lt_i32 s60, -100000\n"
  "s_cselect_b32 s59, s64, s59\n"
  "s_add_u32 s68, s59, s51\n"
  "s_add_u32 s69, s68, 12\n"
  "s_sub_i32 s60, s64, s69\n"
  "s_cmp_gt_i32 s60, 0\n"
  "s_cselect_b32 s69, s64, s69\n"
  "s_add_i32 s60, s60, 100\n"
  "s_max_i32 s60, s60, 0\n"
  "s_min_i32 s3, s60, 200\n"
  "s_cmp_lt_u32 s56, 4\n"
  "s_cselect_b32 s3, 0, s3\n"
  "v_mov_b32 v152, 0\n"
  "v_mov_b32 v153, 0\n"
  "v_mov_b32 v154, 0\n"
  "v_mov_b32 v155, 0\n"
  "v_mov_b32 v156, 0\n"
  "v_mov_b32 v157, 0\n"
  "v_mov_b32 v158, 0\n"
  "v_mov_b32 v159, 0\n"
  "v_mov_b32 v160, 0\n"
  "v_mov_b32 v161, 0\n"
  "v_mov_b32 v162, 0\n"
  "v_mov_b32 v163, 0\n"
  "v_mov_b32 v164, 0\n"
  "v_mov_b32 v165, 0\n"
  "v_mov_b32 v166, 0\n"
  "v_mov_b32 v167, 0\n"
  "v_add_u32 v18, s97, v0\n"
  "s_mov_b32 s62, 0\n"
  "C_WT_%=:\n"
  "s_memrealtime s[64:65]\n"
  "s_waitcnt lgkmcnt(0)\n"
  "s_sub_i32 s60, s64, s68\n"
  "s_cmp_lt_i32 s60, 0\n"
  "s_cbranch_scc1 C_WT_%=\n"
  "C_MP_%=:\n"
  "buffer_load_dwordx4 v[168:171], v2, s[16:19], s57 offen sc1\n"
  "buffer_load_dwordx4 v[172:175], v2, s[16:19], s57 offen offset:1024 sc1\n"
  "buffer_load_dwordx2 v[176:177], v3, s[16:19], s57 offen sc1\n"
  "s_waitcnt vmcnt(2)\n"
  "ds_write_b128 v1, v[168:171]\n"
  "v_max3_u32 v15, v168, v169, v170\n"
  "s_waitcnt vmcnt(1)\n"
  "ds_write_b128 v1, v[172:175] offset:4096\n"
  "v_max3_u32 v15, v171, v172, v15\n"
  "v_max3_u32 v15, v173, v174, v15\n"
  "s_waitcnt vmcnt(0)\n"
  "ds_write_b64 v4, v[176:177]\n"
  "v_max3_u32 v15, v175, v176, v15\n"
  "v_max_u32 v15, v177, v15\n"
  "v_cmp_eq_u32 vcc, -1, v15\n"
  "s_nop 1\n"
  "s_cbranch_vccnz C_MBAD_%=\n"
  "s_waitcnt lgkmcnt(0)\n"
  "s_barrier\n"
  "s_memrealtime s[64:65]\n"
  "ds_read_b128 v[112:115], v20 offset:0\n"
  "ds_read_b128 v[116:119], v20 offset:1024\n"
  "ds_read_b128 v[120:123], v20 offset:2048\n"
  "ds_read_b128 v[124:127], v20 offset:3072\n"
  "ds_read_b128 v[128:131], v20 offset:4096\n"
  "ds_read_b128 v[132:135], v20 offset:5120\n"
  "ds_read_b128 v[136:139], v20 offset:6144\n"
  "ds_read_b128 v[140:143], v20 offset:7168\n"
  "ds_read_b128 v[144:147], v20 offset:8192\n"
  "ds_read_b128 v[148:151], v20 offset:9216\n"
  "s_waitcnt lgkmcnt(0)\n"
  "v_smfmac_f32_16x16x64_bf16 v[152:155], v[112:115], a[0:7], v5\n"
  "s_sub_i32 s60, s64, s68\n"
  "s_add_u32 s61, s64, s50\n"
  "s_cmp_gt_i32 s60, s52\n"
  "s_cselect_b32 s59, s61, s59\n"
  "v_smfmac_f32_16x16x64_bf16 v[156:159], v[112:115], a[8:15], v5\n"
  "v_smfmac_f32_16x16x64_bf16 v[160:163], v[112:115], a[16:23], v5\n"
  "s_add_u32 s59, s59, s48\n"
  "s_add_u32 s60, s59, s49\n"
  "s_min_u32 s60, s60, s78\n"
  "v_mov_b32 v15, s60\n"
  "v_smfmac_f32_16x16x64_bf16 v[164:167], v[112:115], a[24:31], v5\n"
  "v_smfmac_f32_16x16x64_bf16 v[152:155], v[116:119], a[32:39], v5\n"
  "buffer_store_dword v15, v16, s[16:19], s58 offen sc1\n"
  "s_xor_b32 s73, s58, 524288\n"
  "v_mov_b32 v21, -1\n"
  "v_smfmac_f32_16x16x64_bf16 v[156:159], v[116:119], a[40:47], v5\n"
  "v_smfmac_f32_16x16x64_bf16 v[160:163], v[116:119], a[48:55], v5\n"
  "buffer_store_dword v21, v16, s[16:19], s73 offen sc1\n"
  "v_smfmac_f32_16x16x64_bf16 v[164:167], v[116:119], a[56:63], v5\n"
  "v_smfmac_f32_16x16x64_bf16 v[152:155], v[120:123], a[64:71], v5\n"
  "ds_read_b128 v[32:35], v18 offset:0\n"
  "v_smfmac_f32_16x16x64_bf16 v[156:159], v[120:123], a[72:79], v5\n"
  "v_smfmac_f32_16x16x64_bf16 v[160:163], v[120:123], a[80:87], v5\n"
  "ds_read_b128 v[36:39], v18 offset:1024\n"
  "v_smfmac_f32_16x16x64_bf16 v[164:167], v[120:123], a[88:95], v5\n"
  "v_smfmac_f32_16x16x64_bf16 v[152:155], v[124:127], a[96:103], v5\n"
  "ds_read_b128 v[40:43], v18 offset:2048\n"
  "v_smfmac_f32_16x16x64_bf16 v[156:159], v[124:127], a[104:111], v5\n"
  "v_smfmac_f32_16x16x64_bf16 v[160:163], v[124:127], a[112:119], v5\n"
  "ds_read_b128 v[44:47], v18 offset:3072\n"
  "v_smfmac_f32_16x16x64_bf16 v[164:167], v[124:127], a[120:127], v5\n"
  "v_smfmac_f32_16x16x64_bf16 v[152:155], v[128:131], a[128:135], v5\n"
  "ds_read_b128 v[52:55], v18 offset:5120\n"
  "v_smfmac_f32_16x16x64_bf16 v[156:159], v[128:131], a[136:143], v5\n"
  "v_smfmac_f32_16x16x64_bf16 v[160:163], v[128:131], a[144:151], v5\n"
  "ds_read_b128 v[56:59], v18 offset:6144\n"
  "v_smfmac_f32_16x16x64_bf16 v[164:167], v[128:131], a[152:159], v5\n"
  "v_smfmac_f32_16x16x64_bf16 v[152:155], v[132:135], a[160:167], v5\n"
  "ds_read_b128 v[60:63], v18 offset:7168\n"
  "v_smfmac_f32_16x16x64_bf16 v[156:159], v[132:135], a[168:175], v5\n"
  "v_smfmac_f32_16x16x64_bf16 v[160:163], v[132:135], a[176:183], v5\n"
  "ds_read_b128 v[64:67], v18 offset:8192\n"
  "v_smfmac_f32_16x16x64_bf16 v[164:167], v[132:135], a[184:191], v5\n"
  "v_smfmac_f32_16x16x64_bf16 v[152:155], v[136:139], a[192:199], v5\n"
  "ds_read_b128 v[72:75], v18 offset:10240\n"
  "v_smfmac_f32_16x16x64_bf16 v[156:159], v[136:139], a[200:207], v5\n"
  "v_smfmac_f32_16x16x64_bf16 v[160:163], v[136:139], a[208:215], v5\n"
  "ds_read_b128 v[76:79], v18 offset:11264\n"
  "v_smfmac_f32_16x16x64_bf16 v[164:167], v[136:139], a[216:223], v5\n"
  "v_smfmac_f32_16x16x64_bf16 v[152:155], v[140:143], a[224:231], v5\n"
  "ds_read_b128 v[80:83], v18 offset:12288\n"
  "v_smfmac_f32_16x16x64_bf16 v[156:159], v[140:143], a[232:239], v5\n"
  "v_smfmac_f32_16x16x64_bf16 v[160:163], v[140:143], a[240:247], v5\n"
  "ds_read_b128 v[84:87], v18 offset:13312\n"
  "v_smfmac_f32_16x16x64_bf16 v[164:167], v[140:143], a[248:255], v5\n"
  "v_smfmac_f32_16x16x64_bf16 v[152:155], v[144:147], v[192:199], v5\n"
  "ds_read_b128 v[92:95], v18 offset:15360\n"
  "v_smfmac_f32_16x16x64_bf16 v[156:159], v[144:147], v[200:207], v5\n"
  "v_smfmac_f32_16x16x64_bf16 v[160:163], v[144:147], v[208:215], v5\n"
  "ds_read_b128 v[96:99], v18 offset:16384\n"
  "v_smfmac_f32_16x16x64_bf16 v[164:167], v[144:147], v[216:223], v5\n"
  "v_smfmac_f32_16x16x64_bf16 v[152:155], v[148:151], v[224:231], v5\n"
  "s_waitcnt lgkmcnt(10)\n"
  "ds_read_b128 v[100:103], v18 offset:17408\n"
  "v_smfmac_f32_16x16x64_bf16 v[156:159], v[148:151], v[232:239], v5\n"
  "v_smfmac_f32_16x16x64_bf16 v[160:163], v[148:151], v[240:247], v5\n"
  "ds_read_b128 v[104:107], v18 offset:18432\n"
  "v_smfmac_f32_16x16x64_bf16 v[164:167], v[148:151], v[248:255], v5\n"
  "v_mov_b32 v0, v0\n"
  "v_mov_b32 v0, v0\n"
  "v_mov_b32 v0, v0\n"
  "v_mov_b32 v0, v0\n"
  "v_mov_b32 v0, v0\n"
  "v_mov_b32 v0, v0\n"
  "v_permlane32_swap_b32 v152, v160\n"
  "v_permlane32_swap_b32 v153, v161\n"
  "v_permlane32_swap_b32 v154, v162\n"
  "v_permlane32_swap_b32 v155, v163\n"
  "v_permlane32_swap_b32 v156, v164\n"
  "v_permlane32_swap_b32 v157, v165\n"
  "v_permlane32_swap_b32 v158, v166\n"
  "v_permlane32_swap_b32 v159, v167\n"
  "v_pk_add_f32 v[152:153], v[152:153], v[160:161]\n"
  "v_pk_add_f32 v[154:155], v[154:155], v[162:163]\n"
  "v_pk_add_f32 v[156:157], v[156:157], v[164:165]\n"
  "v_pk_add_f32 v[158:159], v[158:159], v[166:167]\n"
  "v_cvt_pk_bf16_f32 v152, v152, v153\n"
  "v_cvt_pk_bf16_f32 v153, v154, v155\n"
  "v_cvt_pk_bf16_f32 v154, v156, v157\n"
  "v_cvt_pk_bf16_f32 v155, v158, v159\n"
  "s_nop 0\n"
  "v_permlane16_swap_b32 v152, v154\n"
  "v_permlane16_swap_b32 v153, v155\n"
  "ds_write_b128 v19, v[152:155] offset:4096\n"
  "s_waitcnt lgkmcnt(0)\n"
  "s_barrier\n"
  "s_cmp_lg_u32 s44, 0\n"
  "s_cbranch_scc1 C_OTHER_%=\n"
  "s_cmp_eq_u32 s56, 63\n"
  "s_cbranch_scc1 C_LAST_%=\n"
  "ds_read_b128 v[48:51], v18 offset:4096\n"
  "ds_read_b128 v[68:71], v18 offset:9216\n"
  "ds_read_b128 v[88:91], v18 offset:14336\n"
  "ds_read_b128 v[108:111], v18 offset:19456\n"
  "s_add_u32 s68, s57, 0\n"
  "s_add_u32 s69, s57, 16384\n"
  "s_add_u32 s70, s57, 32768\n"
  "s_add_u32 s71, s57, 49152\n"
  "s_add_u32 s72, s58, 131072\n"
  "s_mov_b64 s[66:67], 0\n"
  "s_sub_u32 s11, s59, 19\n"
  "s_waitcnt lgkmcnt(0)\n"
  "C_WT2_%=:\n"
  "s_memrealtime s[64:65]\n"
  "s_waitcnt lgkmcnt(0)\n"
  "s_sub_i32 s60, s64, s11\n"
  "s_cmp_lt_i32 s60, 0\n"
  "s_cbranch_scc1 C_WT2_%=\n"
  "s_cmp_gt_i32 s60, 10\n"
  "s_cbranch_scc1 C_WT2C_%=\n"
  "s_memtime vcc\n"
  "s_memrealtime s[28:29]\n"
  "s_nop 2\n"
  "s_memrealtime s[30:31]\n"
  "s_nop 2\n"
  "s_memrealtime s[12:13]\n"
  "s_nop 2\n"
  "s_memrealtime s[14:15]\n"
  "s_nop 2\n"
  "s_memrealtime s[0:1]\n"
  "s_nop 2\n"
  "s_memrealtime s[64:65]\n"
  "s_nop 2\n"
  "s_memrealtime s[74:75]\n"
  "s_mov_b32 s61, 0\n"
  "s_waitcnt lgkmcnt(0)\n"
  "s_cmp_eq_u32 s30, s28\n"
  "s_addc_u32 s61, s61, 0\n"
  "s_cmp_eq_u32 s12, s28\n"
  "s_addc_u32 s61, s61, 0\n"
  "s_cmp_eq_u32 s14, s28\n"
  "s_addc_u32 s61, s61, 0\n"
  "s_cmp_eq_u32 s0, s28\n"
  "s_addc_u32 s61, s61, 0\n"
  "s_cmp_eq_u32 s64, s28\n"
  "s_addc_u32 s61, s61, 0\n"
  "s_cmp_eq_u32 s74, s28\n"
  "s_addc_u32 s61, s61, 0\n"
  "s_sub_i32 s60, s59, s28\n"
  "s_sub_i32 s60, s60, 4\n"
  "s_mul_i32 s60, s60, s6\n"
  "s_ashr_i32 s60, s60, 4\n"
  "s_lshl4_add_u32 s60, s61, s60\n"
  "s_max_i32 s60, s60, 256\n"
  "s_min_i32 s60, s60, 508\n"
  "s_add_u32 s11, vcc_lo, s60\n"
  "s_and_b32 s60, s60, -4\n"
  "s_getpc_b64 s[64:65]\n"
  "s_sub_u32 s60, 532, s60\n"
  "s_add_u32 s64, s64, s60\n"
  "s_addc_u32 s65, s65, 0\n"
  "s_setpc_b64 s[64:65]\n"
  "s_nop 0\n"
  "s_nop 0\n"
  "s_nop 0\n"
  "s_nop 0\n"
  "s_nop 0\n"
  "s_nop 0\n"
  "s_nop 0\n"
  "s_nop 0\n"
  "s_nop 0\n"
  "s_nop 0\n"
  "s_nop 0\n"
  "s_nop 0\n"
  "s_nop 0\n"
  "s_nop 0\n"
  "s_nop 0\n"
  "s_nop 0\n"
  "s_nop 0\n"
  "s_nop 0\n"
  "s_nop 0\n"
  "s_nop 0\n"
  "s_nop 0\n"
  "s_nop 0\n"
  "s_nop 0\n"
  "s_nop 0\n"
  "s_nop 0\n"
  "s_nop 0\n"
  "s_nop 0\n"
  "s_nop 0\n"
  "s_nop 0\n"
  "s_nop 0\n"
  "s_nop 0\n"
  "s_nop 0\n"
  "s_nop 0\n"
  "s_nop 0\n"
  "s_nop 0\n"
  "s_nop 0\n"
  "s_nop 0\n"
  "s_nop 0\n"
  "s_nop 0\n"
  "s_nop 0\n"
  "s_nop 0\n"
  "s_nop 0\n"
  "s_nop 0\n"
  "s_nop 0\n"
  "s_nop 0\n"
  "s_nop 0\n"
  "s_nop 0\n"
  "s_nop 0\n"
  "s_nop 0\n"
  "s_nop 0\n"
  "s_nop 0\n"
  "s_nop 0\n"
  "s_nop 0\n"
  "s_nop 0\n"
  "s_nop 0\n"
  "s_nop 0\n"
  "s_nop 0\n"
  "s_nop 0\n"
  "s_nop 0\n"
  "s_nop 0\n"
  "s_nop 0\n"
  "s_nop 0\n"
  "s_nop 0\n"
  "s_nop 0\n"
  "C_WT2K_%=:\n"
  "buffer_load_dwordx4 v[112:115], v6, s[16:19], s68 offen offset:0 sc1\n"
  "buffer_load_dwordx4 v[116:119], v6, s[16:19], s68 offen offset:1280 sc1\n"
  "buffer_load_dwordx4 v[120:123], v6, s[16:19], s68 offen offset:2560 sc1\n"
  "buffer_load_dwordx4 v[124:127], v6, s[16:19], s68 offen offset:3840 sc1\n"
  "buffer_load_dwordx4 v[128:131], v7, s[16:19], s68 offen sc1\n"
  "buffer_load_dwordx4 v[132:135], v6, s[16:19], s69 offen offset:0 sc1\n"
  "buffer_load_dwordx4 v[136:139], v6, s[16:19], s69 offen offset:1280 sc1\n"
  "buffer_load_dwordx4 v[140:143], v6, s[16:19], s69 offen offset:2560 sc1\n"
  "buffer_load_dwordx4 v[144:147], v6, s[16:19], s69 offen offset:3840 sc1\n"
  "buffer_load_dwordx4 v[148:151], v7, s[16:19], s69 offen sc1\n"
  "buffer_load_dwordx4 v[152:155], v6, s[16:19], s70 offen offset:0 sc1\n"
  "buffer_load_dwordx4 v[156:159], v6, s[16:19], s70 offen offset:1280 sc1\n"
  "buffer_load_dwordx4 v[160:163], v6, s[16:19], s70 offen offset:2560 sc1\n"
  "buffer_load_dwordx4 v[164:167], v6, s[16:19], s70 offen offset:3840 sc1\n"
  "buffer_load_dwordx4 v[168:171], v7, s[16:19], s70 offen sc1\n"
  "buffer_load_dwordx4 v[172:175], v6, s[16:19], s71 offen offset:0 sc1\n"
  "buffer_load_dwordx4 v[176:179], v6, s[16:19], s71 offen offset:1280 sc1\n"
  "buffer_load_dwordx4 v[180:183], v6, s[16:19], s71 offen offset:2560 sc1\n"
  "buffer_load_dwordx4 v[184:187], v6, s[16:19], s71 offen offset:3840 sc1\n"
  "buffer_load_dwordx4 v[188:191], v7, s[16:19], s71 offen sc1\n"
  "C_P2_%=:\n"
  "s_waitcnt vmcnt(19)\n"
  "v_mfma_f32_16x16x32_bf16 v[24:27], v[32:35], v[112:115], 0\n"
  "s_waitcnt vmcnt(18)\n"
  "v_mfma_f32_16x16x32_bf16 v[24:27], v[36:39], v[116:119], v[24:27]\n"
  "s_waitcnt vmcnt(17)\n"
  "v_mfma_f32_16x16x32_bf16 v[24:27], v[40:43], v[120:123], v[24:27]\n"
  "s_waitcnt vmcnt(16)\n"
  "v_mfma_f32_16x16x32_bf16 v[24:27], v[44:47], v[124:127], v[24:27]\n"
  "s_waitcnt vmcnt(15)\n"
  "v_mfma_f32_16x16x32_bf16 v[24:27], v[48:51], v[128:131], v[24:27]\n"
  "s_waitcnt vmcnt(14)\n"
  "v_mfma_f32_16x16x32_bf16 v[24:27], v[52:55], v[132:135], v[24:27]\n"
  "s_waitcnt vmcnt(13)\n"
  "v_mfma_f32_16x16x32_bf16 v[24:27], v[56:59], v[136:139], v[24:27]\n"
  "s_waitcnt vmcnt(12)\n"
  "v_mfma_f32_16x16x32_bf16 v[24:27], v[60:63], v[140:143], v[24:27]\n"
  "s_waitcnt vmcnt(11)\n"
  "v_mfma_f32_16x16x32_bf16 v[24:27], v[64:67], v[144:147], v[24:27]\n"
  "v_mov_b32 v0, v0\n"
  "s_waitcnt vmcnt(10)\n"
  "v_mfma_f32_16x16x32_bf16 v[24:27], v[68:71], v[148:151], v[24:27]\n"
  "v_mov_b32 v0, v0\n"
  "s_waitcnt vmcnt(9)\n"
  "v_mfma_f32_16x16x32_bf16 v[24:27], v[72:75], v[152:155], v[24:27]\n"
  "v_mov_b32 v0, v0\n"
  "s_waitcnt vmcnt(8)\n"
  "v_mfma_f32_16x16x32_bf16 v[24:27], v[76:79], v[156:159], v[24:27]\n"
  "v_mov_b32 v0, v0\n"
  "s_waitcnt vmcnt(7)\n"
  "v_mfma_f32_16x16x32_bf16 v[24:27], v[80:83], v[160:163], v[24:27]\n"
  "v_mov_b32 v0, v0\n"
  "s_waitcnt vmcnt(6)\n"
  "v_mfma_f32_16x16x32_bf16 v[24:27], v[84:87], v[164:167], v[24:27]\n"
  "v_mov_b32 v0, v0\n"
  "s_waitcnt vmcnt(5)\n"
  "v_mfma_f32_16x16x32_bf16 v[24:27], v[88:91], v[168:171], v[24:27]\n"
  "v_mov_b32 v0, v0\n"
  "s_waitcnt vmcnt(4)\n"
  "v_mfma_f32_16x16x32_bf16 v[24:27], v[92:95], v[172:175], v[24:27]\n"
  "v_mov_b32 v0, v0\n"
  "s_waitcnt vmcnt(3)\n"
  "v_mfma_f32_16x16x32_bf16 v[24:27], v[96:99], v[176:179], v[24:27]\n"
  "v_mov_b32 v0, v0\n"
  "s_waitcnt vmcnt(2)\n"
  "v_mfma_f32_16x16x32_bf16 v[24:27], v[100:103], v[180:183], v[24:27]\n"
  "v_mov_b32 v0, v0\n"
  "s_waitcnt vmcnt(1)\n"
  "v_mfma_f32_16x16x32_bf16 v[24:27], v[104:107], v[184:187], v[24:27]\n"
  "v_mov_b32 v0, v0\n"
  "s_waitcnt vmcnt(0)\n"
  "v_mfma_f32_16x16x32_bf16 v[24:27], v[108:111], v[188:191], v[24:27]\n"
  "v_mov_b32 v0, v0\n"
  "v_mov_b32 v0, v0\n"
  "v_mov_b32 v0, v0\n"
  "v_mov_b32 v0, v0\n"
  "v_mov_b32 v0, v0\n"
  "v_mov_b32 v0, v0\n"
  "v_mov_b32 v0, v0\n"
  "v_mov_b32 v0, v0\n"
  "v_add_f32_dpp v24, v26, v24 row_ror:8 row_mask:0xf bank_mask:0xf\n"
  "v_add_f32_dpp v25, v27, v25 row_ror:8 row_mask:0xf bank_mask:0xf\n"
  "v_cmp_u_f32 vcc, v24, v24\n"
  "v_cvt_pk_bf16_f32 v24, v24, v25\n"
  "s_andn2_b64 vcc, vcc, s[66:67]\n"
  "v_min_u32 v24, s78, v24\n"
  "s_cbranch_vccnz C_REDO_%=\n"
  "s_nop 0\n"
  "v_mov_b32_dpp v24, v24 row_ror:8 row_mask:0xf bank_mask:0xc\n"
  "buffer_store_dword v24, v11, s[16:19], s58 offen sc1\n"
  "buffer_store_dword v24, v11, s[16:19], s72 offen sc1\n"
  "s_memtime s[64:65]\n"
  "s_memrealtime s[28:29]\n"
  "s_waitcnt lgkmcnt(0)\n"
  "C_SKIP2_%=:\n"
  "s_add_u32 s60, s56, 2\n"
  "s_bfe_u32 s57, s60, 131073\n"
  "s_lshl_b32 s57, s57, 18\n"
  "s_xor_b32 s60, s60, s47\n"
  "s_and_b32 s60, s60, 1\n"
  "s_lshl_b32 s60, s60, 13\n"
  "s_add_u32 s57, s57, s60\n"
  "s_xor_b32 s75, s96, 40960\n"
  "v_add_u32 v17, s75, v0\n"
  "s_sub_u32 s97, 237568, s97\n"
  "s_mul_i32 s60, s44, 5120\n"
  "s_add_u32 s60, s60, s97\n"
  "v_add_u32 v19, s60, v0\n"
  "s_cmp_ge_u32 s56, 62\n"
  "s_cbranch_scc0 C_DOU0_%=\n"
  "s_add_u32 s72, s73, 131072\n"
  "buffer_store_dword v21, v11, s[16:19], s73 offen sc1\n"
  "buffer_store_dword v21, v11, s[16:19], s72 offen sc1\n"
  "s_branch C_JOIN_%=\n"
  "C_DOU0_%=:\n"
  "ds_read_b128 v[64:67], v17 offset:0\n"
  "ds_read_b128 v[68:71], v17 offset:10240\n"
  "ds_read_b128 v[72:75], v17 offset:20480\n"
  "ds_read_b128 v[76:79], v17 offset:30720\n"
  "s_waitcnt lgkmcnt(3)\n"
  "v_mfma_f32_16x16x32_bf16 v[32:35], v[64:67], a[0:3], 0\n"
  "s_waitcnt lgkmcnt(2)\n"
  "v_mfma_f32_16x16x32_bf16 v[36:39], v[68:71], a[0:3], 0\n"
  "ds_read_b128 v[80:83], v17 offset:1024\n"
  "v_mfma_f32_16x16x32_bf16 v[40:43], v[64:67], a[8:11], 0\n"
  "v_mfma_f32_16x16x32_bf16 v[44:47], v[68:71], a[8:11], 0\n"
  "ds_read_b128 v[84:87], v17 offset:11264\n"
  "v_mfma_f32_16x16x32_bf16 v[48:51], v[64:67], a[16:19], 0\n"
  "v_mfma_f32_16x16x32_bf16 v[52:55], v[68:71], a[16:19], 0\n"
  "ds_read_b128 v[88:91], v17 offset:21504\n"
  "v_mfma_f32_16x16x32_bf16 v[56:59], v[64:67], a[24:27], 0\n"
  "v_mfma_f32_16x16x32_bf16 v[60:63], v[68:71], a[24:27], 0\n"
  "ds_read_b128 v[92:95], v17 offset:31744\n"
  "s_waitcnt lgkmcnt(5)\n"
  "v_mfma_f32_16x16x32_bf16 v[32:35], v[72:75], a[4:7], v[32:35]\n"
  "s_waitcnt lgkmcnt(4)\n"
  "v_mfma_f32_16x16x32_bf16 v[36:39], v[76:79], a[4:7], v[36:39]\n"
  "s_add_u32 s72, s73, 131072\n"
  "buffer_store_dword v21, v11, s[16:19], s73 offen sc1\n"
  "v_mfma_f32_16x16x32_bf16 v[40:43], v[72:75], a[12:15], v[40:43]\n"
  "buffer_store_dword v21, v11, s[16:19], s72 offen sc1\n"
  "v_mfma_f32_16x16x32_bf16 v[44:47], v[76:79], a[12:15], v[44:47]\n"
  "v_mfma_f32_16x16x32_bf16 v[48:51], v[72:75], a[20:23], v[48:51]\n"
  "v_mfma_f32_16x16x32_bf16 v[52:55], v[76:79], a[20:23], v[52:55]\n"
  "v_mfma_f32_16x16x32_bf16 v[56:59], v[72:75], a[28:31], v[56:59]\n"
  "v_mfma_f32_16x16x32_bf16 v[60:63], v[76:79], a[28:31], v[60:63]\n"
  "s_waitcnt lgkmcnt(3)\n"
  "v_mfma_f32_16x16x32_bf16 v[32:35], v[80:83], a[32:35], v[32:35]\n"
  "s_waitcnt lgkmcnt(2)\n"
  "v_mfma_f32_16x16x32_bf16 v[36:39], v[84:87], a[32:35], v[36:39]\n"
  "ds_read_b128 v[64:67], v17 offset:2048\n"
  "s_sub_u32 s60, s11, s4\n"
  "v_mfma_f32_16x16x32_bf16 v[40:43], v[80:83], a[40:43], v[40:43]\n"
  "s_sub_u32 s61, s59, s5\n"
  "v_cvt_f32_u32 v28, s61\n"
  "v_mfma_f32_16x16x32_bf16 v[44:47], v[84:87], a[40:43], v[44:47]\n"
  "ds_read_b128 v[68:71], v17 offset:12288\n"
  "v_cvt_f32_u32 v29, s60\n"
  "v_mfma_f32_16x16x32_bf16 v[48:51], v[80:83], a[48:51], v[48:51]\n"
  "v_rcp_f32 v28, v28\n"
  "v_mfma_f32_16x16x32_bf16 v[52:55], v[84:87], a[48:51], v[52:55]\n"
  "ds_read_b128 v[72:75], v17 offset:22528\n"
  "s_mov_b32 s4, s11\n"
  "v_mfma_f32_16x16x32_bf16 v[56:59], v[80:83], a[56:59], v[56:59]\n"
  "s_mov_b32 s5, s59\n"
  "v_mul_f32 v28, v28, v29\n"
  "v_mfma_f32_16x16x32_bf16 v[60:63], v[84:87], a[56:59], v[60:63]\n"
  "ds_read_b128 v[76:79], v17 offset:32768\n"
  "v_min_f32 v28, 0x41c80000, v28\n"
  "s_waitcnt lgkmcnt(5)\n"
  "v_mfma_f32_16x16x32_bf16 v[32:35], v[88:91], a[36:39], v[32:35]\n"
  "v_max_f32 v28, 0x41600000, v28\n"
  "v_mul_f32 v29, 0x41800000, v28\n"
  "s_waitcnt lgkmcnt(4)\n"
  "v_mfma_f32_16x16x32_bf16 v[36:39], v[92:95], a[36:39], v[36:39]\n"
  "v_rcp_f32 v28, v28\n"
  "v_mfma_f32_16x16x32_bf16 v[40:43], v[88:91], a[44:47], v[40:43]\n"
  "v_cvt_u32_f32 v29, v29\n"
  "s_sub_i32 s60, s64, s11\n"
  "v_mfma_f32_16x16x32_bf16 v[44:47], v[92:95], a[44:47], v[44:47]\n"
  "s_lshl_b32 s60, s60, 2\n"
  "v_readfirstlane_b32 s6, v29\n"
  "v_mfma_f32_16x16x32_bf16 v[48:51], v[88:91], a[52:55], v[48:51]\n"
  "v_cvt_f32_i32 v29, s60\n"
  "s_sub_i32 s60, s28, s59\n"
  "v_mfma_f32_16x16x32_bf16 v[52:55], v[92:95], a[52:55], v[52:55]\n"
  "v_mul_f32 v28, v28, v29\n"
  "s_lshl_b32 s60, s60, 2\n"
  "v_mfma_f32_16x16x32_bf16 v[56:59], v[88:91], a[60:63], v[56:59]\n"
  "v_cvt_i32_f32 v28, v28\n"
  "s_add_u32 s61, s60, 16\n"
  "v_mfma_f32_16x16x32_bf16 v[60:63], v[92:95], a[60:63], v[60:63]\n"
  "v_max_i32 v28, s60, v28\n"
  "v_min_i32 v28, s61, v28\n"
  "s_waitcnt lgkmcnt(3)\n"
  "v_mfma_f32_16x16x32_bf16 v[32:35], v[64:67], a[64:67], v[32:35]\n"
  "s_cmp_lt_u32 s56, 2\n"
  "s_cselect_b64 s[66:67], -1, s[66:67]\n"
  "s_waitcnt lgkmcnt(2)\n"
  "v_mfma_f32_16x16x32_bf16 v[36:39], v[68:71], a[64:67], v[36:39]\n"
  "ds_read_b128 v[80:83], v17 offset:3072\n"
  "s_cmp_eq_u64 s[66:67], 0\n"
  "v_mfma_f32_16x16x32_bf16 v[40:43], v[64:67], a[72:75], v[40:43]\n"
  "s_cselect_b32 s60, s2, 0x80000000\n"
  "v_mov_b32 v29, s60\n"
  "v_mfma_f32_16x16x32_bf16 v[44:47], v[68:71], a[72:75], v[44:47]\n"
  "ds_read_b128 v[84:87], v17 offset:13312\n"
  "s_lshl_b32 s68, s45, 14\n"
  "v_mfma_f32_16x16x32_bf16 v[48:51], v[64:67], a[80:83], v[48:51]\n"
  "s_add_u32 s68, s68, 1852416\n"
  "s_add_u32 s69, s68, 8192\n"
  "v_mfma_f32_16x16x32_bf16 v[52:55], v[68:71], a[80:83], v[52:55]\n"
  "ds_read_b128 v[88:91], v17 offset:23552\n"
  "s_andn2_b32 s60, 0x80000000, s53\n"
  "v_mfma_f32_16x16x32_bf16 v[56:59], v[64:67], a[88:91], v[56:59]\n"
  "v_lshlrev_b32 v96, 3, v0\n"
  "v_or_b32 v96, s60, v96\n"
  "v_mfma_f32_16x16x32_bf16 v[60:63], v[68:71], a[88:91], v[60:63]\n"
  "ds_read_b128 v[92:95], v17 offset:33792\n"
  "s_mov_b64 exec, 1\n"
  "buffer_store_dword v28, v29, s[16:19], 0 offen sc1\n"
  "s_mov_b64 exec, -1\n"
  "s_waitcnt lgkmcnt(5)\n"
  "v_mfma_f32_16x16x32_bf16 v[32:35], v[72:75], a[68:71], v[32:35]\n"
  "s_waitcnt lgkmcnt(4)\n"
  "v_mfma_f32_16x16x32_bf16 v[36:39], v[76:79], a[68:71], v[36:39]\n"
  "v_mfma_f32_16x16x32_bf16 v[40:43], v[72:75], a[76:79], v[40:43]\n"
  "v_mfma_f32_16x16x32_bf16 v[44:47], v[76:79], a[76:79], v[44:47]\n"
  "v_mfma_f32_16x16x32_bf16 v[48:51], v[72:75], a[84:87], v[48:51]\n"
  "v_mfma_f32_16x16x32_bf16 v[52:55], v[76:79], a[84:87], v[52:55]\n"
  "v_mfma_f32_16x16x32_bf16 v[56:59], v[72:75], a[92:95], v[56:59]\n"
  "v_mfma_f32_16x16x32_bf16 v[60:63], v[76:79], a[92:95], v[60:63]\n"
  "s_waitcnt lgkmcnt(3)\n"
  "v_mfma_f32_16x16x32_bf16 v[32:35], v[80:83], a[96:99], v[32:35]\n"
  "s_waitcnt lgkmcnt(2)\n"
  "v_mfma_f32_16x16x32_bf16 v[36:39], v[84:87], a[96:99], v[36:39]\n"
  "ds_read_b128 v[64:67], v17 offset:4096\n"
  "v_mfma_f32_16x16x32_bf16 v[40:43], v[80:83], a[104:107], v[40:43]\n"
  "v_mfma_f32_16x16x32_bf16 v[44:47], v[84:87], a[104:107], v[44:47]\n"
  "ds_read_b128 v[68:71], v17 offset:14336\n"
  "v_mfma_f32_16x16x32_bf16 v[48:51], v[80:83], a[112:115], v[48:51]\n"
  "v_mfma_f32_16x16x32_bf16 v[52:55], v[84:87], a[112:115], v[52:55]\n"
  "ds_read_b128 v[72:75], v17 offset:24576\n"
  "v_mfma_f32_16x16x32_bf16 v[56:59], v[80:83], a[120:123], v[56:59]\n"
  "v_mfma_f32_16x16x32_bf16 v[60:63], v[84:87], a[120:123], v[60:63]\n"
  "ds_read_b128 v[76:79], v17 offset:34816\n"
  "s_waitcnt lgkmcnt(5)\n"
  "v_mfma_f32_16x16x32_bf16 v[32:35], v[88:91], a[100:103], v[32:35]\n"
  "s_waitcnt lgkmcnt(4)\n"
  "v_mfma_f32_16x16x32_bf16 v[36:39], v[92:95], a[100:103], v[36:39]\n"
  "v_mfma_f32_16x16x32_bf16 v[40:43], v[88:91], a[108:111], v[40:43]\n"
  "v_mfma_f32_16x16x32_bf16 v[44:47], v[92:95], a[108:111], v[44:47]\n"
  "v_mfma_f32_16x16x32_bf16 v[48:51], v[88:91], a[116:119], v[48:51]\n"
  "v_mfma_f32_16x16x32_bf16 v[52:55], v[92:95], a[116:119], v[52:55]\n"
  "v_mfma_f32_16x16x32_bf16 v[56:59], v[88:91], a[124:127], v[56:59]\n"
  "v_mfma_f32_16x16x32_bf16 v[60:63], v[92:95], a[124:127], v[60:63]\n"
  "s_waitcnt lgkmcnt(3)\n"
  "v_mfma_f32_16x16x32_bf16 v[32:35], v[64:67], a[128:131], v[32:35]\n"
  "s_waitcnt lgkmcnt(2)\n"
  "v_mfma_f32_16x16x32_bf16 v[36:39], v[68:71], a[128:131], v[36:39]\n"
  "ds_read_b128 v[80:83], v17 offset:5120\n"
  "v_mfma_f32_16x16x32_bf16 v[40:43], v[64:67], a[136:139], v[40:43]\n"
  "v_mfma_f32_16x16x32_bf16 v[44:47], v[68:71], a[136:139], v[44:47]\n"
  "ds_read_b128 v[84:87], v17 offset:15360\n"
  "v_mfma_f32_16x16x32_bf16 v[48:51], v[64:67], a[144:147], v[48:51]\n"
  "v_mfma_f32_16x16x32_bf16 v[52:55], v[68:71], a[144:147], v[52:55]\n"
  "ds_read_b128 v[88:91], v17 offset:25600\n"
  "v_mfma_f32_16x16x32_bf16 v[56:59], v[64:67], a[152:155], v[56:59]\n"
  "v_mfma_f32_16x16x32_bf16 v[60:63], v[68:71], a[152:155], v[60:63]\n"
  "ds_read_b128 v[92:95], v17 offset:35840\n"
  "s_waitcnt lgkmcnt(5)\n"
  "v_mfma_f32_16x16x32_bf16 v[32:35], v[72:75], a[132:135], v[32:35]\n"
  "s_waitcnt lgkmcnt(4)\n"
  "v_mfma_f32_16x16x32_bf16 v[36:39], v[76:79], a[132:135], v[36:39]\n"
  "v_mfma_f32_16x16x32_bf16 v[40:43], v[72:75], a[140:143], v[40:43]\n"
  "buffer_load_dwordx2 v[98:99], v96, s[16:19], s68 offen sc1\n"
  "v_mfma_f32_16x16x32_bf16 v[44:47], v[76:79], a[140:143], v[44:47]\n"
  "buffer_load_dwordx2 v[100:101], v96, s[16:19], s69 offen sc1\n"
  "v_mfma_f32_16x16x32_bf16 v[48:51], v[72:75], a[148:151], v[48:51]\n"
  "v_mfma_f32_16x16x32_bf16 v[52:55], v[76:79], a[148:151], v[52:55]\n"
  "v_mfma_f32_16x16x32_bf16 v[56:59], v[72:75], a[156:159], v[56:59]\n"
  "v_mfma_f32_16x16x32_bf16 v[60:63], v[76:79], a[156:159], v[60:63]\n"
  "s_waitcnt lgkmcnt(3)\n"
  "v_mfma_f32_16x16x32_bf16 v[32:35], v[80:83], a[160:163], v[32:35]\n"
  "s_waitcnt lgkmcnt(2)\n"
  "v_mfma_f32_16x16x32_bf16 v[36:39], v[84:87], a[160:163], v[36:39]\n"
  "ds_read_b128 v[64:67], v17 offset:6144\n"
  "v_mfma_f32_16x16x32_bf16 v[40:43], v[80:83], a[168:171], v[40:43]\n"
  "v_mfma_f32_16x16x32_bf16 v[44:47], v[84:87], a[168:171], v[44:47]\n"
  "ds_read_b128 v[68:71], v17 offset:16384\n"
  "v_mfma_f32_16x16x32_bf16 v[48:51], v[80:83], a[176:179], v[48:51]\n"
  "v_mfma_f32_16x16x32_bf16 v[52:55], v[84:87], a[176:179], v[52:55]\n"
  "ds_read_b128 v[72:75], v17 offset:26624\n"
  "v_mfma_f32_16x16x32_bf16 v[56:59], v[80:83], a[184:187], v[56:59]\n"
  "v_mfma_f32_16x16x32_bf16 v[60:63], v[84:87], a[184:187], v[60:63]\n"
  "ds_read_b128 v[76:79], v17 offset:36864\n"
  "s_waitcnt lgkmcnt(5)\n"
  "v_mfma_f32_16x16x32_bf16 v[32:35], v[88:91], a[164:167], v[32:35]\n"
  "s_waitcnt lgkmcnt(4)\n"
  "v_mfma_f32_16x16x32_bf16 v[36:39], v[92:95], a[164:167], v[36:39]\n"
  "v_mfma_f32_16x16x32_bf16 v[40:43], v[88:91], a[172:175], v[40:43]\n"
  "v_mfma_f32_16x16x32_bf16 v[44:47], v[92:95], a[172:175], v[44:47]\n"
  "v_mfma_f32_16x16x32_bf16 v[48:51], v[88:91], a[180:183], v[48:51]\n"
  "v_mfma_f32_16x16x32_bf16 v[52:55], v[92:95], a[180:183], v[52:55]\n"
  "v_mfma_f32_16x16x32_bf16 v[56:59], v[88:91], a[188:191], v[56:59]\n"
  "v_mfma_f32_16x16x32_bf16 v[60:63], v[92:95], a[188:191], v[60:63]\n"
  "s_waitcnt lgkmcnt(3)\n"
  "v_mfma_f32_16x16x32_bf16 v[32:35], v[64:67], a[192:195], v[32:35]\n"
  "s_waitcnt lgkmcnt(2)\n"
  "v_mfma_f32_16x16x32_bf16 v[36:39], v[68:71], a[192:195], v[36:39]\n"
  "ds_read_b128 v[80:83], v17 offset:7168\n"
  "v_mfma_f32_16x16x32_bf16 v[40:43], v[64:67], a[200:203], v[40:43]\n"
  "v_mfma_f32_16x16x32_bf16 v[44:47], v[68:71], a[200:203], v[44:47]\n"
  "ds_read_b128 v[84:87], v17 offset:17408\n"
  "v_mfma_f32_16x16x32_bf16 v[48:51], v[64:67], a[208:211], v[48:51]\n"
  "v_mfma_f32_16x16x32_bf16 v[52:55], v[68:71], a[208:211], v[52:55]\n"
  "ds_read_b128 v[88:91], v17 offset:27648\n"
  "v_mfma_f32_16x16x32_bf16 v[56:59], v[64:67], a[216:219], v[56:59]\n"
  "v_mfma_f32_16x16x32_bf16 v[60:63], v[68:71], a[216:219], v[60:63]\n"
  "ds_read_b128 v[92:95], v17 offset:37888\n"
  "s_waitcnt lgkmcnt(5)\n"
  "v_mfma_f32_16x16x32_bf16 v[32:35], v[72:75], a[196:199], v[32:35]\n"
  "s_waitcnt lgkmcnt(4)\n"
  "v_mfma_f32_16x16x32_bf16 v[36:39], v[76:79], a[196:199], v[36:39]\n"
  "v_mfma_f32_16x16x32_bf16 v[40:43], v[72:75], a[204:207], v[40:43]\n"
  "v_mfma_f32_16x16x32_bf16 v[44:47], v[76:79], a[204:207], v[44:47]\n"
  "v_mfma_f32_16x16x32_bf16 v[48:51], v[72:75], a[212:215], v[48:51]\n"
  "s_waitcnt vmcnt(0)\n"
  "v_max_i32 v99, v99, v101\n"
  "v_max_i32_dpp v98, v98, v98 row_ror:1 row_mask:0xf bank_mask:0xf\n"
  "v_mfma_f32_16x16x32_bf16 v[52:55], v[76:79], a[212:215], v[52:55]\n"
  "v_max_i32_dpp v99, v99, v99 row_ror:1 row_mask:0xf bank_mask:0xf\n"
  "v_max_i32_dpp v100, v100, v100 row_ror:1 row_mask:0xf bank_mask:0xf\n"
  "v_mfma_f32_16x16x32_bf16 v[56:59], v[72:75], a[220:223], v[56:59]\n"
  "v_max_i32_dpp v98, v98, v98 row_ror:2 row_mask:0xf bank_mask:0xf\n"
  "v_max_i32_dpp v99, v99, v99 row_ror:2 row_mask:0xf bank_mask:0xf\n"
  "v_mfma_f32_16x16x32_bf16 v[60:63], v[76:79], a[220:223], v[60:63]\n"
  "v_max_i32_dpp v100, v100, v100 row_ror:2 row_mask:0xf bank_mask:0xf\n"
  "v_max_i32_dpp v98, v98, v98 row_ror:4 row_mask:0xf bank_mask:0xf\n"
  "s_waitcnt lgkmcnt(3)\n"
  "v_mfma_f32_16x16x32_bf16 v[32:35], v[80:83], a[224:227], v[32:35]\n"
  "v_max_i32_dpp v99, v99, v99 row_ror:4 row_mask:0xf bank_mask:0xf\n"
  "v_max_i32_dpp v100, v100, v100 row_ror:4 row_mask:0xf bank_mask:0xf\n"
  "s_waitcnt lgkmcnt(2)\n"
  "v_mfma_f32_16x16x32_bf16 v[36:39], v[84:87], a[224:227], v[36:39]\n"
  "ds_read_b128 v[64:67], v17 offset:8192\n"
  "v_max_i32_dpp v98, v98, v98 row_ror:8 row_mask:0xf bank_mask:0xf\n"
  "v_mfma_f32_16x16x32_bf16 v[40:43], v[80:83], a[232:235], v[40:43]\n"
  "v_max_i32_dpp v99, v99, v99 row_ror:8 row_mask:0xf bank_mask:0xf\n"
  "v_max_i32_dpp v100, v100, v100 row_ror:8 row_mask:0xf bank_mask:0xf\n"
  "v_mfma_f32_16x16x32_bf16 v[44:47], v[84:87], a[232:235], v[44:47]\n"
  "ds_read_b128 v[68:71], v17 offset:18432\n"
  "v_readlane_b32 s68, v98, 0\n"
  "v_mfma_f32_16x16x32_bf16 v[48:51], v[80:83], a[240:243], v[48:51]\n"
  "v_readlane_b32 s71, v98, 16\n"
  "s_max_i32 s68, s68, s71\n"
  "v_mfma_f32_16x16x32_bf16 v[52:55], v[84:87], a[240:243], v[52:55]\n"
  "ds_read_b128 v[72:75], v17 offset:28672\n"
  "v_readlane_b32 s69, v98, 32\n"
  "v_mfma_f32_16x16x32_bf16 v[56:59], v[80:83], a[248:251], v[56:59]\n"
  "v_readlane_b32 s71, v98, 48\n"
  "s_max_i32 s69, s69, s71\n"
  "v_mfma_f32_16x16x32_bf16 v[60:63], v[84:87], a[248:251], v[60:63]\n"
  "ds_read_b128 v[76:79], v17 offset:38912\n"
  "v_readlane_b32 s71, v100, 0\n"
  "s_waitcnt lgkmcnt(5)\n"
  "v_mfma_f32_16x16x32_bf16 v[32:35], v[88:91], a[228:231], v[32:35]\n"
  "s_max_i32 s69, s69, s71\n"
  "v_readlane_b32 s71, v100, 16\n"
  "s_waitcnt lgkmcnt(4)\n"
  "v_mfma_f32_16x16x32_bf16 v[36:39], v[92:95], a[228:231], v[36:39]\n"
  "s_max_i32 s69, s69, s71\n"
  "v_readlane_b32 s71, v100, 32\n"
  "v_mfma_f32_16x16x32_bf16 v[40:43], v[88:91], a[236:239], v[40:43]\n"
  "s_max_i32 s69, s69, s71\n"
  "v_readlane_b32 s70, v99, 0\n"
  "v_mfma_f32_16x16x32_bf16 v[44:47], v[92:95], a[236:239], v[44:47]\n"
  "v_readlane_b32 s71, v99, 16\n"
  "s_max_i32 s70, s70, s71\n"
  "v_mfma_f32_16x16x32_bf16 v[48:51], v[88:91], a[244:247], v[48:51]\n"
  "v_readlane_b32 s71, v99, 32\n"
  "s_max_i32 s70, s70, s71\n"
  "v_mfma_f32_16x16x32_bf16 v[52:55], v[92:95], a[244:247], v[52:55]\n"
  "v_readlane_b32 s71, v99, 48\n"
  "s_max_i32 s70, s70, s71\n"
  "v_mfma_f32_16x16x32_bf16 v[56:59], v[88:91], a[252:255], v[56:59]\n"
  "s_sub_i32 s71, s68, s9\n"
  "s_ashr_i32 s60, s71, 3\n"
  "v_mfma_f32_16x16x32_bf16 v[60:63], v[92:95], a[252:255], v[60:63]\n"
  "s_min_i32 s60, s60, -1\n"
  "s_min_i32 s71, s71, 6\n"
  "s_waitcnt lgkmcnt(3)\n"
  "v_mfma_f32_16x16x32_bf16 v[32:35], v[64:67], v[192:195], v[32:35]\n"
  "s_max_i32 s71, s71, s60\n"
  "s_add_i32 s71, s71, s9\n"
  "s_waitcnt lgkmcnt(2)\n"
  "v_mfma_f32_16x16x32_bf16 v[36:39], v[68:71], v[192:195], v[36:39]\n"
  "ds_read_b128 v[80:83], v17 offset:9216\n"
  "s_cmp_gt_i32 s9, 0\n"
  "v_mfma_f32_16x16x32_bf16 v[40:43], v[64:67], v[200:203], v[40:43]\n"
  "s_cselect_b32 s9, s71, s68\n"
  "s_sub_i32 s71, s69, s10\n"
  "buffer_load_dwordx2 v[30:31], v10, s[16:19], s57 offen sc1\n"
  "v_mfma_f32_16x16x32_bf16 v[44:47], v[68:71], v[200:203], v[44:47]\n"
  "ds_read_b128 v[84:87], v17 offset:19456\n"
  "s_ashr_i32 s60, s71, 3\n"
  "v_mfma_f32_16x16x32_bf16 v[48:51], v[64:67], v[208:211], v[48:51]\n"
  "s_min_i32 s60, s60, -1\n"
  "s_min_i32 s71, s71, 6\n"
  "v_mfma_f32_16x16x32_bf16 v[52:55], v[68:71], v[208:211], v[52:55]\n"
  "ds_read_b128 v[88:91], v17 offset:29696\n"
  "s_max_i32 s71, s71, s60\n"
  "v_mfma_f32_16x16x32_bf16 v[56:59], v[64:67], v[216:219], v[56:59]\n"
  "s_add_i32 s71, s71, s10\n"
  "s_cmp_gt_i32 s10, 0\n"
  "v_mfma_f32_16x16x32_bf16 v[60:63], v[68:71], v[216:219], v[60:63]\n"
  "ds_read_b128 v[92:95], v17 offset:39936\n"
  "s_cselect_b32 s10, s71, s69\n"
  "s_waitcnt lgkmcnt(5)\n"
  "v_mfma_f32_16x16x32_bf16 v[32:35], v[72:75], v[196:199], v[32:35]\n"
  "s_bfe_u32 s71, s53, 524304\n"
  "s_add_i32 s70, s70, s71\n"
  "s_waitcnt lgkmcnt(4)\n"
  "v_mfma_f32_16x16x32_bf16 v[36:39], v[76:79], v[196:199], v[36:39]\n"
  "s_sub_i32 s70, s70, 99\n"
  "s_ashr_i32 s70, s70, 1\n"
  "v_mfma_f32_16x16x32_bf16 v[40:43], v[72:75], v[204:207], v[40:43]\n"
  "s_min_i32 s70, s70, 4\n"
  "s_max_i32 s70, s70, -1\n"
  "v_mfma_f32_16x16x32_bf16 v[44:47], v[76:79], v[204:207], v[44:47]\n"
  "s_add_i32 s8, s8, s70\n"
  "s_max_i32 s8, s8, 0\n"
  "v_mfma_f32_16x16x32_bf16 v[48:51], v[72:75], v[212:215], v[48:51]\n"
  "s_min_i32 s8, s8, 60\n"
  "s_bfe_u32 s71, s53, 524296\n"
  "v_mfma_f32_16x16x32_bf16 v[52:55], v[76:79], v[212:215], v[52:55]\n"
  "s_add_i32 s71, s71, s10\n"
  "s_lshr_b32 s71, s71, 2\n"
  "v_mfma_f32_16x16x32_bf16 v[56:59], v[72:75], v[220:223], v[56:59]\n"
  "s_add_i32 s71, s71, s8\n"
  "s_cmp_gt_i32 s10, 0\n"
  "v_mfma_f32_16x16x32_bf16 v[60:63], v[76:79], v[220:223], v[60:63]\n"
  "s_cselect_b32 s49, s71, s49\n"
  "s_and_b32 s71, s53, 255\n"
  "s_waitcnt lgkmcnt(3)\n"
  "v_mfma_f32_16x16x32_bf16 v[32:35], v[80:83], v[224:227], v[32:35]\n"
  "s_add_i32 s71, s71, s9\n"
  "s_lshr_b32 s71, s71, 2\n"
  "s_waitcnt lgkmcnt(2)\n"
  "v_mfma_f32_16x16x32_bf16 v[36:39], v[84:87], v[224:227], v[36:39]\n"
  "s_cmp_gt_i32 s9, 0\n"
  "s_cselect_b32 s71, s71, -1\n"
  "v_mfma_f32_16x16x32_bf16 v[40:43], v[80:83], v[232:235], v[40:43]\n"
  "v_mov_b32 v102, s71\n"
  "s_add_u32 s60, s56, 4\n"
  "v_mfma_f32_16x16x32_bf16 v[44:47], v[84:87], v[232:235], v[44:47]\n"
  "s_bfe_u32 s68, s60, 131073\n"
  "s_lshl_b32 s68, s68, 18\n"
  "v_mfma_f32_16x16x32_bf16 v[48:51], v[80:83], v[240:243], v[48:51]\n"
  "s_xor_b32 s60, s60, s47\n"
  "s_and_b32 s60, s60, 1\n"
  "v_mfma_f32_16x16x32_bf16 v[52:55], v[84:87], v[240:243], v[52:55]\n"
  "s_lshl_b32 s60, s60, 13\n"
  "s_add_u32 s68, s68, s60\n"
  "v_mfma_f32_16x16x32_bf16 v[56:59], v[80:83], v[248:251], v[56:59]\n"
  "buffer_store_dword v102, v16, s[16:19], s68 offen offset:4 sc1\n"
  "v_mfma_f32_16x16x32_bf16 v[60:63], v[84:87], v[248:251], v[60:63]\n"
  "s_waitcnt lgkmcnt(1)\n"
  "v_mfma_f32_16x16x32_bf16 v[32:35], v[88:91], v[228:231], v[32:35]\n"
  "s_waitcnt lgkmcnt(0)\n"
  "v_mfma_f32_16x16x32_bf16 v[36:39], v[92:95], v[228:231], v[36:39]\n"
  "v_mfma_f32_16x16x32_bf16 v[40:43], v[88:91], v[236:239], v[40:43]\n"
  "v_mfma_f32_16x16x32_bf16 v[44:47], v[92:95], v[236:239], v[44:47]\n"
  "v_mfma_f32_16x16x32_bf16 v[48:51], v[88:91], v[244:247], v[48:51]\n"
  "v_mfma_f32_16x16x32_bf16 v[52:55], v[92:95], v[244:247], v[52:55]\n"
  "v_mfma_f32_16x16x32_bf16 v[56:59], v[88:91], v[252:255], v[56:59]\n"
  "v_mfma_f32_16x16x32_bf16 v[60:63], v[92:95], v[252:255], v[60:63]\n"
  "v_cvt_pk_bf16_f32 v32, v32, v33\n"
  "v_cvt_pk_bf16_f32 v33, v34, v35\n"
  "v_cvt_pk_bf16_f32 v34, v36, v37\n"
  "v_cvt_pk_bf16_f32 v35, v38, v39\n"
  "ds_write_b128 v19, v[32:35] offset:0\n"
  "v_cvt_pk_bf16_f32 v40, v40, v41\n"
  "v_cvt_pk_bf16_f32 v41, v42, v43\n"
  "v_cvt_pk_bf16_f32 v42, v44, v45\n"
  "v_cvt_pk_bf16_f32 v43, v46, v47\n"
  "ds_write_b128 v19, v[40:43] offset:1024\n"
  "v_cvt_pk_bf16_f32 v48, v48, v49\n"
  "v_cvt_pk_bf16_f32 v49, v50, v51\n"
  "v_cvt_pk_bf16_f32 v50, v52, v53\n"
  "v_cvt_pk_bf16_f32 v51, v54, v55\n"
  "ds_write_b128 v19, v[48:51] offset:2048\n"
  "v_cvt_pk_bf16_f32 v56, v56, v57\n"
  "v_cvt_pk_bf16_f32 v57, v58, v59\n"
  "v_cvt_pk_bf16_f32 v58, v60, v61\n"
  "v_cvt_pk_bf16_f32 v59, v62, v63\n"
  "ds_write_b128 v19, v[56:59] offset:3072\n"
  "s_waitcnt vmcnt(1) lgkmcnt(0)\n"
  "s_branch C_JOIN_%=\n"
  "C_LAST_%=:\n"
  "s_nop 7\n"
  "s_add_u32 s72, s58, 131072\n"
  "buffer_store_dword v23, v11, s[16:19], s58 offen sc1\n"
  "buffer_store_dword v23, v11, s[16:19], s72 offen sc1\n"
  "s_branch C_SKIP2_%=\n"
  "C_OTHER_%=:\n"
  "s_nop 7\n"
  "s_add_u32 s60, s56, 2\n"
  "s_bfe_u32 s57, s60, 131073\n"
  "s_lshl_b32 s57, s57, 18\n"
  "s_xor_b32 s60, s60, s47\n"
  "s_and_b32 s60, s60, 1\n"
  "s_lshl_b32 s60, s60, 13\n"
  "s_add_u32 s57, s57, s60\n"
  "s_xor_b32 s75, s96, 40960\n"
  "v_add_u32 v17, s75, v0\n"
  "s_sub_u32 s97, 237568, s97\n"
  "s_mul_i32 s60, s44, 5120\n"
  "s_add_u32 s60, s60, s97\n"
  "v_add_u32 v19, s60, v0\n"
  "s_lshr_b32 s60, s48, 1\n"
  "s_add_u32 s59, s59, s60\n"
  "s_add_u32 s59, s59, s55\n"
  "s_cmp_eq_u32 s44, 1\n"
  "s_cselect_b32 s98, 0, s44\n"
  "s_and_b32 s68, s56, 7\n"
  "s_mul_i32 s68, s68, 81920\n"
  "s_mul_i32 s60, s98, 20480\n"
  "s_add_u32 s68, s68, s60\n"
  "s_add_u32 s69, s68, 5120\n"
  "s_add_u32 s70, s68, 10240\n"
  "s_add_u32 s71, s68, 15360\n"
  "s_mul_i32 s74, s99, 40960\n"
  "s_mul_i32 s63, s56, 40960\n"
  "s_add_u32 s58, s99, 4\n"
  "s_lshl_b32 s58, s58, 7\n"
  "s_add_u32 s58, s58, 1835008\n"
  "s_mov_b64 s[66:67], 0\n"
  "v_mov_b32 v28, s3\n"
  "v_mov_b32 v29, s2\n"
  "s_cmp_ge_u32 s56, 62\n"
  "s_cbranch_scc0 C_DOU_%=\n"
  "C_WT4_%=:\n"
  "s_memrealtime s[64:65]\n"
  "s_waitcnt lgkmcnt(0)\n"
  "s_sub_i32 s60, s64, s59\n"
  "s_cmp_lt_i32 s60, 0\n"
  "s_cbranch_scc1 C_WT4_%=\n"
  "buffer_load_dword v22, v14, s[32:35], s74 offen sc1\n"
  "s_mov_b64 exec, 1\n"
  "buffer_store_dword v28, v29, s[16:19], 0 offen offset:4 sc1\n"
  "s_mov_b64 exec, -1\n"
  "buffer_load_dword v15, off, s[16:19], s58 sc1\n"
  "buffer_load_dwordx4 v[112:115], v8, s[16:19], s68 offen offset:0 sc1\n"
  "buffer_load_dwordx4 v[116:119], v8, s[16:19], s68 offen offset:1280 sc1\n"
  "buffer_load_dwordx4 v[120:123], v8, s[16:19], s68 offen offset:2560 sc1\n"
  "buffer_load_dwordx4 v[124:127], v8, s[16:19], s68 offen offset:3840 sc1\n"
  "buffer_load_dwordx4 v[128:131], v9, s[16:19], s68 offen sc1\n"
  "buffer_load_dwordx4 v[132:135], v8, s[16:19], s69 offen offset:0 sc1\n"
  "buffer_load_dwordx4 v[136:139], v8, s[16:19], s69 offen offset:1280 sc1\n"
  "buffer_load_dwordx4 v[140:143], v8, s[16:19], s69 offen offset:2560 sc1\n"
  "buffer_load_dwordx4 v[144:147], v8, s[16:19], s69 offen offset:3840 sc1\n"
  "buffer_load_dwordx4 v[148:151], v9, s[16:19], s69 offen sc1\n"
  "buffer_load_dwordx4 v[152:155], v8, s[16:19], s70 offen offset:0 sc1\n"
  "buffer_load_dwordx4 v[156:159], v8, s[16:19], s70 offen offset:1280 sc1\n"
  "buffer_load_dwordx4 v[160:163], v8, s[16:19], s70 offen offset:2560 sc1\n"
  "buffer_load_dwordx4 v[164:167], v8, s[16:19], s70 offen offset:3840 sc1\n"
  "buffer_load_dwordx4 v[168:171], v9, s[16:19], s70 offen sc1\n"
  "buffer_load_dwordx4 v[172:175], v8, s[16:19], s71 offen offset:0 sc1\n"
  "buffer_load_dwordx4 v[176:179], v8, s[16:19], s71 offen offset:1280 sc1\n"
  "buffer_load_dwordx4 v[180:183], v8, s[16:19], s71 offen offset:2560 sc1\n"
  "buffer_load_dwordx4 v[184:187], v8, s[16:19], s71 offen offset:3840 sc1\n"
  "buffer_load_dwordx4 v[188:191], v9, s[16:19], s71 offen sc1\n"
  "buffer_load_dwordx4 v[96:99], v13, s[16:19], s68 offen offset:0 sc1\n"
  "buffer_load_dwordx4 v[100:103], v13, s[16:19], s68 offen offset:1024 sc1\n"
  "buffer_load_dwordx4 v[104:107], v13, s[16:19], s68 offen offset:2048 sc1\n"
  "buffer_load_dwordx4 v[108:111], v13, s[16:19], s68 offen offset:3072 sc1\n"
  "s_branch C_NOU_%=\n"
  "C_DOU_%=:\n"
  "s_nop 7\n"
  "ds_read_b128 v[64:67], v17 offset:0\n"
  "ds_read_b128 v[68:71], v17 offset:10240\n"
  "ds_read_b128 v[72:75], v17 offset:20480\n"
  "ds_read_b128 v[76:79], v17 offset:30720\n"
  "s_waitcnt lgkmcnt(3)\n"
  "v_mfma_f32_16x16x32_bf16 v[32:35], v[64:67], a[0:3], 0\n"
  "s_waitcnt lgkmcnt(2)\n"
  "v_mfma_f32_16x16x32_bf16 v[36:39], v[68:71], a[0:3], 0\n"
  "ds_read_b128 v[80:83], v17 offset:1024\n"
  "v_mfma_f32_16x16x32_bf16 v[40:43], v[64:67], a[8:11], 0\n"
  "v_mfma_f32_16x16x32_bf16 v[44:47], v[68:71], a[8:11], 0\n"
  "ds_read_b128 v[84:87], v17 offset:11264\n"
  "v_mfma_f32_16x16x32_bf16 v[48:51], v[64:67], a[16:19], 0\n"
  "v_mfma_f32_16x16x32_bf16 v[52:55], v[68:71], a[16:19], 0\n"
  "ds_read_b128 v[88:91], v17 offset:21504\n"
  "v_mfma_f32_16x16x32_bf16 v[56:59], v[64:67], a[24:27], 0\n"
  "v_mfma_f32_16x16x32_bf16 v[60:63], v[68:71], a[24:27], 0\n"
  "ds_read_b128 v[92:95], v17 offset:31744\n"
  "s_waitcnt lgkmcnt(5)\n"
  "v_mfma_f32_16x16x32_bf16 v[32:35], v[72:75], a[4:7], v[32:35]\n"
  "s_waitcnt lgkmcnt(4)\n"
  "v_mfma_f32_16x16x32_bf16 v[36:39], v[76:79], a[4:7], v[36:39]\n"
  "v_mfma_f32_16x16x32_bf16 v[40:43], v[72:75], a[12:15], v[40:43]\n"
  "v_mfma_f32_16x16x32_bf16 v[44:47], v[76:79], a[12:15], v[44:47]\n"
  "v_mfma_f32_16x16x32_bf16 v[48:51], v[72:75], a[20:23], v[48:51]\n"
  "v_mfma_f32_16x16x32_bf16 v[52:55], v[76:79], a[20:23], v[52:55]\n"
  "v_mfma_f32_16x16x32_bf16 v[56:59], v[72:75], a[28:31], v[56:59]\n"
  "v_mfma_f32_16x16x32_bf16 v[60:63], v[76:79], a[28:31], v[60:63]\n"
  "s_waitcnt lgkmcnt(3)\n"
  "v_mfma_f32_16x16x32_bf16 v[32:35], v[80:83], a[32:35], v[32:35]\n"
  "s_waitcnt lgkmcnt(2)\n"
  "v_mfma_f32_16x16x32_bf16 v[36:39], v[84:87], a[32:35], v[36:39]\n"
  "ds_read_b128 v[64:67], v17 offset:2048\n"
  "v_mfma_f32_16x16x32_bf16 v[40:43], v[80:83], a[40:43], v[40:43]\n"
  "v_mfma_f32_16x16x32_bf16 v[44:47], v[84:87], a[40:43], v[44:47]\n"
  "ds_read_b128 v[68:71], v17 offset:12288\n"
  "v_mfma_f32_16x16x32_bf16 v[48:51], v[80:83], a[48:51], v[48:51]\n"
  "v_mfma_f32_16x16x32_bf16 v[52:55], v[84:87], a[48:51], v[52:55]\n"
  "ds_read_b128 v[72:75], v17 offset:22528\n"
  "v_mfma_f32_16x16x32_bf16 v[56:59], v[80:83], a[56:59], v[56:59]\n"
  "v_mfma_f32_16x16x32_bf16 v[60:63], v[84:87], a[56:59], v[60:63]\n"
  "ds_read_b128 v[76:79], v17 offset:32768\n"
  "s_waitcnt lgkmcnt(5)\n"
  "v_mfma_f32_16x16x32_bf16 v[32:35], v[88:91], a[36:39], v[32:35]\n"
  "s_waitcnt lgkmcnt(4)\n"
  "v_mfma_f32_16x16x32_bf16 v[36:39], v[92:95], a[36:39], v[36:39]\n"
  "v_mfma_f32_16x16x32_bf16 v[40:43], v[88:91], a[44:47], v[40:43]\n"
  "v_mfma_f32_16x16x32_bf16 v[44:47], v[92:95], a[44:47], v[44:47]\n"
  "v_mfma_f32_16x16x32_bf16 v[48:51], v[88:91], a[52:55], v[48:51]\n"
  "v_mfma_f32_16x16x32_bf16 v[52:55], v[92:95], a[52:55], v[52:55]\n"
  "v_mfma_f32_16x16x32_bf16 v[56:59], v[88:91], a[60:63], v[56:59]\n"
  "v_mfma_f32_16x16x32_bf16 v[60:63], v[92:95], a[60:63], v[60:63]\n"
  "s_waitcnt lgkmcnt(3)\n"
  "v_mfma_f32_16x16x32_bf16 v[32:35], v[64:67], a[64:67], v[32:35]\n"
  "s_waitcnt lgkmcnt(2)\n"
  "v_mfma_f32_16x16x32_bf16 v[36:39], v[68:71], a[64:67], v[36:39]\n"
  "ds_read_b128 v[80:83], v17 offset:3072\n"
  "v_mfma_f32_16x16x32_bf16 v[40:43], v[64:67], a[72:75], v[40:43]\n"
  "v_mfma_f32_16x16x32_bf16 v[44:47], v[68:71], a[72:75], v[44:47]\n"
  "ds_read_b128 v[84:87], v17 offset:13312\n"
  "v_mfma_f32_16x16x32_bf16 v[48:51], v[64:67], a[80:83], v[48:51]\n"
  "v_mfma_f32_16x16x32_bf16 v[52:55], v[68:71], a[80:83], v[52:55]\n"
  "ds_read_b128 v[88:91], v17 offset:23552\n"
  "v_mfma_f32_16x16x32_bf16 v[56:59], v[64:67], a[88:91], v[56:59]\n"
  "v_mfma_f32_16x16x32_bf16 v[60:63], v[68:71], a[88:91], v[60:63]\n"
  "ds_read_b128 v[92:95], v17 offset:33792\n"
  "s_waitcnt lgkmcnt(5)\n"
  "v_mfma_f32_16x16x32_bf16 v[32:35], v[72:75], a[68:71], v[32:35]\n"
  "s_waitcnt lgkmcnt(4)\n"
  "v_mfma_f32_16x16x32_bf16 v[36:39], v[76:79], a[68:71], v[36:39]\n"
  "v_mfma_f32_16x16x32_bf16 v[40:43], v[72:75], a[76:79], v[40:43]\n"
  "v_mfma_f32_16x16x32_bf16 v[44:47], v[76:79], a[76:79], v[44:47]\n"
  "v_mfma_f32_16x16x32_bf16 v[48:51], v[72:75], a[84:87], v[48:51]\n"
  "v_mfma_f32_16x16x32_bf16 v[52:55], v[76:79], a[84:87], v[52:55]\n"
  "v_mfma_f32_16x16x32_bf16 v[56:59], v[72:75], a[92:95], v[56:59]\n"
  "v_mfma_f32_16x16x32_bf16 v[60:63], v[76:79], a[92:95], v[60:63]\n"
  "s_waitcnt lgkmcnt(3)\n"
  "v_mfma_f32_16x16x32_bf16 v[32:35], v[80:83], a[96:99], v[32:35]\n"
  "s_waitcnt lgkmcnt(2)\n"
  "v_mfma_f32_16x16x32_bf16 v[36:39], v[84:87], a[96:99], v[36:39]\n"
  "ds_read_b128 v[64:67], v17 offset:4096\n"
  "v_mfma_f32_16x16x32_bf16 v[40:43], v[80:83], a[104:107], v[40:43]\n"
  "v_mfma_f32_16x16x32_bf16 v[44:47], v[84:87], a[104:107], v[44:47]\n"
  "ds_read_b128 v[68:71], v17 offset:14336\n"
  "v_mfma_f32_16x16x32_bf16 v[48:51], v[80:83], a[112:115], v[48:51]\n"
  "v_mfma_f32_16x16x32_bf16 v[52:55], v[84:87], a[112:115], v[52:55]\n"
  "ds_read_b128 v[72:75], v17 offset:24576\n"
  "v_mfma_f32_16x16x32_bf16 v[56:59], v[80:83], a[120:123], v[56:59]\n"
  "v_mfma_f32_16x16x32_bf16 v[60:63], v[84:87], a[120:123], v[60:63]\n"
  "ds_read_b128 v[76:79], v17 offset:34816\n"
  "s_waitcnt lgkmcnt(5)\n"
  "v_mfma_f32_16x16x32_bf16 v[32:35], v[88:91], a[100:103], v[32:35]\n"
  "s_waitcnt lgkmcnt(4)\n"
  "v_mfma_f32_16x16x32_bf16 v[36:39], v[92:95], a[100:103], v[36:39]\n"
  "v_mfma_f32_16x16x32_bf16 v[40:43], v[88:91], a[108:111], v[40:43]\n"
  "v_mfma_f32_16x16x32_bf16 v[44:47], v[92:95], a[108:111], v[44:47]\n"
  "v_mfma_f32_16x16x32_bf16 v[48:51], v[88:91], a[116:119], v[48:51]\n"
  "v_mfma_f32_16x16x32_bf16 v[52:55], v[92:95], a[116:119], v[52:55]\n"
  "v_mfma_f32_16x16x32_bf16 v[56:59], v[88:91], a[124:127], v[56:59]\n"
  "v_mfma_f32_16x16x32_bf16 v[60:63], v[92:95], a[124:127], v[60:63]\n"
  "s_waitcnt lgkmcnt(3)\n"
  "v_mfma_f32_16x16x32_bf16 v[32:35], v[64:67], a[128:131], v[32:35]\n"
  "s_waitcnt lgkmcnt(2)\n"
  "v_mfma_f32_16x16x32_bf16 v[36:39], v[68:71], a[128:131], v[36:39]\n"
  "ds_read_b128 v[80:83], v17 offset:5120\n"
  "v_mfma_f32_16x16x32_bf16 v[40:43], v[64:67], a[136:139], v[40:43]\n"
  "v_mfma_f32_16x16x32_bf16 v[44:47], v[68:71], a[136:139], v[44:47]\n"
  "ds_read_b128 v[84:87], v17 offset:15360\n"
  "v_mfma_f32_16x16x32_bf16 v[48:51], v[64:67], a[144:147], v[48:51]\n"
  "v_mfma_f32_16x16x32_bf16 v[52:55], v[68:71], a[144:147], v[52:55]\n"
  "ds_read_b128 v[88:91], v17 offset:25600\n"
  "v_mfma_f32_16x16x32_bf16 v[56:59], v[64:67], a[152:155], v[56:59]\n"
  "v_mfma_f32_16x16x32_bf16 v[60:63], v[68:71], a[152:155], v[60:63]\n"
  "ds_read_b128 v[92:95], v17 offset:35840\n"
  "s_waitcnt lgkmcnt(5)\n"
  "v_mfma_f32_16x16x32_bf16 v[32:35], v[72:75], a[132:135], v[32:35]\n"
  "s_waitcnt lgkmcnt(4)\n"
  "v_mfma_f32_16x16x32_bf16 v[36:39], v[76:79], a[132:135], v[36:39]\n"
  "v_mfma_f32_16x16x32_bf16 v[40:43], v[72:75], a[140:143], v[40:43]\n"
  "v_mfma_f32_16x16x32_bf16 v[44:47], v[76:79], a[140:143], v[44:47]\n"
  "v_mfma_f32_16x16x32_bf16 v[48:51], v[72:75], a[148:151], v[48:51]\n"
  "v_mfma_f32_16x16x32_bf16 v[52:55], v[76:79], a[148:151], v[52:55]\n"
  "v_mfma_f32_16x16x32_bf16 v[56:59], v[72:75], a[156:159], v[56:59]\n"
  "v_mfma_f32_16x16x32_bf16 v[60:63], v[76:79], a[156:159], v[60:63]\n"
  "s_waitcnt lgkmcnt(0)\n"
  "C_WT3_%=:\n"
  "s_memrealtime s[64:65]\n"
  "s_waitcnt lgkmcnt(0)\n"
  "s_sub_i32 s60, s64, s59\n"
  "s_cmp_lt_i32 s60, 0\n"
  "s_cbranch_scc1 C_WT3_%=\n"
  "v_mfma_f32_16x16x32_bf16 v[32:35], v[80:83], a[160:163], v[32:35]\n"
  "buffer_load_dword v22, v14, s[32:35], s74 offen sc1\n"
  "v_mfma_f32_16x16x32_bf16 v[36:39], v[84:87], a[160:163], v[36:39]\n"
  "ds_read_b128 v[64:67], v17 offset:6144\n"
  "v_mfma_f32_16x16x32_bf16 v[40:43], v[80:83], a[168:171], v[40:43]\n"
  "v_mfma_f32_16x16x32_bf16 v[44:47], v[84:87], a[168:171], v[44:47]\n"
  "ds_read_b128 v[68:71], v17 offset:16384\n"
  "s_mov_b64 exec, 1\n"
  "buffer_store_dword v28, v29, s[16:19], 0 offen offset:4 sc1\n"
  "s_mov_b64 exec, -1\n"
  "buffer_load_dword v15, off, s[16:19], s58 sc1\n"
  "v_mfma_f32_16x16x32_bf16 v[48:51], v[80:83], a[176:179], v[48:51]\n"
  "v_mfma_f32_16x16x32_bf16 v[52:55], v[84:87], a[176:179], v[52:55]\n"
  "ds_read_b128 v[72:75], v17 offset:26624\n"
  "v_mfma_f32_16x16x32_bf16 v[56:59], v[80:83], a[184:187], v[56:59]\n"
  "buffer_load_dwordx4 v[112:115], v8, s[16:19], s68 offen offset:0 sc1\n"
  "v_mfma_f32_16x16x32_bf16 v[60:63], v[84:87], a[184:187], v[60:63]\n"
  "ds_read_b128 v[76:79], v17 offset:36864\n"
  "v_mfma_f32_16x16x32_bf16 v[32:35], v[88:91], a[164:167], v[32:35]\n"
  "v_mfma_f32_16x16x32_bf16 v[36:39], v[92:95], a[164:167], v[36:39]\n"
  "buffer_load_dwordx4 v[116:119], v8, s[16:19], s68 offen offset:1280 sc1\n"
  "v_mfma_f32_16x16x32_bf16 v[40:43], v[88:91], a[172:175], v[40:43]\n"
  "v_mfma_f32_16x16x32_bf16 v[44:47], v[92:95], a[172:175], v[44:47]\n"
  "v_mfma_f32_16x16x32_bf16 v[48:51], v[88:91], a[180:183], v[48:51]\n"
  "buffer_load_dwordx4 v[120:123], v8, s[16:19], s68 offen offset:2560 sc1\n"
  "v_mfma_f32_16x16x32_bf16 v[52:55], v[92:95], a[180:183], v[52:55]\n"
  "v_mfma_f32_16x16x32_bf16 v[56:59], v[88:91], a[188:191], v[56:59]\n"
  "v_mfma_f32_16x16x32_bf16 v[60:63], v[92:95], a[188:191], v[60:63]\n"
  "buffer_load_dwordx4 v[124:127], v8, s[16:19], s68 offen offset:3840 sc1\n"
  "s_waitcnt lgkmcnt(3)\n"
  "v_mfma_f32_16x16x32_bf16 v[32:35], v[64:67], a[192:195], v[32:35]\n"
  "s_waitcnt lgkmcnt(2)\n"
  "v_mfma_f32_16x16x32_bf16 v[36:39], v[68:71], a[192:195], v[36:39]\n"
  "ds_read_b128 v[80:83], v17 offset:7168\n"
  "v_mfma_f32_16x16x32_bf16 v[40:43], v[64:67], a[200:203], v[40:43]\n"
  "buffer_load_dwordx4 v[128:131], v9, s[16:19], s68 offen sc1\n"
  "v_mfma_f32_16x16x32_bf16 v[44:47], v[68:71], a[200:203], v[44:47]\n"
  "ds_read_b128 v[84:87], v17 offset:17408\n"
  "v_mfma_f32_16x16x32_bf16 v[48:51], v[64:67], a[208:211], v[48:51]\n"
  "v_mfma_f32_16x16x32_bf16 v[52:55], v[68:71], a[208:211], v[52:55]\n"
  "ds_read_b128 v[88:91], v17 offset:27648\n"
  "buffer_load_dwordx4 v[132:135], v8, s[16:19], s69 offen offset:0 sc1\n"
  "v_mfma_f32_16x16x32_bf16 v[56:59], v[64:67], a[216:219], v[56:59]\n"
  "v_mfma_f32_16x16x32_bf16 v[60:63], v[68:71], a[216:219], v[60:63]\n"
  "ds_read_b128 v[92:95], v17 offset:37888\n"
  "s_waitcnt lgkmcnt(5)\n"
  "v_mfma_f32_16x16x32_bf16 v[32:35], v[72:75], a[196:199], v[32:35]\n"
  "buffer_load_dwordx4 v[136:139], v8, s[16:19], s69 offen offset:1280 sc1\n"
  "s_waitcnt lgkmcnt(4)\n"
  "v_mfma_f32_16x16x32_bf16 v[36:39], v[76:79], a[196:199], v[36:39]\n"
  "v_mfma_f32_16x16x32_bf16 v[40:43], v[72:75], a[204:207], v[40:43]\n"
  "v_mfma_f32_16x16x32_bf16 v[44:47], v[76:79], a[204:207], v[44:47]\n"
  "buffer_load_dwordx4 v[140:143], v8, s[16:19], s69 offen offset:2560 sc1\n"
  "v_mfma_f32_16x16x32_bf16 v[48:51], v[72:75], a[212:215], v[48:51]\n"
  "v_mfma_f32_16x16x32_bf16 v[52:55], v[76:79], a[212:215], v[52:55]\n"
  "v_mfma_f32_16x16x32_bf16 v[56:59], v[72:75], a[220:223], v[56:59]\n"
  "buffer_load_dwordx4 v[144:147], v8, s[16:19], s69 offen offset:3840 sc1\n"
  "v_mfma_f32_16x16x32_bf16 v[60:63], v[76:79], a[220:223], v[60:63]\n"
  "s_waitcnt lgkmcnt(3)\n"
  "v_mfma_f32_16x16x32_bf16 v[32:35], v[80:83], a[224:227], v[32:35]\n"
  "s_waitcnt lgkmcnt(2)\n"
  "v_mfma_f32_16x16x32_bf16 v[36:39], v[84:87], a[224:227], v[36:39]\n"
  "ds_read_b128 v[64:67], v17 offset:8192\n"
  "buffer_load_dwordx4 v[148:151], v9, s[16:19], s69 offen sc1\n"
  "v_mfma_f32_16x16x32_bf16 v[40:43], v[80:83], a[232:235], v[40:43]\n"
  "v_mfma_f32_16x16x32_bf16 v[44:47], v[84:87], a[232:235], v[44:47]\n"
  "ds_read_b128 v[68:71], v17 offset:18432\n"
  "v_mfma_f32_16x16x32_bf16 v[48:51], v[80:83], a[240:243], v[48:51]\n"
  "buffer_load_dwordx4 v[152:155], v8, s[16:19], s70 offen offset:0 sc1\n"
  "v_mfma_f32_16x16x32_bf16 v[52:55], v[84:87], a[240:243], v[52:55]\n"
  "ds_read_b128 v[72:75], v17 offset:28672\n"
  "v_mfma_f32_16x16x32_bf16 v[56:59], v[80:83], a[248:251], v[56:59]\n"
  "v_mfma_f32_16x16x32_bf16 v[60:63], v[84:87], a[248:251], v[60:63]\n"
  "ds_read_b128 v[76:79], v17 offset:38912\n"
  "buffer_load_dwordx4 v[156:159], v8, s[16:19], s70 offen offset:1280 sc1\n"
  "s_waitcnt lgkmcnt(5)\n"
  "v_mfma_f32_16x16x32_bf16 v[32:35], v[88:91], a[228:231], v[32:35]\n"
  "s_waitcnt lgkmcnt(4)\n"
  "v_mfma_f32_16x16x32_bf16 v[36:39], v[92:95], a[228:231], v[36:39]\n"
  "v_mfma_f32_16x16x32_bf16 v[40:43], v[88:91], a[236:239], v[40:43]\n"
  "buffer_load_dwordx4 v[160:163], v8, s[16:19], s70 offen offset:2560 sc1\n"
  "v_mfma_f32_16x16x32_bf16 v[44:47], v[92:95], a[236:239], v[44:47]\n"
  "v_mfma_f32_16x16x32_bf16 v[48:51], v[88:91], a[244:247], v[48:51]\n"
  "v_mfma_f32_16x16x32_bf16 v[52:55], v[92:95], a[244:247], v[52:55]\n"
  "buffer_load_dwordx4 v[164:167], v8, s[16:19], s70 offen offset:3840 sc1\n"
  "v_mfma_f32_16x16x32_bf16 v[56:59], v[88:91], a[252:255], v[56:59]\n"
  "v_mfma_f32_16x16x32_bf16 v[60:63], v[92:95], a[252:255], v[60:63]\n"
  "s_waitcnt lgkmcnt(3)\n"
  "v_mfma_f32_16x16x32_bf16 v[32:35], v[64:67], v[192:195], v[32:35]\n"
  "buffer_load_dwordx4 v[168:171], v9, s[16:19], s70 offen sc1\n"
  "s_waitcnt lgkmcnt(2)\n"
  "v_mfma_f32_16x16x32_bf16 v[36:39], v[68:71], v[192:195], v[36:39]\n"
  "ds_read_b128 v[80:83], v17 offset:9216\n"
  "v_mfma_f32_16x16x32_bf16 v[40:43], v[64:67], v[200:203], v[40:43]\n"
  "v_mfma_f32_16x16x32_bf16 v[44:47], v[68:71], v[200:203], v[44:47]\n"
  "ds_read_b128 v[84:87], v17 offset:19456\n"
  "buffer_load_dwordx4 v[172:175], v8, s[16:19], s71 offen offset:0 sc1\n"
  "v_mfma_f32_16x16x32_bf16 v[48:51], v[64:67], v[208:211], v[48:51]\n"
  "v_mfma_f32_16x16x32_bf16 v[52:55], v[68:71], v[208:211], v[52:55]\n"
  "ds_read_b128 v[88:91], v17 offset:29696\n"
  "v_mfma_f32_16x16x32_bf16 v[56:59], v[64:67], v[216:219], v[56:59]\n"
  "buffer_load_dwordx4 v[176:179], v8, s[16:19], s71 offen offset:1280 sc1\n"
  "v_mfma_f32_16x16x32_bf16 v[60:63], v[68:71], v[216:219], v[60:63]\n"
  "ds_read_b128 v[92:95], v17 offset:39936\n"
  "s_waitcnt lgkmcnt(5)\n"
  "v_mfma_f32_16x16x32_bf16 v[32:35], v[72:75], v[196:199], v[32:35]\n"
  "s_waitcnt lgkmcnt(4)\n"
  "v_mfma_f32_16x16x32_bf16 v[36:39], v[76:79], v[196:199], v[36:39]\n"
  "buffer_load_dwordx4 v[180:183], v8, s[16:19], s71 offen offset:2560 sc1\n"
  "v_mfma_f32_16x16x32_bf16 v[40:43], v[72:75], v[204:207], v[40:43]\n"
  "v_mfma_f32_16x16x32_bf16 v[44:47], v[76:79], v[204:207], v[44:47]\n"
  "v_mfma_f32_16x16x32_bf16 v[48:51], v[72:75], v[212:215], v[48:51]\n"
  "buffer_load_dwordx4 v[184:187], v8, s[16:19], s71 offen offset:3840 sc1\n"
  "v_mfma_f32_16x16x32_bf16 v[52:55], v[76:79], v[212:215], v[52:55]\n"
  "v_mfma_f32_16x16x32_bf16 v[56:59], v[72:75], v[220:223], v[56:59]\n"
  "v_mfma_f32_16x16x32_bf16 v[60:63], v[76:79], v[220:223], v[60:63]\n"
  "buffer_load_dwordx4 v[188:191], v9, s[16:19], s71 offen sc1\n"
  "s_waitcnt lgkmcnt(3)\n"
  "v_mfma_f32_16x16x32_bf16 v[32:35], v[80:83], v[224:227], v[32:35]\n"
  "s_waitcnt lgkmcnt(2)\n"
  "v_mfma_f32_16x16x32_bf16 v[36:39], v[84:87], v[224:227], v[36:39]\n"
  "v_mfma_f32_16x16x32_bf16 v[40:43], v[80:83], v[232:235], v[40:43]\n"
  "buffer_load_dwordx4 v[96:99], v13, s[16:19], s68 offen offset:0 sc1\n"
  "v_mfma_f32_16x16x32_bf16 v[44:47], v[84:87], v[232:235], v[44:47]\n"
  "v_mfma_f32_16x16x32_bf16 v[48:51], v[80:83], v[240:243], v[48:51]\n"
  "v_mfma_f32_16x16x32_bf16 v[52:55], v[84:87], v[240:243], v[52:55]\n"
  "buffer_load_dwordx4 v[100:103], v13, s[16:19], s68 offen offset:1024 sc1\n"
  "v_mfma_f32_16x16x32_bf16 v[56:59], v[80:83], v[248:251], v[56:59]\n"
  "v_mfma_f32_16x16x32_bf16 v[60:63], v[84:87], v[248:251], v[60:63]\n"
  "s_waitcnt lgkmcnt(1)\n"
  "v_mfma_f32_16x16x32_bf16 v[32:35], v[88:91], v[228:231], v[32:35]\n"
  "buffer_load_dwordx4 v[104:107], v13, s[16:19], s68 offen offset:2048 sc1\n"
  "s_waitcnt lgkmcnt(0)\n"
  "v_mfma_f32_16x16x32_bf16 v[36:39], v[92:95], v[228:231], v[36:39]\n"
  "v_mfma_f32_16x16x32_bf16 v[40:43], v[88:91], v[236:239], v[40:43]\n"
  "v_mfma_f32_16x16x32_bf16 v[44:47], v[92:95], v[236:239], v[44:47]\n"
  "buffer_load_dwordx4 v[108:111], v13, s[16:19], s68 offen offset:3072 sc1\n"
  "v_mfma_f32_16x16x32_bf16 v[48:51], v[88:91], v[244:247], v[48:51]\n"
  "v_mfma_f32_16x16x32_bf16 v[52:55], v[92:95], v[244:247], v[52:55]\n"
  "v_mfma_f32_16x16x32_bf16 v[56:59], v[88:91], v[252:255], v[56:59]\n"
  "v_mfma_f32_16x16x32_bf16 v[60:63], v[92:95], v[252:255], v[60:63]\n"
  "v_cvt_pk_bf16_f32 v32, v32, v33\n"
  "v_cvt_pk_bf16_f32 v33, v34, v35\n"
  "v_cvt_pk_bf16_f32 v34, v36, v37\n"
  "v_cvt_pk_bf16_f32 v35, v38, v39\n"
  "ds_write_b128 v19, v[32:35] offset:0\n"
  "v_cvt_pk_bf16_f32 v40, v40, v41\n"
  "v_cvt_pk_bf16_f32 v41, v42, v43\n"
  "v_cvt_pk_bf16_f32 v42, v44, v45\n"
  "v_cvt_pk_bf16_f32 v43, v46, v47\n"
  "ds_write_b128 v19, v[40:43] offset:1024\n"
  "v_cvt_pk_bf16_f32 v48, v48, v49\n"
  "v_cvt_pk_bf16_f32 v49, v50, v51\n"
  "v_cvt_pk_bf16_f32 v50, v52, v53\n"
  "v_cvt_pk_bf16_f32 v51, v54, v55\n"
  "ds_write_b128 v19, v[48:51] offset:2048\n"
  "v_cvt_pk_bf16_f32 v56, v56, v57\n"
  "v_cvt_pk_bf16_f32 v57, v58, v59\n"
  "v_cvt_pk_bf16_f32 v58, v60, v61\n"
  "v_cvt_pk_bf16_f32 v59, v62, v63\n"
  "ds_write_b128 v19, v[56:59] offset:3072\n"
  "C_NOU_%=:\n"
  "s_mov_b32 s62, 0\n"
  "s_waitcnt vmcnt(24)\n"
  "v_readfirstlane_b32 s60, v15\n"
  "s_cmp_lg_u32 s60, -1\n"
  "s_cbranch_scc1 C_F4K_%=\n"
  "s_cmp_ge_u32 s56, 60\n"
  "s_cbranch_scc1 C_F4K_%=\n"
  "s_waitcnt vmcnt(0) lgkmcnt(0)\n"
  "C_F4P_%=:\n"
  "buffer_load_dword v15, off, s[16:19], s58 sc1\n"
  "s_waitcnt vmcnt(0)\n"
  "v_readfirstlane_b32 s60, v15\n"
  "s_cmp_lg_u32 s60, -1\n"
  "s_cbranch_scc1 C_F4K_%=\n"
  "s_sleep 4\n"
  "s_add_u32 s62, s62, 1\n"
  "s_cmp_gt_u32 s62, 300000\n"
  "s_cbranch_scc1 C_FAIL_%=\n"
  "s_and_b32 s60, s62, 255\n"
  "s_cmp_lg_u32 s60, 0\n"
  "s_cbranch_scc1 C_F4P_%=\n"
  "v_mov_b32 v15, 0\n"
  "buffer_load_dword v15, v15, s[40:43], 0 offen sc1\n"
  "s_waitcnt vmcnt(0)\n"
  "v_readfirstlane_b32 s60, v15\n"
  "s_cmp_lg_u32 s60, 0\n"
  "s_cbranch_scc1 C_EXIT_%=\n"
  "s_branch C_F4P_%=\n"
  "C_F4K_%=:\n"
  "s_sub_u32 s98, s44, 1\n"
  "s_add_u32 s60, s99, 4\n"
  "s_mul_i32 s72, s60, 40960\n"
  "s_mul_i32 s73, s98, 13312\n"
  "s_add_u32 s72, s72, s73\n"
  "s_add_u32 s73, s73, s96\n"
  "s_mov_b32 m0, s73\n"
  "s_nop 0\n"
  "buffer_load_dwordx4 v0, s[24:27], s72 offen offset:0 sc1 lds\n"
  "buffer_load_dwordx4 v0, s[24:27], s72 offen offset:1024 sc1 lds\n"
  "buffer_load_dwordx4 v0, s[24:27], s72 offen offset:2048 sc1 lds\n"
  "buffer_load_dwordx4 v0, s[24:27], s72 offen offset:3072 sc1 lds\n"
  "s_add_u32 s72, s72, 4096\n"
  "s_add_u32 s73, s73, 4096\n"
  "s_mov_b32 m0, s73\n"
  "s_nop 0\n"
  "buffer_load_dwordx4 v0, s[24:27], s72 offen offset:0 sc1 lds\n"
  "buffer_load_dwordx4 v0, s[24:27], s72 offen offset:1024 sc1 lds\n"
  "buffer_load_dwordx4 v0, s[24:27], s72 offen offset:2048 sc1 lds\n"
  "buffer_load_dwordx4 v0, s[24:27], s72 offen offset:3072 sc1 lds\n"
  "s_add_u32 s72, s72, 4096\n"
  "s_add_u32 s73, s73, 4096\n"
  "s_mov_b32 m0, s73\n"
  "s_nop 0\n"
  "buffer_load_dwordx4 v0, s[24:27], s72 offen offset:0 sc1 lds\n"
  "buffer_load_dwordx4 v0, s[24:27], s72 offen offset:1024 sc1 lds\n"
  "buffer_load_dwordx4 v0, s[24:27], s72 offen offset:2048 sc1 lds\n"
  "buffer_load_dwordx4 v0, s[24:27], s72 offen offset:3072 sc1 lds\n"
  "s_add_u32 s72, s72, 4096\n"
  "s_add_u32 s73, s73, 4096\n"
  "s_mov_b32 m0, s73\n"
  "s_nop 0\n"
  "buffer_load_dwordx4 v0, s[24:27], s72 offen offset:0 sc1 lds\n"
  "buffer_load_dwordx4 v0, s[24:27], s72 offen offset:1024 sc1 lds\n"
  "buffer_load_dwordx2 v[30:31], v10, s[16:19], s57 offen sc1\n"
  "s_cmp_eq_u32 s44, 1\n"
  "s_cbranch_scc1 C_W1_%=\n"
  "ds_read_b128 v[32:35], v18 offset:0\n"
  "ds_read_b128 v[36:39], v18 offset:1024\n"
  "ds_read_b128 v[40:43], v18 offset:2048\n"
  "ds_read_b128 v[44:47], v18 offset:3072\n"
  "ds_read_b128 v[48:51], v18 offset:4096\n"
  "ds_read_b128 v[52:55], v18 offset:5120\n"
  "ds_read_b128 v[56:59], v18 offset:6144\n"
  "ds_read_b128 v[60:63], v18 offset:7168\n"
  "ds_read_b128 v[64:67], v18 offset:8192\n"
  "ds_read_b128 v[68:71], v18 offset:9216\n"
  "s_waitcnt lgkmcnt(10)\n"
  "ds_read_b128 v[72:75], v18 offset:10240\n"
  "ds_read_b128 v[76:79], v18 offset:11264\n"
  "ds_read_b128 v[80:83], v18 offset:12288\n"
  "ds_read_b128 v[84:87], v18 offset:13312\n"
  "s_waitcnt lgkmcnt(10)\n"
  "ds_read_b128 v[88:91], v18 offset:14336\n"
  "ds_read_b128 v[92:95], v18 offset:15360\n"
  "s_waitcnt vmcnt(18)\n"
  "ds_read_b128 v[96:99], v18 offset:16384\n"
  "s_waitcnt vmcnt(17)\n"
  "ds_read_b128 v[100:103], v18 offset:17408\n"
  "s_waitcnt vmcnt(16) lgkmcnt(10)\n"
  "ds_read_b128 v[104:107], v18 offset:18432\n"
  "s_waitcnt vmcnt(15)\n"
  "ds_read_b128 v[108:111], v18 offset:19456\n"
  "s_waitcnt lgkmcnt(0)\n"
  "C_X2_%=:\n"
  "v_mfma_f32_16x16x32_bf16 v[24:27], v[32:35], v[112:115], 0\n"
  "v_mfma_f32_16x16x32_bf16 v[24:27], v[36:39], v[116:119], v[24:27]\n"
  "v_mfma_f32_16x16x32_bf16 v[24:27], v[40:43], v[120:123], v[24:27]\n"
  "v_mfma_f32_16x16x32_bf16 v[24:27], v[44:47], v[124:127], v[24:27]\n"
  "v_mfma_f32_16x16x32_bf16 v[24:27], v[48:51], v[128:131], v[24:27]\n"
  "v_mfma_f32_16x16x32_bf16 v[24:27], v[52:55], v[132:135], v[24:27]\n"
  "v_mfma_f32_16x16x32_bf16 v[24:27], v[56:59], v[136:139], v[24:27]\n"
  "v_mfma_f32_16x16x32_bf16 v[24:27], v[60:63], v[140:143], v[24:27]\n"
  "v_mfma_f32_16x16x32_bf16 v[24:27], v[64:67], v[144:147], v[24:27]\n"
  "v_mfma_f32_16x16x32_bf16 v[24:27], v[68:71], v[148:151], v[24:27]\n"
  "v_mfma_f32_16x16x32_bf16 v[24:27], v[72:75], v[152:155], v[24:27]\n"
  "v_mfma_f32_16x16x32_bf16 v[24:27], v[76:79], v[156:159], v[24:27]\n"
  "v_mfma_f32_16x16x32_bf16 v[24:27], v[80:83], v[160:163], v[24:27]\n"
  "v_mfma_f32_16x16x32_bf16 v[24:27], v[84:87], v[164:167], v[24:27]\n"
  "v_mfma_f32_16x16x32_bf16 v[24:27], v[88:91], v[168:171], v[24:27]\n"
  "v_mfma_f32_16x16x32_bf16 v[24:27], v[92:95], v[172:175], v[24:27]\n"
  "v_mfma_f32_16x16x32_bf16 v[24:27], v[96:99], v[176:179], v[24:27]\n"
  "v_mfma_f32_16x16x32_bf16 v[24:27], v[100:103], v[180:183], v[24:27]\n"
  "v_mfma_f32_16x16x32_bf16 v[24:27], v[104:107], v[184:187], v[24:27]\n"
  "v_mfma_f32_16x16x32_bf16 v[24:27], v[108:111], v[188:191], v[24:27]\n"
  "v_mov_b32 v0, v0\n"
  "v_mov_b32 v0, v0\n"
  "v_mov_b32 v0, v0\n"
  "v_mov_b32 v0, v0\n"
  "v_mov_b32 v0, v0\n"
  "v_mov_b32 v0, v0\n"
  "v_mov_b32 v0, v0\n"
  "v_mov_b32 v0, v0\n"
  "v_add_f32_dpp v24, v26, v24 row_ror:8 row_mask:0xf bank_mask:0xf\n"
  "v_add_f32_dpp v25, v27, v25 row_ror:8 row_mask:0xf bank_mask:0xf\n"
  "v_cmp_u_f32 vcc, v24, v24\n"
  "v_lshlrev_b32 v28, 16, v22\n"
  "v_and_b32 v29, 0xffff0000, v22\n"
  "s_andn2_b64 vcc, vcc, s[66:67]\n"
  "v_add_f32 v24, v24, v28\n"
  "v_add_f32 v25, v25, v29\n"
  "s_cbranch_vccnz C_XREDO_%=\n"
  "v_cvt_pk_bf16_f32 v24, v24, v25\n"
  "buffer_store_dword v24, v12, s[36:39], s63 offen\n"
  "v_min_u32 v25, s78, v24\n"
  "buffer_store_dword v25, v14, s[20:23], s63 offen sc1\n"
  "s_waitcnt vmcnt(2)\n"
  "s_branch C_JOIN_%=\n"
  "C_W1_%=:\n"
  "ds_read_b64 v[32:33], v18 offset:0\n"
  "ds_read_b64 v[34:35], v18 offset:1024\n"
  "ds_read_b64 v[36:37], v18 offset:2048\n"
  "ds_read_b64 v[38:39], v18 offset:3072\n"
  "ds_read_b128 v[64:67], v18 offset:4096\n"
  "ds_read_b64 v[40:41], v18 offset:5120\n"
  "ds_read_b64 v[42:43], v18 offset:6144\n"
  "ds_read_b64 v[44:45], v18 offset:7168\n"
  "ds_read_b64 v[46:47], v18 offset:8192\n"
  "ds_read_b128 v[68:71], v18 offset:9216\n"
  "s_waitcnt lgkmcnt(10)\n"
  "ds_read_b64 v[48:49], v18 offset:10240\n"
  "ds_read_b64 v[50:51], v18 offset:11264\n"
  "ds_read_b64 v[52:53], v18 offset:12288\n"
  "ds_read_b64 v[54:55], v18 offset:13312\n"
  "s_waitcnt lgkmcnt(10)\n"
  "ds_read_b128 v[72:75], v18 offset:14336\n"
  "ds_read_b64 v[56:57], v18 offset:15360\n"
  "ds_read_b64 v[58:59], v18 offset:16384\n"
  "ds_read_b64 v[60:61], v18 offset:17408\n"
  "s_waitcnt lgkmcnt(10)\n"
  "ds_read_b64 v[62:63], v18 offset:18432\n"
  "ds_read_b128 v[76:79], v18 offset:19456\n"
  "s_waitcnt lgkmcnt(0)\n"
  "C_X1_%=:\n"
  "s_waitcnt vmcnt(38)\n"
  "v_mfma_f32_16x16x16_bf16 v[80:83], v[32:33], v[112:113], 0\n"
  "v_mfma_f32_16x16x16_bf16 v[84:87], v[32:33], v[114:115], 0\n"
  "s_waitcnt vmcnt(37)\n"
  "v_mfma_f32_16x16x16_bf16 v[80:83], v[34:35], v[116:117], v[80:83]\n"
  "v_mfma_f32_16x16x16_bf16 v[84:87], v[34:35], v[118:119], v[84:87]\n"
  "s_waitcnt vmcnt(36)\n"
  "v_mfma_f32_16x16x16_bf16 v[80:83], v[36:37], v[120:121], v[80:83]\n"
  "v_mfma_f32_16x16x16_bf16 v[84:87], v[36:37], v[122:123], v[84:87]\n"
  "s_waitcnt vmcnt(35)\n"
  "v_mfma_f32_16x16x16_bf16 v[80:83], v[38:39], v[124:125], v[80:83]\n"
  "v_mfma_f32_16x16x16_bf16 v[84:87], v[38:39], v[126:127], v[84:87]\n"
  "s_waitcnt vmcnt(33)\n"
  "v_mfma_f32_16x16x16_bf16 v[80:83], v[40:41], v[132:133], v[80:83]\n"
  "v_mfma_f32_16x16x16_bf16 v[84:87], v[40:41], v[134:135], v[84:87]\n"
  "s_waitcnt vmcnt(32)\n"
  "v_mfma_f32_16x16x16_bf16 v[80:83], v[42:43], v[136:137], v[80:83]\n"
  "v_mfma_f32_16x16x16_bf16 v[84:87], v[42:43], v[138:139], v[84:87]\n"
  "s_waitcnt vmcnt(31)\n"
  "v_mfma_f32_16x16x16_bf16 v[80:83], v[44:45], v[140:141], v[80:83]\n"
  "v_mfma_f32_16x16x16_bf16 v[84:87], v[44:45], v[142:143], v[84:87]\n"
  "s_waitcnt vmcnt(30)\n"
  "v_mfma_f32_16x16x16_bf16 v[80:83], v[46:47], v[144:145], v[80:83]\n"
  "v_mfma_f32_16x16x16_bf16 v[84:87], v[46:47], v[146:147], v[84:87]\n"
  "s_waitcnt vmcnt(28)\n"
  "v_mfma_f32_16x16x16_bf16 v[80:83], v[48:49], v[152:153], v[80:83]\n"
  "v_mfma_f32_16x16x16_bf16 v[84:87], v[48:49], v[154:155], v[84:87]\n"
  "s_waitcnt vmcnt(27)\n"
  "v_mfma_f32_16x16x16_bf16 v[80:83], v[50:51], v[156:157], v[80:83]\n"
  "v_mfma_f32_16x16x16_bf16 v[84:87], v[50:51], v[158:159], v[84:87]\n"
  "s_waitcnt vmcnt(26)\n"
  "v_mfma_f32_16x16x16_bf16 v[80:83], v[52:53], v[160:161], v[80:83]\n"
  "v_mfma_f32_16x16x16_bf16 v[84:87], v[52:53], v[162:163], v[84:87]\n"
  "s_waitcnt vmcnt(25)\n"
  "v_mfma_f32_16x16x16_bf16 v[80:83], v[54:55], v[164:165], v[80:83]\n"
  "v_mfma_f32_16x16x16_bf16 v[84:87], v[54:55], v[166:167], v[84:87]\n"
  "s_waitcnt vmcnt(23)\n"
  "v_mfma_f32_16x16x16_bf16 v[80:83], v[56:57], v[172:173], v[80:83]\n"
  "v_mfma_f32_16x16x16_bf16 v[84:87], v[56:57], v[174:175], v[84:87]\n"
  "s_waitcnt vmcnt(22)\n"
  "v_mfma_f32_16x16x16_bf16 v[80:83], v[58:59], v[176:177], v[80:83]\n"
  "v_mfma_f32_16x16x16_bf16 v[84:87], v[58:59], v[178:179], v[84:87]\n"
  "s_waitcnt vmcnt(21)\n"
  "v_mfma_f32_16x16x16_bf16 v[80:83], v[60:61], v[180:181], v[80:83]\n"
  "v_mfma_f32_16x16x16_bf16 v[84:87], v[60:61], v[182:183], v[84:87]\n"
  "s_waitcnt vmcnt(20)\n"
  "v_mfma_f32_16x16x16_bf16 v[80:83], v[62:63], v[184:185], v[80:83]\n"
  "v_mfma_f32_16x16x16_bf16 v[84:87], v[62:63], v[186:187], v[84:87]\n"
  "v_mfma_f32_16x16x32_bf16 v[88:91], v[64:67], v[128:131], 0\n"
  "s_waitcnt vmcnt(18)\n"
  "v_mfma_f32_16x16x32_bf16 v[92:95], v[64:67], v[96:99], 0\n"
  "v_mfma_f32_16x16x32_bf16 v[88:91], v[68:71], v[148:151], v[88:91]\n"
  "s_waitcnt vmcnt(17)\n"
  "v_mfma_f32_16x16x32_bf16 v[92:95], v[68:71], v[100:103], v[92:95]\n"
  "v_mfma_f32_16x16x32_bf16 v[88:91], v[72:75], v[168:171], v[88:91]\n"
  "s_waitcnt vmcnt(16)\n"
  "v_mfma_f32_16x16x32_bf16 v[92:95], v[72:75], v[104:107], v[92:95]\n"
  "v_mfma_f32_16x16x32_bf16 v[88:91], v[76:79], v[188:191], v[88:91]\n"
  "s_waitcnt vmcnt(15)\n"
  "v_mfma_f32_16x16x32_bf16 v[92:95], v[76:79], v[108:111], v[92:95]\n"
  "v_mov_b32 v0, v0\n"
  "v_mov_b32 v0, v0\n"
  "v_mov_b32 v0, v0\n"
  "v_mov_b32 v0, v0\n"
  "v_mov_b32 v0, v0\n"
  "v_mov_b32 v0, v0\n"
  "v_pk_add_f32 v[80:81], v[80:81], v[88:89]\n"
  "v_pk_add_f32 v[82:83], v[82:83], v[90:91]\n"
  "v_pk_add_f32 v[84:85], v[84:85], v[92:93]\n"
  "v_pk_add_f32 v[86:87], v[86:87], v[94:95]\n"
  "v_add_f32_dpp v24, v82, v80 row_ror:8 row_mask:0xf bank_mask:0x3\n"
  "v_add_f32_dpp v25, v83, v81 row_ror:8 row_mask:0xf bank_mask:0x3\n"
  "v_add_f32_dpp v24, v84, v86 row_ror:8 row_mask:0xf bank_mask:0xc\n"
  "v_add_f32_dpp v25, v85, v87 row_ror:8 row_mask:0xf bank_mask:0xc\n"
  "v_cmp_u_f32 vcc, v24, v24\n"
  "v_lshlrev_b32 v28, 16, v22\n"
  "v_and_b32 v29, 0xffff0000, v22\n"
  "s_andn2_b64 vcc, vcc, s[66:67]\n"
  "v_add_f32 v24, v24, v28\n"
  "v_add_f32 v25, v25, v29\n"
  "s_cbranch_vccnz C_X1REDO_%=\n"
  "v_cvt_pk_bf16_f32 v24, v24, v25\n"
  "buffer_store_dword v24, v12, s[36:39], s63 offen\n"
  "v_min_u32 v25, s78, v24\n"
  "buffer_store_dword v25, v14, s[20:23], s63 offen sc1\n"
  "s_waitcnt vmcnt(2)\n"
  "C_JOIN_%=:\n"
  "s_mov_b32 s96, s75\n"
  "s_add_u32 s56, s56, 2\n"
  "s_add_u32 s99, s99, 2\n"
  "s_cmp_lt_u32 s56, 64\n"
  "s_cbranch_scc1 C_LOOP_%=\n"
  "s_waitcnt vmcnt(0)\n"
  "s_mov_b32 s38, 2621440\n"
  "s_mov_b32 s22, 0\n"
  "s_add_u32 s46, s46, 64\n"
  "s_cmp_lt_u32 s46, 128\n"
  "s_cbranch_scc1 C_LAYER_%=\n"
  "s_branch C_EXIT_%=\n"
  "C_WT2C_%=:\n"
  "C_WT2D_%=:\n"
  "s_memrealtime s[64:65]\n"
  "s_waitcnt lgkmcnt(0)\n"
  "s_sub_i32 s60, s64, s59\n"
  "s_cmp_lt_i32 s60, 0\n"
  "s_cbranch_scc1 C_WT2D_%=\n"
  "s_mov_b32 s59, s64\n"
  "s_memtime s[64:65]\n"
  "s_waitcnt lgkmcnt(0)\n"
  "s_mov_b32 s11, s64\n"
  "s_branch C_WT2K_%=\n"
  "C_MBAD_%=:\n"
  "s_nop 7\n"
  "s_add_u32 s62, s62, 1\n"
  "s_cmp_gt_u32 s62, 300000\n"
  "s_cbranch_scc1 C_FAIL_%=\n"
  "s_and_b32 s60, s62, 255\n"
  "s_cmp_lg_u32 s60, 0\n"
  "s_cbranch_scc1 C_MP_%=\n"
  "v_mov_b32 v15, 0\n"
  "buffer_load_dword v15, v15, s[40:43], 0 offen sc1\n"
  "s_waitcnt vmcnt(0)\n"
  "v_readfirstlane_b32 s60, v15\n"
  "s_cmp_lg_u32 s60, 0\n"
  "s_cbranch_scc1 C_EXIT_%=\n"
  "s_branch C_MP_%=\n"
  "C_REDO_%=:\n"
  "s_nop 7\n"
  "s_waitcnt vmcnt(0) lgkmcnt(0)\n"
  "s_mov_b32 s62, 0\n"
  "C_REDOP_%=:\n"
  "s_nop 7\n"
  "buffer_load_dwordx4 v[112:115], v6, s[16:19], s68 offen offset:0 sc1\n"
  "buffer_load_dwordx4 v[116:119], v6, s[16:19], s68 offen offset:1280 sc1\n"
  "buffer_load_dwordx4 v[120:123], v6, s[16:19], s68 offen offset:2560 sc1\n"
  "buffer_load_dwordx4 v[124:127], v6, s[16:19], s68 offen offset:3840 sc1\n"
  "buffer_load_dwordx4 v[128:131], v7, s[16:19], s68 offen sc1\n"
  "buffer_load_dwordx4 v[132:135], v6, s[16:19], s69 offen offset:0 sc1\n"
  "buffer_load_dwordx4 v[136:139], v6, s[16:19], s69 offen offset:1280 sc1\n"
  "buffer_load_dwordx4 v[140:143], v6, s[16:19], s69 offen offset:2560 sc1\n"
  "buffer_load_dwordx4 v[144:147], v6, s[16:19], s69 offen offset:3840 sc1\n"
  "buffer_load_dwordx4 v[148:151], v7, s[16:19], s69 offen sc1\n"
  "buffer_load_dwordx4 v[152:155], v6, s[16:19], s70 offen offset:0 sc1\n"
  "buffer_load_dwordx4 v[156:159], v6, s[16:19], s70 offen offset:1280 sc1\n"
  "buffer_load_dwordx4 v[160:163], v6, s[16:19], s70 offen offset:2560 sc1\n"
  "buffer_load_dwordx4 v[164:167], v6, s[16:19], s70 offen offset:3840 sc1\n"
  "buffer_load_dwordx4 v[168:171], v7, s[16:19], s70 offen sc1\n"
  "buffer_load_dwordx4 v[172:175], v6, s[16:19], s71 offen offset:0 sc1\n"
  "buffer_load_dwordx4 v[176:179], v6, s[16:19], s71 offen offset:1280 sc1\n"
  "buffer_load_dwordx4 v[180:183], v6, s[16:19], s71 offen offset:2560 sc1\n"
  "buffer_load_dwordx4 v[184:187], v6, s[16:19], s71 offen offset:3840 sc1\n"
  "buffer_load_dwordx4 v[188:191], v7, s[16:19], s71 offen sc1\n"
  "s_waitcnt vmcnt(19)\n"
  "v_max3_u32 v15, v112, v113, v114\n"
  "s_waitcnt vmcnt(18)\n"
  "v_max3_u32 v15, v115, v116, v15\n"
  "v_max3_u32 v15, v117, v118, v15\n"
  "s_waitcnt vmcnt(17)\n"
  "v_max3_u32 v15, v119, v120, v15\n"
  "v_max3_u32 v15, v121, v122, v15\n"
  "s_waitcnt vmcnt(16)\n"
  "v_max3_u32 v15, v123, v124, v15\n"
  "v_max3_u32 v15, v125, v126, v15\n"
  "s_waitcnt vmcnt(15)\n"
  "v_max3_u32 v15, v127, v128, v15\n"
  "v_max3_u32 v15, v129, v130, v15\n"
  "s_waitcnt vmcnt(14)\n"
  "v_max3_u32 v15, v131, v132, v15\n"
  "v_max3_u32 v15, v133, v134, v15\n"
  "s_waitcnt vmcnt(13)\n"
  "v_max3_u32 v15, v135, v136, v15\n"
  "v_max3_u32 v15, v137, v138, v15\n"
  "s_waitcnt vmcnt(12)\n"
  "v_max3_u32 v15, v139, v140, v15\n"
  "v_max3_u32 v15, v141, v142, v15\n"
  "s_waitcnt vmcnt(11)\n"
  "v_max3_u32 v15, v143, v144, v15\n"
  "v_max3_u32 v15, v145, v146, v15\n"
  "s_waitcnt vmcnt(10)\n"
  "v_max3_u32 v15, v147, v148, v15\n"
  "v_max3_u32 v15, v149, v150, v15\n"
  "s_waitcnt vmcnt(9)\n"
  "v_max3_u32 v15, v151, v152, v15\n"
  "v_max3_u32 v15, v153, v154, v15\n"
  "s_waitcnt vmcnt(8)\n"
  "v_max3_u32 v15, v155, v156, v15\n"
  "v_max3_u32 v15, v157, v158, v15\n"
  "s_waitcnt vmcnt(7)\n"
  "v_max3_u32 v15, v159, v160, v15\n"
  "v_max3_u32 v15, v161, v162, v15\n"
  "s_waitcnt vmcnt(6)\n"
  "v_max3_u32 v15, v163, v164, v15\n"
  "v_max3_u32 v15, v165, v166, v15\n"
  "s_waitcnt vmcnt(5)\n"
  "v_max3_u32 v15, v167, v168, v15\n"
  "v_max3_u32 v15, v169, v170, v15\n"
  "s_waitcnt vmcnt(4)\n"
  "v_max3_u32 v15, v171, v172, v15\n"
  "v_max3_u32 v15, v173, v174, v15\n"
  "s_waitcnt vmcnt(3)\n"
  "v_max3_u32 v15, v175, v176, v15\n"
  "v_max3_u32 v15, v177, v178, v15\n"
  "s_waitcnt vmcnt(2)\n"
  "v_max3_u32 v15, v179, v180, v15\n"
  "v_max3_u32 v15, v181, v182, v15\n"
  "s_waitcnt vmcnt(1)\n"
  "v_max3_u32 v15, v183, v184, v15\n"
  "v_max3_u32 v15, v185, v186, v15\n"
  "s_waitcnt vmcnt(0)\n"
  "v_max3_u32 v15, v187, v188, v15\n"
  "v_max3_u32 v15, v189, v190, v15\n"
  "v_max_u32 v15, v191, v15\n"
  "s_waitcnt vmcnt(0) lgkmcnt(0)\n"
  "v_cmp_eq_u32 vcc, -1, v15\n"
  "s_nop 1\n"
  "s_mov_b64 s[66:67], -1\n"
  "s_cbranch_vccz C_P2_%=\n"
  "s_add_u32 s62, s62, 1\n"
  "s_cmp_gt_u32 s62, 300000\n"
  "s_cbranch_scc1 C_FAIL_%=\n"
  "s_and_b32 s60, s62, 255\n"
  "s_cmp_lg_u32 s60, 0\n"
  "s_cbranch_scc1 C_REDOP_%=\n"
  "v_mov_b32 v15, 0\n"
  "buffer_load_dword v15, v15, s[40:43], 0 offen sc1\n"
  "s_waitcnt vmcnt(0)\n"
  "v_readfirstlane_b32 s60, v15\n"
  "s_cmp_lg_u32 s60, 0\n"
  "s_cbranch_scc1 C_EXIT_%=\n"
  "s_branch C_REDOP_%=\n"
  "C_XREDO_%=:\n"
  "s_nop 7\n"
  "s_waitcnt vmcnt(0) lgkmcnt(0)\n"
  "s_mov_b32 s62, 0\n"
  "C_XREDOP_%=:\n"
  "s_nop 7\n"
  "buffer_load_dwordx4 v[112:115], v8, s[16:19], s68 offen offset:0 sc1\n"
  "buffer_load_dwordx4 v[116:119], v8, s[16:19], s68 offen offset:1280 sc1\n"
  "buffer_load_dwordx4 v[120:123], v8, s[16:19], s68 offen offset:2560 sc1\n"
  "buffer_load_dwordx4 v[124:127], v8, s[16:19], s68 offen offset:3840 sc1\n"
  "buffer_load_dwordx4 v[128:131], v9, s[16:19], s68 offen sc1\n"
  "buffer_load_dwordx4 v[132:135], v8, s[16:19], s69 offen offset:0 sc1\n"
  "buffer_load_dwordx4 v[136:139], v8, s[16:19], s69 offen offset:1280 sc1\n"
  "buffer_load_dwordx4 v[140:143], v8, s[16:19], s69 offen offset:2560 sc1\n"
  "buffer_load_dwordx4 v[144:147], v8, s[16:19], s69 offen offset:3840 sc1\n"
  "buffer_load_dwordx4 v[148:151], v9, s[16:19], s69 offen sc1\n"
  "buffer_load_dwordx4 v[152:155], v8, s[16:19], s70 offen offset:0 sc1\n"
  "buffer_load_dwordx4 v[156:159], v8, s[16:19], s70 offen offset:1280 sc1\n"
  "buffer_load_dwordx4 v[160:163], v8, s[16:19], s70 offen offset:2560 sc1\n"
  "buffer_load_dwordx4 v[164:167], v8, s[16:19], s70 offen offset:3840 sc1\n"
  "buffer_load_dwordx4 v[168:171], v9, s[16:19], s70 offen sc1\n"
  "buffer_load_dwordx4 v[172:175], v8, s[16:19], s71 offen offset:0 sc1\n"
  "buffer_load_dwordx4 v[176:179], v8, s[16:19], s71 offen offset:1280 sc1\n"
  "buffer_load_dwordx4 v[180:183], v8, s[16:19], s71 offen offset:2560 sc1\n"
  "buffer_load_dwordx4 v[184:187], v8, s[16:19], s71 offen offset:3840 sc1\n"
  "buffer_load_dwordx4 v[188:191], v9, s[16:19], s71 offen sc1\n"
  "s_waitcnt vmcnt(19)\n"
  "v_max3_u32 v15, v112, v113, v114\n"
  "s_waitcnt vmcnt(18)\n"
  "v_max3_u32 v15, v115, v116, v15\n"
  "v_max3_u32 v15, v117, v118, v15\n"
  "s_waitcnt vmcnt(17)\n"
  "v_max3_u32 v15, v119, v120, v15\n"
  "v_max3_u32 v15, v121, v122, v15\n"
  "s_waitcnt vmcnt(16)\n"
  "v_max3_u32 v15, v123, v124, v15\n"
  "v_max3_u32 v15, v125, v126, v15\n"
  "s_waitcnt vmcnt(15)\n"
  "v_max3_u32 v15, v127, v128, v15\n"
  "v_max3_u32 v15, v129, v130, v15\n"
  "s_waitcnt vmcnt(14)\n"
  "v_max3_u32 v15, v131, v132, v15\n"
  "v_max3_u32 v15, v133, v134, v15\n"
  "s_waitcnt vmcnt(13)\n"
  "v_max3_u32 v15, v135, v136, v15\n"
  "v_max3_u32 v15, v137, v138, v15\n"
  "s_waitcnt vmcnt(12)\n"
  "v_max3_u32 v15, v139, v140, v15\n"
  "v_max3_u32 v15, v141, v142, v15\n"
  "s_waitcnt vmcnt(11)\n"
  "v_max3_u32 v15, v143, v144, v15\n"
  "v_max3_u32 v15, v145, v146, v15\n"
  "s_waitcnt vmcnt(10)\n"
  "v_max3_u32 v15, v147, v148, v15\n"
  "v_max3_u32 v15, v149, v150, v15\n"
  "s_waitcnt vmcnt(9)\n"
  "v_max3_u32 v15, v151, v152, v15\n"
  "v_max3_u32 v15, v153, v154, v15\n"
  "s_waitcnt vmcnt(8)\n"
  "v_max3_u32 v15, v155, v156, v15\n"
  "v_max3_u32 v15, v157, v158, v15\n"
  "s_waitcnt vmcnt(7)\n"
  "v_max3_u32 v15, v159, v160, v15\n"
  "v_max3_u32 v15, v161, v162, v15\n"
  "s_waitcnt vmcnt(6)\n"
  "v_max3_u32 v15, v163, v164, v15\n"
  "v_max3_u32 v15, v165, v166, v15\n"
  "s_waitcnt vmcnt(5)\n"
  "v_max3_u32 v15, v167, v168, v15\n"
  "v_max3_u32 v15, v169, v170, v15\n"
  "s_waitcnt vmcnt(4)\n"
  "v_max3_u32 v15, v171, v172, v15\n"
  "v_max3_u32 v15, v173, v174, v15\n"
  "s_waitcnt vmcnt(3)\n"
  "v_max3_u32 v15, v175, v176, v15\n"
  "v_max3_u32 v15, v177, v178, v15\n"
  "s_waitcnt vmcnt(2)\n"
  "v_max3_u32 v15, v179, v180, v15\n"
  "v_max3_u32 v15, v181, v182, v15\n"
  "s_waitcnt vmcnt(1)\n"
  "v_max3_u32 v15, v183, v184, v15\n"
  "v_max3_u32 v15, v185, v186, v15\n"
  "s_waitcnt vmcnt(0)\n"
  "v_max3_u32 v15, v187, v188, v15\n"
  "v_max3_u32 v15, v189, v190, v15\n"
  "v_max_u32 v15, v191, v15\n"
  "s_waitcnt vmcnt(0) lgkmcnt(0)\n"
  "v_cmp_eq_u32 vcc, -1, v15\n"
  "s_nop 1\n"
  "s_mov_b64 s[66:67], -1\n"
  "s_cbranch_vccz C_X2_%=\n"
  "s_add_u32 s62, s62, 1\n"
  "s_cmp_gt_u32 s62, 300000\n"
  "s_cbranch_scc1 C_FAIL_%=\n"
  "s_and_b32 s60, s62, 255\n"
  "s_cmp_lg_u32 s60, 0\n"
  "s_cbranch_scc1 C_XREDOP_%=\n"
  "v_mov_b32 v15, 0\n"
  "buffer_load_dword v15, v15, s[40:43], 0 offen sc1\n"
  "s_waitcnt vmcnt(0)\n"
  "v_readfirstlane_b32 s60, v15\n"
  "s_cmp_lg_u32 s60, 0\n"
  "s_cbranch_scc1 C_EXIT_%=\n"
  "s_branch C_XREDOP_%=\n"
  "C_X1REDO_%=:\n"
  "s_nop 7\n"
  "s_waitcnt vmcnt(0) lgkmcnt(0)\n"
  "s_mov_b32 s62, 0\n"
  "C_X1REDOP_%=:\n"
  "s_nop 7\n"
  "buffer_load_dwordx4 v[112:115], v8, s[16:19], s68 offen offset:0 sc1\n"
  "buffer_load_dwordx4 v[116:119], v8, s[16:19], s68 offen offset:1280 sc1\n"
  "buffer_load_dwordx4 v[120:123], v8, s[16:19], s68 offen offset:2560 sc1\n"
  "buffer_load_dwordx4 v[124:127], v8, s[16:19], s68 offen offset:3840 sc1\n"
  "buffer_load_dwordx4 v[128:131], v9, s[16:19], s68 offen sc1\n"
  "buffer_load_dwordx4 v[132:135], v8, s[16:19], s69 offen offset:0 sc1\n"
  "buffer_load_dwordx4 v[136:139], v8, s[16:19], s69 offen offset:1280 sc1\n"
  "buffer_load_dwordx4 v[140:143], v8, s[16:19], s69 offen offset:2560 sc1\n"
  "buffer_load_dwordx4 v[144:147], v8, s[16:19], s69 offen offset:3840 sc1\n"
  "buffer_load_dwordx4 v[148:151], v9, s[16:19], s69 offen sc1\n"
  "buffer_load_dwordx4 v[152:155], v8, s[16:19], s70 offen offset:0 sc1\n"
  "buffer_load_dwordx4 v[156:159], v8, s[16:19], s70 offen offset:1280 sc1\n"
  "buffer_load_dwordx4 v[160:163], v8, s[16:19], s70 offen offset:2560 sc1\n"
  "buffer_load_dwordx4 v[164:167], v8, s[16:19], s70 offen offset:3840 sc1\n"
  "buffer_load_dwordx4 v[168:171], v9, s[16:19], s70 offen sc1\n"
  "buffer_load_dwordx4 v[172:175], v8, s[16:19], s71 offen offset:0 sc1\n"
  "buffer_load_dwordx4 v[176:179], v8, s[16:19], s71 offen offset:1280 sc1\n"
  "buffer_load_dwordx4 v[180:183], v8, s[16:19], s71 offen offset:2560 sc1\n"
  "buffer_load_dwordx4 v[184:187], v8, s[16:19], s71 offen offset:3840 sc1\n"
  "buffer_load_dwordx4 v[188:191], v9, s[16:19], s71 offen sc1\n"
  "buffer_load_dwordx4 v[96:99], v13, s[16:19], s68 offen offset:0 sc1\n"
  "buffer_load_dwordx4 v[100:103], v13, s[16:19], s68 offen offset:1024 sc1\n"
  "buffer_load_dwordx4 v[104:107], v13, s[16:19], s68 offen offset:2048 sc1\n"
  "buffer_load_dwordx4 v[108:111], v13, s[16:19], s68 offen offset:3072 sc1\n"
  "s_waitcnt vmcnt(3)\n"
  "v_max3_u32 v15, v96, v97, v98\n"
  "s_waitcnt vmcnt(2)\n"
  "v_max3_u32 v15, v99, v100, v15\n"
  "v_max3_u32 v15, v101, v102, v15\n"
  "s_waitcnt vmcnt(1)\n"
  "v_max3_u32 v15, v103, v104, v15\n"
  "v_max3_u32 v15, v105, v106, v15\n"
  "s_waitcnt vmcnt(0)\n"
  "v_max3_u32 v15, v107, v108, v15\n"
  "v_max3_u32 v15, v109, v110, v15\n"
  "v_max3_u32 v15, v111, v112, v15\n"
  "v_max3_u32 v15, v113, v114, v15\n"
  "v_max3_u32 v15, v115, v116, v15\n"
  "v_max3_u32 v15, v117, v118, v15\n"
  "v_max3_u32 v15, v119, v120, v15\n"
  "v_max3_u32 v15, v121, v122, v15\n"
  "v_max3_u32 v15, v123, v124, v15\n"
  "v_max3_u32 v15, v125, v126, v15\n"
  "v_max3_u32 v15, v127, v128, v15\n"
  "v_max3_u32 v15, v129, v130, v15\n"
  "v_max3_u32 v15, v131, v132, v15\n"
  "v_max3_u32 v15, v133, v134, v15\n"
  "v_max3_u32 v15, v135, v136, v15\n"
  "v_max3_u32 v15, v137, v138, v15\n"
  "v_max3_u32 v15, v139, v140, v15\n"
  "v_max3_u32 v15, v141, v142, v15\n"
  "v_max3_u32 v15, v143, v144, v15\n"
  "v_max3_u32 v15, v145, v146, v15\n"
  "v_max3_u32 v15, v147, v148, v15\n"
  "v_max3_u32 v15, v149, v150, v15\n"
  "v_max3_u32 v15, v151, v152, v15\n"
  "v_max3_u32 v15, v153, v154, v15\n"
  "v_max3_u32 v15, v155, v156, v15\n"
  "v_max3_u32 v15, v157, v158, v15\n"
  "v_max3_u32 v15, v159, v160, v15\n"
  "v_max3_u32 v15, v161, v162, v15\n"
  "v_max3_u32 v15, v163, v164, v15\n"
  "v_max3_u32 v15, v165, v166, v15\n"
  "v_max3_u32 v15, v167, v168, v15\n"
  "v_max3_u32 v15, v169, v170, v15\n"
  "v_max3_u32 v15, v171, v172, v15\n"
  "v_max3_u32 v15, v173, v174, v15\n"
  "v_max3_u32 v15, v175, v176, v15\n"
  "v_max3_u32 v15, v177, v178, v15\n"
  "v_max3_u32 v15, v179, v180, v15\n"
  "v_max3_u32 v15, v181, v182, v15\n"
  "v_max3_u32 v15, v183, v184, v15\n"
  "v_max3_u32 v15, v185, v186, v15\n"
  "v_max3_u32 v15, v187, v188, v15\n"
  "v_max3_u32 v15, v189, v190, v15\n"
  "v_max_u32 v15, v191, v15\n"
  "s_waitcnt vmcnt(0) lgkmcnt(0)\n"
  "v_cmp_eq_u32 vcc, -1, v15\n"
  "s_nop 1\n"
  "s_mov_b64 s[66:67], -1\n"
  "s_cbranch_vccz C_X1_%=\n"
  "s_add_u32 s62, s62, 1\n"
  "s_cmp_gt_u32 s62, 300000\n"
  "s_cbranch_scc1 C_FAIL_%=\n"
  "s_and_b32 s60, s62, 255\n"
  "s_cmp_lg_u32 s60, 0\n"
  "s_cbranch_scc1 C_X1REDOP_%=\n"
  "v_mov_b32 v15, 0\n"
  "buffer_load_dword v15, v15, s[40:43], 0 offen sc1\n"
  "s_waitcnt vmcnt(0)\n"
  "v_readfirstlane_b32 s60, v15\n"
  "s_cmp_lg_u32 s60, 0\n"
  "s_cbranch_scc1 C_EXIT_%=\n"
  "s_branch C_X1REDOP_%=\n"
  "C_FAIL_%=:\n"
  "s_nop 7\n"
  "v_mov_b32 v32, 0\n"
  "v_mov_b32 v33, 1\n"
  "buffer_store_dword v33, v32, s[40:43], 0 offen sc1\n"
  "C_EXIT_%=:\n"
  "s_nop 7\n"
  "s_waitcnt vmcnt(0)\n"
  "s_endpgm\n"
      :: "{s[16:31]}"(sa), "{s[32:47]}"(sb), "{s[48:55]}"(sc), "{s79}"(dbgoff), "{s2}"(sto), "{v[0:15]}"(va), "{v[16:23]}"(vb), "{v[24:31]}"(vc) : "v255", "a255", "s99", "vcc", "memory");
}

static int envi(const char*, int d) { return d; }
extern "C" void kernel_launch(void* const* d_in, const int* in_sizes, int n_in, void* d_out, int out_size, void* d_ws, size_t ws_size, hipStream_t stream) {
  Args a;
  a.x0 = (const u16*)d_in[0]; a.wqkv = (const u16*)d_in[1]; a.wo = (const u16*)d_in[2]; a.mem0 = (const u16*)d_in[4];
  a.Wq = (const u16*)d_in[5]; a.Wk = (const u16*)d_in[6]; a.Wv = (const u16*)d_in[7]; a.Wo = (const u16*)d_in[8];
  a.out = (u16*)d_out;
  a.ws = (u8*)(((uintptr_t)d_ws + (2u << 20) - 1) & ~(uintptr_t)((2u << 20) - 1));
  a.marg = envi("R_MP", 48) | (envi("R_MM", 80) << 8) | (envi("R_G", 16) << 16); a.pad = 0;
  a.dp = envi("R_DP", 125); a.dm = envi("R_DM", 72); a.skew = envi("R_SKEW", 0); a.delta = envi("R_DELTA", 10); a.lat = envi("R_LAT", 50); a.pubw = envi("R_PUBW", -2);
  (void)hipFuncSetAttribute((const void*)k_pre, hipFuncAttributeMaxDynamicSharedMemorySize, 81920);
  (void)hipFuncSetAttribute((const void*)k_main, hipFuncAttributeMaxDynamicSharedMemorySize, 163840);
  k_pre<<<512, 256, 81920, stream>>>(a);
  if (!envi("R_PREONLY", 0)) k_main<<<256, 256, 163840, stream>>>(a);
}
```

```cpp
#include <hip/hip_runtime.h>
#include <hip/hip_bf16.h>
#include <stdint.h>

typedef __attribute__((ext_vector_type(8))) __bf16 bf16x8;
typedef __attribute__((ext_vector_type(4))) __bf16 bf16x4;
typedef __attribute__((ext_vector_type(2))) __bf16 bf16x2;
typedef __attribute__((ext_vector_type(4))) short s16x4;
typedef __attribute__((ext_vector_type(4))) float f32x4;
typedef __attribute__((ext_vector_type(4))) unsigned u32x4;
typedef __attribute__((ext_vector_type(2))) unsigned u32x2;
typedef __attribute__((ext_vector_type(16))) unsigned u32x16;
typedef __attribute__((ext_vector_type(8))) unsigned u32x8;
typedef unsigned short u16;
typedef unsigned char u8;
#define GAS __attribute__((address_space(1)))
#define LAS __attribute__((address_space(3)))
#define CAT4(a, b) __builtin_shufflevector(a, b, 0,1,2,3,4,5,6,7)
#define MFMA32(a, b, c) __builtin_amdgcn_mfma_f32_16x16x32_bf16(a, b, c, 0, 0, 0)
#define Z4 ((f32x4){0.f, 0.f, 0.f, 0.f})
#define ARM4 ((u32x4){0xFFFFFFFFu, 0xFFFFFFFFu, 0xFFFFFFFFu, 0xFFFFFFFFu})
#define DEV __device__ __forceinline__
#define SB() __builtin_amdgcn_sched_barrier(0)

constexpr unsigned SZ_X = 2048 * 640 * 2, SEGB = 40960;
constexpr unsigned SZ_RING = 2u << 20, PXB = 1u << 20, FLB = 7u << 18, CFB = FLB + 400 * 128, RING_FILL = CFB + 160 * 128;
constexpr size_t OFF_O = SZ_RING;
constexpr size_t OFF_XR = OFF_O + 2 * SZ_X;
constexpr size_t OFF_X2 = OFF_XR + 2 * SZ_X;
constexpr size_t OFF_KVP = OFF_X2 + SZ_X;
constexpr size_t OFF_XNF = OFF_KVP + (1u << 20);
constexpr size_t OFF_PQ = OFF_XNF + 2 * SZ_X + 4 * SEGB;
constexpr size_t OFF_XN0 = OFF_PQ + (4u << 20);
constexpr size_t OFF_WQF = OFF_XN0 + SZ_X;
constexpr size_t OFF_WOF = OFF_WQF + SZ_X;
constexpr size_t OFF_CF = OFF_WOF + (1u << 20);
constexpr size_t OFF_CTL = OFF_CF + (size_t)32 * 640 * 640 * 2;

#define DBGOUT(o)
#define TL(i)
#define TP(i)
struct Args { const u16* x0; const u16* wqkv; const u16* wo; const u16* mem0; const u16* Wq; const u16* Wk; const u16* Wv; const u16* Wo; u16* out; u8* ws; int dp, dm, skew, delta, lat, pubw, marg, pad; };

DEV float rbf(float f) { return (float)(__bf16)f; }
DEV bf16x4 cvt4(f32x4 v) { bf16x4 r; r[0] = (__bf16)v[0]; r[1] = (__bf16)v[1]; r[2] = (__bf16)v[2]; r[3] = (__bf16)v[3]; return r; }
DEV int pi16(int p) { return 8 * ((p >> 1) & 1) + 2 * (p >> 2) + (p & 1); }
DEV int drow(int u, int gb, int j) { return 16 * (2 * u + (gb >> 1)) + pi16(8 * (gb & 1) + j); }
DEV bf16x8 tr8(const u8* p0, const u8* p1) {
  s16x4 a = __builtin_amdgcn_ds_read_tr16_b64_v4i16((LAS s16x4*)p0), b = __builtin_amdgcn_ds_read_tr16_b64_v4i16((LAS s16x4*)p1);
  return __builtin_bit_cast(bf16x8, CAT4(a, b));
}
DEV unsigned mx4(u32x4 v) { return max(max(v.x, v.y), max(v.z, v.w)); }
DEV u32x4 canon4(u32x4 v) { return __builtin_elementwise_min(v, (u32x4){0xFFFFFFFEu, 0xFFFFFFFEu, 0xFFFFFFFEu, 0xFFFFFFFEu}); }
DEV float lo16(unsigned v) { return __uint_as_float(v << 16); }
DEV float hi16(unsigned v) { return __uint_as_float(v & 0xFFFF0000u); }
DEV unsigned pk2(float a, float b) { bf16x2 p; p[0] = (__bf16)a; p[1] = (__bf16)b; return __builtin_bit_cast(unsigned, p); }
DEV unsigned pkc(float a, float b) { return min(pk2(a, b), 0xFFFFFFFEu); }
DEV __amdgpu_buffer_rsrc_t rsrc(const void* p, unsigned bytes) { return __builtin_amdgcn_make_buffer_rsrc((void*)p, 0, (int)bytes, 0x00020000); }
DEV bool spin_fail(unsigned& spins, unsigned* ctl) {
  if (++spins > 300000u) { __hip_atomic_store((GAS unsigned*)ctl, 1u, __ATOMIC_RELAXED, __HIP_MEMORY_SCOPE_AGENT); return true; }
  return (spins & 63u) == 0u && __builtin_amdgcn_readfirstlane(__hip_atomic_load((GAS unsigned*)ctl, __ATOMIC_RELAXED, __HIP_MEMORY_SCOPE_AGENT));
}

#define OPQ(x) asm volatile("" : "+v"(x))
struct LinS { float sN[3]; f32x4 sT[2]; };
#define WF(i, t) (*(const bf16x8*)(wl + ((i) * 4 + (t)) * 1024))
DEV void lin_sums(const u8* wl, LinS& w) {
  bf16x8 one;
#pragma unroll
  for (int j = 0; j < 8; ++j) one[j] = (__bf16)1.f;
  f32x4 n0 = Z4, n1 = Z4, n2 = Z4, t0 = Z4, t1 = Z4;
#pragma unroll 4
  for (int i = 0; i < 20; ++i) { n0 = MFMA32(one, WF(i, 0), n0); n1 = MFMA32(one, WF(i, 1), n1); n2 = MFMA32(one, WF(i, 2), n2); t0 = MFMA32(WF(i, 3), one, t0); t1 = MFMA32(WF(i, 2), one, t1); }
  w.sN[0] = n0[0]; w.sN[1] = n1[0]; w.sN[2] = n2[0]; w.sT[0] = t0; w.sT[1] = t1;
}
template <int AUX>
DEV void lin_load(__amdgpu_buffer_rsrc_t rx, int tile, u32x4 (&x)[20], int lane) {
#pragma unroll
  for (int i = 0; i < 20; ++i) { x[i] = __builtin_amdgcn_raw_buffer_load_b128(rx, lane * 16 + (i & 3) * 1024, tile * 20480 + (i >> 2) * 4096, AUX); SB(); }
}
DEV float phi_r(float t) {
  float e = __builtin_amdgcn_exp2f(fminf(t * 1.4426950408889634f, 0.f)), p = fmaxf(t + 1e-6f, 1e-6f);
  OPQ(p);
  return p + e;
}
template <bool RAW>
DEV bool lin_tile(u32x4 (&x)[20], __amdgpu_buffer_rsrc_t rx, int tile, const u8* wl, const LinS& w, f32x4 (&kv)[4], __amdgpu_buffer_rsrc_t rq, int h, int lane, bool check, unsigned pfo, unsigned (&dbg)[3]) {
  const int g = lane >> 4, n16 = lane & 15;
  f32x4 n0 = Z4, n1 = Z4, n2 = Z4, t0 = Z4, t1 = Z4, gr = Z4;
  asm volatile("" ::: "memory");
  bf16x8 wf[3][4];
#pragma unroll
  for (int t = 0; t < 8; ++t) wf[t >> 2][t & 3] = WF(t >> 2, t & 3);
  SB();
#pragma unroll
  for (int i = 0; i < 20; ++i) {
    const bf16x8 xf = __builtin_bit_cast(bf16x8, x[i]);
    const int c = i % 3, n = (i + 2) % 3, j = i < 18 ? i + 2 : i;
    n0 = MFMA32(xf, wf[c][0], n0); if (i < 18) wf[n][0] = WF(j, 0); SB();
    n1 = MFMA32(xf, wf[c][1], n1); if (i < 18) wf[n][1] = WF(j, 1); SB();
    n2 = MFMA32(xf, wf[c][2], n2); if (i < 18) wf[n][2] = WF(j, 2); SB();
    t1 = MFMA32(wf[c][2], xf, t1); if (i < 18) wf[n][3] = WF(j, 3); SB();
    if (RAW) gr = MFMA32(xf, xf, gr);
    t0 = MFMA32(wf[c][3], xf, t0); SB();
    x[i] = __builtin_amdgcn_raw_buffer_load_b128(rx, pfo + (i & 3) * 1024, (tile + 4) * 20480 + (i >> 2) * 4096, RAW ? 16 : 0);
    SB();
  }
  if (RAW) {
    const float dg = (n16 & 2) ? ((n16 & 1) ? gr[3] : gr[2]) : ((n16 & 1) ? gr[1] : gr[0]);
    float s1 = __shfl(t1[0], 48 + n16), s2 = __shfl(dg, (n16 >> 2) * 16 + n16);
    if (check && __any(s1 != s1)) return false;
    OPQ(s1);
    float mu = s1 * (1.f / 640.f);
    OPQ(mu);
    float rs = __builtin_amdgcn_rsqf(fmaxf(s2 * (1.f / 640.f) - mu * mu, 0.f) + 1e-5f), mr = mu * rs;
    OPQ(rs); OPQ(mr);
    float mi[4], ri[4];
#pragma unroll
    for (int i = 0; i < 4; ++i) { mi[i] = __shfl(mr, 4 * g + i); ri[i] = __shfl(rs, 4 * g + i); }
#pragma unroll
    for (int i = 0; i < 4; ++i) {
      float a0 = mr * w.sT[0][i]; OPQ(a0); float a1 = mr * w.sT[1][i]; OPQ(a1);
      float b0 = mi[i] * w.sN[0]; OPQ(b0); float b1 = mi[i] * w.sN[1]; OPQ(b1); float b2 = mi[i] * w.sN[2]; OPQ(b2);
      a0 = __builtin_fmaf(t0[i], rs, -a0); a1 = __builtin_fmaf(t1[i], rs, -a1);
      b0 = __builtin_fmaf(n0[i], ri[i], -b0); b1 = __builtin_fmaf(n1[i], ri[i], -b1); b2 = __builtin_fmaf(n2[i], ri[i], -b2);
      OPQ(a0); OPQ(a1); OPQ(b0); OPQ(b1); OPQ(b2);
      t0[i] = a0; t1[i] = a1; n0[i] = b0; n1[i] = b1; n2[i] = b2;
    }
  }
  const float cst = n16 == 12 ? 1.f : 0.f;
#pragma unroll
  for (int i = 0; i < 4; ++i) {
    n0[i] = phi_r(n0[i]);
    const float pm = phi_r(n2[i]), pq = phi_r(t1[i]);
    n2[i] = n16 < 4 ? pm : (n16 < 8 ? n2[i] : cst);
    t0[i] = phi_r(t0[i]);
    t1[i] = g == 2 ? pq : 0.f;
  }
  const bf16x4 z = cvt4(Z4);
  const bf16x8 K = CAT4(cvt4(n0), z), V = CAT4(cvt4(n1), z), M = CAT4(cvt4(n2), z);
  kv[0] = MFMA32(K, V, kv[0]); kv[1] = MFMA32(K, M, kv[1]); kv[2] = MFMA32(M, V, kv[2]); kv[3] = MFMA32(M, M, kv[3]);
  __builtin_amdgcn_raw_buffer_store_b128(__builtin_bit_cast(u32x4, CAT4(cvt4(t0), cvt4(t1))), rq, lane * 16, (tile * 32 + h) * 1024, 16);
  return true;
}
#define LH 0
#define LC 4
#define FOLD1 1
constexpr int NKV = (LH ? 2 : 0) + (LC ? 5 : 0) + 1;
static_assert(NKV <= 7, "fill");
#define SRD(vec, i, ptr, bytes) do { unsigned long long p_ = (unsigned long long)(ptr); vec[i] = (unsigned)p_; vec[i + 1] = (unsigned)(p_ >> 32) & 0xFFFFu; vec[i + 2] = (unsigned)(bytes); vec[i + 3] = 0x00020000u; } while (0)
DEV void lin_first(const Args& a) {
  const unsigned wave = __builtin_amdgcn_readfirstlane(threadIdx.x >> 6), lane = threadIdx.x & 63;
  const unsigned jj = blockIdx.x >> 3, h = (blockIdx.x & 7) * 4 + (jj & 3);
  const unsigned q = jj >> 2, rg = (LH ? q : q - 2) - ((LC == 0 && q == 7) ? 5 : 0);
  constexpr unsigned LA = 32 - 2 * LH - 5 * LC;
  const unsigned first = q < 2 ? LA + q * LH : q < 7 ? LA + 2 * LH + (q - 2) * LC : 0, cnt = q < 2 ? LH : q < 7 ? LC : LA;
  u32x16 sa; u32x8 sb;
  SRD(sa, 0, a.ws + OFF_XN0, SZ_X); SRD(sa, 4, a.ws + OFF_WQF + (size_t)h * 81920, 81920); SRD(sa, 8, a.ws + OFF_PQ, 4u << 20); SRD(sa, 12, a.ws + OFF_KVP, 1u << 20);
  sb[0] = wave; sb[1] = first * 4 + wave; sb[2] = cnt; sb[3] = h * 1024; sb[4] = (h * NKV + rg) * 4096; sb[5] = sb[6] = sb[7] = 0;
  const u32x2 va = {lane * 16, (lane & 15) == 12 ? 0x3F800000u : 0u};
  asm volatile(
  "s_waitcnt vmcnt(0) lgkmcnt(0)\n s_nop 7\n"
  "s_mul_i32 s37, s32, 20480\n"
  "s_mov_b32 m0, s37\n"
  "s_nop 0\n"
  "buffer_load_dwordx4 v8, s[20:23], s37 offen offset:0 lds\n"
  "buffer_load_dwordx4 v8, s[20:23], s37 offen offset:1024 lds\n"
  "buffer_load_dwordx4 v8, s[20:23], s37 offen offset:2048 lds\n"
  "buffer_load_dwordx4 v8, s[20:23], s37 offen offset:3072 lds\n"
  "s_add_u32 s37, s37, 4096\n"
  "s_mov_b32 m0, s37\n"
  "s_nop 0\n"
  "buffer_load_dwordx4 v8, s[20:23], s37 offen offset:0 lds\n"
  "buffer_load_dwordx4 v8, s[20:23], s37 offen offset:1024 lds\n"
  "buffer_load_dwordx4 v8, s[20:23], s37 offen offset:2048 lds\n"
  "buffer_load_dwordx4 v8, s[20:23], s37 offen offset:3072 lds\n"
  "s_add_u32 s37, s37, 4096\n"
  "s_mov_b32 m0, s37\n"
  "s_nop 0\n"
  "buffer_load_dwordx4 v8, s[20:23], s37 offen offset:0 lds\n"
  "buffer_load_dwordx4 v8, s[20:23], s37 offen offset:1024 lds\n"
  "buffer_load_dwordx4 v8, s[20:23], s37 offen offset:2048 lds\n"
  "buffer_load_dwordx4 v8, s[20:23], s37 offen offset:3072 lds\n"
  "s_add_u32 s37, s37, 4096\n"
  "s_mov_b32 m0, s37\n"
  "s_nop 0\n"
  "buffer_load_dwordx4 v8, s[20:23], s37 offen offset:0 lds\n"
  "buffer_load_dwordx4 v8, s[20:23], s37 offen offset:1024 lds\n"
  "buffer_load_dwordx4 v8, s[20:23], s37 offen offset:2048 lds\n"
  "buffer_load_dwordx4 v8, s[20:23], s37 offen offset:3072 lds\n"
  "s_add_u32 s37, s37, 4096\n"
  "s_mov_b32 m0, s37\n"
  "s_nop 0\n"
  "buffer_load_dwordx4 v8, s[20:23], s37 offen offset:0 lds\n"
  "buffer_load_dwordx4 v8, s[20:23], s37 offen offset:1024 lds\n"
  "buffer_load_dwordx4 v8, s[20:23], s37 offen offset:2048 lds\n"
  "buffer_load_dwordx4 v8, s[20:23], s37 offen offset:3072 lds\n"
  "s_mul_i32 s42, s33, 20480\n"
  "s_add_u32 s43, s42, 4096\n"
  "s_add_u32 s44, s42, 8192\n"
  "s_add_u32 s45, s42, 12288\n"
  "s_add_u32 s46, s42, 16384\n"
  "buffer_load_dwordx4 v[16:19], v8, s[16:19], s42 offen offset:0\n"
  "buffer_load_dwordx4 v[20:23], v8, s[16:19], s42 offen offset:1024\n"
  "buffer_load_dwordx4 v[24:27], v8, s[16:19], s42 offen offset:2048\n"
  "buffer_load_dwordx4 v[28:31], v8, s[16:19], s42 offen offset:3072\n"
  "buffer_load_dwordx4 v[32:35], v8, s[16:19], s43 offen offset:0\n"
  "buffer_load_dwordx4 v[36:39], v8, s[16:19], s43 offen offset:1024\n"
  "buffer_load_dwordx4 v[40:43], v8, s[16:19], s43 offen offset:2048\n"
  "buffer_load_dwordx4 v[44:47], v8, s[16:19], s43 offen offset:3072\n"
  "buffer_load_dwordx4 v[48:51], v8, s[16:19], s44 offen offset:0\n"
  "buffer_load_dwordx4 v[52:55], v8, s[16:19], s44 offen offset:1024\n"
  "buffer_load_dwordx4 v[56:59], v8, s[16:19], s44 offen offset:2048\n"
  "buffer_load_dwordx4 v[60:63], v8, s[16:19], s44 offen offset:3072\n"
  "buffer_load_dwordx4 v[64:67], v8, s[16:19], s45 offen offset:0\n"
  "buffer_load_dwordx4 v[68:71], v8, s[16:19], s45 offen offset:1024\n"
  "buffer_load_dwordx4 v[72:75], v8, s[16:19], s45 offen offset:2048\n"
  "buffer_load_dwordx4 v[76:79], v8, s[16:19], s45 offen offset:3072\n"
  "buffer_load_dwordx4 v[80:83], v8, s[16:19], s46 offen offset:0\n"
  "buffer_load_dwordx4 v[84:87], v8, s[16:19], s46 offen offset:1024\n"
  "buffer_load_dwordx4 v[88:91], v8, s[16:19], s46 offen offset:2048\n"
  "buffer_load_dwordx4 v[92:95], v8, s[16:19], s46 offen offset:3072\n"
  "s_mov_b32 s40, 0x3fb8aa3b\n"
  "s_mov_b32 s41, 0x358637bd\n"
  "v_and_b32 v152, 240, v8\n"
  "v_cmp_gt_u32 s[48:49], 64, v152\n"
  "s_mov_b32 s38, 128\n"
  "v_cmp_gt_u32 s[50:51], s38, v152\n"
  "v_lshrrev_b32 v152, 8, v8\n"
  "v_cmp_eq_u32 s[52:53], 2, v152\n"
  "v_mov_b64 v[136:137], 0\n"
  "v_mov_b64 v[138:139], 0\n"
  "v_mov_b64 v[140:141], 0\n"
  "v_mov_b64 v[142:143], 0\n"
  "v_mov_b64 v[144:145], 0\n"
  "v_mov_b64 v[146:147], 0\n"
  "v_mov_b64 v[148:149], 0\n"
  "v_mov_b64 v[150:151], 0\n"
  "v_mov_b64 v[154:155], 0\n"
  "v_mov_b64 v[158:159], 0\n"
  "v_mov_b64 v[162:163], 0\n"
  "s_waitcnt vmcnt(20)\n"
  "s_barrier\n"
  "v_add_u32 v10, 40960, v8\n"
  "ds_read_b128 a[0:3], v8 offset:0\n"
  "ds_read_b128 a[4:7], v8 offset:1024\n"
  "ds_read_b128 a[8:11], v8 offset:2048\n"
  "ds_read_b128 a[12:15], v8 offset:3072\n"
  "ds_read_b128 a[16:19], v8 offset:4096\n"
  "ds_read_b128 a[20:23], v8 offset:5120\n"
  "ds_read_b128 a[24:27], v8 offset:6144\n"
  "ds_read_b128 a[28:31], v8 offset:7168\n"
  "ds_read_b128 a[32:35], v8 offset:8192\n"
  "ds_read_b128 a[36:39], v8 offset:9216\n"
  "ds_read_b128 a[40:43], v8 offset:10240\n"
  "ds_read_b128 a[44:47], v8 offset:11264\n"
  "ds_read_b128 a[48:51], v8 offset:12288\n"
  "ds_read_b128 a[52:55], v8 offset:13312\n"
  "s_waitcnt lgkmcnt(10)\n"
  "ds_read_b128 a[56:59], v8 offset:14336\n"
  "ds_read_b128 a[60:63], v8 offset:15360\n"
  "ds_read_b128 a[64:67], v8 offset:16384\n"
  "ds_read_b128 a[68:71], v8 offset:17408\n"
  "s_waitcnt lgkmcnt(10)\n"
  "ds_read_b128 a[72:75], v8 offset:18432\n"
  "ds_read_b128 a[76:79], v8 offset:19456\n"
  "ds_read_b128 a[80:83], v8 offset:20480\n"
  "ds_read_b128 a[84:87], v8 offset:21504\n"
  "s_waitcnt lgkmcnt(10)\n"
  "ds_read_b128 a[88:91], v8 offset:22528\n"
  "ds_read_b128 a[92:95], v8 offset:23552\n"
  "ds_read_b128 a[96:99], v8 offset:24576\n"
  "ds_read_b128 a[100:103], v8 offset:25600\n"
  "s_waitcnt lgkmcnt(10)\n"
  "ds_read_b128 a[104:107], v8 offset:26624\n"
  "ds_read_b128 a[108:111], v8 offset:27648\n"
  "ds_read_b128 a[112:115], v8 offset:28672\n"
  "ds_read_b128 a[116:119], v8 offset:29696\n"
  "s_waitcnt lgkmcnt(10)\n"
  "ds_read_b128 a[120:123], v8 offset:30720\n"
  "ds_read_b128 a[124:127], v8 offset:31744\n"
  "ds_read_b128 a[128:131], v8 offset:32768\n"
  "ds_read_b128 a[132:135], v8 offset:33792\n"
  "s_waitcnt lgkmcnt(10)\n"
  "ds_read_b128 a[136:139], v8 offset:34816\n"
  "ds_read_b128 a[140:143], v8 offset:35840\n"
  "ds_read_b128 a[144:147], v8 offset:36864\n"
  "ds_read_b128 a[148:151], v8 offset:37888\n"
  "s_waitcnt lgkmcnt(10)\n"
  "ds_read_b128 a[152:155], v8 offset:38912\n"
  "ds_read_b128 a[156:159], v8 offset:39936\n"
  "ds_read_b128 a[160:163], v10 offset:0\n"
  "ds_read_b128 a[164:167], v10 offset:1024\n"
  "s_waitcnt lgkmcnt(10)\n"
  "ds_read_b128 a[168:171], v10 offset:2048\n"
  "ds_read_b128 a[172:175], v10 offset:3072\n"
  "ds_read_b128 a[176:179], v10 offset:4096\n"
  "ds_read_b128 a[180:183], v10 offset:5120\n"
  "s_waitcnt lgkmcnt(10)\n"
  "ds_read_b128 a[184:187], v10 offset:6144\n"
  "ds_read_b128 a[188:191], v10 offset:7168\n"
  "ds_read_b128 a[192:195], v10 offset:8192\n"
  "ds_read_b128 a[196:199], v10 offset:9216\n"
  "s_waitcnt lgkmcnt(10)\n"
  "ds_read_b128 a[200:203], v10 offset:10240\n"
  "ds_read_b128 a[204:207], v10 offset:11264\n"
  "ds_read_b128 a[208:211], v10 offset:12288\n"
  "ds_read_b128 a[212:215], v10 offset:13312\n"
  "s_waitcnt lgkmcnt(10)\n"
  "ds_read_b128 a[216:219], v10 offset:14336\n"
  "ds_read_b128 a[220:223], v10 offset:15360\n"
  "ds_read_b128 a[224:227], v10 offset:16384\n"
  "ds_read_b128 a[228:231], v10 offset:17408\n"
  "s_waitcnt lgkmcnt(10)\n"
  "ds_read_b128 a[232:235], v10 offset:18432\n"
  "ds_read_b128 a[236:239], v10 offset:19456\n"
  "ds_read_b128 a[240:243], v10 offset:20480\n"
  "ds_read_b128 a[244:247], v10 offset:21504\n"
  "s_waitcnt lgkmcnt(10)\n"
  "ds_read_b128 a[248:251], v10 offset:22528\n"
  "ds_read_b128 a[252:255], v10 offset:23552\n"
  "ds_read_b128 v[184:187], v10 offset:24576\n"
  "ds_read_b128 v[188:191], v10 offset:25600\n"
  "s_waitcnt lgkmcnt(10)\n"
  "ds_read_b128 v[192:195], v10 offset:26624\n"
  "ds_read_b128 v[196:199], v10 offset:27648\n"
  "ds_read_b128 v[200:203], v10 offset:28672\n"
  "ds_read_b128 v[204:207], v10 offset:29696\n"
  "s_waitcnt lgkmcnt(10)\n"
  "ds_read_b128 v[208:211], v10 offset:30720\n"
  "ds_read_b128 v[212:215], v10 offset:31744\n"
  "ds_read_b128 v[216:219], v10 offset:32768\n"
  "ds_read_b128 v[220:223], v10 offset:33792\n"
  "s_waitcnt lgkmcnt(10)\n"
  "ds_read_b128 v[224:227], v10 offset:34816\n"
  "ds_read_b128 v[228:231], v10 offset:35840\n"
  "ds_read_b128 v[232:235], v10 offset:36864\n"
  "ds_read_b128 v[236:239], v10 offset:37888\n"
  "s_waitcnt lgkmcnt(10)\n"
  "ds_read_b128 v[240:243], v10 offset:38912\n"
  "ds_read_b128 v[244:247], v10 offset:39936\n"
  "s_waitcnt lgkmcnt(0)\n"
  "s_cmp_gt_u32 s34, 1\n"
  "s_cselect_b32 s37, 0, 0x80000000\n"
  "v_or_b32 v10, s37, v8\n"
  "s_lshl_b32 s38, s33, 15\n"
  "s_add_u32 s33, s33, 4\n"
  "s_mul_i32 s42, s33, 20480\n"
  "s_add_u32 s43, s42, 4096\n"
  "s_add_u32 s44, s42, 8192\n"
  "s_add_u32 s45, s42, 12288\n"
  "s_add_u32 s46, s42, 16384\n"
  "s_waitcnt vmcnt(19)\n"
  "v_mfma_f32_16x16x32_bf16 v[96:99], v[16:19], a[0:3], 0\n"
  "v_mfma_f32_16x16x32_bf16 v[100:103], v[16:19], a[4:7], 0\n"
  "v_mfma_f32_16x16x32_bf16 v[104:107], v[16:19], a[8:11], 0\n"
  "v_mfma_f32_16x16x32_bf16 v[108:111], a[8:11], v[16:19], 0\n"
  "v_mfma_f32_16x16x32_bf16 v[112:115], a[12:15], v[16:19], 0\n"
  "s_waitcnt vmcnt(18)\n"
  "v_mfma_f32_16x16x32_bf16 v[96:99], v[20:23], a[16:19], v[96:99]\n"
  "v_mfma_f32_16x16x32_bf16 v[100:103], v[20:23], a[20:23], v[100:103]\n"
  "v_mfma_f32_16x16x32_bf16 v[104:107], v[20:23], a[24:27], v[104:107]\n"
  "v_mfma_f32_16x16x32_bf16 v[108:111], a[24:27], v[20:23], v[108:111]\n"
  "v_mfma_f32_16x16x32_bf16 v[112:115], a[28:31], v[20:23], v[112:115]\n"
  "s_waitcnt vmcnt(17)\n"
  "v_mfma_f32_16x16x32_bf16 v[96:99], v[24:27], a[32:35], v[96:99]\n"
  "v_mfma_f32_16x16x32_bf16 v[100:103], v[24:27], a[36:39], v[100:103]\n"
  "v_mfma_f32_16x16x32_bf16 v[104:107], v[24:27], a[40:43], v[104:107]\n"
  "v_mfma_f32_16x16x32_bf16 v[108:111], a[40:43], v[24:27], v[108:111]\n"
  "v_mfma_f32_16x16x32_bf16 v[112:115], a[44:47], v[24:27], v[112:115]\n"
  "s_waitcnt vmcnt(16)\n"
  "v_mfma_f32_16x16x32_bf16 v[96:99], v[28:31], a[48:51], v[96:99]\n"
  "v_mfma_f32_16x16x32_bf16 v[100:103], v[28:31], a[52:55], v[100:103]\n"
  "v_mfma_f32_16x16x32_bf16 v[104:107], v[28:31], a[56:59], v[104:107]\n"
  "v_mfma_f32_16x16x32_bf16 v[108:111], a[56:59], v[28:31], v[108:111]\n"
  "v_mfma_f32_16x16x32_bf16 v[112:115], a[60:63], v[28:31], v[112:115]\n"
  "s_waitcnt vmcnt(15)\n"
  "v_mfma_f32_16x16x32_bf16 v[96:99], v[32:35], a[64:67], v[96:99]\n"
  "v_mfma_f32_16x16x32_bf16 v[100:103], v[32:35], a[68:71], v[100:103]\n"
  "v_mfma_f32_16x16x32_bf16 v[104:107], v[32:35], a[72:75], v[104:107]\n"
  "v_mfma_f32_16x16x32_bf16 v[108:111], a[72:75], v[32:35], v[108:111]\n"
  "v_mfma_f32_16x16x32_bf16 v[112:115], a[76:79], v[32:35], v[112:115]\n"
  "s_waitcnt vmcnt(14)\n"
  "v_mfma_f32_16x16x32_bf16 v[96:99], v[36:39], a[80:83], v[96:99]\n"
  "v_mfma_f32_16x16x32_bf16 v[100:103], v[36:39], a[84:87], v[100:103]\n"
  "v_mfma_f32_16x16x32_bf16 v[104:107], v[36:39], a[88:91], v[104:107]\n"
  "v_mfma_f32_16x16x32_bf16 v[108:111], a[88:91], v[36:39], v[108:111]\n"
  "v_mfma_f32_16x16x32_bf16 v[112:115], a[92:95], v[36:39], v[112:115]\n"
  "s_waitcnt vmcnt(13)\n"
  "v_mfma_f32_16x16x32_bf16 v[96:99], v[40:43], a[96:99], v[96:99]\n"
  "v_mfma_f32_16x16x32_bf16 v[100:103], v[40:43], a[100:103], v[100:103]\n"
  "v_mfma_f32_16x16x32_bf16 v[104:107], v[40:43], a[104:107], v[104:107]\n"
  "v_mfma_f32_16x16x32_bf16 v[108:111], a[104:107], v[40:43], v[108:111]\n"
  "v_mfma_f32_16x16x32_bf16 v[112:115], a[108:111], v[40:43], v[112:115]\n"
  "s_waitcnt vmcnt(12)\n"
  "v_mfma_f32_16x16x32_bf16 v[96:99], v[44:47], a[112:115], v[96:99]\n"
  "v_mfma_f32_16x16x32_bf16 v[100:103], v[44:47], a[116:119], v[100:103]\n"
  "v_mfma_f32_16x16x32_bf16 v[104:107], v[44:47], a[120:123], v[104:107]\n"
  "v_mfma_f32_16x16x32_bf16 v[108:111], a[120:123], v[44:47], v[108:111]\n"
  "v_mfma_f32_16x16x32_bf16 v[112:115], a[124:127], v[44:47], v[112:115]\n"
  "s_waitcnt vmcnt(11)\n"
  "v_mfma_f32_16x16x32_bf16 v[96:99], v[48:51], a[128:131], v[96:99]\n"
  "v_mfma_f32_16x16x32_bf16 v[100:103], v[48:51], a[132:135], v[100:103]\n"
  "v_mfma_f32_16x16x32_bf16 v[104:107], v[48:51], a[136:139], v[104:107]\n"
  "v_mfma_f32_16x16x32_bf16 v[108:111], a[136:139], v[48:51], v[108:111]\n"
  "v_mfma_f32_16x16x32_bf16 v[112:115], a[140:143], v[48:51], v[112:115]\n"
  "s_waitcnt vmcnt(10)\n"
  "v_mfma_f32_16x16x32_bf16 v[96:99], v[52:55], a[144:147], v[96:99]\n"
  "v_mfma_f32_16x16x32_bf16 v[100:103], v[52:55], a[148:151], v[100:103]\n"
  "v_mfma_f32_16x16x32_bf16 v[104:107], v[52:55], a[152:155], v[104:107]\n"
  "v_mfma_f32_16x16x32_bf16 v[108:111], a[152:155], v[52:55], v[108:111]\n"
  "v_mfma_f32_16x16x32_bf16 v[112:115], a[156:159], v[52:55], v[112:115]\n"
  "s_waitcnt vmcnt(9)\n"
  "v_mfma_f32_16x16x32_bf16 v[96:99], v[56:59], a[160:163], v[96:99]\n"
  "v_mfma_f32_16x16x32_bf16 v[100:103], v[56:59], a[164:167], v[100:103]\n"
  "buffer_load_dwordx4 v[16:19], v10, s[16:19], s42 offen offset:0\n"
  "v_mfma_f32_16x16x32_bf16 v[104:107], v[56:59], a[168:171], v[104:107]\n"
  "v_mfma_f32_16x16x32_bf16 v[108:111], a[168:171], v[56:59], v[108:111]\n"
  "v_mfma_f32_16x16x32_bf16 v[112:115], a[172:175], v[56:59], v[112:115]\n"
  "s_waitcnt vmcnt(9)\n"
  "v_mfma_f32_16x16x32_bf16 v[96:99], v[60:63], a[176:179], v[96:99]\n"
  "v_mfma_f32_16x16x32_bf16 v[100:103], v[60:63], a[180:183], v[100:103]\n"
  "buffer_load_dwordx4 v[20:23], v10, s[16:19], s42 offen offset:1024\n"
  "v_mfma_f32_16x16x32_bf16 v[104:107], v[60:63], a[184:187], v[104:107]\n"
  "v_mfma_f32_16x16x32_bf16 v[108:111], a[184:187], v[60:63], v[108:111]\n"
  "buffer_load_dwordx4 v[56:59], v10, s[16:19], s44 offen offset:2048\n"
  "v_mfma_f32_16x16x32_bf16 v[112:115], a[188:191], v[60:63], v[112:115]\n"
  "s_waitcnt vmcnt(10)\n"
  "v_mfma_f32_16x16x32_bf16 v[96:99], v[64:67], a[192:195], v[96:99]\n"
  "v_mfma_f32_16x16x32_bf16 v[100:103], v[64:67], a[196:199], v[100:103]\n"
  "buffer_load_dwordx4 v[24:27], v10, s[16:19], s42 offen offset:2048\n"
  "v_mfma_f32_16x16x32_bf16 v[104:107], v[64:67], a[200:203], v[104:107]\n"
  "v_mfma_f32_16x16x32_bf16 v[108:111], a[200:203], v[64:67], v[108:111]\n"
  "buffer_load_dwordx4 v[60:63], v10, s[16:19], s44 offen offset:3072\n"
  "v_mfma_f32_16x16x32_bf16 v[112:115], a[204:207], v[64:67], v[112:115]\n"
  "s_waitcnt vmcnt(11)\n"
  "v_mfma_f32_16x16x32_bf16 v[96:99], v[68:71], a[208:211], v[96:99]\n"
  "v_mfma_f32_16x16x32_bf16 v[100:103], v[68:71], a[212:215], v[100:103]\n"
  "buffer_load_dwordx4 v[28:31], v10, s[16:19], s42 offen offset:3072\n"
  "v_mfma_f32_16x16x32_bf16 v[104:107], v[68:71], a[216:219], v[104:107]\n"
  "v_mfma_f32_16x16x32_bf16 v[108:111], a[216:219], v[68:71], v[108:111]\n"
  "buffer_load_dwordx4 v[64:67], v10, s[16:19], s45 offen offset:0\n"
  "v_mfma_f32_16x16x32_bf16 v[112:115], a[220:223], v[68:71], v[112:115]\n"
  "s_waitcnt vmcnt(12)\n"
  "v_mfma_f32_16x16x32_bf16 v[96:99], v[72:75], a[224:227], v[96:99]\n"
  "v_mfma_f32_16x16x32_bf16 v[100:103], v[72:75], a[228:231], v[100:103]\n"
  "buffer_load_dwordx4 v[32:35], v10, s[16:19], s43 offen offset:0\n"
  "v_mfma_f32_16x16x32_bf16 v[104:107], v[72:75], a[232:235], v[104:107]\n"
  "v_mfma_f32_16x16x32_bf16 v[108:111], a[232:235], v[72:75], v[108:111]\n"
  "buffer_load_dwordx4 v[68:71], v10, s[16:19], s45 offen offset:1024\n"
  "v_mfma_f32_16x16x32_bf16 v[112:115], a[236:239], v[72:75], v[112:115]\n"
  "s_waitcnt vmcnt(13)\n"
  "v_mfma_f32_16x16x32_bf16 v[96:99], v[76:79], a[240:243], v[96:99]\n"
  "v_mfma_f32_16x16x32_bf16 v[100:103], v[76:79], a[244:247], v[100:103]\n"
  "buffer_load_dwordx4 v[36:39], v10, s[16:19], s43 offen offset:1024\n"
  "v_mfma_f32_16x16x32_bf16 v[104:107], v[76:79], a[248:251], v[104:107]\n"
  "v_mfma_f32_16x16x32_bf16 v[108:111], a[248:251], v[76:79], v[108:111]\n"
  "buffer_load_dwordx4 v[72:75], v10, s[16:19], s45 offen offset:2048\n"
  "v_mfma_f32_16x16x32_bf16 v[112:115], a[252:255], v[76:79], v[112:115]\n"
  "s_waitcnt vmcnt(14)\n"
  "v_mfma_f32_16x16x32_bf16 v[96:99], v[80:83], v[184:187], v[96:99]\n"
  "v_mfma_f32_16x16x32_bf16 v[100:103], v[80:83], v[188:191], v[100:103]\n"
  "buffer_load_dwordx4 v[40:43], v10, s[16:19], s43 offen offset:2048\n"
  "v_mfma_f32_16x16x32_bf16 v[104:107], v[80:83], v[192:195], v[104:107]\n"
  "v_mfma_f32_16x16x32_bf16 v[108:111], v[192:195], v[80:83], v[108:111]\n"
  "buffer_load_dwordx4 v[76:79], v10, s[16:19], s45 offen offset:3072\n"
  "v_mfma_f32_16x16x32_bf16 v[112:115], v[196:199], v[80:83], v[112:115]\n"
  "s_waitcnt vmcnt(15)\n"
  "v_mfma_f32_16x16x32_bf16 v[96:99], v[84:87], v[200:203], v[96:99]\n"
  "v_mfma_f32_16x16x32_bf16 v[100:103], v[84:87], v[204:207], v[100:103]\n"
  "buffer_load_dwordx4 v[44:47], v10, s[16:19], s43 offen offset:3072\n"
  "v_mfma_f32_16x16x32_bf16 v[104:107], v[84:87], v[208:211], v[104:107]\n"
  "v_mfma_f32_16x16x32_bf16 v[108:111], v[208:211], v[84:87], v[108:111]\n"
  "buffer_load_dwordx4 v[80:83], v10, s[16:19], s46 offen offset:0\n"
  "v_mfma_f32_16x16x32_bf16 v[112:115], v[212:215], v[84:87], v[112:115]\n"
  "s_waitcnt vmcnt(16)\n"
  "v_mfma_f32_16x16x32_bf16 v[96:99], v[88:91], v[216:219], v[96:99]\n"
  "v_mfma_f32_16x16x32_bf16 v[100:103], v[88:91], v[220:223], v[100:103]\n"
  "buffer_load_dwordx4 v[48:51], v10, s[16:19], s44 offen offset:0\n"
  "v_mfma_f32_16x16x32_bf16 v[104:107], v[88:91], v[224:227], v[104:107]\n"
  "v_mfma_f32_16x16x32_bf16 v[108:111], v[224:227], v[88:91], v[108:111]\n"
  "buffer_load_dwordx4 v[84:87], v10, s[16:19], s46 offen offset:1024\n"
  "v_mfma_f32_16x16x32_bf16 v[112:115], v[228:231], v[88:91], v[112:115]\n"
  "s_waitcnt vmcnt(17)\n"
  "v_mfma_f32_16x16x32_bf16 v[96:99], v[92:95], v[232:235], v[96:99]\n"
  "v_mfma_f32_16x16x32_bf16 v[100:103], v[92:95], v[236:239], v[100:103]\n"
  "buffer_load_dwordx4 v[52:55], v10, s[16:19], s44 offen offset:1024\n"
  "v_mfma_f32_16x16x32_bf16 v[104:107], v[92:95], v[240:243], v[104:107]\n"
  "v_mfma_f32_16x16x32_bf16 v[108:111], v[240:243], v[92:95], v[108:111]\n"
  "buffer_load_dwordx4 v[88:91], v10, s[16:19], s46 offen offset:2048\n"
  "v_mfma_f32_16x16x32_bf16 v[112:115], v[244:247], v[92:95], v[112:115]\n"
  "buffer_load_dwordx4 v[92:95], v10, s[16:19], s46 offen offset:3072\n"
  "s_add_u32 s39, s38, s35\n"
  "s_sub_u32 s34, s34, 1\n"
  "s_cmp_eq_u32 s34, 0\n"
  "L_LP_%=:\n"
  "s_cmp_gt_u32 s34, 1\n"
  "s_cselect_b32 s37, 0, 0x80000000\n"
  "v_or_b32 v10, s37, v8\n"
  "s_lshl_b32 s38, s33, 15\n"
  "s_add_u32 s33, s33, 4\n"
  "s_mul_i32 s42, s33, 20480\n"
  "s_add_u32 s43, s42, 4096\n"
  "s_add_u32 s44, s42, 8192\n"
  "s_add_u32 s45, s42, 12288\n"
  "s_add_u32 s46, s42, 16384\n"
  "s_waitcnt vmcnt(19)\n"
  "v_mfma_f32_16x16x32_bf16 v[116:119], v[16:19], a[0:3], 0\n"
  "v_mfma_f32_16x16x32_bf16 v[120:123], v[16:19], a[4:7], 0\n"
  "v_cvt_pk_bf16_f32 v156, v100, v101\n"
  "v_mfma_f32_16x16x32_bf16 v[124:127], v[16:19], a[8:11], 0\n"
  "v_cvt_pk_bf16_f32 v157, v102, v103\n"
  "v_mul_f32 v168, s40, v96\n"
  "v_mfma_f32_16x16x32_bf16 v[128:131], a[8:11], v[16:19], 0\n"
  "v_mul_f32 v169, s40, v97\n"
  "v_mul_f32 v170, s40, v98\n"
  "v_mul_f32 v171, s40, v99\n"
  "v_mfma_f32_16x16x32_bf16 v[132:135], a[12:15], v[16:19], 0\n"
  "v_exp_f32 v168, v168 clamp\n"
  "s_waitcnt vmcnt(18)\n"
  "v_mfma_f32_16x16x32_bf16 v[116:119], v[20:23], a[16:19], v[116:119]\n"
  "v_exp_f32 v169, v169 clamp\n"
  "v_mfma_f32_16x16x32_bf16 v[120:123], v[20:23], a[20:23], v[120:123]\n"
  "v_exp_f32 v170, v170 clamp\n"
  "v_exp_f32 v171, v171 clamp\n"
  "v_mfma_f32_16x16x32_bf16 v[124:127], v[20:23], a[24:27], v[124:127]\n"
  "v_add_f32 v96, s41, v96\n"
  "v_add_f32 v97, s41, v97\n"
  "v_mfma_f32_16x16x32_bf16 v[128:131], a[24:27], v[20:23], v[128:131]\n"
  "v_add_f32 v98, s41, v98\n"
  "v_add_f32 v99, s41, v99\n"
  "v_max_f32 v96, s41, v96\n"
  "v_mfma_f32_16x16x32_bf16 v[132:135], a[28:31], v[20:23], v[132:135]\n"
  "v_max_f32 v97, s41, v97\n"
  "v_max_f32 v98, s41, v98\n"
  "s_waitcnt vmcnt(16)\n"
  "v_mfma_f32_16x16x32_bf16 v[116:119], v[24:27], a[32:35], v[116:119]\n"
  "v_max_f32 v99, s41, v99\n"
  "v_add_f32 v96, v96, v168\n"
  "v_mfma_f32_16x16x32_bf16 v[120:123], v[24:27], a[36:39], v[120:123]\n"
  "v_add_f32 v97, v97, v169\n"
  "v_add_f32 v98, v98, v170\n"
  "v_add_f32 v99, v99, v171\n"
  "v_mfma_f32_16x16x32_bf16 v[124:127], v[24:27], a[40:43], v[124:127]\n"
  "v_cvt_pk_bf16_f32 v152, v96, v97\n"
  "v_cvt_pk_bf16_f32 v153, v98, v99\n"
  "v_mfma_f32_16x16x32_bf16 v[128:131], a[40:43], v[24:27], v[128:131]\n"
  "v_mul_f32 v168, s40, v104\n"
  "v_mul_f32 v169, s40, v105\n"
  "v_mul_f32 v170, s40, v106\n"
  "v_mfma_f32_16x16x32_bf16 v[132:135], a[44:47], v[24:27], v[132:135]\n"
  "v_mul_f32 v171, s40, v107\n"
  "v_exp_f32 v168, v168 clamp\n"
  "s_waitcnt vmcnt(14)\n"
  "v_mfma_f32_16x16x32_bf16 v[116:119], v[28:31], a[48:51], v[116:119]\n"
  "v_exp_f32 v169, v169 clamp\n"
  "v_mfma_f32_16x16x32_bf16 v[120:123], v[28:31], a[52:55], v[120:123]\n"
  "v_exp_f32 v170, v170 clamp\n"
  "v_exp_f32 v171, v171 clamp\n"
  "v_mfma_f32_16x16x32_bf16 v[124:127], v[28:31], a[56:59], v[124:127]\n"
  "v_add_f32 v100, s41, v104\n"
  "v_add_f32 v101, s41, v105\n"
  "v_mfma_f32_16x16x32_bf16 v[128:131], a[56:59], v[28:31], v[128:131]\n"
  "v_add_f32 v102, s41, v106\n"
  "v_add_f32 v103, s41, v107\n"
  "v_max_f32 v100, s41, v100\n"
  "v_mfma_f32_16x16x32_bf16 v[132:135], a[60:63], v[28:31], v[132:135]\n"
  "v_max_f32 v101, s41, v101\n"
  "v_max_f32 v102, s41, v102\n"
  "s_waitcnt vmcnt(12)\n"
  "v_mfma_f32_16x16x32_bf16 v[116:119], v[32:35], a[64:67], v[116:119]\n"
  "v_max_f32 v103, s41, v103\n"
  "v_add_f32 v100, v100, v168\n"
  "v_mfma_f32_16x16x32_bf16 v[120:123], v[32:35], a[68:71], v[120:123]\n"
  "v_add_f32 v101, v101, v169\n"
  "v_add_f32 v102, v102, v170\n"
  "v_add_f32 v103, v103, v171\n"
  "v_mfma_f32_16x16x32_bf16 v[124:127], v[32:35], a[72:75], v[124:127]\n"
  "v_cndmask_b32 v104, v9, v104, s[50:51]\n"
  "v_cndmask_b32 v105, v9, v105, s[50:51]\n"
  "v_mfma_f32_16x16x32_bf16 v[128:131], a[72:75], v[32:35], v[128:131]\n"
  "v_cndmask_b32 v106, v9, v106, s[50:51]\n"
  "v_cndmask_b32 v107, v9, v107, s[50:51]\n"
  "v_cndmask_b32 v104, v104, v100, s[48:49]\n"
  "v_mfma_f32_16x16x32_bf16 v[132:135], a[76:79], v[32:35], v[132:135]\n"
  "v_cndmask_b32 v105, v105, v101, s[48:49]\n"
  "v_cndmask_b32 v106, v106, v102, s[48:49]\n"
  "s_waitcnt vmcnt(10)\n"
  "v_mfma_f32_16x16x32_bf16 v[116:119], v[36:39], a[80:83], v[116:119]\n"
  "v_cndmask_b32 v107, v107, v103, s[48:49]\n"
  "v_cvt_pk_bf16_f32 v160, v104, v105\n"
  "v_mfma_f32_16x16x32_bf16 v[120:123], v[36:39], a[84:87], v[120:123]\n"
  "v_cvt_pk_bf16_f32 v161, v106, v107\n"
  "v_mul_f32 v168, s40, v112\n"
  "v_mul_f32 v169, s40, v113\n"
  "v_mfma_f32_16x16x32_bf16 v[124:127], v[36:39], a[88:91], v[124:127]\n"
  "v_mul_f32 v170, s40, v114\n"
  "v_mul_f32 v171, s40, v115\n"
  "v_mfma_f32_16x16x32_bf16 v[128:131], a[88:91], v[36:39], v[128:131]\n"
  "v_exp_f32 v168, v168 clamp\n"
  "v_exp_f32 v169, v169 clamp\n"
  "v_mfma_f32_16x16x32_bf16 v[132:135], a[92:95], v[36:39], v[132:135]\n"
  "v_exp_f32 v170, v170 clamp\n"
  "s_waitcnt vmcnt(8)\n"
  "v_mfma_f32_16x16x32_bf16 v[116:119], v[40:43], a[96:99], v[116:119]\n"
  "v_exp_f32 v171, v171 clamp\n"
  "v_mfma_f32_16x16x32_bf16 v[120:123], v[40:43], a[100:103], v[120:123]\n"
  "v_add_f32 v112, s41, v112\n"
  "v_add_f32 v113, s41, v113\n"
  "v_add_f32 v114, s41, v114\n"
  "v_mfma_f32_16x16x32_bf16 v[124:127], v[40:43], a[104:107], v[124:127]\n"
  "v_add_f32 v115, s41, v115\n"
  "v_max_f32 v112, s41, v112\n"
  "v_mfma_f32_16x16x32_bf16 v[128:131], a[104:107], v[40:43], v[128:131]\n"
  "v_max_f32 v113, s41, v113\n"
  "v_max_f32 v114, s41, v114\n"
  "v_max_f32 v115, s41, v115\n"
  "v_mfma_f32_16x16x32_bf16 v[132:135], a[108:111], v[40:43], v[132:135]\n"
  "v_add_f32 v112, v112, v168\n"
  "v_add_f32 v113, v113, v169\n"
  "s_waitcnt vmcnt(6)\n"
  "v_mfma_f32_16x16x32_bf16 v[116:119], v[44:47], a[112:115], v[116:119]\n"
  "v_add_f32 v114, v114, v170\n"
  "v_add_f32 v115, v115, v171\n"
  "v_mfma_f32_16x16x32_bf16 v[120:123], v[44:47], a[116:119], v[120:123]\n"
  "v_mfma_f32_16x16x32_bf16 v[136:139], v[152:155], v[156:159], v[136:139]\n"
  "v_mfma_f32_16x16x32_bf16 v[140:143], v[152:155], v[160:163], v[140:143]\n"
  "v_mfma_f32_16x16x32_bf16 v[144:147], v[160:163], v[156:159], v[144:147]\n"
  "v_mfma_f32_16x16x32_bf16 v[124:127], v[44:47], a[120:123], v[124:127]\n"
  "v_mfma_f32_16x16x32_bf16 v[148:151], v[160:163], v[160:163], v[148:151]\n"
  "v_cvt_pk_bf16_f32 v164, v112, v113\n"
  "v_mfma_f32_16x16x32_bf16 v[128:131], a[120:123], v[44:47], v[128:131]\n"
  "v_cvt_pk_bf16_f32 v165, v114, v115\n"
  "v_mul_f32 v168, s40, v108\n"
  "v_mul_f32 v169, s40, v109\n"
  "v_mfma_f32_16x16x32_bf16 v[132:135], a[124:127], v[44:47], v[132:135]\n"
  "v_mul_f32 v170, s40, v110\n"
  "v_mul_f32 v171, s40, v111\n"
  "s_waitcnt vmcnt(4)\n"
  "v_mfma_f32_16x16x32_bf16 v[116:119], v[48:51], a[128:131], v[116:119]\n"
  "v_exp_f32 v168, v168 clamp\n"
  "v_mfma_f32_16x16x32_bf16 v[120:123], v[48:51], a[132:135], v[120:123]\n"
  "v_exp_f32 v169, v169 clamp\n"
  "v_exp_f32 v170, v170 clamp\n"
  "v_mfma_f32_16x16x32_bf16 v[124:127], v[48:51], a[136:139], v[124:127]\n"
  "v_exp_f32 v171, v171 clamp\n"
  "v_mfma_f32_16x16x32_bf16 v[128:131], a[136:139], v[48:51], v[128:131]\n"
  "v_add_f32 v108, s41, v108\n"
  "v_add_f32 v109, s41, v109\n"
  "v_add_f32 v110, s41, v110\n"
  "v_mfma_f32_16x16x32_bf16 v[132:135], a[140:143], v[48:51], v[132:135]\n"
  "v_add_f32 v111, s41, v111\n"
  "v_max_f32 v108, s41, v108\n"
  "s_waitcnt vmcnt(2)\n"
  "v_mfma_f32_16x16x32_bf16 v[116:119], v[52:55], a[144:147], v[116:119]\n"
  "v_max_f32 v109, s41, v109\n"
  "v_max_f32 v110, s41, v110\n"
  "v_mfma_f32_16x16x32_bf16 v[120:123], v[52:55], a[148:151], v[120:123]\n"
  "v_max_f32 v111, s41, v111\n"
  "v_add_f32 v108, v108, v168\n"
  "v_add_f32 v109, v109, v169\n"
  "v_mfma_f32_16x16x32_bf16 v[124:127], v[52:55], a[152:155], v[124:127]\n"
  "v_add_f32 v110, v110, v170\n"
  "v_add_f32 v111, v111, v171\n"
  "v_mfma_f32_16x16x32_bf16 v[128:131], a[152:155], v[52:55], v[128:131]\n"
  "v_cvt_pk_bf16_f32 v166, v108, v109\n"
  "v_cvt_pk_bf16_f32 v167, v110, v111\n"
  "v_cndmask_b32 v166, 0, v166, s[52:53]\n"
  "v_mfma_f32_16x16x32_bf16 v[132:135], a[156:159], v[52:55], v[132:135]\n"
  "v_cndmask_b32 v167, 0, v167, s[52:53]\n"
  "v_mfma_f32_16x16x32_bf16 v[116:119], v[56:59], a[160:163], v[116:119]\n"
  "buffer_store_dwordx4 v[164:167], v8, s[24:27], s39 offen sc1\n"
  "v_mfma_f32_16x16x32_bf16 v[120:123], v[56:59], a[164:167], v[120:123]\n"
  "buffer_load_dwordx4 v[16:19], v10, s[16:19], s42 offen offset:0\n"
  "v_mfma_f32_16x16x32_bf16 v[124:127], v[56:59], a[168:171], v[124:127]\n"
  "v_mfma_f32_16x16x32_bf16 v[128:131], a[168:171], v[56:59], v[128:131]\n"
  "v_mfma_f32_16x16x32_bf16 v[132:135], a[172:175], v[56:59], v[132:135]\n"
  "v_mfma_f32_16x16x32_bf16 v[116:119], v[60:63], a[176:179], v[116:119]\n"
  "v_mfma_f32_16x16x32_bf16 v[120:123], v[60:63], a[180:183], v[120:123]\n"
  "buffer_load_dwordx4 v[20:23], v10, s[16:19], s42 offen offset:1024\n"
  "v_mfma_f32_16x16x32_bf16 v[124:127], v[60:63], a[184:187], v[124:127]\n"
  "v_mfma_f32_16x16x32_bf16 v[128:131], a[184:187], v[60:63], v[128:131]\n"
  "buffer_load_dwordx4 v[56:59], v10, s[16:19], s44 offen offset:2048\n"
  "v_mfma_f32_16x16x32_bf16 v[132:135], a[188:191], v[60:63], v[132:135]\n"
  "v_mfma_f32_16x16x32_bf16 v[116:119], v[64:67], a[192:195], v[116:119]\n"
  "v_mfma_f32_16x16x32_bf16 v[120:123], v[64:67], a[196:199], v[120:123]\n"
  "buffer_load_dwordx4 v[24:27], v10, s[16:19], s42 offen offset:2048\n"
  "v_mfma_f32_16x16x32_bf16 v[124:127], v[64:67], a[200:203], v[124:127]\n"
  "v_mfma_f32_16x16x32_bf16 v[128:131], a[200:203], v[64:67], v[128:131]\n"
  "buffer_load_dwordx4 v[60:63], v10, s[16:19], s44 offen offset:3072\n"
  "v_mfma_f32_16x16x32_bf16 v[132:135], a[204:207], v[64:67], v[132:135]\n"
  "v_mfma_f32_16x16x32_bf16 v[116:119], v[68:71], a[208:211], v[116:119]\n"
  "v_mfma_f32_16x16x32_bf16 v[120:123], v[68:71], a[212:215], v[120:123]\n"
  "buffer_load_dwordx4 v[28:31], v10, s[16:19], s42 offen offset:3072\n"
  "v_mfma_f32_16x16x32_bf16 v[124:127], v[68:71], a[216:219], v[124:127]\n"
  "v_mfma_f32_16x16x32_bf16 v[128:131], a[216:219], v[68:71], v[128:131]\n"
  "buffer_load_dwordx4 v[64:67], v10, s[16:19], s45 offen offset:0\n"
  "v_mfma_f32_16x16x32_bf16 v[132:135], a[220:223], v[68:71], v[132:135]\n"
  "v_mfma_f32_16x16x32_bf16 v[116:119], v[72:75], a[224:227], v[116:119]\n"
  "v_mfma_f32_16x16x32_bf16 v[120:123], v[72:75], a[228:231], v[120:123]\n"
  "buffer_load_dwordx4 v[32:35], v10, s[16:19], s43 offen offset:0\n"
  "v_mfma_f32_16x16x32_bf16 v[124:127], v[72:75], a[232:235], v[124:127]\n"
  "v_mfma_f32_16x16x32_bf16 v[128:131], a[232:235], v[72:75], v[128:131]\n"
  "buffer_load_dwordx4 v[68:71], v10, s[16:19], s45 offen offset:1024\n"
  "v_mfma_f32_16x16x32_bf16 v[132:135], a[236:239], v[72:75], v[132:135]\n"
  "v_mfma_f32_16x16x32_bf16 v[116:119], v[76:79], a[240:243], v[116:119]\n"
  "v_mfma_f32_16x16x32_bf16 v[120:123], v[76:79], a[244:247], v[120:123]\n"
  "buffer_load_dwordx4 v[36:39], v10, s[16:19], s43 offen offset:1024\n"
  "v_mfma_f32_16x16x32_bf16 v[124:127], v[76:79], a[248:251], v[124:127]\n"
  "v_mfma_f32_16x16x32_bf16 v[128:131], a[248:251], v[76:79], v[128:131]\n"
  "buffer_load_dwordx4 v[72:75], v10, s[16:19], s45 offen offset:2048\n"
  "v_mfma_f32_16x16x32_bf16 v[132:135], a[252:255], v[76:79], v[132:135]\n"
  "v_mfma_f32_16x16x32_bf16 v[116:119], v[80:83], v[184:187], v[116:119]\n"
  "v_mfma_f32_16x16x32_bf16 v[120:123], v[80:83], v[188:191], v[120:123]\n"
  "buffer_load_dwordx4 v[40:43], v10, s[16:19], s43 offen offset:2048\n"
  "v_mfma_f32_16x16x32_bf16 v[124:127], v[80:83], v[192:195], v[124:127]\n"
  "v_mfma_f32_16x16x32_bf16 v[128:131], v[192:195], v[80:83], v[128:131]\n"
  "buffer_load_dwordx4 v[76:79], v10, s[16:19], s45 offen offset:3072\n"
  "v_mfma_f32_16x16x32_bf16 v[132:135], v[196:199], v[80:83], v[132:135]\n"
  "v_mfma_f32_16x16x32_bf16 v[116:119], v[84:87], v[200:203], v[116:119]\n"
  "v_mfma_f32_16x16x32_bf16 v[120:123], v[84:87], v[204:207], v[120:123]\n"
  "buffer_load_dwordx4 v[44:47], v10, s[16:19], s43 offen offset:3072\n"
  "v_mfma_f32_16x16x32_bf16 v[124:127], v[84:87], v[208:211], v[124:127]\n"
  "v_mfma_f32_16x16x32_bf16 v[128:131], v[208:211], v[84:87], v[128:131]\n"
  "buffer_load_dwordx4 v[80:83], v10, s[16:19], s46 offen offset:0\n"
  "v_mfma_f32_16x16x32_bf16 v[132:135], v[212:215], v[84:87], v[132:135]\n"
  "s_waitcnt vmcnt(17)\n"
  "v_mfma_f32_16x16x32_bf16 v[116:119], v[88:91], v[216:219], v[116:119]\n"
  "v_mfma_f32_16x16x32_bf16 v[120:123], v[88:91], v[220:223], v[120:123]\n"
  "buffer_load_dwordx4 v[48:51], v10, s[16:19], s44 offen offset:0\n"
  "v_mfma_f32_16x16x32_bf16 v[124:127], v[88:91], v[224:227], v[124:127]\n"
  "v_mfma_f32_16x16x32_bf16 v[128:131], v[224:227], v[88:91], v[128:131]\n"
  "buffer_load_dwordx4 v[84:87], v10, s[16:19], s46 offen offset:1024\n"
  "v_mfma_f32_16x16x32_bf16 v[132:135], v[228:231], v[88:91], v[132:135]\n"
  "s_waitcnt vmcnt(18)\n"
  "v_mfma_f32_16x16x32_bf16 v[116:119], v[92:95], v[232:235], v[116:119]\n"
  "v_mfma_f32_16x16x32_bf16 v[120:123], v[92:95], v[236:239], v[120:123]\n"
  "buffer_load_dwordx4 v[52:55], v10, s[16:19], s44 offen offset:1024\n"
  "v_mfma_f32_16x16x32_bf16 v[124:127], v[92:95], v[240:243], v[124:127]\n"
  "v_mfma_f32_16x16x32_bf16 v[128:131], v[240:243], v[92:95], v[128:131]\n"
  "buffer_load_dwordx4 v[88:91], v10, s[16:19], s46 offen offset:2048\n"
  "v_mfma_f32_16x16x32_bf16 v[132:135], v[244:247], v[92:95], v[132:135]\n"
  "buffer_load_dwordx4 v[92:95], v10, s[16:19], s46 offen offset:3072\n"
  "s_add_u32 s39, s38, s35\n"
  "s_sub_u32 s34, s34, 1\n"
  "s_cmp_eq_u32 s34, 0\n"
  "s_cbranch_scc1 L_E1_%=\n"
  "s_cmp_gt_u32 s34, 1\n"
  "s_cselect_b32 s37, 0, 0x80000000\n"
  "v_or_b32 v10, s37, v8\n"
  "s_lshl_b32 s38, s33, 15\n"
  "s_add_u32 s33, s33, 4\n"
  "s_mul_i32 s42, s33, 20480\n"
  "s_add_u32 s43, s42, 4096\n"
  "s_add_u32 s44, s42, 8192\n"
  "s_add_u32 s45, s42, 12288\n"
  "s_add_u32 s46, s42, 16384\n"
  "s_waitcnt vmcnt(19)\n"
  "v_mfma_f32_16x16x32_bf16 v[96:99], v[16:19], a[0:3], 0\n"
  "v_mfma_f32_16x16x32_bf16 v[100:103], v[16:19], a[4:7], 0\n"
  "v_cvt_pk_bf16_f32 v156, v120, v121\n"
  "v_mfma_f32_16x16x32_bf16 v[104:107], v[16:19], a[8:11], 0\n"
  "v_cvt_pk_bf16_f32 v157, v122, v123\n"
  "v_mul_f32 v168, s40, v116\n"
  "v_mfma_f32_16x16x32_bf16 v[108:111], a[8:11], v[16:19], 0\n"
  "v_mul_f32 v169, s40, v117\n"
  "v_mul_f32 v170, s40, v118\n"
  "v_mul_f32 v171, s40, v119\n"
  "v_mfma_f32_16x16x32_bf16 v[112:115], a[12:15], v[16:19], 0\n"
  "v_exp_f32 v168, v168 clamp\n"
  "s_waitcnt vmcnt(18)\n"
  "v_mfma_f32_16x16x32_bf16 v[96:99], v[20:23], a[16:19], v[96:99]\n"
  "v_exp_f32 v169, v169 clamp\n"
  "v_mfma_f32_16x16x32_bf16 v[100:103], v[20:23], a[20:23], v[100:103]\n"
  "v_exp_f32 v170, v170 clamp\n"
  "v_exp_f32 v171, v171 clamp\n"
  "v_mfma_f32_16x16x32_bf16 v[104:107], v[20:23], a[24:27], v[104:107]\n"
  "v_add_f32 v116, s41, v116\n"
  "v_add_f32 v117, s41, v117\n"
  "v_mfma_f32_16x16x32_bf16 v[108:111], a[24:27], v[20:23], v[108:111]\n"
  "v_add_f32 v118, s41, v118\n"
  "v_add_f32 v119, s41, v119\n"
  "v_max_f32 v116, s41, v116\n"
  "v_mfma_f32_16x16x32_bf16 v[112:115], a[28:31], v[20:23], v[112:115]\n"
  "v_max_f32 v117, s41, v117\n"
  "v_max_f32 v118, s41, v118\n"
  "s_waitcnt vmcnt(16)\n"
  "v_mfma_f32_16x16x32_bf16 v[96:99], v[24:27], a[32:35], v[96:99]\n"
  "v_max_f32 v119, s41, v119\n"
  "v_add_f32 v116, v116, v168\n"
  "v_mfma_f32_16x16x32_bf16 v[100:103], v[24:27], a[36:39], v[100:103]\n"
  "v_add_f32 v117, v117, v169\n"
  "v_add_f32 v118, v118, v170\n"
  "v_add_f32 v119, v119, v171\n"
  "v_mfma_f32_16x16x32_bf16 v[104:107], v[24:27], a[40:43], v[104:107]\n"
  "v_cvt_pk_bf16_f32 v152, v116, v117\n"
  "v_cvt_pk_bf16_f32 v153, v118, v119\n"
  "v_mfma_f32_16x16x32_bf16 v[108:111], a[40:43], v[24:27], v[108:111]\n"
  "v_mul_f32 v168, s40, v124\n"
  "v_mul_f32 v169, s40, v125\n"
  "v_mul_f32 v170, s40, v126\n"
  "v_mfma_f32_16x16x32_bf16 v[112:115], a[44:47], v[24:27], v[112:115]\n"
  "v_mul_f32 v171, s40, v127\n"
  "v_exp_f32 v168, v168 clamp\n"
  "s_waitcnt vmcnt(14)\n"
  "v_mfma_f32_16x16x32_bf16 v[96:99], v[28:31], a[48:51], v[96:99]\n"
  "v_exp_f32 v169, v169 clamp\n"
  "v_mfma_f32_16x16x32_bf16 v[100:103], v[28:31], a[52:55], v[100:103]\n"
  "v_exp_f32 v170, v170 clamp\n"
  "v_exp_f32 v171, v171 clamp\n"
  "v_mfma_f32_16x16x32_bf16 v[104:107], v[28:31], a[56:59], v[104:107]\n"
  "v_add_f32 v120, s41, v124\n"
  "v_add_f32 v121, s41, v125\n"
  "v_mfma_f32_16x16x32_bf16 v[108:111], a[56:59], v[28:31], v[108:111]\n"
  "v_add_f32 v122, s41, v126\n"
  "v_add_f32 v123, s41, v127\n"
  "v_max_f32 v120, s41, v120\n"
  "v_mfma_f32_16x16x32_bf16 v[112:115], a[60:63], v[28:31], v[112:115]\n"
  "v_max_f32 v121, s41, v121\n"
  "v_max_f32 v122, s41, v122\n"
  "s_waitcnt vmcnt(12)\n"
  "v_mfma_f32_16x16x32_bf16 v[96:99], v[32:35], a[64:67], v[96:99]\n"
  "v_max_f32 v123, s41, v123\n"
  "v_add_f32 v120, v120, v168\n"
  "v_mfma_f32_16x16x32_bf16 v[100:103], v[32:35], a[68:71], v[100:103]\n"
  "v_add_f32 v121, v121, v169\n"
  "v_add_f32 v122, v122, v170\n"
  "v_add_f32 v123, v123, v171\n"
  "v_mfma_f32_16x16x32_bf16 v[104:107], v[32:35], a[72:75], v[104:107]\n"
  "v_cndmask_b32 v124, v9, v124, s[50:51]\n"
  "v_cndmask_b32 v125, v9, v125, s[50:51]\n"
  "v_mfma_f32_16x16x32_bf16 v[108:111], a[72:75], v[32:35], v[108:111]\n"
  "v_cndmask_b32 v126, v9, v126, s[50:51]\n"
  "v_cndmask_b32 v127, v9, v127, s[50:51]\n"
  "v_cndmask_b32 v124, v124, v120, s[48:49]\n"
  "v_mfma_f32_16x16x32_bf16 v[112:115], a[76:79], v[32:35], v[112:115]\n"
  "v_cndmask_b32 v125, v125, v121, s[48:49]\n"
  "v_cndmask_b32 v126, v126, v122, s[48:49]\n"
  "s_waitcnt vmcnt(10)\n"
  "v_mfma_f32_16x16x32_bf16 v[96:99], v[36:39], a[80:83], v[96:99]\n"
  "v_cndmask_b32 v127, v127, v123, s[48:49]\n"
  "v_cvt_pk_bf16_f32 v160, v124, v125\n"
  "v_mfma_f32_16x16x32_bf16 v[100:103], v[36:39], a[84:87], v[100:103]\n"
  "v_cvt_pk_bf16_f32 v161, v126, v127\n"
  "v_mul_f32 v168, s40, v132\n"
  "v_mul_f32 v169, s40, v133\n"
  "v_mfma_f32_16x16x32_bf16 v[104:107], v[36:39], a[88:91], v[104:107]\n"
  "v_mul_f32 v170, s40, v134\n"
  "v_mul_f32 v171, s40, v135\n"
  "v_mfma_f32_16x16x32_bf16 v[108:111], a[88:91], v[36:39], v[108:111]\n"
  "v_exp_f32 v168, v168 clamp\n"
  "v_exp_f32 v169, v169 clamp\n"
  "v_mfma_f32_16x16x32_bf16 v[112:115], a[92:95], v[36:39], v[112:115]\n"
  "v_exp_f32 v170, v170 clamp\n"
  "s_waitcnt vmcnt(8)\n"
  "v_mfma_f32_16x16x32_bf16 v[96:99], v[40:43], a[96:99], v[96:99]\n"
  "v_exp_f32 v171, v171 clamp\n"
  "v_mfma_f32_16x16x32_bf16 v[100:103], v[40:43], a[100:103], v[100:103]\n"
  "v_add_f32 v132, s41, v132\n"
  "v_add_f32 v133, s41, v133\n"
  "v_add_f32 v134, s41, v134\n"
  "v_mfma_f32_16x16x32_bf16 v[104:107], v[40:43], a[104:107], v[104:107]\n"
  "v_add_f32 v135, s41, v135\n"
  "v_max_f32 v132, s41, v132\n"
  "v_mfma_f32_16x16x32_bf16 v[108:111], a[104:107], v[40:43], v[108:111]\n"
  "v_max_f32 v133, s41, v133\n"
  "v_max_f32 v134, s41, v134\n"
  "v_max_f32 v135, s41, v135\n"
  "v_mfma_f32_16x16x32_bf16 v[112:115], a[108:111], v[40:43], v[112:115]\n"
  "v_add_f32 v132, v132, v168\n"
  "v_add_f32 v133, v133, v169\n"
  "s_waitcnt vmcnt(6)\n"
  "v_mfma_f32_16x16x32_bf16 v[96:99], v[44:47], a[112:115], v[96:99]\n"
  "v_add_f32 v134, v134, v170\n"
  "v_add_f32 v135, v135, v171\n"
  "v_mfma_f32_16x16x32_bf16 v[100:103], v[44:47], a[116:119], v[100:103]\n"
  "v_mfma_f32_16x16x32_bf16 v[136:139], v[152:155], v[156:159], v[136:139]\n"
  "v_mfma_f32_16x16x32_bf16 v[140:143], v[152:155], v[160:163], v[140:143]\n"
  "v_mfma_f32_16x16x32_bf16 v[144:147], v[160:163], v[156:159], v[144:147]\n"
  "v_mfma_f32_16x16x32_bf16 v[104:107], v[44:47], a[120:123], v[104:107]\n"
  "v_mfma_f32_16x16x32_bf16 v[148:151], v[160:163], v[160:163], v[148:151]\n"
  "v_cvt_pk_bf16_f32 v164, v132, v133\n"
  "v_mfma_f32_16x16x32_bf16 v[108:111], a[120:123], v[44:47], v[108:111]\n"
  "v_cvt_pk_bf16_f32 v165, v134, v135\n"
  "v_mul_f32 v168, s40, v128\n"
  "v_mul_f32 v169, s40, v129\n"
  "v_mfma_f32_16x16x32_bf16 v[112:115], a[124:127], v[44:47], v[112:115]\n"
  "v_mul_f32 v170, s40, v130\n"
  "v_mul_f32 v171, s40, v131\n"
  "s_waitcnt vmcnt(4)\n"
  "v_mfma_f32_16x16x32_bf16 v[96:99], v[48:51], a[128:131], v[96:99]\n"
  "v_exp_f32 v168, v168 clamp\n"
  "v_mfma_f32_16x16x32_bf16 v[100:103], v[48:51], a[132:135], v[100:103]\n"
  "v_exp_f32 v169, v169 clamp\n"
  "v_exp_f32 v170, v170 clamp\n"
  "v_mfma_f32_16x16x32_bf16 v[104:107], v[48:51], a[136:139], v[104:107]\n"
  "v_exp_f32 v171, v171 clamp\n"
  "v_mfma_f32_16x16x32_bf16 v[108:111], a[136:139], v[48:51], v[108:111]\n"
  "v_add_f32 v128, s41, v128\n"
  "v_add_f32 v129, s41, v129\n"
  "v_add_f32 v130, s41, v130\n"
  "v_mfma_f32_16x16x32_bf16 v[112:115], a[140:143], v[48:51], v[112:115]\n"
  "v_add_f32 v131, s41, v131\n"
  "v_max_f32 v128, s41, v128\n"
  "s_waitcnt vmcnt(2)\n"
  "v_mfma_f32_16x16x32_bf16 v[96:99], v[52:55], a[144:147], v[96:99]\n"
  "v_max_f32 v129, s41, v129\n"
  "v_max_f32 v130, s41, v130\n"
  "v_mfma_f32_16x16x32_bf16 v[100:103], v[52:55], a[148:151], v[100:103]\n"
  "v_max_f32 v131, s41, v131\n"
  "v_add_f32 v128, v128, v168\n"
  "v_add_f32 v129, v129, v169\n"
  "v_mfma_f32_16x16x32_bf16 v[104:107], v[52:55], a[152:155], v[104:107]\n"
  "v_add_f32 v130, v130, v170\n"
  "v_add_f32 v131, v131, v171\n"
  "v_mfma_f32_16x16x32_bf16 v[108:111], a[152:155], v[52:55], v[108:111]\n"
  "v_cvt_pk_bf16_f32 v166, v128, v129\n"
  "v_cvt_pk_bf16_f32 v167, v130, v131\n"
  "v_cndmask_b32 v166, 0, v166, s[52:53]\n"
  "v_mfma_f32_16x16x32_bf16 v[112:115], a[156:159], v[52:55], v[112:115]\n"
  "v_cndmask_b32 v167, 0, v167, s[52:53]\n"
  "v_mfma_f32_16x16x32_bf16 v[96:99], v[56:59], a[160:163], v[96:99]\n"
  "buffer_store_dwordx4 v[164:167], v8, s[24:27], s39 offen sc1\n"
  "v_mfma_f32_16x16x32_bf16 v[100:103], v[56:59], a[164:167], v[100:103]\n"
  "buffer_load_dwordx4 v[16:19], v10, s[16:19], s42 offen offset:0\n"
  "v_mfma_f32_16x16x32_bf16 v[104:107], v[56:59], a[168:171], v[104:107]\n"
  "v_mfma_f32_16x16x32_bf16 v[108:111], a[168:171], v[56:59], v[108:111]\n"
  "v_mfma_f32_16x16x32_bf16 v[112:115], a[172:175], v[56:59], v[112:115]\n"
  "v_mfma_f32_16x16x32_bf16 v[96:99], v[60:63], a[176:179], v[96:99]\n"
  "v_mfma_f32_16x16x32_bf16 v[100:103], v[60:63], a[180:183], v[100:103]\n"
  "buffer_load_dwordx4 v[20:23], v10, s[16:19], s42 offen offset:1024\n"
  "v_mfma_f32_16x16x32_bf16 v[104:107], v[60:63], a[184:187], v[104:107]\n"
  "v_mfma_f32_16x16x32_bf16 v[108:111], a[184:187], v[60:63], v[108:111]\n"
  "buffer_load_dwordx4 v[56:59], v10, s[16:19], s44 offen offset:2048\n"
  "v_mfma_f32_16x16x32_bf16 v[112:115], a[188:191], v[60:63], v[112:115]\n"
  "v_mfma_f32_16x16x32_bf16 v[96:99], v[64:67], a[192:195], v[96:99]\n"
  "v_mfma_f32_16x16x32_bf16 v[100:103], v[64:67], a[196:199], v[100:103]\n"
  "buffer_load_dwordx4 v[24:27], v10, s[16:19], s42 offen offset:2048\n"
  "v_mfma_f32_16x16x32_bf16 v[104:107], v[64:67], a[200:203], v[104:107]\n"
  "v_mfma_f32_16x16x32_bf16 v[108:111], a[200:203], v[64:67], v[108:111]\n"
  "buffer_load_dwordx4 v[60:63], v10, s[16:19], s44 offen offset:3072\n"
  "v_mfma_f32_16x16x32_bf16 v[112:115], a[204:207], v[64:67], v[112:115]\n"
  "v_mfma_f32_16x16x32_bf16 v[96:99], v[68:71], a[208:211], v[96:99]\n"
  "v_mfma_f32_16x16x32_bf16 v[100:103], v[68:71], a[212:215], v[100:103]\n"
  "buffer_load_dwordx4 v[28:31], v10, s[16:19], s42 offen offset:3072\n"
  "v_mfma_f32_16x16x32_bf16 v[104:107], v[68:71], a[216:219], v[104:107]\n"
  "v_mfma_f32_16x16x32_bf16 v[108:111], a[216:219], v[68:71], v[108:111]\n"
  "buffer_load_dwordx4 v[64:67], v10, s[16:19], s45 offen offset:0\n"
  "v_mfma_f32_16x16x32_bf16 v[112:115], a[220:223], v[68:71], v[112:115]\n"
  "v_mfma_f32_16x16x32_bf16 v[96:99], v[72:75], a[224:227], v[96:99]\n"
  "v_mfma_f32_16x16x32_bf16 v[100:103], v[72:75], a[228:231], v[100:103]\n"
  "buffer_load_dwordx4 v[32:35], v10, s[16:19], s43 offen offset:0\n"
  "v_mfma_f32_16x16x32_bf16 v[104:107], v[72:75], a[232:235], v[104:107]\n"
  "v_mfma_f32_16x16x32_bf16 v[108:111], a[232:235], v[72:75], v[108:111]\n"
  "buffer_load_dwordx4 v[68:71], v10, s[16:19], s45 offen offset:1024\n"
  "v_mfma_f32_16x16x32_bf16 v[112:115], a[236:239], v[72:75], v[112:115]\n"
  "v_mfma_f32_16x16x32_bf16 v[96:99], v[76:79], a[240:243], v[96:99]\n"
  "v_mfma_f32_16x16x32_bf16 v[100:103], v[76:79], a[244:247], v[100:103]\n"
  "buffer_load_dwordx4 v[36:39], v10, s[16:19], s43 offen offset:1024\n"
  "v_mfma_f32_16x16x32_bf16 v[104:107], v[76:79], a[248:251], v[104:107]\n"
  "v_mfma_f32_16x16x32_bf16 v[108:111], a[248:251], v[76:79], v[108:111]\n"
  "buffer_load_dwordx4 v[72:75], v10, s[16:19], s45 offen offset:2048\n"
  "v_mfma_f32_16x16x32_bf16 v[112:115], a[252:255], v[76:79], v[112:115]\n"
  "v_mfma_f32_16x16x32_bf16 v[96:99], v[80:83], v[184:187], v[96:99]\n"
  "v_mfma_f32_16x16x32_bf16 v[100:103], v[80:83], v[188:191], v[100:103]\n"
  "buffer_load_dwordx4 v[40:43], v10, s[16:19], s43 offen offset:2048\n"
  "v_mfma_f32_16x16x32_bf16 v[104:107], v[80:83], v[192:195], v[104:107]\n"
  "v_mfma_f32_16x16x32_bf16 v[108:111], v[192:195], v[80:83], v[108:111]\n"
  "buffer_load_dwordx4 v[76:79], v10, s[16:19], s45 offen offset:3072\n"
  "v_mfma_f32_16x16x32_bf16 v[112:115], v[196:199], v[80:83], v[112:115]\n"
  "v_mfma_f32_16x16x32_bf16 v[96:99], v[84:87], v[200:203], v[96:99]\n"
  "v_mfma_f32_16x16x32_bf16 v[100:103], v[84:87], v[204:207], v[100:103]\n"
  "buffer_load_dwordx4 v[44:47], v10, s[16:19], s43 offen offset:3072\n"
  "v_mfma_f32_16x16x32_bf16 v[104:107], v[84:87], v[208:211], v[104:107]\n"
  "v_mfma_f32_16x16x32_bf16 v[108:111], v[208:211], v[84:87], v[108:111]\n"
  "buffer_load_dwordx4 v[80:83], v10, s[16:19], s46 offen offset:0\n"
  "v_mfma_f32_16x16x32_bf16 v[112:115], v[212:215], v[84:87], v[112:115]\n"
  "s_waitcnt vmcnt(17)\n"
  "v_mfma_f32_16x16x32_bf16 v[96:99], v[88:91], v[216:219], v[96:99]\n"
  "v_mfma_f32_16x16x32_bf16 v[100:103], v[88:91], v[220:223], v[100:103]\n"
  "buffer_load_dwordx4 v[48:51], v10, s[16:19], s44 offen offset:0\n"
  "v_mfma_f32_16x16x32_bf16 v[104:107], v[88:91], v[224:227], v[104:107]\n"
  "v_mfma_f32_16x16x32_bf16 v[108:111], v[224:227], v[88:91], v[108:111]\n"
  "buffer_load_dwordx4 v[84:87], v10, s[16:19], s46 offen offset:1024\n"
  "v_mfma_f32_16x16x32_bf16 v[112:115], v[228:231], v[88:91], v[112:115]\n"
  "s_waitcnt vmcnt(18)\n"
  "v_mfma_f32_16x16x32_bf16 v[96:99], v[92:95], v[232:235], v[96:99]\n"
  "v_mfma_f32_16x16x32_bf16 v[100:103], v[92:95], v[236:239], v[100:103]\n"
  "buffer_load_dwordx4 v[52:55], v10, s[16:19], s44 offen offset:1024\n"
  "v_mfma_f32_16x16x32_bf16 v[104:107], v[92:95], v[240:243], v[104:107]\n"
  "v_mfma_f32_16x16x32_bf16 v[108:111], v[240:243], v[92:95], v[108:111]\n"
  "buffer_load_dwordx4 v[88:91], v10, s[16:19], s46 offen offset:2048\n"
  "v_mfma_f32_16x16x32_bf16 v[112:115], v[244:247], v[92:95], v[112:115]\n"
  "buffer_load_dwordx4 v[92:95], v10, s[16:19], s46 offen offset:3072\n"
  "s_add_u32 s39, s38, s35\n"
  "s_sub_u32 s34, s34, 1\n"
  "s_cmp_eq_u32 s34, 0\n"
  "s_cbranch_scc0 L_LP_%=\n"
  "v_cvt_pk_bf16_f32 v156, v100, v101\n"
  "v_cvt_pk_bf16_f32 v157, v102, v103\n"
  "v_mul_f32 v168, s40, v96\n"
  "v_mul_f32 v169, s40, v97\n"
  "v_mul_f32 v170, s40, v98\n"
  "v_mul_f32 v171, s40, v99\n"
  "v_exp_f32 v168, v168 clamp\n"
  "v_exp_f32 v169, v169 clamp\n"
  "v_exp_f32 v170, v170 clamp\n"
  "v_exp_f32 v171, v171 clamp\n"
  "v_add_f32 v96, s41, v96\n"
  "v_add_f32 v97, s41, v97\n"
  "v_add_f32 v98, s41, v98\n"
  "v_add_f32 v99, s41, v99\n"
  "v_max_f32 v96, s41, v96\n"
  "v_max_f32 v97, s41, v97\n"
  "v_max_f32 v98, s41, v98\n"
  "v_max_f32 v99, s41, v99\n"
  "v_add_f32 v96, v96, v168\n"
  "v_add_f32 v97, v97, v169\n"
  "v_add_f32 v98, v98, v170\n"
  "v_add_f32 v99, v99, v171\n"
  "v_cvt_pk_bf16_f32 v152, v96, v97\n"
  "v_cvt_pk_bf16_f32 v153, v98, v99\n"
  "v_mul_f32 v168, s40, v104\n"
  "v_mul_f32 v169, s40, v105\n"
  "v_mul_f32 v170, s40, v106\n"
  "v_mul_f32 v171, s40, v107\n"
  "v_exp_f32 v168, v168 clamp\n"
  "v_exp_f32 v169, v169 clamp\n"
  "v_exp_f32 v170, v170 clamp\n"
  "v_exp_f32 v171, v171 clamp\n"
  "v_add_f32 v100, s41, v104\n"
  "v_add_f32 v101, s41, v105\n"
  "v_add_f32 v102, s41, v106\n"
  "v_add_f32 v103, s41, v107\n"
  "v_max_f32 v100, s41, v100\n"
  "v_max_f32 v101, s41, v101\n"
  "v_max_f32 v102, s41, v102\n"
  "v_max_f32 v103, s41, v103\n"
  "v_add_f32 v100, v100, v168\n"
  "v_add_f32 v101, v101, v169\n"
  "v_add_f32 v102, v102, v170\n"
  "v_add_f32 v103, v103, v171\n"
  "v_cndmask_b32 v104, v9, v104, s[50:51]\n"
  "v_cndmask_b32 v105, v9, v105, s[50:51]\n"
  "v_cndmask_b32 v106, v9, v106, s[50:51]\n"
  "v_cndmask_b32 v107, v9, v107, s[50:51]\n"
  "v_cndmask_b32 v104, v104, v100, s[48:49]\n"
  "v_cndmask_b32 v105, v105, v101, s[48:49]\n"
  "v_cndmask_b32 v106, v106, v102, s[48:49]\n"
  "v_cndmask_b32 v107, v107, v103, s[48:49]\n"
  "v_cvt_pk_bf16_f32 v160, v104, v105\n"
  "v_cvt_pk_bf16_f32 v161, v106, v107\n"
  "v_mul_f32 v168, s40, v112\n"
  "v_mul_f32 v169, s40, v113\n"
  "v_mul_f32 v170, s40, v114\n"
  "v_mul_f32 v171, s40, v115\n"
  "v_exp_f32 v168, v168 clamp\n"
  "v_exp_f32 v169, v169 clamp\n"
  "v_exp_f32 v170, v170 clamp\n"
  "v_exp_f32 v171, v171 clamp\n"
  "v_add_f32 v112, s41, v112\n"
  "v_add_f32 v113, s41, v113\n"
  "v_add_f32 v114, s41, v114\n"
  "v_add_f32 v115, s41, v115\n"
  "v_max_f32 v112, s41, v112\n"
  "v_max_f32 v113, s41, v113\n"
  "v_max_f32 v114, s41, v114\n"
  "v_max_f32 v115, s41, v115\n"
  "v_add_f32 v112, v112, v168\n"
  "v_add_f32 v113, v113, v169\n"
  "v_add_f32 v114, v114, v170\n"
  "v_add_f32 v115, v115, v171\n"
  "v_mfma_f32_16x16x32_bf16 v[136:139], v[152:155], v[156:159], v[136:139]\n"
  "v_mfma_f32_16x16x32_bf16 v[140:143], v[152:155], v[160:163], v[140:143]\n"
  "v_mfma_f32_16x16x32_bf16 v[144:147], v[160:163], v[156:159], v[144:147]\n"
  "v_mfma_f32_16x16x32_bf16 v[148:151], v[160:163], v[160:163], v[148:151]\n"
  "v_cvt_pk_bf16_f32 v164, v112, v113\n"
  "v_cvt_pk_bf16_f32 v165, v114, v115\n"
  "v_mul_f32 v168, s40, v108\n"
  "v_mul_f32 v169, s40, v109\n"
  "v_mul_f32 v170, s40, v110\n"
  "v_mul_f32 v171, s40, v111\n"
  "v_exp_f32 v168, v168 clamp\n"
  "v_exp_f32 v169, v169 clamp\n"
  "v_exp_f32 v170, v170 clamp\n"
  "v_exp_f32 v171, v171 clamp\n"
  "v_add_f32 v108, s41, v108\n"
  "v_add_f32 v109, s41, v109\n"
  "v_add_f32 v110, s41, v110\n"
  "v_add_f32 v111, s41, v111\n"
  "v_max_f32 v108, s41, v108\n"
  "v_max_f32 v109, s41, v109\n"
  "v_max_f32 v110, s41, v110\n"
  "v_max_f32 v111, s41, v111\n"
  "v_add_f32 v108, v108, v168\n"
  "v_add_f32 v109, v109, v169\n"
  "v_add_f32 v110, v110, v170\n"
  "v_add_f32 v111, v111, v171\n"
  "v_cvt_pk_bf16_f32 v166, v108, v109\n"
  "v_cvt_pk_bf16_f32 v167, v110, v111\n"
  "v_cndmask_b32 v166, 0, v166, s[52:53]\n"
  "v_cndmask_b32 v167, 0, v167, s[52:53]\n"
  "buffer_store_dwordx4 v[164:167], v8, s[24:27], s39 offen sc1\n"
  "s_branch L_DN_%=\n"
  "L_E1_%=:\n"
  "s_nop 7\n"
  "v_cvt_pk_bf16_f32 v156, v120, v121\n"
  "v_cvt_pk_bf16_f32 v157, v122, v123\n"
  "v_mul_f32 v168, s40, v116\n"
  "v_mul_f32 v169, s40, v117\n"
  "v_mul_f32 v170, s40, v118\n"
  "v_mul_f32 v171, s40, v119\n"
  "v_exp_f32 v168, v168 clamp\n"
  "v_exp_f32 v169, v169 clamp\n"
  "v_exp_f32 v170, v170 clamp\n"
  "v_exp_f32 v171, v171 clamp\n"
  "v_add_f32 v116, s41, v116\n"
  "v_add_f32 v117, s41, v117\n"
  "v_add_f32 v118, s41, v118\n"
  "v_add_f32 v119, s41, v119\n"
  "v_max_f32 v116, s41, v116\n"
  "v_max_f32 v117, s41, v117\n"
  "v_max_f32 v118, s41, v118\n"
  "v_max_f32 v119, s41, v119\n"
  "v_add_f32 v116, v116, v168\n"
  "v_add_f32 v117, v117, v169\n"
  "v_add_f32 v118, v118, v170\n"
  "v_add_f32 v119, v119, v171\n"
  "v_cvt_pk_bf16_f32 v152, v116, v117\n"
  "v_cvt_pk_bf16_f32 v153, v118, v119\n"
  "v_mul_f32 v168, s40, v124\n"
  "v_mul_f32 v169, s40, v125\n"
  "v_mul_f32 v170, s40, v126\n"
  "v_mul_f32 v171, s40, v127\n"
  "v_exp_f32 v168, v168 clamp\n"
  "v_exp_f32 v169, v169 clamp\n"
  "v_exp_f32 v170, v170 clamp\n"
  "v_exp_f32 v171, v171 clamp\n"
  "v_add_f32 v120, s41, v124\n"
  "v_add_f32 v121, s41, v125\n"
  "v_add_f32 v122, s41, v126\n"
  "v_add_f32 v123, s41, v127\n"
  "v_max_f32 v120, s41, v120\n"
  "v_max_f32 v121, s41, v121\n"
  "v_max_f32 v122, s41, v122\n"
  "v_max_f32 v123, s41, v123\n"
  "v_add_f32 v120, v120, v168\n"
  "v_add_f32 v121, v121, v169\n"
  "v_add_f32 v122, v122, v170\n"
  "v_add_f32 v123, v123, v171\n"
  "v_cndmask_b32 v124, v9, v124, s[50:51]\n"
  "v_cndmask_b32 v125, v9, v125, s[50:51]\n"
  "v_cndmask_b32 v126, v9, v126, s[50:51]\n"
  "v_cndmask_b32 v127, v9, v127, s[50:51]\n"
  "v_cndmask_b32 v124, v124, v120, s[48:49]\n"
  "v_cndmask_b32 v125, v125, v121, s[48:49]\n"
  "v_cndmask_b32 v126, v126, v122, s[48:49]\n"
  "v_cndmask_b32 v127, v127, v123, s[48:49]\n"
  "v_cvt_pk_bf16_f32 v160, v124, v125\n"
  "v_cvt_pk_bf16_f32 v161, v126, v127\n"
  "v_mul_f32 v168, s40, v132\n"
  "v_mul_f32 v169, s40, v133\n"
  "v_mul_f32 v170, s40, v134\n"
  "v_mul_f32 v171, s40, v135\n"
  "v_exp_f32 v168, v168 clamp\n"
  "v_exp_f32 v169, v169 clamp\n"
  "v_exp_f32 v170, v170 clamp\n"
  "v_exp_f32 v171, v171 clamp\n"
  "v_add_f32 v132, s41, v132\n"
  "v_add_f32 v133, s41, v133\n"
  "v_add_f32 v134, s41, v134\n"
  "v_add_f32 v135, s41, v135\n"
  "v_max_f32 v132, s41, v132\n"
  "v_max_f32 v133, s41, v133\n"
  "v_max_f32 v134, s41, v134\n"
  "v_max_f32 v135, s41, v135\n"
  "v_add_f32 v132, v132, v168\n"
  "v_add_f32 v133, v133, v169\n"
  "v_add_f32 v134, v134, v170\n"
  "v_add_f32 v135, v135, v171\n"
  "v_mfma_f32_16x16x32_bf16 v[136:139], v[152:155], v[156:159], v[136:139]\n"
  "v_mfma_f32_16x16x32_bf16 v[140:143], v[152:155], v[160:163], v[140:143]\n"
  "v_mfma_f32_16x16x32_bf16 v[144:147], v[160:163], v[156:159], v[144:147]\n"
  "v_mfma_f32_16x16x32_bf16 v[148:151], v[160:163], v[160:163], v[148:151]\n"
  "v_cvt_pk_bf16_f32 v164, v132, v133\n"
  "v_cvt_pk_bf16_f32 v165, v134, v135\n"
  "v_mul_f32 v168, s40, v128\n"
  "v_mul_f32 v169, s40, v129\n"
  "v_mul_f32 v170, s40, v130\n"
  "v_mul_f32 v171, s40, v131\n"
  "v_exp_f32 v168, v168 clamp\n"
  "v_exp_f32 v169, v169 clamp\n"
  "v_exp_f32 v170, v170 clamp\n"
  "v_exp_f32 v171, v171 clamp\n"
  "v_add_f32 v128, s41, v128\n"
  "v_add_f32 v129, s41, v129\n"
  "v_add_f32 v130, s41, v130\n"
  "v_add_f32 v131, s41, v131\n"
  "v_max_f32 v128, s41, v128\n"
  "v_max_f32 v129, s41, v129\n"
  "v_max_f32 v130, s41, v130\n"
  "v_max_f32 v131, s41, v131\n"
  "v_add_f32 v128, v128, v168\n"
  "v_add_f32 v129, v129, v169\n"
  "v_add_f32 v130, v130, v170\n"
  "v_add_f32 v131, v131, v171\n"
  "v_cvt_pk_bf16_f32 v166, v128, v129\n"
  "v_cvt_pk_bf16_f32 v167, v130, v131\n"
  "v_cndmask_b32 v166, 0, v166, s[52:53]\n"
  "v_cndmask_b32 v167, 0, v167, s[52:53]\n"
  "buffer_store_dwordx4 v[164:167], v8, s[24:27], s39 offen sc1\n"
  "L_DN_%=:\n"
  "s_nop 7\n"
  "s_lshl_b32 s37, s32, 12\n"
  "s_add_u32 s37, s37, 81920\n"
  "v_add_u32 v10, s37, v8\n"
  "ds_write_b128 v10, v[136:139] offset:0\n"
  "ds_write_b128 v10, v[140:143] offset:1024\n"
  "ds_write_b128 v10, v[144:147] offset:2048\n"
  "ds_write_b128 v10, v[148:151] offset:3072\n"
  "s_lshl_b32 s38, s32, 10\n"
  "s_add_u32 s37, s38, 81920\n"
  "v_add_u32 v10, s37, v8\n"
  "s_add_u32 s36, s36, s38\n"
  "s_waitcnt vmcnt(0)\n"
  "s_waitcnt lgkmcnt(0)\n"
  "s_barrier\n"
  "ds_read_b128 v[16:19], v10 offset:0\n"
  "ds_read_b128 v[20:23], v10 offset:4096\n"
  "ds_read_b128 v[24:27], v10 offset:8192\n"
  "ds_read_b128 v[28:31], v10 offset:12288\n"
  "s_waitcnt lgkmcnt(2)\n"
  "v_add_f32 v16, v16, v20\n"
  "v_add_f32 v17, v17, v21\n"
  "v_add_f32 v18, v18, v22\n"
  "v_add_f32 v19, v19, v23\n"
  "s_waitcnt lgkmcnt(0)\n"
  "v_add_f32 v24, v24, v28\n"
  "v_add_f32 v25, v25, v29\n"
  "v_add_f32 v26, v26, v30\n"
  "v_add_f32 v27, v27, v31\n"
  "v_add_f32 v16, v16, v24\n"
  "v_add_f32 v17, v17, v25\n"
  "v_add_f32 v18, v18, v26\n"
  "v_add_f32 v19, v19, v27\n"
  "v_min_u32 v16, -2, v16\n"
  "v_min_u32 v17, -2, v17\n"
  "v_min_u32 v18, -2, v18\n"
  "v_min_u32 v19, -2, v19\n"
  "buffer_store_dwordx4 v[16:19], v8, s[28:31], s36 offen sc1\n"
  "s_barrier\n"
  "s_nop 3\n"
      : "+{s[32:39]}"(sb) : "{s[16:31]}"(sa), "{v[8:9]}"(va) : "v10", "v11", "v12", "v13", "v14", "v15", "v16", "v17", "v18", "v19", "v20", "v21", "v22", "v23", "v24", "v25", "v26", "v27", "v28", "v29", "v30", "v31", "v32", "v33", "v34", "v35", "v36", "v37", "v38", "v39", "v40", "v41", "v42", "v43", "v44", "v45", "v46", "v47", "v48", "v49", "v50", "v51", "v52", "v53", "v54", "v55", "v56", "v57", "v58", "v59", "v60", "v61", "v62", "v63", "v64", "v65", "v66", "v67", "v68", "v69", "v70", "v71", "v72", "v73", "v74", "v75", "v76", "v77", "v78", "v79", "v80", "v81", "v82", "v83", "v84", "v85", "v86", "v87", "v88", "v89", "v90", "v91", "v92", "v93", "v94", "v95", "v96", "v97", "v98", "v99", "v100", "v101", "v102", "v103", "v104", "v105", "v106", "v107", "v108", "v109", "v110", "v111", "v112", "v113", "v114", "v115", "v116", "v117", "v118", "v119", "v120", "v121", "v122", "v123", "v124", "v125", "v126", "v127", "v128", "v129", "v130", "v131", "v132", "v133", "v134", "v135", "v136", "v137", "v138", "v139", "v140", "v141", "v142", "v143", "v144", "v145", "v146", "v147", "v148", "v149", "v150", "v151", "v152", "v153", "v154", "v155", "v156", "v157", "v158", "v159", "v160", "v161", "v162", "v163", "v164", "v165", "v166", "v167", "v168", "v169", "v170", "v171", "v172", "v173", "v174", "v175", "v176", "v177", "v178", "v179", "v180", "v181", "v182", "v183", "v184", "v185", "v186", "v187", "v188", "v189", "v190", "v191", "v192", "v193", "v194", "v195", "v196", "v197", "v198", "v199", "v200", "v201", "v202", "v203", "v204", "v205", "v206", "v207", "v208", "v209", "v210", "v211", "v212", "v213", "v214", "v215", "v216", "v217", "v218", "v219", "v220", "v221", "v222", "v223", "v224", "v225", "v226", "v227", "v228", "v229", "v230", "v231", "v232", "v233", "v234", "v235", "v236", "v237", "v238", "v239", "v240", "v241", "v242", "v243", "v244", "v245", "v246", "v247", "a0", "a1", "a2", "a3", "a4", "a5", "a6", "a7", "a8", "a9", "a10", "a11", "a12", "a13", "a14", "a15", "a16", "a17", "a18", "a19", "a20", "a21", "a22", "a23", "a24", "a25", "a26", "a27", "a28", "a29", "a30", "a31", "a32", "a33", "a34", "a35", "a36", "a37", "a38", "a39", "a40", "a41", "a42", "a43", "a44", "a45", "a46", "a47", "a48", "a49", "a50", "a51", "a52", "a53", "a54", "a55", "a56", "a57", "a58", "a59", "a60", "a61", "a62", "a63", "a64", "a65", "a66", "a67", "a68", "a69", "a70", "a71", "a72", "a73", "a74", "a75", "a76", "a77", "a78", "a79", "a80", "a81", "a82", "a83", "a84", "a85", "a86", "a87", "a88", "a89", "a90", "a91", "a92", "a93", "a94", "a95", "a96", "a97", "a98", "a99", "a100", "a101", "a102", "a103", "a104", "a105", "a106", "a107", "a108", "a109", "a110", "a111", "a112", "a113", "a114", "a115", "a116", "a117", "a118", "a119", "a120", "a121", "a122", "a123", "a124", "a125", "a126", "a127", "a128", "a129", "a130", "a131", "a132", "a133", "a134", "a135", "a136", "a137", "a138", "a139", "a140", "a141", "a142", "a143", "a144", "a145", "a146", "a147", "a148", "a149", "a150", "a151", "a152", "a153", "a154", "a155", "a156", "a157", "a158", "a159", "a160", "a161", "a162", "a163", "a164", "a165", "a166", "a167", "a168", "a169", "a170", "a171", "a172", "a173", "a174", "a175", "a176", "a177", "a178", "a179", "a180", "a181", "a182", "a183", "a184", "a185", "a186", "a187", "a188", "a189", "a190", "a191", "a192", "a193", "a194", "a195", "a196", "a197", "a198", "a199", "a200", "a201", "a202", "a203", "a204", "a205", "a206", "a207", "a208", "a209", "a210", "a211", "a212", "a213", "a214", "a215", "a216", "a217", "a218", "a219", "a220", "a221", "a222", "a223", "a224", "a225", "a226", "a227", "a228", "a229", "a230", "a231", "a232", "a233", "a234", "a235", "a236", "a237", "a238", "a239", "a240", "a241", "a242", "a243", "a244", "a245", "a246", "a247", "a248", "a249", "a250", "a251", "a252", "a253", "a254", "a255", "s40", "s41", "s42", "s43", "s44", "s45", "s46", "s47", "s48", "s49", "s50", "s51", "s52", "s53", "vcc", "scc", "memory");
}

DEV void fold_vo(const Args& a) {
  const unsigned wave = __builtin_amdgcn_readfirstlane(threadIdx.x >> 6), lane = threadIdx.x & 63, g = lane >> 4, n16 = lane & 15;
  const unsigned j2 = (blockIdx.x >> 3) - 8, h = (blockIdx.x & 7) * 4 + (j2 & 3), rc = j2 >> 2;
  const unsigned vo = rc * 128 * 12288 + h * 384;
  u32x16 sd, vd, so;
  SRD(sd, 0, (const u8*)a.Wv + vo, 7864320 - vo); SRD(sd, 4, (const u8*)a.Wo + h * 245760, 245760); SRD(sd, 8, a.ws, OFF_CTL);
  sd[12] = OFF_CF + ((((wave & 1) * 32 + (h >> 3)) * 10 + rc * 2 + (wave >> 1)) * 4 + ((h & 7) >> 1)) * 2048 + (h & 1) * 512;
  sd[13] = CFB + (h * 5 + rc) * 128; sd[14] = wave; sd[15] = 0;
#pragma unroll
  for (int j = 0; j < 3; ++j) { const unsigned n = lane + 64 * j, rw = n / 24; vd[j] = rw * 12288 + ((n % 24) ^ ((rw >> 1) & 3)) * 16; }
#pragma unroll
  for (int j = 0; j < 2; ++j) {
    vd[3 + j] = g * 1280 + (n16 ^ ((g + 4 * j) * 2)) * 16;
    const unsigned rw = drow(0, n16 >> 2, 4 * j + (n16 & 3));
    vd[5 + j] = 98304 + ((wave >> 1) * 8 + (rw >> 3)) * 3136 + (rw & 7) * 384 + (g ^ ((rw >> 1) & 3)) * 16;
  }
#pragma unroll
  for (int j = 0; j < 4; ++j) vd[7 + j] = (8 * g + (n16 >> 2)) * 256 + (((wave & 1) * 4 + j) ^ ((n16 >> 2) + 4 * (g & 1))) * 32 + (lane & 3) * 8;
  vd[11] = (n16 >> 3) * 327680 + g * 128 + (n16 & 7) * 16; vd[12] = vd[13] = vd[14] = vd[15] = 0;
  asm volatile(
  "s_waitcnt lgkmcnt(0)\n s_nop 7\n"
  "s_mul_i32 s40, s30, 393216\n"
  "s_mul_i32 s41, s30, 12544\n"
  "s_add_u32 m0, s41, 98304\n"
  "s_nop 0\n"
  "buffer_load_dwordx4 v32, s[16:19], s40 offen nt lds\n"
  "s_add_u32 m0, s41, 99328\n"
  "s_nop 0\n"
  "buffer_load_dwordx4 v33, s[16:19], s40 offen nt lds\n"
  "s_add_u32 m0, s41, 100352\n"
  "s_nop 0\n"
  "buffer_load_dwordx4 v34, s[16:19], s40 offen nt lds\n"
  "s_add_u32 s40, s40, 98304\n"
  "s_add_u32 m0, s41, 101440\n"
  "s_nop 0\n"
  "buffer_load_dwordx4 v32, s[16:19], s40 offen nt lds\n"
  "s_add_u32 m0, s41, 102464\n"
  "s_nop 0\n"
  "buffer_load_dwordx4 v33, s[16:19], s40 offen nt lds\n"
  "s_add_u32 m0, s41, 103488\n"
  "s_nop 0\n"
  "buffer_load_dwordx4 v34, s[16:19], s40 offen nt lds\n"
  "s_add_u32 s40, s40, 98304\n"
  "s_add_u32 m0, s41, 104576\n"
  "s_nop 0\n"
  "buffer_load_dwordx4 v32, s[16:19], s40 offen nt lds\n"
  "s_add_u32 m0, s41, 105600\n"
  "s_nop 0\n"
  "buffer_load_dwordx4 v33, s[16:19], s40 offen nt lds\n"
  "s_add_u32 m0, s41, 106624\n"
  "s_nop 0\n"
  "buffer_load_dwordx4 v34, s[16:19], s40 offen nt lds\n"
  "s_add_u32 s40, s40, 98304\n"
  "s_add_u32 m0, s41, 107712\n"
  "s_nop 0\n"
  "buffer_load_dwordx4 v32, s[16:19], s40 offen nt lds\n"
  "s_add_u32 m0, s41, 108736\n"
  "s_nop 0\n"
  "buffer_load_dwordx4 v33, s[16:19], s40 offen nt lds\n"
  "s_add_u32 m0, s41, 109760\n"
  "s_nop 0\n"
  "buffer_load_dwordx4 v34, s[16:19], s40 offen nt lds\n"
  "s_mul_i32 s42, s30, 61440\n"
  "s_mul_i32 s43, s30, 12288\n"
  "s_add_u32 s41, s42, 0\n"
  "s_add_u32 m0, s43, 0\n"
  "s_nop 0\n"
  "buffer_load_dwordx4 v35, s[20:23], s41 offen lds\n"
  "s_add_u32 s41, s42, 5120\n"
  "s_add_u32 m0, s43, 1024\n"
  "s_nop 0\n"
  "buffer_load_dwordx4 v35, s[20:23], s41 offen lds\n"
  "s_add_u32 s41, s42, 10240\n"
  "s_add_u32 m0, s43, 2048\n"
  "s_nop 0\n"
  "buffer_load_dwordx4 v36, s[20:23], s41 offen lds\n"
  "s_add_u32 s41, s42, 15360\n"
  "s_add_u32 m0, s43, 3072\n"
  "s_nop 0\n"
  "buffer_load_dwordx4 v36, s[20:23], s41 offen lds\n"
  "s_add_u32 s41, s42, 20480\n"
  "s_add_u32 m0, s43, 4096\n"
  "s_nop 0\n"
  "buffer_load_dwordx4 v35, s[20:23], s41 offen lds\n"
  "s_add_u32 s41, s42, 25600\n"
  "s_add_u32 m0, s43, 5120\n"
  "s_nop 0\n"
  "buffer_load_dwordx4 v35, s[20:23], s41 offen lds\n"
  "s_add_u32 s41, s42, 30720\n"
  "s_add_u32 m0, s43, 6144\n"
  "s_nop 0\n"
  "buffer_load_dwordx4 v36, s[20:23], s41 offen lds\n"
  "s_add_u32 s41, s42, 35840\n"
  "s_add_u32 m0, s43, 7168\n"
  "s_nop 0\n"
  "buffer_load_dwordx4 v36, s[20:23], s41 offen lds\n"
  "s_add_u32 s41, s42, 40960\n"
  "s_add_u32 m0, s43, 8192\n"
  "s_nop 0\n"
  "buffer_load_dwordx4 v35, s[20:23], s41 offen lds\n"
  "s_add_u32 s41, s42, 46080\n"
  "s_add_u32 m0, s43, 9216\n"
  "s_nop 0\n"
  "buffer_load_dwordx4 v35, s[20:23], s41 offen lds\n"
  "s_add_u32 s41, s42, 51200\n"
  "s_add_u32 m0, s43, 10240\n"
  "s_nop 0\n"
  "buffer_load_dwordx4 v36, s[20:23], s41 offen lds\n"
  "s_add_u32 s41, s42, 56320\n"
  "s_add_u32 m0, s43, 11264\n"
  "s_nop 0\n"
  "buffer_load_dwordx4 v36, s[20:23], s41 offen lds\n"
  "s_add_u32 s41, s42, 256\n"
  "s_add_u32 m0, s43, 49152\n"
  "s_nop 0\n"
  "buffer_load_dwordx4 v35, s[20:23], s41 offen lds\n"
  "s_add_u32 s41, s42, 5376\n"
  "s_add_u32 m0, s43, 50176\n"
  "s_nop 0\n"
  "buffer_load_dwordx4 v35, s[20:23], s41 offen lds\n"
  "s_add_u32 s41, s42, 10496\n"
  "s_add_u32 m0, s43, 51200\n"
  "s_nop 0\n"
  "buffer_load_dwordx4 v36, s[20:23], s41 offen lds\n"
  "s_add_u32 s41, s42, 15616\n"
  "s_add_u32 m0, s43, 52224\n"
  "s_nop 0\n"
  "buffer_load_dwordx4 v36, s[20:23], s41 offen lds\n"
  "s_add_u32 s41, s42, 20736\n"
  "s_add_u32 m0, s43, 53248\n"
  "s_nop 0\n"
  "buffer_load_dwordx4 v35, s[20:23], s41 offen lds\n"
  "s_add_u32 s41, s42, 25856\n"
  "s_add_u32 m0, s43, 54272\n"
  "s_nop 0\n"
  "buffer_load_dwordx4 v35, s[20:23], s41 offen lds\n"
  "s_add_u32 s41, s42, 30976\n"
  "s_add_u32 m0, s43, 55296\n"
  "s_nop 0\n"
  "buffer_load_dwordx4 v36, s[20:23], s41 offen lds\n"
  "s_add_u32 s41, s42, 36096\n"
  "s_add_u32 m0, s43, 56320\n"
  "s_nop 0\n"
  "buffer_load_dwordx4 v36, s[20:23], s41 offen lds\n"
  "s_add_u32 s41, s42, 41216\n"
  "s_add_u32 m0, s43, 57344\n"
  "s_nop 0\n"
  "buffer_load_dwordx4 v35, s[20:23], s41 offen lds\n"
  "s_add_u32 s41, s42, 46336\n"
  "s_add_u32 m0, s43, 58368\n"
  "s_nop 0\n"
  "buffer_load_dwordx4 v35, s[20:23], s41 offen lds\n"
  "s_add_u32 s41, s42, 51456\n"
  "s_add_u32 m0, s43, 59392\n"
  "s_nop 0\n"
  "buffer_load_dwordx4 v36, s[20:23], s41 offen lds\n"
  "s_add_u32 s41, s42, 56576\n"
  "s_add_u32 m0, s43, 60416\n"
  "s_nop 0\n"
  "buffer_load_dwordx4 v36, s[20:23], s41 offen lds\n"
  "s_waitcnt vmcnt(24)\n"
  "s_barrier\n"
  "ds_read_b128 a[0:3], v37 offset:0\n"
  "ds_read_b128 a[4:7], v37 offset:64\n"
  "ds_read_b128 a[8:11], v37 offset:128\n"
  "ds_read_b128 a[12:15], v37 offset:192\n"
  "ds_read_b128 a[16:19], v37 offset:256\n"
  "ds_read_b128 a[20:23], v37 offset:320\n"
  "ds_read_b128 a[24:27], v38 offset:0\n"
  "ds_read_b128 a[28:31], v38 offset:64\n"
  "ds_read_b128 a[32:35], v38 offset:128\n"
  "ds_read_b128 a[36:39], v38 offset:192\n"
  "ds_read_b128 a[40:43], v38 offset:256\n"
  "ds_read_b128 a[44:47], v38 offset:320\n"
  "ds_read_b128 a[48:51], v37 offset:12544\n"
  "ds_read_b128 a[52:55], v37 offset:12608\n"
  "s_waitcnt lgkmcnt(10)\n"
  "ds_read_b128 a[56:59], v37 offset:12672\n"
  "ds_read_b128 a[60:63], v37 offset:12736\n"
  "ds_read_b128 a[64:67], v37 offset:12800\n"
  "ds_read_b128 a[68:71], v37 offset:12864\n"
  "s_waitcnt lgkmcnt(10)\n"
  "ds_read_b128 a[72:75], v38 offset:12544\n"
  "ds_read_b128 a[76:79], v38 offset:12608\n"
  "ds_read_b128 a[80:83], v38 offset:12672\n"
  "ds_read_b128 a[84:87], v38 offset:12736\n"
  "s_waitcnt lgkmcnt(10)\n"
  "ds_read_b128 a[88:91], v38 offset:12800\n"
  "ds_read_b128 a[92:95], v38 offset:12864\n"
  "s_waitcnt vmcnt(12)\n"
  "s_waitcnt lgkmcnt(0)\n"
  "s_barrier\n"
  "v_add_u32 v48, 0, v39\n"
  "v_add_u32 v49, 0, v40\n"
  "v_add_u32 v50, 0, v41\n"
  "v_add_u32 v51, 0, v42\n"
  "ds_read_b64_tr_b16 v[56:57], v48 offset:0\n"
  "ds_read_b64_tr_b16 v[58:59], v48 offset:1024\n"
  "ds_read_b64_tr_b16 v[60:61], v48 offset:8192\n"
  "ds_read_b64_tr_b16 v[62:63], v48 offset:9216\n"
  "ds_read_b64_tr_b16 v[64:65], v48 offset:16384\n"
  "ds_read_b64_tr_b16 v[66:67], v48 offset:17408\n"
  "ds_read_b64_tr_b16 v[68:69], v48 offset:24576\n"
  "ds_read_b64_tr_b16 v[70:71], v48 offset:25600\n"
  "ds_read_b64_tr_b16 v[72:73], v48 offset:32768\n"
  "ds_read_b64_tr_b16 v[74:75], v48 offset:33792\n"
  "ds_read_b64_tr_b16 v[76:77], v48 offset:40960\n"
  "ds_read_b64_tr_b16 v[78:79], v48 offset:41984\n"
  "s_waitcnt lgkmcnt(10)\n"
  "v_mfma_f32_16x16x32_bf16 v[104:107], a[0:3], v[56:59], 0\n"
  "ds_read_b64_tr_b16 v[80:81], v49 offset:0\n"
  "v_mfma_f32_16x16x32_bf16 v[108:111], a[24:27], v[56:59], 0\n"
  "ds_read_b64_tr_b16 v[82:83], v49 offset:1024\n"
  "v_mfma_f32_16x16x32_bf16 v[112:115], a[48:51], v[56:59], 0\n"
  "ds_read_b64_tr_b16 v[84:85], v49 offset:8192\n"
  "v_mfma_f32_16x16x32_bf16 v[116:119], a[72:75], v[56:59], 0\n"
  "ds_read_b64_tr_b16 v[86:87], v49 offset:9216\n"
  "s_waitcnt lgkmcnt(12)\n"
  "v_mfma_f32_16x16x32_bf16 v[104:107], a[4:7], v[60:63], v[104:107]\n"
  "ds_read_b64_tr_b16 v[88:89], v49 offset:16384\n"
  "v_mfma_f32_16x16x32_bf16 v[108:111], a[28:31], v[60:63], v[108:111]\n"
  "ds_read_b64_tr_b16 v[90:91], v49 offset:17408\n"
  "v_mfma_f32_16x16x32_bf16 v[112:115], a[52:55], v[60:63], v[112:115]\n"
  "s_waitcnt lgkmcnt(10)\n"
  "ds_read_b64_tr_b16 v[92:93], v49 offset:24576\n"
  "v_mfma_f32_16x16x32_bf16 v[116:119], a[76:79], v[60:63], v[116:119]\n"
  "ds_read_b64_tr_b16 v[94:95], v49 offset:25600\n"
  "v_mfma_f32_16x16x32_bf16 v[104:107], a[8:11], v[64:67], v[104:107]\n"
  "ds_read_b64_tr_b16 v[96:97], v49 offset:32768\n"
  "v_mfma_f32_16x16x32_bf16 v[108:111], a[32:35], v[64:67], v[108:111]\n"
  "ds_read_b64_tr_b16 v[98:99], v49 offset:33792\n"
  "v_mfma_f32_16x16x32_bf16 v[112:115], a[56:59], v[64:67], v[112:115]\n"
  "s_waitcnt lgkmcnt(10)\n"
  "ds_read_b64_tr_b16 v[100:101], v49 offset:40960\n"
  "v_mfma_f32_16x16x32_bf16 v[116:119], a[80:83], v[64:67], v[116:119]\n"
  "ds_read_b64_tr_b16 v[102:103], v49 offset:41984\n"
  "v_mfma_f32_16x16x32_bf16 v[104:107], a[12:15], v[68:71], v[104:107]\n"
  "v_mfma_f32_16x16x32_bf16 v[108:111], a[36:39], v[68:71], v[108:111]\n"
  "v_mfma_f32_16x16x32_bf16 v[112:115], a[60:63], v[68:71], v[112:115]\n"
  "v_mfma_f32_16x16x32_bf16 v[116:119], a[84:87], v[68:71], v[116:119]\n"
  "v_mfma_f32_16x16x32_bf16 v[104:107], a[16:19], v[72:75], v[104:107]\n"
  "v_mfma_f32_16x16x32_bf16 v[108:111], a[40:43], v[72:75], v[108:111]\n"
  "v_mfma_f32_16x16x32_bf16 v[112:115], a[64:67], v[72:75], v[112:115]\n"
  "s_add_u32 s41, s42, 512\n"
  "s_add_u32 m0, s43, 98304\n"
  "s_nop 0\n"
  "buffer_load_dwordx4 v35, s[20:23], s41 offen lds\n"
  "v_mfma_f32_16x16x32_bf16 v[116:119], a[88:91], v[72:75], v[116:119]\n"
  "v_mfma_f32_16x16x32_bf16 v[104:107], a[20:23], v[76:79], v[104:107]\n"
  "s_add_u32 s41, s42, 5632\n"
  "s_add_u32 m0, s43, 99328\n"
  "s_nop 0\n"
  "buffer_load_dwordx4 v35, s[20:23], s41 offen lds\n"
  "v_mfma_f32_16x16x32_bf16 v[108:111], a[44:47], v[76:79], v[108:111]\n"
  "v_mfma_f32_16x16x32_bf16 v[112:115], a[68:71], v[76:79], v[112:115]\n"
  "s_add_u32 s41, s42, 10752\n"
  "s_add_u32 m0, s43, 100352\n"
  "s_nop 0\n"
  "buffer_load_dwordx4 v36, s[20:23], s41 offen lds\n"
  "v_mfma_f32_16x16x32_bf16 v[116:119], a[92:95], v[76:79], v[116:119]\n"
  "s_waitcnt lgkmcnt(10)\n"
  "v_mfma_f32_16x16x32_bf16 v[120:123], a[0:3], v[80:83], 0\n"
  "ds_read_b64_tr_b16 v[56:57], v50 offset:0\n"
  "v_mfma_f32_16x16x32_bf16 v[124:127], a[24:27], v[80:83], 0\n"
  "ds_read_b64_tr_b16 v[58:59], v50 offset:1024\n"
  "v_mfma_f32_16x16x32_bf16 v[128:131], a[48:51], v[80:83], 0\n"
  "ds_read_b64_tr_b16 v[60:61], v50 offset:8192\n"
  "v_mfma_f32_16x16x32_bf16 v[132:135], a[72:75], v[80:83], 0\n"
  "ds_read_b64_tr_b16 v[62:63], v50 offset:9216\n"
  "s_waitcnt lgkmcnt(12)\n"
  "v_mfma_f32_16x16x32_bf16 v[120:123], a[4:7], v[84:87], v[120:123]\n"
  "ds_read_b64_tr_b16 v[64:65], v50 offset:16384\n"
  "v_mfma_f32_16x16x32_bf16 v[124:127], a[28:31], v[84:87], v[124:127]\n"
  "ds_read_b64_tr_b16 v[66:67], v50 offset:17408\n"
  "v_mfma_f32_16x16x32_bf16 v[128:131], a[52:55], v[84:87], v[128:131]\n"
  "s_waitcnt lgkmcnt(10)\n"
  "ds_read_b64_tr_b16 v[68:69], v50 offset:24576\n"
  "v_mfma_f32_16x16x32_bf16 v[132:135], a[76:79], v[84:87], v[132:135]\n"
  "ds_read_b64_tr_b16 v[70:71], v50 offset:25600\n"
  "v_mfma_f32_16x16x32_bf16 v[120:123], a[8:11], v[88:91], v[120:123]\n"
  "ds_read_b64_tr_b16 v[72:73], v50 offset:32768\n"
  "v_cvt_pk_bf16_f32 v104, v104, v105\n"
  "v_mfma_f32_16x16x32_bf16 v[124:127], a[32:35], v[88:91], v[124:127]\n"
  "ds_read_b64_tr_b16 v[74:75], v50 offset:33792\n"
  "v_cvt_pk_bf16_f32 v105, v106, v107\n"
  "v_mfma_f32_16x16x32_bf16 v[128:131], a[56:59], v[88:91], v[128:131]\n"
  "s_waitcnt lgkmcnt(10)\n"
  "ds_read_b64_tr_b16 v[76:77], v50 offset:40960\n"
  "v_cvt_pk_bf16_f32 v106, v108, v109\n"
  "v_mfma_f32_16x16x32_bf16 v[132:135], a[80:83], v[88:91], v[132:135]\n"
  "ds_read_b64_tr_b16 v[78:79], v50 offset:41984\n"
  "v_cvt_pk_bf16_f32 v107, v110, v111\n"
  "v_mfma_f32_16x16x32_bf16 v[120:123], a[12:15], v[92:95], v[120:123]\n"
  "v_cvt_pk_bf16_f32 v112, v112, v113\n"
  "v_cvt_pk_bf16_f32 v113, v114, v115\n"
  "v_mfma_f32_16x16x32_bf16 v[124:127], a[36:39], v[92:95], v[124:127]\n"
  "v_cvt_pk_bf16_f32 v114, v116, v117\n"
  "v_cvt_pk_bf16_f32 v115, v118, v119\n"
  "v_mfma_f32_16x16x32_bf16 v[128:131], a[60:63], v[92:95], v[128:131]\n"
  "s_add_u32 s40, s28, 0\n"
  "buffer_store_dwordx4 v[104:107], v43, s[24:27], s40 offen sc1\n"
  "v_mfma_f32_16x16x32_bf16 v[132:135], a[84:87], v[92:95], v[132:135]\n"
  "buffer_store_dwordx4 v[112:115], v43, s[24:27], s40 offen offset:1024 sc1\n"
  "v_mfma_f32_16x16x32_bf16 v[120:123], a[16:19], v[96:99], v[120:123]\n"
  "v_mfma_f32_16x16x32_bf16 v[124:127], a[40:43], v[96:99], v[124:127]\n"
  "v_mfma_f32_16x16x32_bf16 v[128:131], a[64:67], v[96:99], v[128:131]\n"
  "s_add_u32 s41, s42, 15872\n"
  "s_add_u32 m0, s43, 101376\n"
  "s_nop 0\n"
  "buffer_load_dwordx4 v36, s[20:23], s41 offen lds\n"
  "v_mfma_f32_16x16x32_bf16 v[132:135], a[88:91], v[96:99], v[132:135]\n"
  "v_mfma_f32_16x16x32_bf16 v[120:123], a[20:23], v[100:103], v[120:123]\n"
  "s_add_u32 s41, s42, 20992\n"
  "s_add_u32 m0, s43, 102400\n"
  "s_nop 0\n"
  "buffer_load_dwordx4 v35, s[20:23], s41 offen lds\n"
  "v_mfma_f32_16x16x32_bf16 v[124:127], a[44:47], v[100:103], v[124:127]\n"
  "v_mfma_f32_16x16x32_bf16 v[128:131], a[68:71], v[100:103], v[128:131]\n"
  "s_add_u32 s41, s42, 26112\n"
  "s_add_u32 m0, s43, 103424\n"
  "s_nop 0\n"
  "buffer_load_dwordx4 v35, s[20:23], s41 offen lds\n"
  "v_mfma_f32_16x16x32_bf16 v[132:135], a[92:95], v[100:103], v[132:135]\n"
  "s_waitcnt lgkmcnt(10)\n"
  "v_mfma_f32_16x16x32_bf16 v[104:107], a[0:3], v[56:59], 0\n"
  "ds_read_b64_tr_b16 v[80:81], v51 offset:0\n"
  "v_mfma_f32_16x16x32_bf16 v[108:111], a[24:27], v[56:59], 0\n"
  "ds_read_b64_tr_b16 v[82:83], v51 offset:1024\n"
  "v_mfma_f32_16x16x32_bf16 v[112:115], a[48:51], v[56:59], 0\n"
  "ds_read_b64_tr_b16 v[84:85], v51 offset:8192\n"
  "v_mfma_f32_16x16x32_bf16 v[116:119], a[72:75], v[56:59], 0\n"
  "ds_read_b64_tr_b16 v[86:87], v51 offset:9216\n"
  "s_waitcnt lgkmcnt(12)\n"
  "v_mfma_f32_16x16x32_bf16 v[104:107], a[4:7], v[60:63], v[104:107]\n"
  "ds_read_b64_tr_b16 v[88:89], v51 offset:16384\n"
  "v_mfma_f32_16x16x32_bf16 v[108:111], a[28:31], v[60:63], v[108:111]\n"
  "ds_read_b64_tr_b16 v[90:91], v51 offset:17408\n"
  "v_mfma_f32_16x16x32_bf16 v[112:115], a[52:55], v[60:63], v[112:115]\n"
  "s_waitcnt lgkmcnt(10)\n"
  "ds_read_b64_tr_b16 v[92:93], v51 offset:24576\n"
  "v_mfma_f32_16x16x32_bf16 v[116:119], a[76:79], v[60:63], v[116:119]\n"
  "ds_read_b64_tr_b16 v[94:95], v51 offset:25600\n"
  "v_mfma_f32_16x16x32_bf16 v[104:107], a[8:11], v[64:67], v[104:107]\n"
  "ds_read_b64_tr_b16 v[96:97], v51 offset:32768\n"
  "v_cvt_pk_bf16_f32 v120, v120, v121\n"
  "v_mfma_f32_16x16x32_bf16 v[108:111], a[32:35], v[64:67], v[108:111]\n"
  "ds_read_b64_tr_b16 v[98:99], v51 offset:33792\n"
  "v_cvt_pk_bf16_f32 v121, v122, v123\n"
  "v_mfma_f32_16x16x32_bf16 v[112:115], a[56:59], v[64:67], v[112:115]\n"
  "s_waitcnt lgkmcnt(10)\n"
  "ds_read_b64_tr_b16 v[100:101], v51 offset:40960\n"
  "v_cvt_pk_bf16_f32 v122, v124, v125\n"
  "v_mfma_f32_16x16x32_bf16 v[116:119], a[80:83], v[64:67], v[116:119]\n"
  "ds_read_b64_tr_b16 v[102:103], v51 offset:41984\n"
  "v_cvt_pk_bf16_f32 v123, v126, v127\n"
  "v_mfma_f32_16x16x32_bf16 v[104:107], a[12:15], v[68:71], v[104:107]\n"
  "v_cvt_pk_bf16_f32 v128, v128, v129\n"
  "v_cvt_pk_bf16_f32 v129, v130, v131\n"
  "v_mfma_f32_16x16x32_bf16 v[108:111], a[36:39], v[68:71], v[108:111]\n"
  "v_cvt_pk_bf16_f32 v130, v132, v133\n"
  "v_cvt_pk_bf16_f32 v131, v134, v135\n"
  "v_mfma_f32_16x16x32_bf16 v[112:115], a[60:63], v[68:71], v[112:115]\n"
  "s_add_u32 s40, s28, 655360\n"
  "buffer_store_dwordx4 v[120:123], v43, s[24:27], s40 offen sc1\n"
  "v_mfma_f32_16x16x32_bf16 v[116:119], a[84:87], v[68:71], v[116:119]\n"
  "buffer_store_dwordx4 v[128:131], v43, s[24:27], s40 offen offset:1024 sc1\n"
  "v_mfma_f32_16x16x32_bf16 v[104:107], a[16:19], v[72:75], v[104:107]\n"
  "v_mfma_f32_16x16x32_bf16 v[108:111], a[40:43], v[72:75], v[108:111]\n"
  "v_mfma_f32_16x16x32_bf16 v[112:115], a[64:67], v[72:75], v[112:115]\n"
  "s_add_u32 s41, s42, 31232\n"
  "s_add_u32 m0, s43, 104448\n"
  "s_nop 0\n"
  "buffer_load_dwordx4 v36, s[20:23], s41 offen lds\n"
  "v_mfma_f32_16x16x32_bf16 v[116:119], a[88:91], v[72:75], v[116:119]\n"
  "v_mfma_f32_16x16x32_bf16 v[104:107], a[20:23], v[76:79], v[104:107]\n"
  "s_add_u32 s41, s42, 36352\n"
  "s_add_u32 m0, s43, 105472\n"
  "s_nop 0\n"
  "buffer_load_dwordx4 v36, s[20:23], s41 offen lds\n"
  "v_mfma_f32_16x16x32_bf16 v[108:111], a[44:47], v[76:79], v[108:111]\n"
  "v_mfma_f32_16x16x32_bf16 v[112:115], a[68:71], v[76:79], v[112:115]\n"
  "s_add_u32 s41, s42, 41472\n"
  "s_add_u32 m0, s43, 106496\n"
  "s_nop 0\n"
  "buffer_load_dwordx4 v35, s[20:23], s41 offen lds\n"
  "v_mfma_f32_16x16x32_bf16 v[116:119], a[92:95], v[76:79], v[116:119]\n"
  "s_waitcnt lgkmcnt(10)\n"
  "v_mfma_f32_16x16x32_bf16 v[120:123], a[0:3], v[80:83], 0\n"
  "v_mfma_f32_16x16x32_bf16 v[124:127], a[24:27], v[80:83], 0\n"
  "v_mfma_f32_16x16x32_bf16 v[128:131], a[48:51], v[80:83], 0\n"
  "v_mfma_f32_16x16x32_bf16 v[132:135], a[72:75], v[80:83], 0\n"
  "s_waitcnt lgkmcnt(8)\n"
  "v_mfma_f32_16x16x32_bf16 v[120:123], a[4:7], v[84:87], v[120:123]\n"
  "v_mfma_f32_16x16x32_bf16 v[124:127], a[28:31], v[84:87], v[124:127]\n"
  "v_mfma_f32_16x16x32_bf16 v[128:131], a[52:55], v[84:87], v[128:131]\n"
  "v_mfma_f32_16x16x32_bf16 v[132:135], a[76:79], v[84:87], v[132:135]\n"
  "s_waitcnt lgkmcnt(6)\n"
  "v_mfma_f32_16x16x32_bf16 v[120:123], a[8:11], v[88:91], v[120:123]\n"
  "v_cvt_pk_bf16_f32 v104, v104, v105\n"
  "v_mfma_f32_16x16x32_bf16 v[124:127], a[32:35], v[88:91], v[124:127]\n"
  "v_cvt_pk_bf16_f32 v105, v106, v107\n"
  "v_mfma_f32_16x16x32_bf16 v[128:131], a[56:59], v[88:91], v[128:131]\n"
  "v_cvt_pk_bf16_f32 v106, v108, v109\n"
  "v_mfma_f32_16x16x32_bf16 v[132:135], a[80:83], v[88:91], v[132:135]\n"
  "v_cvt_pk_bf16_f32 v107, v110, v111\n"
  "s_waitcnt lgkmcnt(4)\n"
  "v_mfma_f32_16x16x32_bf16 v[120:123], a[12:15], v[92:95], v[120:123]\n"
  "v_cvt_pk_bf16_f32 v112, v112, v113\n"
  "v_cvt_pk_bf16_f32 v113, v114, v115\n"
  "v_mfma_f32_16x16x32_bf16 v[124:127], a[36:39], v[92:95], v[124:127]\n"
  "v_cvt_pk_bf16_f32 v114, v116, v117\n"
  "v_cvt_pk_bf16_f32 v115, v118, v119\n"
  "v_mfma_f32_16x16x32_bf16 v[128:131], a[60:63], v[92:95], v[128:131]\n"
  "s_add_u32 s40, s28, 1310720\n"
  "buffer_store_dwordx4 v[104:107], v43, s[24:27], s40 offen sc1\n"
  "v_mfma_f32_16x16x32_bf16 v[132:135], a[84:87], v[92:95], v[132:135]\n"
  "buffer_store_dwordx4 v[112:115], v43, s[24:27], s40 offen offset:1024 sc1\n"
  "s_waitcnt lgkmcnt(2)\n"
  "v_mfma_f32_16x16x32_bf16 v[120:123], a[16:19], v[96:99], v[120:123]\n"
  "v_mfma_f32_16x16x32_bf16 v[124:127], a[40:43], v[96:99], v[124:127]\n"
  "v_mfma_f32_16x16x32_bf16 v[128:131], a[64:67], v[96:99], v[128:131]\n"
  "s_add_u32 s41, s42, 46592\n"
  "s_add_u32 m0, s43, 107520\n"
  "s_nop 0\n"
  "buffer_load_dwordx4 v35, s[20:23], s41 offen lds\n"
  "v_mfma_f32_16x16x32_bf16 v[132:135], a[88:91], v[96:99], v[132:135]\n"
  "s_waitcnt lgkmcnt(0)\n"
  "v_mfma_f32_16x16x32_bf16 v[120:123], a[20:23], v[100:103], v[120:123]\n"
  "s_add_u32 s41, s42, 51712\n"
  "s_add_u32 m0, s43, 108544\n"
  "s_nop 0\n"
  "buffer_load_dwordx4 v36, s[20:23], s41 offen lds\n"
  "v_mfma_f32_16x16x32_bf16 v[124:127], a[44:47], v[100:103], v[124:127]\n"
  "v_mfma_f32_16x16x32_bf16 v[128:131], a[68:71], v[100:103], v[128:131]\n"
  "s_add_u32 s41, s42, 56832\n"
  "s_add_u32 m0, s43, 109568\n"
  "s_nop 0\n"
  "buffer_load_dwordx4 v36, s[20:23], s41 offen lds\n"
  "v_mfma_f32_16x16x32_bf16 v[132:135], a[92:95], v[100:103], v[132:135]\n"
  "s_waitcnt vmcnt(18)\n"
  "s_barrier\n"
  "v_add_u32 v48, 49152, v39\n"
  "v_add_u32 v49, 49152, v40\n"
  "v_add_u32 v50, 49152, v41\n"
  "v_add_u32 v51, 49152, v42\n"
  "ds_read_b64_tr_b16 v[56:57], v48 offset:0\n"
  "ds_read_b64_tr_b16 v[58:59], v48 offset:1024\n"
  "ds_read_b64_tr_b16 v[60:61], v48 offset:8192\n"
  "ds_read_b64_tr_b16 v[62:63], v48 offset:9216\n"
  "ds_read_b64_tr_b16 v[64:65], v48 offset:16384\n"
  "ds_read_b64_tr_b16 v[66:67], v48 offset:17408\n"
  "ds_read_b64_tr_b16 v[68:69], v48 offset:24576\n"
  "ds_read_b64_tr_b16 v[70:71], v48 offset:25600\n"
  "ds_read_b64_tr_b16 v[72:73], v48 offset:32768\n"
  "ds_read_b64_tr_b16 v[74:75], v48 offset:33792\n"
  "ds_read_b64_tr_b16 v[76:77], v48 offset:40960\n"
  "ds_read_b64_tr_b16 v[78:79], v48 offset:41984\n"
  "s_waitcnt lgkmcnt(10)\n"
  "v_mfma_f32_16x16x32_bf16 v[104:107], a[0:3], v[56:59], 0\n"
  "ds_read_b64_tr_b16 v[80:81], v49 offset:0\n"
  "v_mfma_f32_16x16x32_bf16 v[108:111], a[24:27], v[56:59], 0\n"
  "ds_read_b64_tr_b16 v[82:83], v49 offset:1024\n"
  "v_mfma_f32_16x16x32_bf16 v[112:115], a[48:51], v[56:59], 0\n"
  "ds_read_b64_tr_b16 v[84:85], v49 offset:8192\n"
  "v_mfma_f32_16x16x32_bf16 v[116:119], a[72:75], v[56:59], 0\n"
  "ds_read_b64_tr_b16 v[86:87], v49 offset:9216\n"
  "s_waitcnt lgkmcnt(12)\n"
  "v_mfma_f32_16x16x32_bf16 v[104:107], a[4:7], v[60:63], v[104:107]\n"
  "ds_read_b64_tr_b16 v[88:89], v49 offset:16384\n"
  "v_mfma_f32_16x16x32_bf16 v[108:111], a[28:31], v[60:63], v[108:111]\n"
  "ds_read_b64_tr_b16 v[90:91], v49 offset:17408\n"
  "v_mfma_f32_16x16x32_bf16 v[112:115], a[52:55], v[60:63], v[112:115]\n"
  "s_waitcnt lgkmcnt(10)\n"
  "ds_read_b64_tr_b16 v[92:93], v49 offset:24576\n"
  "v_mfma_f32_16x16x32_bf16 v[116:119], a[76:79], v[60:63], v[116:119]\n"
  "ds_read_b64_tr_b16 v[94:95], v49 offset:25600\n"
  "v_mfma_f32_16x16x32_bf16 v[104:107], a[8:11], v[64:67], v[104:107]\n"
  "ds_read_b64_tr_b16 v[96:97], v49 offset:32768\n"
  "v_cvt_pk_bf16_f32 v120, v120, v121\n"
  "v_mfma_f32_16x16x32_bf16 v[108:111], a[32:35], v[64:67], v[108:111]\n"
  "ds_read_b64_tr_b16 v[98:99], v49 offset:33792\n"
  "v_cvt_pk_bf16_f32 v121, v122, v123\n"
  "v_mfma_f32_16x16x32_bf16 v[112:115], a[56:59], v[64:67], v[112:115]\n"
  "s_waitcnt lgkmcnt(10)\n"
  "ds_read_b64_tr_b16 v[100:101], v49 offset:40960\n"
  "v_cvt_pk_bf16_f32 v122, v124, v125\n"
  "v_mfma_f32_16x16x32_bf16 v[116:119], a[80:83], v[64:67], v[116:119]\n"
  "ds_read_b64_tr_b16 v[102:103], v49 offset:41984\n"
  "v_cvt_pk_bf16_f32 v123, v126, v127\n"
  "v_mfma_f32_16x16x32_bf16 v[104:107], a[12:15], v[68:71], v[104:107]\n"
  "v_cvt_pk_bf16_f32 v128, v128, v129\n"
  "v_cvt_pk_bf16_f32 v129, v130, v131\n"
  "v_mfma_f32_16x16x32_bf16 v[108:111], a[36:39], v[68:71], v[108:111]\n"
  "v_cvt_pk_bf16_f32 v130, v132, v133\n"
  "v_cvt_pk_bf16_f32 v131, v134, v135\n"
  "v_mfma_f32_16x16x32_bf16 v[112:115], a[60:63], v[68:71], v[112:115]\n"
  "s_add_u32 s40, s28, 1966080\n"
  "buffer_store_dwordx4 v[120:123], v43, s[24:27], s40 offen sc1\n"
  "v_mfma_f32_16x16x32_bf16 v[116:119], a[84:87], v[68:71], v[116:119]\n"
  "buffer_store_dwordx4 v[128:131], v43, s[24:27], s40 offen offset:1024 sc1\n"
  "v_mfma_f32_16x16x32_bf16 v[104:107], a[16:19], v[72:75], v[104:107]\n"
  "v_mfma_f32_16x16x32_bf16 v[108:111], a[40:43], v[72:75], v[108:111]\n"
  "v_mfma_f32_16x16x32_bf16 v[112:115], a[64:67], v[72:75], v[112:115]\n"
  "s_add_u32 s41, s42, 768\n"
  "s_add_u32 m0, s43, 0\n"
  "s_nop 0\n"
  "buffer_load_dwordx4 v35, s[20:23], s41 offen lds\n"
  "v_mfma_f32_16x16x32_bf16 v[116:119], a[88:91], v[72:75], v[116:119]\n"
  "v_mfma_f32_16x16x32_bf16 v[104:107], a[20:23], v[76:79], v[104:107]\n"
  "s_add_u32 s41, s42, 5888\n"
  "s_add_u32 m0, s43, 1024\n"
  "s_nop 0\n"
  "buffer_load_dwordx4 v35, s[20:23], s41 offen lds\n"
  "v_mfma_f32_16x16x32_bf16 v[108:111], a[44:47], v[76:79], v[108:111]\n"
  "v_mfma_f32_16x16x32_bf16 v[112:115], a[68:71], v[76:79], v[112:115]\n"
  "s_add_u32 s41, s42, 11008\n"
  "s_add_u32 m0, s43, 2048\n"
  "s_nop 0\n"
  "buffer_load_dwordx4 v36, s[20:23], s41 offen lds\n"
  "v_mfma_f32_16x16x32_bf16 v[116:119], a[92:95], v[76:79], v[116:119]\n"
  "s_waitcnt lgkmcnt(10)\n"
  "v_mfma_f32_16x16x32_bf16 v[120:123], a[0:3], v[80:83], 0\n"
  "ds_read_b64_tr_b16 v[56:57], v50 offset:0\n"
  "v_mfma_f32_16x16x32_bf16 v[124:127], a[24:27], v[80:83], 0\n"
  "ds_read_b64_tr_b16 v[58:59], v50 offset:1024\n"
  "v_mfma_f32_16x16x32_bf16 v[128:131], a[48:51], v[80:83], 0\n"
  "ds_read_b64_tr_b16 v[60:61], v50 offset:8192\n"
  "v_mfma_f32_16x16x32_bf16 v[132:135], a[72:75], v[80:83], 0\n"
  "ds_read_b64_tr_b16 v[62:63], v50 offset:9216\n"
  "s_waitcnt lgkmcnt(12)\n"
  "v_mfma_f32_16x16x32_bf16 v[120:123], a[4:7], v[84:87], v[120:123]\n"
  "ds_read_b64_tr_b16 v[64:65], v50 offset:16384\n"
  "v_mfma_f32_16x16x32_bf16 v[124:127], a[28:31], v[84:87], v[124:127]\n"
  "ds_read_b64_tr_b16 v[66:67], v50 offset:17408\n"
  "v_mfma_f32_16x16x32_bf16 v[128:131], a[52:55], v[84:87], v[128:131]\n"
  "s_waitcnt lgkmcnt(10)\n"
  "ds_read_b64_tr_b16 v[68:69], v50 offset:24576\n"
  "v_mfma_f32_16x16x32_bf16 v[132:135], a[76:79], v[84:87], v[132:135]\n"
  "ds_read_b64_tr_b16 v[70:71], v50 offset:25600\n"
  "v_mfma_f32_16x16x32_bf16 v[120:123], a[8:11], v[88:91], v[120:123]\n"
  "ds_read_b64_tr_b16 v[72:73], v50 offset:32768\n"
  "v_cvt_pk_bf16_f32 v104, v104, v105\n"
  "v_mfma_f32_16x16x32_bf16 v[124:127], a[32:35], v[88:91], v[124:127]\n"
  "ds_read_b64_tr_b16 v[74:75], v50 offset:33792\n"
  "v_cvt_pk_bf16_f32 v105, v106, v107\n"
  "v_mfma_f32_16x16x32_bf16 v[128:131], a[56:59], v[88:91], v[128:131]\n"
  "s_waitcnt lgkmcnt(10)\n"
  "ds_read_b64_tr_b16 v[76:77], v50 offset:40960\n"
  "v_cvt_pk_bf16_f32 v106, v108, v109\n"
  "v_mfma_f32_16x16x32_bf16 v[132:135], a[80:83], v[88:91], v[132:135]\n"
  "ds_read_b64_tr_b16 v[78:79], v50 offset:41984\n"
  "v_cvt_pk_bf16_f32 v107, v110, v111\n"
  "v_mfma_f32_16x16x32_bf16 v[120:123], a[12:15], v[92:95], v[120:123]\n"
  "v_cvt_pk_bf16_f32 v112, v112, v113\n"
  "v_cvt_pk_bf16_f32 v113, v114, v115\n"
  "v_mfma_f32_16x16x32_bf16 v[124:127], a[36:39], v[92:95], v[124:127]\n"
  "v_cvt_pk_bf16_f32 v114, v116, v117\n"
  "v_cvt_pk_bf16_f32 v115, v118, v119\n"
  "v_mfma_f32_16x16x32_bf16 v[128:131], a[60:63], v[92:95], v[128:131]\n"
  "s_add_u32 s40, s28, 5242880\n"
  "buffer_store_dwordx4 v[104:107], v43, s[24:27], s40 offen sc1\n"
  "v_mfma_f32_16x16x32_bf16 v[132:135], a[84:87], v[92:95], v[132:135]\n"
  "buffer_store_dwordx4 v[112:115], v43, s[24:27], s40 offen offset:1024 sc1\n"
  "v_mfma_f32_16x16x32_bf16 v[120:123], a[16:19], v[96:99], v[120:123]\n"
  "v_mfma_f32_16x16x32_bf16 v[124:127], a[40:43], v[96:99], v[124:127]\n"
  "v_mfma_f32_16x16x32_bf16 v[128:131], a[64:67], v[96:99], v[128:131]\n"
  "s_add_u32 s41, s42, 16128\n"
  "s_add_u32 m0, s43, 3072\n"
  "s_nop 0\n"
  "buffer_load_dwordx4 v36, s[20:23], s41 offen lds\n"
  "v_mfma_f32_16x16x32_bf16 v[132:135], a[88:91], v[96:99], v[132:135]\n"
  "v_mfma_f32_16x16x32_bf16 v[120:123], a[20:23], v[100:103], v[120:123]\n"
  "s_add_u32 s41, s42, 21248\n"
  "s_add_u32 m0, s43, 4096\n"
  "s_nop 0\n"
  "buffer_load_dwordx4 v35, s[20:23], s41 offen lds\n"
  "v_mfma_f32_16x16x32_bf16 v[124:127], a[44:47], v[100:103], v[124:127]\n"
  "v_mfma_f32_16x16x32_bf16 v[128:131], a[68:71], v[100:103], v[128:131]\n"
  "s_add_u32 s41, s42, 26368\n"
  "s_add_u32 m0, s43, 5120\n"
  "s_nop 0\n"
  "buffer_load_dwordx4 v35, s[20:23], s41 offen lds\n"
  "v_mfma_f32_16x16x32_bf16 v[132:135], a[92:95], v[100:103], v[132:135]\n"
  "s_waitcnt lgkmcnt(10)\n"
  "v_mfma_f32_16x16x32_bf16 v[104:107], a[0:3], v[56:59], 0\n"
  "ds_read_b64_tr_b16 v[80:81], v51 offset:0\n"
  "v_mfma_f32_16x16x32_bf16 v[108:111], a[24:27], v[56:59], 0\n"
  "ds_read_b64_tr_b16 v[82:83], v51 offset:1024\n"
  "v_mfma_f32_16x16x32_bf16 v[112:115], a[48:51], v[56:59], 0\n"
  "ds_read_b64_tr_b16 v[84:85], v51 offset:8192\n"
  "v_mfma_f32_16x16x32_bf16 v[116:119], a[72:75], v[56:59], 0\n"
  "ds_read_b64_tr_b16 v[86:87], v51 offset:9216\n"
  "s_waitcnt lgkmcnt(12)\n"
  "v_mfma_f32_16x16x32_bf16 v[104:107], a[4:7], v[60:63], v[104:107]\n"
  "ds_read_b64_tr_b16 v[88:89], v51 offset:16384\n"
  "v_mfma_f32_16x16x32_bf16 v[108:111], a[28:31], v[60:63], v[108:111]\n"
  "ds_read_b64_tr_b16 v[90:91], v51 offset:17408\n"
  "v_mfma_f32_16x16x32_bf16 v[112:115], a[52:55], v[60:63], v[112:115]\n"
  "s_waitcnt lgkmcnt(10)\n"
  "ds_read_b64_tr_b16 v[92:93], v51 offset:24576\n"
  "v_mfma_f32_16x16x32_bf16 v[116:119], a[76:79], v[60:63], v[116:119]\n"
  "ds_read_b64_tr_b16 v[94:95], v51 offset:25600\n"
  "v_mfma_f32_16x16x32_bf16 v[104:107], a[8:11], v[64:67], v[104:107]\n"
  "ds_read_b64_tr_b16 v[96:97], v51 offset:32768\n"
  "v_cvt_pk_bf16_f32 v120, v120, v121\n"
  "v_mfma_f32_16x16x32_bf16 v[108:111], a[32:35], v[64:67], v[108:111]\n"
  "ds_read_b64_tr_b16 v[98:99], v51 offset:33792\n"
  "v_cvt_pk_bf16_f32 v121, v122, v123\n"
  "v_mfma_f32_16x16x32_bf16 v[112:115], a[56:59], v[64:67], v[112:115]\n"
  "s_waitcnt lgkmcnt(10)\n"
  "ds_read_b64_tr_b16 v[100:101], v51 offset:40960\n"
  "v_cvt_pk_bf16_f32 v122, v124, v125\n"
  "v_mfma_f32_16x16x32_bf16 v[116:119], a[80:83], v[64:67], v[116:119]\n"
  "ds_read_b64_tr_b16 v[102:103], v51 offset:41984\n"
  "v_cvt_pk_bf16_f32 v123, v126, v127\n"
  "v_mfma_f32_16x16x32_bf16 v[104:107], a[12:15], v[68:71], v[104:107]\n"
  "v_cvt_pk_bf16_f32 v128, v128, v129\n"
  "v_cvt_pk_bf16_f32 v129, v130, v131\n"
  "v_mfma_f32_16x16x32_bf16 v[108:111], a[36:39], v[68:71], v[108:111]\n"
  "v_cvt_pk_bf16_f32 v130, v132, v133\n"
  "v_cvt_pk_bf16_f32 v131, v134, v135\n"
  "v_mfma_f32_16x16x32_bf16 v[112:115], a[60:63], v[68:71], v[112:115]\n"
  "s_add_u32 s40, s28, 5898240\n"
  "buffer_store_dwordx4 v[120:123], v43, s[24:27], s40 offen sc1\n"
  "v_mfma_f32_16x16x32_bf16 v[116:119], a[84:87], v[68:71], v[116:119]\n"
  "buffer_store_dwordx4 v[128:131], v43, s[24:27], s40 offen offset:1024 sc1\n"
  "v_mfma_f32_16x16x32_bf16 v[104:107], a[16:19], v[72:75], v[104:107]\n"
  "v_mfma_f32_16x16x32_bf16 v[108:111], a[40:43], v[72:75], v[108:111]\n"
  "v_mfma_f32_16x16x32_bf16 v[112:115], a[64:67], v[72:75], v[112:115]\n"
  "s_add_u32 s41, s42, 31488\n"
  "s_add_u32 m0, s43, 6144\n"
  "s_nop 0\n"
  "buffer_load_dwordx4 v36, s[20:23], s41 offen lds\n"
  "v_mfma_f32_16x16x32_bf16 v[116:119], a[88:91], v[72:75], v[116:119]\n"
  "v_mfma_f32_16x16x32_bf16 v[104:107], a[20:23], v[76:79], v[104:107]\n"
  "s_add_u32 s41, s42, 36608\n"
  "s_add_u32 m0, s43, 7168\n"
  "s_nop 0\n"
  "buffer_load_dwordx4 v36, s[20:23], s41 offen lds\n"
  "v_mfma_f32_16x16x32_bf16 v[108:111], a[44:47], v[76:79], v[108:111]\n"
  "v_mfma_f32_16x16x32_bf16 v[112:115], a[68:71], v[76:79], v[112:115]\n"
  "s_add_u32 s41, s42, 41728\n"
  "s_add_u32 m0, s43, 8192\n"
  "s_nop 0\n"
  "buffer_load_dwordx4 v35, s[20:23], s41 offen lds\n"
  "v_mfma_f32_16x16x32_bf16 v[116:119], a[92:95], v[76:79], v[116:119]\n"
  "s_waitcnt lgkmcnt(10)\n"
  "v_mfma_f32_16x16x32_bf16 v[120:123], a[0:3], v[80:83], 0\n"
  "v_mfma_f32_16x16x32_bf16 v[124:127], a[24:27], v[80:83], 0\n"
  "v_mfma_f32_16x16x32_bf16 v[128:131], a[48:51], v[80:83], 0\n"
  "v_mfma_f32_16x16x32_bf16 v[132:135], a[72:75], v[80:83], 0\n"
  "s_waitcnt lgkmcnt(8)\n"
  "v_mfma_f32_16x16x32_bf16 v[120:123], a[4:7], v[84:87], v[120:123]\n"
  "v_mfma_f32_16x16x32_bf16 v[124:127], a[28:31], v[84:87], v[124:127]\n"
  "v_mfma_f32_16x16x32_bf16 v[128:131], a[52:55], v[84:87], v[128:131]\n"
  "v_mfma_f32_16x16x32_bf16 v[132:135], a[76:79], v[84:87], v[132:135]\n"
  "s_waitcnt lgkmcnt(6)\n"
  "v_mfma_f32_16x16x32_bf16 v[120:123], a[8:11], v[88:91], v[120:123]\n"
  "v_cvt_pk_bf16_f32 v104, v104, v105\n"
  "v_mfma_f32_16x16x32_bf16 v[124:127], a[32:35], v[88:91], v[124:127]\n"
  "v_cvt_pk_bf16_f32 v105, v106, v107\n"
  "v_mfma_f32_16x16x32_bf16 v[128:131], a[56:59], v[88:91], v[128:131]\n"
  "v_cvt_pk_bf16_f32 v106, v108, v109\n"
  "v_mfma_f32_16x16x32_bf16 v[132:135], a[80:83], v[88:91], v[132:135]\n"
  "v_cvt_pk_bf16_f32 v107, v110, v111\n"
  "s_waitcnt lgkmcnt(4)\n"
  "v_mfma_f32_16x16x32_bf16 v[120:123], a[12:15], v[92:95], v[120:123]\n"
  "v_cvt_pk_bf16_f32 v112, v112, v113\n"
  "v_cvt_pk_bf16_f32 v113, v114, v115\n"
  "v_mfma_f32_16x16x32_bf16 v[124:127], a[36:39], v[92:95], v[124:127]\n"
  "v_cvt_pk_bf16_f32 v114, v116, v117\n"
  "v_cvt_pk_bf16_f32 v115, v118, v119\n"
  "v_mfma_f32_16x16x32_bf16 v[128:131], a[60:63], v[92:95], v[128:131]\n"
  "s_add_u32 s40, s28, 6553600\n"
  "buffer_store_dwordx4 v[104:107], v43, s[24:27], s40 offen sc1\n"
  "v_mfma_f32_16x16x32_bf16 v[132:135], a[84:87], v[92:95], v[132:135]\n"
  "buffer_store_dwordx4 v[112:115], v43, s[24:27], s40 offen offset:1024 sc1\n"
  "s_waitcnt lgkmcnt(2)\n"
  "v_mfma_f32_16x16x32_bf16 v[120:123], a[16:19], v[96:99], v[120:123]\n"
  "v_mfma_f32_16x16x32_bf16 v[124:127], a[40:43], v[96:99], v[124:127]\n"
  "v_mfma_f32_16x16x32_bf16 v[128:131], a[64:67], v[96:99], v[128:131]\n"
  "s_add_u32 s41, s42, 46848\n"
  "s_add_u32 m0, s43, 9216\n"
  "s_nop 0\n"
  "buffer_load_dwordx4 v35, s[20:23], s41 offen lds\n"
  "v_mfma_f32_16x16x32_bf16 v[132:135], a[88:91], v[96:99], v[132:135]\n"
  "s_waitcnt lgkmcnt(0)\n"
  "v_mfma_f32_16x16x32_bf16 v[120:123], a[20:23], v[100:103], v[120:123]\n"
  "s_add_u32 s41, s42, 51968\n"
  "s_add_u32 m0, s43, 10240\n"
  "s_nop 0\n"
  "buffer_load_dwordx4 v36, s[20:23], s41 offen lds\n"
  "v_mfma_f32_16x16x32_bf16 v[124:127], a[44:47], v[100:103], v[124:127]\n"
  "v_mfma_f32_16x16x32_bf16 v[128:131], a[68:71], v[100:103], v[128:131]\n"
  "s_add_u32 s41, s42, 57088\n"
  "s_add_u32 m0, s43, 11264\n"
  "s_nop 0\n"
  "buffer_load_dwordx4 v36, s[20:23], s41 offen lds\n"
  "v_mfma_f32_16x16x32_bf16 v[132:135], a[92:95], v[100:103], v[132:135]\n"
  "s_waitcnt vmcnt(20)\n"
  "s_barrier\n"
  "v_add_u32 v48, 98304, v39\n"
  "v_add_u32 v49, 98304, v40\n"
  "v_add_u32 v50, 98304, v41\n"
  "v_add_u32 v51, 98304, v42\n"
  "ds_read_b64_tr_b16 v[56:57], v48 offset:0\n"
  "ds_read_b64_tr_b16 v[58:59], v48 offset:1024\n"
  "ds_read_b64_tr_b16 v[60:61], v48 offset:8192\n"
  "ds_read_b64_tr_b16 v[62:63], v48 offset:9216\n"
  "ds_read_b64_tr_b16 v[64:65], v48 offset:16384\n"
  "ds_read_b64_tr_b16 v[66:67], v48 offset:17408\n"
  "ds_read_b64_tr_b16 v[68:69], v48 offset:24576\n"
  "ds_read_b64_tr_b16 v[70:71], v48 offset:25600\n"
  "ds_read_b64_tr_b16 v[72:73], v48 offset:32768\n"
  "ds_read_b64_tr_b16 v[74:75], v48 offset:33792\n"
  "ds_read_b64_tr_b16 v[76:77], v48 offset:40960\n"
  "ds_read_b64_tr_b16 v[78:79], v48 offset:41984\n"
  "s_waitcnt lgkmcnt(10)\n"
  "v_mfma_f32_16x16x32_bf16 v[104:107], a[0:3], v[56:59], 0\n"
  "ds_read_b64_tr_b16 v[80:81], v49 offset:0\n"
  "v_mfma_f32_16x16x32_bf16 v[108:111], a[24:27], v[56:59], 0\n"
  "ds_read_b64_tr_b16 v[82:83], v49 offset:1024\n"
  "v_mfma_f32_16x16x32_bf16 v[112:115], a[48:51], v[56:59], 0\n"
  "ds_read_b64_tr_b16 v[84:85], v49 offset:8192\n"
  "v_mfma_f32_16x16x32_bf16 v[116:119], a[72:75], v[56:59], 0\n"
  "ds_read_b64_tr_b16 v[86:87], v49 offset:9216\n"
  "s_waitcnt lgkmcnt(12)\n"
  "v_mfma_f32_16x16x32_bf16 v[104:107], a[4:7], v[60:63], v[104:107]\n"
  "ds_read_b64_tr_b16 v[88:89], v49 offset:16384\n"
  "v_mfma_f32_16x16x32_bf16 v[108:111], a[28:31], v[60:63], v[108:111]\n"
  "ds_read_b64_tr_b16 v[90:91], v49 offset:17408\n"
  "v_mfma_f32_16x16x32_bf16 v[112:115], a[52:55], v[60:63], v[112:115]\n"
  "s_waitcnt lgkmcnt(10)\n"
  "ds_read_b64_tr_b16 v[92:93], v49 offset:24576\n"
  "v_mfma_f32_16x16x32_bf16 v[116:119], a[76:79], v[60:63], v[116:119]\n"
  "ds_read_b64_tr_b16 v[94:95], v49 offset:25600\n"
  "v_mfma_f32_16x16x32_bf16 v[104:107], a[8:11], v[64:67], v[104:107]\n"
  "ds_read_b64_tr_b16 v[96:97], v49 offset:32768\n"
  "v_cvt_pk_bf16_f32 v120, v120, v121\n"
  "v_mfma_f32_16x16x32_bf16 v[108:111], a[32:35], v[64:67], v[108:111]\n"
  "ds_read_b64_tr_b16 v[98:99], v49 offset:33792\n"
  "v_cvt_pk_bf16_f32 v121, v122, v123\n"
  "v_mfma_f32_16x16x32_bf16 v[112:115], a[56:59], v[64:67], v[112:115]\n"
  "s_waitcnt lgkmcnt(10)\n"
  "ds_read_b64_tr_b16 v[100:101], v49 offset:40960\n"
  "v_cvt_pk_bf16_f32 v122, v124, v125\n"
  "v_mfma_f32_16x16x32_bf16 v[116:119], a[80:83], v[64:67], v[116:119]\n"
  "ds_read_b64_tr_b16 v[102:103], v49 offset:41984\n"
  "v_cvt_pk_bf16_f32 v123, v126, v127\n"
  "v_mfma_f32_16x16x32_bf16 v[104:107], a[12:15], v[68:71], v[104:107]\n"
  "v_cvt_pk_bf16_f32 v128, v128, v129\n"
  "v_cvt_pk_bf16_f32 v129, v130, v131\n"
  "v_mfma_f32_16x16x32_bf16 v[108:111], a[36:39], v[68:71], v[108:111]\n"
  "v_cvt_pk_bf16_f32 v130, v132, v133\n"
  "v_cvt_pk_bf16_f32 v131, v134, v135\n"
  "v_mfma_f32_16x16x32_bf16 v[112:115], a[60:63], v[68:71], v[112:115]\n"
  "s_add_u32 s40, s28, 7208960\n"
  "buffer_store_dwordx4 v[120:123], v43, s[24:27], s40 offen sc1\n"
  "v_mfma_f32_16x16x32_bf16 v[116:119], a[84:87], v[68:71], v[116:119]\n"
  "buffer_store_dwordx4 v[128:131], v43, s[24:27], s40 offen offset:1024 sc1\n"
  "v_mfma_f32_16x16x32_bf16 v[104:107], a[16:19], v[72:75], v[104:107]\n"
  "v_mfma_f32_16x16x32_bf16 v[108:111], a[40:43], v[72:75], v[108:111]\n"
  "v_mfma_f32_16x16x32_bf16 v[112:115], a[64:67], v[72:75], v[112:115]\n"
  "s_add_u32 s41, s42, 1024\n"
  "s_add_u32 m0, s43, 49152\n"
  "s_nop 0\n"
  "buffer_load_dwordx4 v35, s[20:23], s41 offen lds\n"
  "v_mfma_f32_16x16x32_bf16 v[116:119], a[88:91], v[72:75], v[116:119]\n"
  "v_mfma_f32_16x16x32_bf16 v[104:107], a[20:23], v[76:79], v[104:107]\n"
  "s_add_u32 s41, s42, 6144\n"
  "s_add_u32 m0, s43, 50176\n"
  "s_nop 0\n"
  "buffer_load_dwordx4 v35, s[20:23], s41 offen lds\n"
  "v_mfma_f32_16x16x32_bf16 v[108:111], a[44:47], v[76:79], v[108:111]\n"
  "v_mfma_f32_16x16x32_bf16 v[112:115], a[68:71], v[76:79], v[112:115]\n"
  "s_add_u32 s41, s42, 11264\n"
  "s_add_u32 m0, s43, 51200\n"
  "s_nop 0\n"
  "buffer_load_dwordx4 v36, s[20:23], s41 offen lds\n"
  "v_mfma_f32_16x16x32_bf16 v[116:119], a[92:95], v[76:79], v[116:119]\n"
  "s_waitcnt lgkmcnt(10)\n"
  "v_mfma_f32_16x16x32_bf16 v[120:123], a[0:3], v[80:83], 0\n"
  "ds_read_b64_tr_b16 v[56:57], v50 offset:0\n"
  "v_mfma_f32_16x16x32_bf16 v[124:127], a[24:27], v[80:83], 0\n"
  "ds_read_b64_tr_b16 v[58:59], v50 offset:1024\n"
  "v_mfma_f32_16x16x32_bf16 v[128:131], a[48:51], v[80:83], 0\n"
  "ds_read_b64_tr_b16 v[60:61], v50 offset:8192\n"
  "v_mfma_f32_16x16x32_bf16 v[132:135], a[72:75], v[80:83], 0\n"
  "ds_read_b64_tr_b16 v[62:63], v50 offset:9216\n"
  "s_waitcnt lgkmcnt(12)\n"
  "v_mfma_f32_16x16x32_bf16 v[120:123], a[4:7], v[84:87], v[120:123]\n"
  "ds_read_b64_tr_b16 v[64:65], v50 offset:16384\n"
  "v_mfma_f32_16x16x32_bf16 v[124:127], a[28:31], v[84:87], v[124:127]\n"
  "ds_read_b64_tr_b16 v[66:67], v50 offset:17408\n"
  "v_mfma_f32_16x16x32_bf16 v[128:131], a[52:55], v[84:87], v[128:131]\n"
  "s_waitcnt lgkmcnt(10)\n"
  "ds_read_b64_tr_b16 v[68:69], v50 offset:24576\n"
  "v_mfma_f32_16x16x32_bf16 v[132:135], a[76:79], v[84:87], v[132:135]\n"
  "ds_read_b64_tr_b16 v[70:71], v50 offset:25600\n"
  "v_mfma_f32_16x16x32_bf16 v[120:123], a[8:11], v[88:91], v[120:123]\n"
  "ds_read_b64_tr_b16 v[72:73], v50 offset:32768\n"
  "v_cvt_pk_bf16_f32 v104, v104, v105\n"
  "v_mfma_f32_16x16x32_bf16 v[124:127], a[32:35], v[88:91], v[124:127]\n"
  "ds_read_b64_tr_b16 v[74:75], v50 offset:33792\n"
  "v_cvt_pk_bf16_f32 v105, v106, v107\n"
  "v_mfma_f32_16x16x32_bf16 v[128:131], a[56:59], v[88:91], v[128:131]\n"
  "s_waitcnt lgkmcnt(10)\n"
  "ds_read_b64_tr_b16 v[76:77], v50 offset:40960\n"
  "v_cvt_pk_bf16_f32 v106, v108, v109\n"
  "v_mfma_f32_16x16x32_bf16 v[132:135], a[80:83], v[88:91], v[132:135]\n"
  "ds_read_b64_tr_b16 v[78:79], v50 offset:41984\n"
  "v_cvt_pk_bf16_f32 v107, v110, v111\n"
  "v_mfma_f32_16x16x32_bf16 v[120:123], a[12:15], v[92:95], v[120:123]\n"
  "v_cvt_pk_bf16_f32 v112, v112, v113\n"
  "v_cvt_pk_bf16_f32 v113, v114, v115\n"
  "v_mfma_f32_16x16x32_bf16 v[124:127], a[36:39], v[92:95], v[124:127]\n"
  "v_cvt_pk_bf16_f32 v114, v116, v117\n"
  "v_cvt_pk_bf16_f32 v115, v118, v119\n"
  "v_mfma_f32_16x16x32_bf16 v[128:131], a[60:63], v[92:95], v[128:131]\n"
  "s_add_u32 s40, s28, 10485760\n"
  "buffer_store_dwordx4 v[104:107], v43, s[24:27], s40 offen sc1\n"
  "v_mfma_f32_16x16x32_bf16 v[132:135], a[84:87], v[92:95], v[132:135]\n"
  "buffer_store_dwordx4 v[112:115], v43, s[24:27], s40 offen offset:1024 sc1\n"
  "v_mfma_f32_16x16x32_bf16 v[120:123], a[16:19], v[96:99], v[120:123]\n"
  "v_mfma_f32_16x16x32_bf16 v[124:127], a[40:43], v[96:99], v[124:127]\n"
  "v_mfma_f32_16x16x32_bf16 v[128:131], a[64:67], v[96:99], v[128:131]\n"
  "s_add_u32 s41, s42, 16384\n"
  "s_add_u32 m0, s43, 52224\n"
  "s_nop 0\n"
  "buffer_load_dwordx4 v36, s[20:23], s41 offen lds\n"
  "v_mfma_f32_16x16x32_bf16 v[132:135], a[88:91], v[96:99], v[132:135]\n"
  "v_mfma_f32_16x16x32_bf16 v[120:123], a[20:23], v[100:103], v[120:123]\n"
  "s_add_u32 s41, s42, 21504\n"
  "s_add_u32 m0, s43, 53248\n"
  "s_nop 0\n"
  "buffer_load_dwordx4 v35, s[20:23], s41 offen lds\n"
  "v_mfma_f32_16x16x32_bf16 v[124:127], a[44:47], v[100:103], v[124:127]\n"
  "v_mfma_f32_16x16x32_bf16 v[128:131], a[68:71], v[100:103], v[128:131]\n"
  "s_add_u32 s41, s42, 26624\n"
  "s_add_u32 m0, s43, 54272\n"
  "s_nop 0\n"
  "buffer_load_dwordx4 v35, s[20:23], s41 offen lds\n"
  "v_mfma_f32_16x16x32_bf16 v[132:135], a[92:95], v[100:103], v[132:135]\n"
  "s_waitcnt lgkmcnt(10)\n"
  "v_mfma_f32_16x16x32_bf16 v[104:107], a[0:3], v[56:59], 0\n"
  "ds_read_b64_tr_b16 v[80:81], v51 offset:0\n"
  "v_mfma_f32_16x16x32_bf16 v[108:111], a[24:27], v[56:59], 0\n"
  "ds_read_b64_tr_b16 v[82:83], v51 offset:1024\n"
  "v_mfma_f32_16x16x32_bf16 v[112:115], a[48:51], v[56:59], 0\n"
  "ds_read_b64_tr_b16 v[84:85], v51 offset:8192\n"
  "v_mfma_f32_16x16x32_bf16 v[116:119], a[72:75], v[56:59], 0\n"
  "ds_read_b64_tr_b16 v[86:87], v51 offset:9216\n"
  "s_waitcnt lgkmcnt(12)\n"
  "v_mfma_f32_16x16x32_bf16 v[104:107], a[4:7], v[60:63], v[104:107]\n"
  "ds_read_b64_tr_b16 v[88:89], v51 offset:16384\n"
  "v_mfma_f32_16x16x32_bf16 v[108:111], a[28:31], v[60:63], v[108:111]\n"
  "ds_read_b64_tr_b16 v[90:91], v51 offset:17408\n"
  "v_mfma_f32_16x16x32_bf16 v[112:115], a[52:55], v[60:63], v[112:115]\n"
  "s_waitcnt lgkmcnt(10)\n"
  "ds_read_b64_tr_b16 v[92:93], v51 offset:24576\n"
  "v_mfma_f32_16x16x32_bf16 v[116:119], a[76:79], v[60:63], v[116:119]\n"
  "ds_read_b64_tr_b16 v[94:95], v51 offset:25600\n"
  "v_mfma_f32_16x16x32_bf16 v[104:107], a[8:11], v[64:67], v[104:107]\n"
  "ds_read_b64_tr_b16 v[96:97], v51 offset:32768\n"
  "v_cvt_pk_bf16_f32 v120, v120, v121\n"
  "v_mfma_f32_16x16x32_bf16 v[108:111], a[32:35], v[64:67], v[108:111]\n"
  "ds_read_b64_tr_b16 v[98:99], v51 offset:33792\n"
  "v_cvt_pk_bf16_f32 v121, v122, v123\n"
  "v_mfma_f32_16x16x32_bf16 v[112:115], a[56:59], v[64:67], v[112:115]\n"
  "s_waitcnt lgkmcnt(10)\n"
  "ds_read_b64_tr_b16 v[100:101], v51 offset:40960\n"
  "v_cvt_pk_bf16_f32 v122, v124, v125\n"
  "v_mfma_f32_16x16x32_bf16 v[116:119], a[80:83], v[64:67], v[116:119]\n"
  "ds_read_b64_tr_b16 v[102:103], v51 offset:41984\n"
  "v_cvt_pk_bf16_f32 v123, v126, v127\n"
  "v_mfma_f32_16x16x32_bf16 v[104:107], a[12:15], v[68:71], v[104:107]\n"
  "v_cvt_pk_bf16_f32 v128, v128, v129\n"
  "v_cvt_pk_bf16_f32 v129, v130, v131\n"
  "v_mfma_f32_16x16x32_bf16 v[108:111], a[36:39], v[68:71], v[108:111]\n"
  "v_cvt_pk_bf16_f32 v130, v132, v133\n"
  "v_cvt_pk_bf16_f32 v131, v134, v135\n"
  "v_mfma_f32_16x16x32_bf16 v[112:115], a[60:63], v[68:71], v[112:115]\n"
  "s_add_u32 s40, s28, 11141120\n"
  "buffer_store_dwordx4 v[120:123], v43, s[24:27], s40 offen sc1\n"
  "v_mfma_f32_16x16x32_bf16 v[116:119], a[84:87], v[68:71], v[116:119]\n"
  "buffer_store_dwordx4 v[128:131], v43, s[24:27], s40 offen offset:1024 sc1\n"
  "v_mfma_f32_16x16x32_bf16 v[104:107], a[16:19], v[72:75], v[104:107]\n"
  "v_mfma_f32_16x16x32_bf16 v[108:111], a[40:43], v[72:75], v[108:111]\n"
  "v_mfma_f32_16x16x32_bf16 v[112:115], a[64:67], v[72:75], v[112:115]\n"
  "s_add_u32 s41, s42, 31744\n"
  "s_add_u32 m0, s43, 55296\n"
  "s_nop 0\n"
  "buffer_load_dwordx4 v36, s[20:23], s41 offen lds\n"
  "v_mfma_f32_16x16x32_bf16 v[116:119], a[88:91], v[72:75], v[116:119]\n"
  "v_mfma_f32_16x16x32_bf16 v[104:107], a[20:23], v[76:79], v[104:107]\n"
  "s_add_u32 s41, s42, 36864\n"
  "s_add_u32 m0, s43, 56320\n"
  "s_nop 0\n"
  "buffer_load_dwordx4 v36, s[20:23], s41 offen lds\n"
  "v_mfma_f32_16x16x32_bf16 v[108:111], a[44:47], v[76:79], v[108:111]\n"
  "v_mfma_f32_16x16x32_bf16 v[112:115], a[68:71], v[76:79], v[112:115]\n"
  "s_add_u32 s41, s42, 41984\n"
  "s_add_u32 m0, s43, 57344\n"
  "s_nop 0\n"
  "buffer_load_dwordx4 v35, s[20:23], s41 offen lds\n"
  "v_mfma_f32_16x16x32_bf16 v[116:119], a[92:95], v[76:79], v[116:119]\n"
  "s_waitcnt lgkmcnt(10)\n"
  "v_mfma_f32_16x16x32_bf16 v[120:123], a[0:3], v[80:83], 0\n"
  "v_mfma_f32_16x16x32_bf16 v[124:127], a[24:27], v[80:83], 0\n"
  "v_mfma_f32_16x16x32_bf16 v[128:131], a[48:51], v[80:83], 0\n"
  "v_mfma_f32_16x16x32_bf16 v[132:135], a[72:75], v[80:83], 0\n"
  "s_waitcnt lgkmcnt(8)\n"
  "v_mfma_f32_16x16x32_bf16 v[120:123], a[4:7], v[84:87], v[120:123]\n"
  "v_mfma_f32_16x16x32_bf16 v[124:127], a[28:31], v[84:87], v[124:127]\n"
  "v_mfma_f32_16x16x32_bf16 v[128:131], a[52:55], v[84:87], v[128:131]\n"
  "v_mfma_f32_16x16x32_bf16 v[132:135], a[76:79], v[84:87], v[132:135]\n"
  "s_waitcnt lgkmcnt(6)\n"
  "v_mfma_f32_16x16x32_bf16 v[120:123], a[8:11], v[88:91], v[120:123]\n"
  "v_cvt_pk_bf16_f32 v104, v104, v105\n"
  "v_mfma_f32_16x16x32_bf16 v[124:127], a[32:35], v[88:91], v[124:127]\n"
  "v_cvt_pk_bf16_f32 v105, v106, v107\n"
  "v_mfma_f32_16x16x32_bf16 v[128:131], a[56:59], v[88:91], v[128:131]\n"
  "v_cvt_pk_bf16_f32 v106, v108, v109\n"
  "v_mfma_f32_16x16x32_bf16 v[132:135], a[80:83], v[88:91], v[132:135]\n"
  "v_cvt_pk_bf16_f32 v107, v110, v111\n"
  "s_waitcnt lgkmcnt(4)\n"
  "v_mfma_f32_16x16x32_bf16 v[120:123], a[12:15], v[92:95], v[120:123]\n"
  "v_cvt_pk_bf16_f32 v112, v112, v113\n"
  "v_cvt_pk_bf16_f32 v113, v114, v115\n"
  "v_mfma_f32_16x16x32_bf16 v[124:127], a[36:39], v[92:95], v[124:127]\n"
  "v_cvt_pk_bf16_f32 v114, v116, v117\n"
  "v_cvt_pk_bf16_f32 v115, v118, v119\n"
  "v_mfma_f32_16x16x32_bf16 v[128:131], a[60:63], v[92:95], v[128:131]\n"
  "s_add_u32 s40, s28, 11796480\n"
  "buffer_store_dwordx4 v[104:107], v43, s[24:27], s40 offen sc1\n"
  "v_mfma_f32_16x16x32_bf16 v[132:135], a[84:87], v[92:95], v[132:135]\n"
  "buffer_store_dwordx4 v[112:115], v43, s[24:27], s40 offen offset:1024 sc1\n"
  "s_waitcnt lgkmcnt(2)\n"
  "v_mfma_f32_16x16x32_bf16 v[120:123], a[16:19], v[96:99], v[120:123]\n"
  "v_mfma_f32_16x16x32_bf16 v[124:127], a[40:43], v[96:99], v[124:127]\n"
  "v_mfma_f32_16x16x32_bf16 v[128:131], a[64:67], v[96:99], v[128:131]\n"
  "s_add_u32 s41, s42, 47104\n"
  "s_add_u32 m0, s43, 58368\n"
  "s_nop 0\n"
  "buffer_load_dwordx4 v35, s[20:23], s41 offen lds\n"
  "v_mfma_f32_16x16x32_bf16 v[132:135], a[88:91], v[96:99], v[132:135]\n"
  "s_waitcnt lgkmcnt(0)\n"
  "v_mfma_f32_16x16x32_bf16 v[120:123], a[20:23], v[100:103], v[120:123]\n"
  "s_add_u32 s41, s42, 52224\n"
  "s_add_u32 m0, s43, 59392\n"
  "s_nop 0\n"
  "buffer_load_dwordx4 v36, s[20:23], s41 offen lds\n"
  "v_mfma_f32_16x16x32_bf16 v[124:127], a[44:47], v[100:103], v[124:127]\n"
  "v_mfma_f32_16x16x32_bf16 v[128:131], a[68:71], v[100:103], v[128:131]\n"
  "s_add_u32 s41, s42, 57344\n"
  "s_add_u32 m0, s43, 60416\n"
  "s_nop 0\n"
  "buffer_load_dwordx4 v36, s[20:23], s41 offen lds\n"
  "v_mfma_f32_16x16x32_bf16 v[132:135], a[92:95], v[100:103], v[132:135]\n"
  "s_waitcnt vmcnt(20)\n"
  "s_barrier\n"
  "v_add_u32 v48, 0, v39\n"
  "v_add_u32 v49, 0, v40\n"
  "v_add_u32 v50, 0, v41\n"
  "v_add_u32 v51, 0, v42\n"
  "ds_read_b64_tr_b16 v[56:57], v48 offset:0\n"
  "ds_read_b64_tr_b16 v[58:59], v48 offset:1024\n"
  "ds_read_b64_tr_b16 v[60:61], v48 offset:8192\n"
  "ds_read_b64_tr_b16 v[62:63], v48 offset:9216\n"
  "ds_read_b64_tr_b16 v[64:65], v48 offset:16384\n"
  "ds_read_b64_tr_b16 v[66:67], v48 offset:17408\n"
  "ds_read_b64_tr_b16 v[68:69], v48 offset:24576\n"
  "ds_read_b64_tr_b16 v[70:71], v48 offset:25600\n"
  "ds_read_b64_tr_b16 v[72:73], v48 offset:32768\n"
  "ds_read_b64_tr_b16 v[74:75], v48 offset:33792\n"
  "ds_read_b64_tr_b16 v[76:77], v48 offset:40960\n"
  "ds_read_b64_tr_b16 v[78:79], v48 offset:41984\n"
  "s_waitcnt lgkmcnt(10)\n"
  "v_mfma_f32_16x16x32_bf16 v[104:107], a[0:3], v[56:59], 0\n"
  "ds_read_b64_tr_b16 v[80:81], v49 offset:0\n"
  "v_mfma_f32_16x16x32_bf16 v[108:111], a[24:27], v[56:59], 0\n"
  "ds_read_b64_tr_b16 v[82:83], v49 offset:1024\n"
  "v_mfma_f32_16x16x32_bf16 v[112:115], a[48:51], v[56:59], 0\n"
  "ds_read_b64_tr_b16 v[84:85], v49 offset:8192\n"
  "v_mfma_f32_16x16x32_bf16 v[116:119], a[72:75], v[56:59], 0\n"
  "ds_read_b64_tr_b16 v[86:87], v49 offset:9216\n"
  "s_waitcnt lgkmcnt(12)\n"
  "v_mfma_f32_16x16x32_bf16 v[104:107], a[4:7], v[60:63], v[104:107]\n"
  "ds_read_b64_tr_b16 v[88:89], v49 offset:16384\n"
  "v_mfma_f32_16x16x32_bf16 v[108:111], a[28:31], v[60:63], v[108:111]\n"
  "ds_read_b64_tr_b16 v[90:91], v49 offset:17408\n"
  "v_mfma_f32_16x16x32_bf16 v[112:115], a[52:55], v[60:63], v[112:115]\n"
  "s_waitcnt lgkmcnt(10)\n"
  "ds_read_b64_tr_b16 v[92:93], v49 offset:24576\n"
  "v_mfma_f32_16x16x32_bf16 v[116:119], a[76:79], v[60:63], v[116:119]\n"
  "ds_read_b64_tr_b16 v[94:95], v49 offset:25600\n"
  "v_mfma_f32_16x16x32_bf16 v[104:107], a[8:11], v[64:67], v[104:107]\n"
  "ds_read_b64_tr_b16 v[96:97], v49 offset:32768\n"
  "v_cvt_pk_bf16_f32 v120, v120, v121\n"
  "v_mfma_f32_16x16x32_bf16 v[108:111], a[32:35], v[64:67], v[108:111]\n"
  "ds_read_b64_tr_b16 v[98:99], v49 offset:33792\n"
  "v_cvt_pk_bf16_f32 v121, v122, v123\n"
  "v_mfma_f32_16x16x32_bf16 v[112:115], a[56:59], v[64:67], v[112:115]\n"
  "s_waitcnt lgkmcnt(10)\n"
  "ds_read_b64_tr_b16 v[100:101], v49 offset:40960\n"
  "v_cvt_pk_bf16_f32 v122, v124, v125\n"
  "v_mfma_f32_16x16x32_bf16 v[116:119], a[80:83], v[64:67], v[116:119]\n"
  "ds_read_b64_tr_b16 v[102:103], v49 offset:41984\n"
  "v_cvt_pk_bf16_f32 v123, v126, v127\n"
  "v_mfma_f32_16x16x32_bf16 v[104:107], a[12:15], v[68:71], v[104:107]\n"
  "v_cvt_pk_bf16_f32 v128, v128, v129\n"
  "v_cvt_pk_bf16_f32 v129, v130, v131\n"
  "v_mfma_f32_16x16x32_bf16 v[108:111], a[36:39], v[68:71], v[108:111]\n"
  "v_cvt_pk_bf16_f32 v130, v132, v133\n"
  "v_cvt_pk_bf16_f32 v131, v134, v135\n"
  "v_mfma_f32_16x16x32_bf16 v[112:115], a[60:63], v[68:71], v[112:115]\n"
  "s_add_u32 s40, s28, 12451840\n"
  "buffer_store_dwordx4 v[120:123], v43, s[24:27], s40 offen sc1\n"
  "v_mfma_f32_16x16x32_bf16 v[116:119], a[84:87], v[68:71], v[116:119]\n"
  "buffer_store_dwordx4 v[128:131], v43, s[24:27], s40 offen offset:1024 sc1\n"
  "v_mfma_f32_16x16x32_bf16 v[104:107], a[16:19], v[72:75], v[104:107]\n"
  "v_mfma_f32_16x16x32_bf16 v[108:111], a[40:43], v[72:75], v[108:111]\n"
  "v_mfma_f32_16x16x32_bf16 v[112:115], a[64:67], v[72:75], v[112:115]\n"
  "v_mfma_f32_16x16x32_bf16 v[116:119], a[88:91], v[72:75], v[116:119]\n"
  "v_mfma_f32_16x16x32_bf16 v[104:107], a[20:23], v[76:79], v[104:107]\n"
  "v_mfma_f32_16x16x32_bf16 v[108:111], a[44:47], v[76:79], v[108:111]\n"
  "v_mfma_f32_16x16x32_bf16 v[112:115], a[68:71], v[76:79], v[112:115]\n"
  "v_mfma_f32_16x16x32_bf16 v[116:119], a[92:95], v[76:79], v[116:119]\n"
  "s_waitcnt lgkmcnt(10)\n"
  "v_mfma_f32_16x16x32_bf16 v[120:123], a[0:3], v[80:83], 0\n"
  "ds_read_b64_tr_b16 v[56:57], v50 offset:0\n"
  "v_mfma_f32_16x16x32_bf16 v[124:127], a[24:27], v[80:83], 0\n"
  "ds_read_b64_tr_b16 v[58:59], v50 offset:1024\n"
  "v_mfma_f32_16x16x32_bf16 v[128:131], a[48:51], v[80:83], 0\n"
  "ds_read_b64_tr_b16 v[60:61], v50 offset:8192\n"
  "v_mfma_f32_16x16x32_bf16 v[132:135], a[72:75], v[80:83], 0\n"
  "ds_read_b64_tr_b16 v[62:63], v50 offset:9216\n"
  "s_waitcnt lgkmcnt(12)\n"
  "v_mfma_f32_16x16x32_bf16 v[120:123], a[4:7], v[84:87], v[120:123]\n"
  "ds_read_b64_tr_b16 v[64:65], v50 offset:16384\n"
  "v_mfma_f32_16x16x32_bf16 v[124:127], a[28:31], v[84:87], v[124:127]\n"
  "ds_read_b64_tr_b16 v[66:67], v50 offset:17408\n"
  "v_mfma_f32_16x16x32_bf16 v[128:131], a[52:55], v[84:87], v[128:131]\n"
  "s_waitcnt lgkmcnt(10)\n"
  "ds_read_b64_tr_b16 v[68:69], v50 offset:24576\n"
  "v_mfma_f32_16x16x32_bf16 v[132:135], a[76:79], v[84:87], v[132:135]\n"
  "ds_read_b64_tr_b16 v[70:71], v50 offset:25600\n"
  "v_mfma_f32_16x16x32_bf16 v[120:123], a[8:11], v[88:91], v[120:123]\n"
  "ds_read_b64_tr_b16 v[72:73], v50 offset:32768\n"
  "v_cvt_pk_bf16_f32 v104, v104, v105\n"
  "v_mfma_f32_16x16x32_bf16 v[124:127], a[32:35], v[88:91], v[124:127]\n"
  "ds_read_b64_tr_b16 v[74:75], v50 offset:33792\n"
  "v_cvt_pk_bf16_f32 v105, v106, v107\n"
  "v_mfma_f32_16x16x32_bf16 v[128:131], a[56:59], v[88:91], v[128:131]\n"
  "s_waitcnt lgkmcnt(10)\n"
  "ds_read_b64_tr_b16 v[76:77], v50 offset:40960\n"
  "v_cvt_pk_bf16_f32 v106, v108, v109\n"
  "v_mfma_f32_16x16x32_bf16 v[132:135], a[80:83], v[88:91], v[132:135]\n"
  "ds_read_b64_tr_b16 v[78:79], v50 offset:41984\n"
  "v_cvt_pk_bf16_f32 v107, v110, v111\n"
  "v_mfma_f32_16x16x32_bf16 v[120:123], a[12:15], v[92:95], v[120:123]\n"
  "v_cvt_pk_bf16_f32 v112, v112, v113\n"
  "v_cvt_pk_bf16_f32 v113, v114, v115\n"
  "v_mfma_f32_16x16x32_bf16 v[124:127], a[36:39], v[92:95], v[124:127]\n"
  "v_cvt_pk_bf16_f32 v114, v116, v117\n"
  "v_cvt_pk_bf16_f32 v115, v118, v119\n"
  "v_mfma_f32_16x16x32_bf16 v[128:131], a[60:63], v[92:95], v[128:131]\n"
  "s_add_u32 s40, s28, 15728640\n"
  "buffer_store_dwordx4 v[104:107], v43, s[24:27], s40 offen sc1\n"
  "v_mfma_f32_16x16x32_bf16 v[132:135], a[84:87], v[92:95], v[132:135]\n"
  "buffer_store_dwordx4 v[112:115], v43, s[24:27], s40 offen offset:1024 sc1\n"
  "v_mfma_f32_16x16x32_bf16 v[120:123], a[16:19], v[96:99], v[120:123]\n"
  "v_mfma_f32_16x16x32_bf16 v[124:127], a[40:43], v[96:99], v[124:127]\n"
  "v_mfma_f32_16x16x32_bf16 v[128:131], a[64:67], v[96:99], v[128:131]\n"
  "v_mfma_f32_16x16x32_bf16 v[132:135], a[88:91], v[96:99], v[132:135]\n"
  "v_mfma_f32_16x16x32_bf16 v[120:123], a[20:23], v[100:103], v[120:123]\n"
  "v_mfma_f32_16x16x32_bf16 v[124:127], a[44:47], v[100:103], v[124:127]\n"
  "v_mfma_f32_16x16x32_bf16 v[128:131], a[68:71], v[100:103], v[128:131]\n"
  "v_mfma_f32_16x16x32_bf16 v[132:135], a[92:95], v[100:103], v[132:135]\n"
  "s_waitcnt lgkmcnt(10)\n"
  "v_mfma_f32_16x16x32_bf16 v[104:107], a[0:3], v[56:59], 0\n"
  "ds_read_b64_tr_b16 v[80:81], v51 offset:0\n"
  "v_mfma_f32_16x16x32_bf16 v[108:111], a[24:27], v[56:59], 0\n"
  "ds_read_b64_tr_b16 v[82:83], v51 offset:1024\n"
  "v_mfma_f32_16x16x32_bf16 v[112:115], a[48:51], v[56:59], 0\n"
  "ds_read_b64_tr_b16 v[84:85], v51 offset:8192\n"
  "v_mfma_f32_16x16x32_bf16 v[116:119], a[72:75], v[56:59], 0\n"
  "ds_read_b64_tr_b16 v[86:87], v51 offset:9216\n"
  "s_waitcnt lgkmcnt(12)\n"
  "v_mfma_f32_16x16x32_bf16 v[104:107], a[4:7], v[60:63], v[104:107]\n"
  "ds_read_b64_tr_b16 v[88:89], v51 offset:16384\n"
  "v_mfma_f32_16x16x32_bf16 v[108:111], a[28:31], v[60:63], v[108:111]\n"
  "ds_read_b64_tr_b16 v[90:91], v51 offset:17408\n"
  "v_mfma_f32_16x16x32_bf16 v[112:115], a[52:55], v[60:63], v[112:115]\n"
  "s_waitcnt lgkmcnt(10)\n"
  "ds_read_b64_tr_b16 v[92:93], v51 offset:24576\n"
  "v_mfma_f32_16x16x32_bf16 v[116:119], a[76:79], v[60:63], v[116:119]\n"
  "ds_read_b64_tr_b16 v[94:95], v51 offset:25600\n"
  "v_mfma_f32_16x16x32_bf16 v[104:107], a[8:11], v[64:67], v[104:107]\n"
  "ds_read_b64_tr_b16 v[96:97], v51 offset:32768\n"
  "v_cvt_pk_bf16_f32 v120, v120, v121\n"
  "v_mfma_f32_16x16x32_bf16 v[108:111], a[32:35], v[64:67], v[108:111]\n"
  "ds_read_b64_tr_b16 v[98:99], v51 offset:33792\n"
  "v_cvt_pk_bf16_f32 v121, v122, v123\n"
  "v_mfma_f32_16x16x32_bf16 v[112:115], a[56:59], v[64:67], v[112:115]\n"
  "s_waitcnt lgkmcnt(10)\n"
  "ds_read_b64_tr_b16 v[100:101], v51 offset:40960\n"
  "v_cvt_pk_bf16_f32 v122, v124, v125\n"
  "v_mfma_f32_16x16x32_bf16 v[116:119], a[80:83], v[64:67], v[116:119]\n"
  "ds_read_b64_tr_b16 v[102:103], v51 offset:41984\n"
  "v_cvt_pk_bf16_f32 v123, v126, v127\n"
  "v_mfma_f32_16x16x32_bf16 v[104:107], a[12:15], v[68:71], v[104:107]\n"
  "v_cvt_pk_bf16_f32 v128, v128, v129\n"
  "v_cvt_pk_bf16_f32 v129, v130, v131\n"
  "v_mfma_f32_16x16x32_bf16 v[108:111], a[36:39], v[68:71], v[108:111]\n"
  "v_cvt_pk_bf16_f32 v130, v132, v133\n"
  "v_cvt_pk_bf16_f32 v131, v134, v135\n"
  "v_mfma_f32_16x16x32_bf16 v[112:115], a[60:63], v[68:71], v[112:115]\n"
  "s_add_u32 s40, s28, 16384000\n"
  "buffer_store_dwordx4 v[120:123], v43, s[24:27], s40 offen sc1\n"
  "v_mfma_f32_16x16x32_bf16 v[116:119], a[84:87], v[68:71], v[116:119]\n"
  "buffer_store_dwordx4 v[128:131], v43, s[24:27], s40 offen offset:1024 sc1\n"
  "v_mfma_f32_16x16x32_bf16 v[104:107], a[16:19], v[72:75], v[104:107]\n"
  "v_mfma_f32_16x16x32_bf16 v[108:111], a[40:43], v[72:75], v[108:111]\n"
  "v_mfma_f32_16x16x32_bf16 v[112:115], a[64:67], v[72:75], v[112:115]\n"
  "v_mfma_f32_16x16x32_bf16 v[116:119], a[88:91], v[72:75], v[116:119]\n"
  "v_mfma_f32_16x16x32_bf16 v[104:107], a[20:23], v[76:79], v[104:107]\n"
  "v_mfma_f32_16x16x32_bf16 v[108:111], a[44:47], v[76:79], v[108:111]\n"
  "v_mfma_f32_16x16x32_bf16 v[112:115], a[68:71], v[76:79], v[112:115]\n"
  "v_mfma_f32_16x16x32_bf16 v[116:119], a[92:95], v[76:79], v[116:119]\n"
  "s_waitcnt lgkmcnt(10)\n"
  "v_mfma_f32_16x16x32_bf16 v[120:123], a[0:3], v[80:83], 0\n"
  "v_mfma_f32_16x16x32_bf16 v[124:127], a[24:27], v[80:83], 0\n"
  "v_mfma_f32_16x16x32_bf16 v[128:131], a[48:51], v[80:83], 0\n"
  "v_mfma_f32_16x16x32_bf16 v[132:135], a[72:75], v[80:83], 0\n"
  "s_waitcnt lgkmcnt(8)\n"
  "v_mfma_f32_16x16x32_bf16 v[120:123], a[4:7], v[84:87], v[120:123]\n"
  "v_mfma_f32_16x16x32_bf16 v[124:127], a[28:31], v[84:87], v[124:127]\n"
  "v_mfma_f32_16x16x32_bf16 v[128:131], a[52:55], v[84:87], v[128:131]\n"
  "v_mfma_f32_16x16x32_bf16 v[132:135], a[76:79], v[84:87], v[132:135]\n"
  "s_waitcnt lgkmcnt(6)\n"
  "v_mfma_f32_16x16x32_bf16 v[120:123], a[8:11], v[88:91], v[120:123]\n"
  "v_cvt_pk_bf16_f32 v104, v104, v105\n"
  "v_mfma_f32_16x16x32_bf16 v[124:127], a[32:35], v[88:91], v[124:127]\n"
  "v_cvt_pk_bf16_f32 v105, v106, v107\n"
  "v_mfma_f32_16x16x32_bf16 v[128:131], a[56:59], v[88:91], v[128:131]\n"
  "v_cvt_pk_bf16_f32 v106, v108, v109\n"
  "v_mfma_f32_16x16x32_bf16 v[132:135], a[80:83], v[88:91], v[132:135]\n"
  "v_cvt_pk_bf16_f32 v107, v110, v111\n"
  "s_waitcnt lgkmcnt(4)\n"
  "v_mfma_f32_16x16x32_bf16 v[120:123], a[12:15], v[92:95], v[120:123]\n"
  "v_cvt_pk_bf16_f32 v112, v112, v113\n"
  "v_cvt_pk_bf16_f32 v113, v114, v115\n"
  "v_mfma_f32_16x16x32_bf16 v[124:127], a[36:39], v[92:95], v[124:127]\n"
  "v_cvt_pk_bf16_f32 v114, v116, v117\n"
  "v_cvt_pk_bf16_f32 v115, v118, v119\n"
  "v_mfma_f32_16x16x32_bf16 v[128:131], a[60:63], v[92:95], v[128:131]\n"
  "s_add_u32 s40, s28, 17039360\n"
  "buffer_store_dwordx4 v[104:107], v43, s[24:27], s40 offen sc1\n"
  "v_mfma_f32_16x16x32_bf16 v[132:135], a[84:87], v[92:95], v[132:135]\n"
  "buffer_store_dwordx4 v[112:115], v43, s[24:27], s40 offen offset:1024 sc1\n"
  "s_waitcnt lgkmcnt(2)\n"
  "v_mfma_f32_16x16x32_bf16 v[120:123], a[16:19], v[96:99], v[120:123]\n"
  "v_mfma_f32_16x16x32_bf16 v[124:127], a[40:43], v[96:99], v[124:127]\n"
  "v_mfma_f32_16x16x32_bf16 v[128:131], a[64:67], v[96:99], v[128:131]\n"
  "v_mfma_f32_16x16x32_bf16 v[132:135], a[88:91], v[96:99], v[132:135]\n"
  "s_waitcnt lgkmcnt(0)\n"
  "v_mfma_f32_16x16x32_bf16 v[120:123], a[20:23], v[100:103], v[120:123]\n"
  "v_mfma_f32_16x16x32_bf16 v[124:127], a[44:47], v[100:103], v[124:127]\n"
  "v_mfma_f32_16x16x32_bf16 v[128:131], a[68:71], v[100:103], v[128:131]\n"
  "v_mfma_f32_16x16x32_bf16 v[132:135], a[92:95], v[100:103], v[132:135]\n"
  "s_waitcnt vmcnt(8)\n"
  "s_barrier\n"
  "v_add_u32 v48, 49152, v39\n"
  "v_add_u32 v49, 49152, v40\n"
  "v_add_u32 v50, 49152, v41\n"
  "v_add_u32 v51, 49152, v42\n"
  "ds_read_b64_tr_b16 v[56:57], v48 offset:0\n"
  "ds_read_b64_tr_b16 v[58:59], v48 offset:1024\n"
  "ds_read_b64_tr_b16 v[60:61], v48 offset:8192\n"
  "ds_read_b64_tr_b16 v[62:63], v48 offset:9216\n"
  "ds_read_b64_tr_b16 v[64:65], v48 offset:16384\n"
  "ds_read_b64_tr_b16 v[66:67], v48 offset:17408\n"
  "ds_read_b64_tr_b16 v[68:69], v48 offset:24576\n"
  "ds_read_b64_tr_b16 v[70:71], v48 offset:25600\n"
  "ds_read_b64_tr_b16 v[72:73], v48 offset:32768\n"
  "ds_read_b64_tr_b16 v[74:75], v48 offset:33792\n"
  "ds_read_b64_tr_b16 v[76:77], v48 offset:40960\n"
  "ds_read_b64_tr_b16 v[78:79], v48 offset:41984\n"
  "s_waitcnt lgkmcnt(10)\n"
  "v_mfma_f32_16x16x32_bf16 v[104:107], a[0:3], v[56:59], 0\n"
  "ds_read_b64_tr_b16 v[80:81], v49 offset:0\n"
  "v_mfma_f32_16x16x32_bf16 v[108:111], a[24:27], v[56:59], 0\n"
  "ds_read_b64_tr_b16 v[82:83], v49 offset:1024\n"
  "v_mfma_f32_16x16x32_bf16 v[112:115], a[48:51], v[56:59], 0\n"
  "ds_read_b64_tr_b16 v[84:85], v49 offset:8192\n"
  "v_mfma_f32_16x16x32_bf16 v[116:119], a[72:75], v[56:59], 0\n"
  "ds_read_b64_tr_b16 v[86:87], v49 offset:9216\n"
  "s_waitcnt lgkmcnt(12)\n"
  "v_mfma_f32_16x16x32_bf16 v[104:107], a[4:7], v[60:63], v[104:107]\n"
  "ds_read_b64_tr_b16 v[88:89], v49 offset:16384\n"
  "v_mfma_f32_16x16x32_bf16 v[108:111], a[28:31], v[60:63], v[108:111]\n"
  "ds_read_b64_tr_b16 v[90:91], v49 offset:17408\n"
  "v_mfma_f32_16x16x32_bf16 v[112:115], a[52:55], v[60:63], v[112:115]\n"
  "s_waitcnt lgkmcnt(10)\n"
  "ds_read_b64_tr_b16 v[92:93], v49 offset:24576\n"
  "v_mfma_f32_16x16x32_bf16 v[116:119], a[76:79], v[60:63], v[116:119]\n"
  "ds_read_b64_tr_b16 v[94:95], v49 offset:25600\n"
  "v_mfma_f32_16x16x32_bf16 v[104:107], a[8:11], v[64:67], v[104:107]\n"
  "ds_read_b64_tr_b16 v[96:97], v49 offset:32768\n"
  "v_cvt_pk_bf16_f32 v120, v120, v121\n"
  "v_mfma_f32_16x16x32_bf16 v[108:111], a[32:35], v[64:67], v[108:111]\n"
  "ds_read_b64_tr_b16 v[98:99], v49 offset:33792\n"
  "v_cvt_pk_bf16_f32 v121, v122, v123\n"
  "v_mfma_f32_16x16x32_bf16 v[112:115], a[56:59], v[64:67], v[112:115]\n"
  "s_waitcnt lgkmcnt(10)\n"
  "ds_read_b64_tr_b16 v[100:101], v49 offset:40960\n"
  "v_cvt_pk_bf16_f32 v122, v124, v125\n"
  "v_mfma_f32_16x16x32_bf16 v[116:119], a[80:83], v[64:67], v[116:119]\n"
  "ds_read_b64_tr_b16 v[102:103], v49 offset:41984\n"
  "v_cvt_pk_bf16_f32 v123, v126, v127\n"
  "v_mfma_f32_16x16x32_bf16 v[104:107], a[12:15], v[68:71], v[104:107]\n"
  "v_cvt_pk_bf16_f32 v128, v128, v129\n"
  "v_cvt_pk_bf16_f32 v129, v130, v131\n"
  "v_mfma_f32_16x16x32_bf16 v[108:111], a[36:39], v[68:71], v[108:111]\n"
  "v_cvt_pk_bf16_f32 v130, v132, v133\n"
  "v_cvt_pk_bf16_f32 v131, v134, v135\n"
  "v_mfma_f32_16x16x32_bf16 v[112:115], a[60:63], v[68:71], v[112:115]\n"
  "s_add_u32 s40, s28, 17694720\n"
  "buffer_store_dwordx4 v[120:123], v43, s[24:27], s40 offen sc1\n"
  "v_mfma_f32_16x16x32_bf16 v[116:119], a[84:87], v[68:71], v[116:119]\n"
  "buffer_store_dwordx4 v[128:131], v43, s[24:27], s40 offen offset:1024 sc1\n"
  "v_mfma_f32_16x16x32_bf16 v[104:107], a[16:19], v[72:75], v[104:107]\n"
  "v_mfma_f32_16x16x32_bf16 v[108:111], a[40:43], v[72:75], v[108:111]\n"
  "v_mfma_f32_16x16x32_bf16 v[112:115], a[64:67], v[72:75], v[112:115]\n"
  "v_mfma_f32_16x16x32_bf16 v[116:119], a[88:91], v[72:75], v[116:119]\n"
  "v_mfma_f32_16x16x32_bf16 v[104:107], a[20:23], v[76:79], v[104:107]\n"
  "v_mfma_f32_16x16x32_bf16 v[108:111], a[44:47], v[76:79], v[108:111]\n"
  "v_mfma_f32_16x16x32_bf16 v[112:115], a[68:71], v[76:79], v[112:115]\n"
  "v_mfma_f32_16x16x32_bf16 v[116:119], a[92:95], v[76:79], v[116:119]\n"
  "s_waitcnt lgkmcnt(10)\n"
  "v_mfma_f32_16x16x32_bf16 v[120:123], a[0:3], v[80:83], 0\n"
  "ds_read_b64_tr_b16 v[56:57], v50 offset:0\n"
  "v_mfma_f32_16x16x32_bf16 v[124:127], a[24:27], v[80:83], 0\n"
  "ds_read_b64_tr_b16 v[58:59], v50 offset:1024\n"
  "v_mfma_f32_16x16x32_bf16 v[128:131], a[48:51], v[80:83], 0\n"
  "ds_read_b64_tr_b16 v[60:61], v50 offset:8192\n"
  "v_mfma_f32_16x16x32_bf16 v[132:135], a[72:75], v[80:83], 0\n"
  "ds_read_b64_tr_b16 v[62:63], v50 offset:9216\n"
  "s_waitcnt lgkmcnt(12)\n"
  "v_mfma_f32_16x16x32_bf16 v[120:123], a[4:7], v[84:87], v[120:123]\n"
  "ds_read_b64_tr_b16 v[64:65], v50 offset:16384\n"
  "v_mfma_f32_16x16x32_bf16 v[124:127], a[28:31], v[84:87], v[124:127]\n"
  "ds_read_b64_tr_b16 v[66:67], v50 offset:17408\n"
  "v_mfma_f32_16x16x32_bf16 v[128:131], a[52:55], v[84:87], v[128:131]\n"
  "s_waitcnt lgkmcnt(10)\n"
  "ds_read_b64_tr_b16 v[68:69], v50 offset:24576\n"
  "v_mfma_f32_16x16x32_bf16 v[132:135], a[76:79], v[84:87], v[132:135]\n"
  "ds_read_b64_tr_b16 v[70:71], v50 offset:25600\n"
  "v_mfma_f32_16x16x32_bf16 v[120:123], a[8:11], v[88:91], v[120:123]\n"
  "ds_read_b64_tr_b16 v[72:73], v50 offset:32768\n"
  "v_cvt_pk_bf16_f32 v104, v104, v105\n"
  "v_mfma_f32_16x16x32_bf16 v[124:127], a[32:35], v[88:91], v[124:127]\n"
  "ds_read_b64_tr_b16 v[74:75], v50 offset:33792\n"
  "v_cvt_pk_bf16_f32 v105, v106, v107\n"
  "v_mfma_f32_16x16x32_bf16 v[128:131], a[56:59], v[88:91], v[128:131]\n"
  "s_waitcnt lgkmcnt(10)\n"
  "ds_read_b64_tr_b16 v[76:77], v50 offset:40960\n"
  "v_cvt_pk_bf16_f32 v106, v108, v109\n"
  "v_mfma_f32_16x16x32_bf16 v[132:135], a[80:83], v[88:91], v[132:135]\n"
  "ds_read_b64_tr_b16 v[78:79], v50 offset:41984\n"
  "v_cvt_pk_bf16_f32 v107, v110, v111\n"
  "v_mfma_f32_16x16x32_bf16 v[120:123], a[12:15], v[92:95], v[120:123]\n"
  "v_cvt_pk_bf16_f32 v112, v112, v113\n"
  "v_cvt_pk_bf16_f32 v113, v114, v115\n"
  "v_mfma_f32_16x16x32_bf16 v[124:127], a[36:39], v[92:95], v[124:127]\n"
  "v_cvt_pk_bf16_f32 v114, v116, v117\n"
  "v_cvt_pk_bf16_f32 v115, v118, v119\n"
  "v_mfma_f32_16x16x32_bf16 v[128:131], a[60:63], v[92:95], v[128:131]\n"
  "s_add_u32 s40, s28, 20971520\n"
  "buffer_store_dwordx4 v[104:107], v43, s[24:27], s40 offen sc1\n"
  "v_mfma_f32_16x16x32_bf16 v[132:135], a[84:87], v[92:95], v[132:135]\n"
  "buffer_store_dwordx4 v[112:115], v43, s[24:27], s40 offen offset:1024 sc1\n"
  "v_mfma_f32_16x16x32_bf16 v[120:123], a[16:19], v[96:99], v[120:123]\n"
  "v_mfma_f32_16x16x32_bf16 v[124:127], a[40:43], v[96:99], v[124:127]\n"
  "v_mfma_f32_16x16x32_bf16 v[128:131], a[64:67], v[96:99], v[128:131]\n"
  "v_mfma_f32_16x16x32_bf16 v[132:135], a[88:91], v[96:99], v[132:135]\n"
  "v_mfma_f32_16x16x32_bf16 v[120:123], a[20:23], v[100:103], v[120:123]\n"
  "v_mfma_f32_16x16x32_bf16 v[124:127], a[44:47], v[100:103], v[124:127]\n"
  "v_mfma_f32_16x16x32_bf16 v[128:131], a[68:71], v[100:103], v[128:131]\n"
  "v_mfma_f32_16x16x32_bf16 v[132:135], a[92:95], v[100:103], v[132:135]\n"
  "s_waitcnt lgkmcnt(10)\n"
  "v_mfma_f32_16x16x32_bf16 v[104:107], a[0:3], v[56:59], 0\n"
  "ds_read_b64_tr_b16 v[80:81], v51 offset:0\n"
  "v_mfma_f32_16x16x32_bf16 v[108:111], a[24:27], v[56:59], 0\n"
  "ds_read_b64_tr_b16 v[82:83], v51 offset:1024\n"
  "v_mfma_f32_16x16x32_bf16 v[112:115], a[48:51], v[56:59], 0\n"
  "ds_read_b64_tr_b16 v[84:85], v51 offset:8192\n"
  "v_mfma_f32_16x16x32_bf16 v[116:119], a[72:75], v[56:59], 0\n"
  "ds_read_b64_tr_b16 v[86:87], v51 offset:9216\n"
  "s_waitcnt lgkmcnt(12)\n"
  "v_mfma_f32_16x16x32_bf16 v[104:107], a[4:7], v[60:63], v[104:107]\n"
  "ds_read_b64_tr_b16 v[88:89], v51 offset:16384\n"
  "v_mfma_f32_16x16x32_bf16 v[108:111], a[28:31], v[60:63], v[108:111]\n"
  "ds_read_b64_tr_b16 v[90:91], v51 offset:17408\n"
  "v_mfma_f32_16x16x32_bf16 v[112:115], a[52:55], v[60:63], v[112:115]\n"
  "s_waitcnt lgkmcnt(10)\n"
  "ds_read_b64_tr_b16 v[92:93], v51 offset:24576\n"
  "v_mfma_f32_16x16x32_bf16 v[116:119], a[76:79], v[60:63], v[116:119]\n"
  "ds_read_b64_tr_b16 v[94:95], v51 offset:25600\n"
  "v_mfma_f32_16x16x32_bf16 v[104:107], a[8:11], v[64:67], v[104:107]\n"
  "ds_read_b64_tr_b16 v[96:97], v51 offset:32768\n"
  "v_cvt_pk_bf16_f32 v120, v120, v121\n"
  "v_mfma_f32_16x16x32_bf16 v[108:111], a[32:35], v[64:67], v[108:111]\n"
  "ds_read_b64_tr_b16 v[98:99], v51 offset:33792\n"
  "v_cvt_pk_bf16_f32 v121, v122, v123\n"
  "v_mfma_f32_16x16x32_bf16 v[112:115], a[56:59], v[64:67], v[112:115]\n"
  "s_waitcnt lgkmcnt(10)\n"
  "ds_read_b64_tr_b16 v[100:101], v51 offset:40960\n"
  "v_cvt_pk_bf16_f32 v122, v124, v125\n"
  "v_mfma_f32_16x16x32_bf16 v[116:119], a[80:83], v[64:67], v[116:119]\n"
  "ds_read_b64_tr_b16 v[102:103], v51 offset:41984\n"
  "v_cvt_pk_bf16_f32 v123, v126, v127\n"
  "v_mfma_f32_16x16x32_bf16 v[104:107], a[12:15], v[68:71], v[104:107]\n"
  "v_cvt_pk_bf16_f32 v128, v128, v129\n"
  "v_cvt_pk_bf16_f32 v129, v130, v131\n"
  "v_mfma_f32_16x16x32_bf16 v[108:111], a[36:39], v[68:71], v[108:111]\n"
  "v_cvt_pk_bf16_f32 v130, v132, v133\n"
  "v_cvt_pk_bf16_f32 v131, v134, v135\n"
  "v_mfma_f32_16x16x32_bf16 v[112:115], a[60:63], v[68:71], v[112:115]\n"
  "s_add_u32 s40, s28, 21626880\n"
  "buffer_store_dwordx4 v[120:123], v43, s[24:27], s40 offen sc1\n"
  "v_mfma_f32_16x16x32_bf16 v[116:119], a[84:87], v[68:71], v[116:119]\n"
  "buffer_store_dwordx4 v[128:131], v43, s[24:27], s40 offen offset:1024 sc1\n"
  "v_mfma_f32_16x16x32_bf16 v[104:107], a[16:19], v[72:75], v[104:107]\n"
  "v_mfma_f32_16x16x32_bf16 v[108:111], a[40:43], v[72:75], v[108:111]\n"
  "v_mfma_f32_16x16x32_bf16 v[112:115], a[64:67], v[72:75], v[112:115]\n"
  "v_mfma_f32_16x16x32_bf16 v[116:119], a[88:91], v[72:75], v[116:119]\n"
  "v_mfma_f32_16x16x32_bf16 v[104:107], a[20:23], v[76:79], v[104:107]\n"
  "v_mfma_f32_16x16x32_bf16 v[108:111], a[44:47], v[76:79], v[108:111]\n"
  "v_mfma_f32_16x16x32_bf16 v[112:115], a[68:71], v[76:79], v[112:115]\n"
  "v_mfma_f32_16x16x32_bf16 v[116:119], a[92:95], v[76:79], v[116:119]\n"
  "s_waitcnt lgkmcnt(10)\n"
  "v_mfma_f32_16x16x32_bf16 v[120:123], a[0:3], v[80:83], 0\n"
  "v_mfma_f32_16x16x32_bf16 v[124:127], a[24:27], v[80:83], 0\n"
  "v_mfma_f32_16x16x32_bf16 v[128:131], a[48:51], v[80:83], 0\n"
  "v_mfma_f32_16x16x32_bf16 v[132:135], a[72:75], v[80:83], 0\n"
  "s_waitcnt lgkmcnt(8)\n"
  "v_mfma_f32_16x16x32_bf16 v[120:123], a[4:7], v[84:87], v[120:123]\n"
  "v_mfma_f32_16x16x32_bf16 v[124:127], a[28:31], v[84:87], v[124:127]\n"
  "v_mfma_f32_16x16x32_bf16 v[128:131], a[52:55], v[84:87], v[128:131]\n"
  "v_mfma_f32_16x16x32_bf16 v[132:135], a[76:79], v[84:87], v[132:135]\n"
  "s_waitcnt lgkmcnt(6)\n"
  "v_mfma_f32_16x16x32_bf16 v[120:123], a[8:11], v[88:91], v[120:123]\n"
  "v_cvt_pk_bf16_f32 v104, v104, v105\n"
  "v_mfma_f32_16x16x32_bf16 v[124:127], a[32:35], v[88:91], v[124:127]\n"
  "v_cvt_pk_bf16_f32 v105, v106, v107\n"
  "v_mfma_f32_16x16x32_bf16 v[128:131], a[56:59], v[88:91], v[128:131]\n"
  "v_cvt_pk_bf16_f32 v106, v108, v109\n"
  "v_mfma_f32_16x16x32_bf16 v[132:135], a[80:83], v[88:91], v[132:135]\n"
  "v_cvt_pk_bf16_f32 v107, v110, v111\n"
  "s_waitcnt lgkmcnt(4)\n"
  "v_mfma_f32_16x16x32_bf16 v[120:123], a[12:15], v[92:95], v[120:123]\n"
  "v_cvt_pk_bf16_f32 v112, v112, v113\n"
  "v_cvt_pk_bf16_f32 v113, v114, v115\n"
  "v_mfma_f32_16x16x32_bf16 v[124:127], a[36:39], v[92:95], v[124:127]\n"
  "v_cvt_pk_bf16_f32 v114, v116, v117\n"
  "v_cvt_pk_bf16_f32 v115, v118, v119\n"
  "v_mfma_f32_16x16x32_bf16 v[128:131], a[60:63], v[92:95], v[128:131]\n"
  "s_add_u32 s40, s28, 22282240\n"
  "buffer_store_dwordx4 v[104:107], v43, s[24:27], s40 offen sc1\n"
  "v_mfma_f32_16x16x32_bf16 v[132:135], a[84:87], v[92:95], v[132:135]\n"
  "buffer_store_dwordx4 v[112:115], v43, s[24:27], s40 offen offset:1024 sc1\n"
  "s_waitcnt lgkmcnt(2)\n"
  "v_mfma_f32_16x16x32_bf16 v[120:123], a[16:19], v[96:99], v[120:123]\n"
  "v_mfma_f32_16x16x32_bf16 v[124:127], a[40:43], v[96:99], v[124:127]\n"
  "v_mfma_f32_16x16x32_bf16 v[128:131], a[64:67], v[96:99], v[128:131]\n"
  "v_mfma_f32_16x16x32_bf16 v[132:135], a[88:91], v[96:99], v[132:135]\n"
  "s_waitcnt lgkmcnt(0)\n"
  "v_mfma_f32_16x16x32_bf16 v[120:123], a[20:23], v[100:103], v[120:123]\n"
  "v_mfma_f32_16x16x32_bf16 v[124:127], a[44:47], v[100:103], v[124:127]\n"
  "v_mfma_f32_16x16x32_bf16 v[128:131], a[68:71], v[100:103], v[128:131]\n"
  "v_mfma_f32_16x16x32_bf16 v[132:135], a[92:95], v[100:103], v[132:135]\n"
  "v_mov_b32 v32, v32\n"
  "v_mov_b32 v32, v32\n"
  "v_mov_b32 v32, v32\n"
  "v_mov_b32 v32, v32\n"
  "v_mov_b32 v32, v32\n"
  "v_cvt_pk_bf16_f32 v120, v120, v121\n"
  "v_cvt_pk_bf16_f32 v121, v122, v123\n"
  "v_cvt_pk_bf16_f32 v122, v124, v125\n"
  "v_cvt_pk_bf16_f32 v123, v126, v127\n"
  "v_cvt_pk_bf16_f32 v128, v128, v129\n"
  "v_cvt_pk_bf16_f32 v129, v130, v131\n"
  "v_cvt_pk_bf16_f32 v130, v132, v133\n"
  "v_cvt_pk_bf16_f32 v131, v134, v135\n"
  "s_add_u32 s40, s28, 22937600\n"
  "buffer_store_dwordx4 v[120:123], v43, s[24:27], s40 offen sc1\n"
  "buffer_store_dwordx4 v[128:131], v43, s[24:27], s40 offen offset:1024 sc1\n"
  "s_waitcnt vmcnt(0)\n"
  "s_barrier\n"
  "v_mov_b32 v63, 1\n"
  "s_cmp_eq_u32 s30, 0\n"
  "s_cselect_b32 s40, s29, 0x80000000\n"
  "v_mov_b32 v62, s40\n"
  "s_mov_b64 exec, 1\n"
  "buffer_store_dword v63, v62, s[24:27], 0 offen sc1\n"
  "s_mov_b64 exec, -1\n"
  "s_nop 3\n"
      : "+{v[32:47]}"(vd), "={s[48:63]}"(so) : "{s[16:31]}"(sd) : "v48", "v49", "v50", "v51", "v52", "v53", "v54", "v55", "v56", "v57", "v58", "v59", "v60", "v61", "v62", "v63", "v64", "v65", "v66", "v67", "v68", "v69", "v70", "v71", "v72", "v73", "v74", "v75", "v76", "v77", "v78", "v79", "v80", "v81", "v82", "v83", "v84", "v85", "v86", "v87", "v88", "v89", "v90", "v91", "v92", "v93", "v94", "v95", "v96", "v97", "v98", "v99", "v100", "v101", "v102", "v103", "v104", "v105", "v106", "v107", "v108", "v109", "v110", "v111", "v112", "v113", "v114", "v115", "v116", "v117", "v118", "v119", "v120", "v121", "v122", "v123", "v124", "v125", "v126", "v127", "v128", "v129", "v130", "v131", "v132", "v133", "v134", "v135", "v136", "v137", "v138", "v139", "v140", "v141", "v142", "v143", "v144", "v145", "v146", "v147", "v148", "v149", "v150", "v151", "v152", "v153", "v154", "v155", "v156", "v157", "v158", "v159", "v160", "v161", "v162", "v163", "v164", "v165", "v166", "v167", "v168", "v169", "v170", "v171", "v172", "v173", "v174", "v175", "v176", "v177", "v178", "v179", "v180", "v181", "v182", "v183", "v184", "v185", "v186", "v187", "v188", "v189", "v190", "v191", "v192", "v193", "v194", "v195", "v196", "v197", "v198", "v199", "v200", "v201", "v202", "v203", "v204", "v205", "v206", "v207", "v208", "v209", "v210", "v211", "v212", "v213", "v214", "v215", "v216", "v217", "v218", "v219", "v220", "v221", "v222", "v223", "v224", "v225", "v226", "v227", "v228", "v229", "v230", "v231", "v232", "v233", "v234", "v235", "v236", "v237", "v238", "v239", "v240", "v241", "v242", "v243", "v244", "v245", "v246", "v247", "a0", "a1", "a2", "a3", "a4", "a5", "a6", "a7", "a8", "a9", "a10", "a11", "a12", "a13", "a14", "a15", "a16", "a17", "a18", "a19", "a20", "a21", "a22", "a23", "a24", "a25", "a26", "a27", "a28", "a29", "a30", "a31", "a32", "a33", "a34", "a35", "a36", "a37", "a38", "a39", "a40", "a41", "a42", "a43", "a44", "a45", "a46", "a47", "a48", "a49", "a50", "a51", "a52", "a53", "a54", "a55", "a56", "a57", "a58", "a59", "a60", "a61", "a62", "a63", "a64", "a65", "a66", "a67", "a68", "a69", "a70", "a71", "a72", "a73", "a74", "a75", "a76", "a77", "a78", "a79", "a80", "a81", "a82", "a83", "a84", "a85", "a86", "a87", "a88", "a89", "a90", "a91", "a92", "a93", "a94", "a95", "a96", "a97", "a98", "a99", "a100", "a101", "a102", "a103", "a104", "a105", "a106", "a107", "a108", "a109", "a110", "a111", "a112", "a113", "a114", "a115", "a116", "a117", "a118", "a119", "a120", "a121", "a122", "a123", "a124", "a125", "a126", "a127", "a128", "a129", "a130", "a131", "a132", "a133", "a134", "a135", "a136", "a137", "a138", "a139", "a140", "a141", "a142", "a143", "a144", "a145", "a146", "a147", "a148", "a149", "a150", "a151", "a152", "a153", "a154", "a155", "a156", "a157", "a158", "a159", "a160", "a161", "a162", "a163", "a164", "a165", "a166", "a167", "a168", "a169", "a170", "a171", "a172", "a173", "a174", "a175", "a176", "a177", "a178", "a179", "a180", "a181", "a182", "a183", "a184", "a185", "a186", "a187", "a188", "a189", "a190", "a191", "a192", "a193", "a194", "a195", "a196", "a197", "a198", "a199", "a200", "a201", "a202", "a203", "a204", "a205", "a206", "a207", "a208", "a209", "a210", "a211", "a212", "a213", "a214", "a215", "a216", "a217", "a218", "a219", "a220", "a221", "a222", "a223", "a224", "a225", "a226", "a227", "a228", "a229", "a230", "a231", "a232", "a233", "a234", "a235", "a236", "a237", "a238", "a239", "a240", "a241", "a242", "a243", "a244", "a245", "a246", "a247", "a248", "a249", "a250", "a251", "a252", "a253", "a254", "a255", "s40", "s41", "s42", "s43", "s44", "s45", "s46", "vcc", "scc", "memory");
}

static_assert(RING_FILL <= 466 * 4096 && 466 * 4096 <= SZ_RING - 16384, "ring fill");
__global__ void __launch_bounds__(256) k_pre(Args a) {
  __shared__ __attribute__((aligned(16))) u8 smem[160 * 144];
  const int tid = threadIdx.x, wave = __builtin_amdgcn_readfirstlane(tid >> 6), lane = tid & 63, g = lane >> 4, n16 = lane & 15;
  const int b = blockIdx.x;
  TP(0);
    if (b == 256) {
      unsigned best[2] = {1000000u, 1000000u};
      for (int rep = 0; rep < 3; ++rep)
        for (int cc = 0; cc < 2; ++cc) {
          unsigned* p = (unsigned*)(a.ws + SZ_RING - 16384 + cc * 8192 + wave * 2048);
          unsigned v, z = 0;
          const unsigned c0 = (unsigned)__builtin_amdgcn_s_memtime();
          if (lane == 0) asm volatile("global_atomic_or %0, %1, %2, off sc0\n s_waitcnt vmcnt(0)" : "=&v"(v) : "v"(p), "v"(z) : "memory");
          const unsigned c1 = (unsigned)__builtin_amdgcn_s_memtime();
          best[cc] = min(best[cc], c1 - c0);
        }
      if (lane == 0) *(u32x2*)(smem + wave * 8) = (u32x2){best[0], best[1]};
      __syncthreads();
      if (tid == 0) {
        const u32x4 p0 = *(const u32x4*)smem, p1 = *(const u32x4*)(smem + 16);
        const unsigned b0 = min(min(p0.x, p0.z), min(p1.x, p1.z)), b1 = min(min(p0.y, p0.w), min(p1.y, p1.w));
        *(u32x4*)(a.ws + OFF_CTL) = (u32x4){0u, b1 < b0 ? 1u : 0u, b0, b1};
      }
      return;
    }
    if (b == 297) {
      unsigned long long pc;
      asm volatile("s_getpc_b64 s[20:21]\n s_add_u32 s20, s20, _Z6k_main4Args@rel32@lo+4\n s_addc_u32 s21, s21, _Z6k_main4Args@rel32@hi+12" : "={s[20:21]}"(pc) :: "scc");
      const __amdgpu_buffer_rsrc_t rc = rsrc((const void*)pc, 69632);
      unsigned t = 0;
#pragma unroll
      for (int i = 0; i < 3; ++i) t ^= __builtin_amdgcn_raw_buffer_load_b32(rc, tid * 128 + i * 32768, 0, 0);
      asm volatile("" :: "v"(t));
      return;
    }
    if (b > 256) {
      const int cb = (b - 257) >> 1, hf = (b - 257) & 1;
      u32x4 v[5];
#pragma unroll
      for (int i = 0; i < 5; ++i) v[i] = *(const u32x4*)(a.wo + (size_t)(hf * 320 + (tid >> 2) + 64 * i) * 640 + cb * 32 + (tid & 3) * 8);
#pragma unroll
      for (int i = 0; i < 5; ++i) *(u32x4*)(smem + ((tid >> 2) + 64 * i) * 64 + (tid & 3) * 16) = v[i];
      __syncthreads();
      const u8* ob = smem + (32 * (wave >> 1) + 8 * g + (n16 >> 2)) * 64 + (lane & 3) * 16 + (wave & 1) * 8;
#pragma unroll
      for (int i = 0; i < 5; ++i) *(bf16x8*)(a.ws + OFF_WOF + (size_t)cb * 40960 + hf * 20480 + i * 4096 + tid * 16) = tr8(ob + i * 4096, ob + i * 4096 + 256);
      return;
    }
    if (b < 128) {
      const int row = b * 16 + wave * 4 + g;
      u32x4 x[5];
#pragma unroll
      for (int i = 0; i < 5; ++i) x[i] = *(const u32x4*)(a.x0 + (size_t)row * 640 + (i * 16 + n16) * 8);
      float s = 0.f;
#pragma unroll
      for (int i = 0; i < 5; ++i)
#pragma unroll
        for (int d = 0; d < 4; ++d) { const unsigned xd = x[i][d]; s += lo16(xd) + hi16(xd); }
      s += __shfl_xor(s, 1); s += __shfl_xor(s, 2); s += __shfl_xor(s, 4); s += __shfl_xor(s, 8);
      float mu = s * (1.f / 640.f), q = 0.f;
      OPQ(mu);
#pragma unroll
      for (int i = 0; i < 5; ++i)
#pragma unroll
        for (int d = 0; d < 4; ++d) { const unsigned xd = x[i][d]; const float d0 = lo16(xd) - mu, d1 = hi16(xd) - mu; q += d0 * d0 + d1 * d1; }
      q += __shfl_xor(q, 1); q += __shfl_xor(q, 2); q += __shfl_xor(q, 4); q += __shfl_xor(q, 8);
      float rs = __builtin_amdgcn_rsqf(q * (1.f / 640.f) + 1e-5f);
      OPQ(rs);
#pragma unroll
      for (int i = 0; i < 5; ++i) {
        u32x4 y;
#pragma unroll
        for (int d = 0; d < 4; ++d) { const unsigned xd = x[i][d]; y[d] = pk2((lo16(xd) - mu) * rs, (hi16(xd) - mu) * rs); }
        *(u32x4*)(smem + (i * 16 + n16) * 272 + (wave * 4 + g) * 16) = y;
      }
      __syncthreads();
#pragma unroll
      for (int i = 0; i < 5; ++i) *(u32x4*)(a.ws + OFF_XN0 + (size_t)b * 20480 + i * 4096 + tid * 16) = *(const u32x4*)(smem + (i * 16 + (tid >> 4)) * 272 + (tid & 15) * 16);
    } else {
      const int h = (b - 128) >> 2, kq = b & 3, pc = tid & 15, tile = pc >> 2, p = pc & 3;
      const int col = 20 * h + (tile == 0 ? 640 + 4 * p : tile == 1 ? 1280 + 4 * p : tile == 3 ? 4 * p : (p == 0 ? 656 : p == 1 ? 1296 : 16));
      u32x2 v[10];
#pragma unroll
      for (int i = 0; i < 10; ++i) v[i] = *(const u32x2*)(a.wqkv + (size_t)(kq * 160 + (tid >> 4) + 16 * i) * 1920 + col);
#pragma unroll
      for (int i = 0; i < 10; ++i) *(u32x2*)(smem + ((tid >> 4) + 16 * i) * 144 + pc * 8) = pc == 11 ? (u32x2){0x3F80u, 0u} : v[i];
      __syncthreads();
      const u8* p0 = smem + (8 * g + (n16 >> 2)) * 144 + wave * 32 + (lane & 3) * 8;
#pragma unroll
      for (int i = 0; i < 5; ++i) *(bf16x8*)(a.ws + OFF_WQF + (size_t)h * 81920 + (kq * 5 + i) * 4096 + tid * 16) = tr8(p0 + i * 32 * 144, p0 + (i * 32 + 4) * 144);
    }
    TP(4);
#pragma unroll
    for (int i = 0; i < 4; ++i) {
      const unsigned n = i * 256 + b;
      const size_t p = n < 466 ? (size_t)n * 4096 : n < 626 ? OFF_O + (n - 466) * 4096 : n < 786 ? OFF_XR + (n - 626) * 4096 : OFF_KVP + (n - 786) * 4096;
      if (n < 786 + 32 * NKV) *(u32x4*)(a.ws + p + tid * 16) = ARM4;
    }
  TP(5);
}

template <int N, int STRIDE>
DEV bool poll_frags(__amdgpu_buffer_rsrc_t r, unsigned vo, unsigned so, u32x4 (&v)[N], unsigned* ctl) {
  for (unsigned spins = 0;;) {
    unsigned m = 0;
#pragma unroll
    for (int i = 0; i < N; ++i) v[i] = __builtin_amdgcn_raw_buffer_load_b128(r, vo + ((i * STRIDE) & 4095), so + ((i * STRIDE) & ~4095), 16);
#pragma unroll
    for (int i = 0; i < N; ++i) m = max(m, mx4(v[i]));
    if (__all(m != 0xFFFFFFFFu)) return true;
    __builtin_amdgcn_s_sleep(N == 1 ? 4 : 2);
    if (spin_fail(spins, ctl)) return false;
  }
}
DEV bool lin_out(const Args& a, u8* smem, const int id, const int layer, const f32x4 ks, const bf16x8 (&pq)[4], const bf16x8 (&wo)[20][2]) {
  int tid = threadIdx.x;
  OPQ(tid);
  const int wave = __builtin_amdgcn_readfirstlane(tid >> 6), lane = tid & 63;
  const int hd = (id & 7) * 4 + (id >> 3);
  unsigned* ctl = (unsigned*)(a.ws + OFF_CTL);
  u8* KS = smem + 81920;
  const __amdgpu_buffer_rsrc_t rq = rsrc(a.ws + OFF_PQ, 4u << 20), rX2 = rsrc(a.ws + OFF_X2, SZ_X);
  auto ldq = [&](int rd) { return __builtin_bit_cast(bf16x8, __builtin_amdgcn_raw_buffer_load_b128(rq, lane * 16, ((rd * 4 + wave) * 32 + hd) * 1024, 16)); };
  const int g = lane >> 4, n16 = lane & 15;
  const __amdgpu_buffer_rsrc_t rO = rsrc(a.ws + OFF_O, 2 * SZ_X), rR = rsrc(a.ws + OFF_XR, 2 * SZ_X), rN = rsrc(a.ws + OFF_XNF, 2 * SZ_X);
  const unsigned oo0 = hd * 640 + lane * 8, oo4 = g == 0 ? oo0 + 512 : 0x80000000u;
    *(f32x4*)(KS + 16384 + tid * 16) = ks;
    __syncthreads();
    bf16x8 E0, E1, EZ;
    {
      const u8* S = KS + 16384;
      const int c1 = 4 + (n16 & 3);
      f32x4 a0 = *(const f32x4*)(S + lane * 16), a1 = *(const f32x4*)(S + 2048 + n16 * 16);
      f32x4 b0 = *(const f32x4*)(S + 1024 + (g * 16 + c1) * 16), b1 = *(const f32x4*)(S + 3072 + c1 * 16);
      f32x4 z0 = *(const f32x4*)(S + 1024 + (g * 16 + 12) * 16), z1 = *(const f32x4*)(S + 3072 + 12 * 16);
      if (g != 2) { a1 = Z4; b1 = Z4; z1 = Z4; }
      E0 = CAT4(cvt4(a0), cvt4(a1)); E1 = CAT4(cvt4(b0), cvt4(b1)); EZ = CAT4(cvt4(z0), cvt4(z1));
    }
    TL(1 + layer * 8);
    auto attn_out = [&](bf16x8 pq, int rd) {
      const f32x4 n0 = MFMA32(E0, pq, Z4), n1 = MFMA32(E1, pq, Z4), dn = MFMA32(EZ, pq, Z4);
      const float r = __builtin_amdgcn_rcpf(dn[0] + 1e-6f);
      const unsigned so = (layer * 128 + rd * 4 + wave) * 20480;
      __builtin_amdgcn_raw_buffer_store_b64((u32x2){pkc(n0[0] * r, n0[1] * r), pkc(n0[2] * r, n0[3] * r)}, rO, oo0, so, 16);
      __builtin_amdgcn_raw_buffer_store_b64((u32x2){pkc(n1[0] * r, n1[1] * r), pkc(n1[2] * r, n1[3] * r)}, rO, oo4, so, 16);
    };
    auto arm = [&](int rd) {
      if (layer * 32 + rd < 56) {
        const unsigned so = (layer * 128 + rd * 4 + 32 + wave) * 20480;
        __builtin_amdgcn_raw_buffer_store_b64((u32x2){0xFFFFFFFFu, 0xFFFFFFFFu}, rO, oo0, so, 16);
        __builtin_amdgcn_raw_buffer_store_b64((u32x2){0xFFFFFFFFu, 0xFFFFFFFFu}, rO, oo4, so, 16);
      }
    };
    auto batch = [&](int r0) {
      if (r0 == 4) { arm(0); arm(1); arm(2); arm(3); }
      bf16x8 p[7];
#pragma unroll
      for (int i = 0; i < 7; ++i) p[i] = ldq(r0 + i);
      asm volatile("s_waitcnt vmcnt(0)" ::: "memory");
#pragma unroll
      for (int i = 0; i < 7; ++i) { attn_out(p[i], r0 + i); arm(r0 + i); }
    };
    attn_out(pq[0], 0); attn_out(pq[1], 1); attn_out(pq[2], 2); attn_out(pq[3], 3);
    if (id < 20) {
      const unsigned cb = (id % 5) * 4 + wave;
      const __amdgpu_buffer_rsrc_t rs_ = rsrc(layer == 0 ? (const u8*)a.x0 : a.ws + OFF_X2, SZ_X);
      const unsigned ro = layer == 0 ? n16 * 1280 + cb * 64 + g * 16 : cb * 1024 + lane * 16;
      auto ldres = [&](unsigned tile) { return __builtin_amdgcn_raw_buffer_load_b128(rs_, ro, tile * 20480, 16); };
      u32x4 o[5] = {ARM4, ARM4, ARM4, ARM4, ARM4}, res = ldres(id / 5);
#pragma unroll 1
      for (int rd = 0; rd < 32; ++rd) {
        if (rd == 0) TL(2 + layer * 8);
        if (rd == 1) TL(3 + layer * 8);
        if (rd == 8) TL(4 + layer * 8);
        const unsigned tile = rd * 4 + id / 5, so = (layer * 128 + tile) * 20480;
        f32x4 c0 = {lo16(res.x), hi16(res.x), lo16(res.y), hi16(res.y)}, c1 = {lo16(res.z), hi16(res.z), lo16(res.w), hi16(res.w)};
        OPQ(c0); OPQ(c1);
        if (!__all(max(max(max(mx4(o[0]), mx4(o[1])), max(mx4(o[2]), mx4(o[3]))), mx4(o[4])) != 0xFFFFFFFFu) && !poll_frags<5, 1024>(rO, lane * 16, so + wave * 5120, o, ctl)) return false;
        u8* ot = smem + 102400 + (rd & 1) * 20480 + lane * 16;
#pragma unroll
        for (int k = 0; k < 5; ++k) *(u32x4*)(ot + (wave * 5 + k) * 1024) = o[k];
        SB();
        res = ldres(rd < 31 ? tile + 4 : tile);
        SB();
#pragma unroll
        for (int k = 0; k < 5; ++k) o[k] = __builtin_amdgcn_raw_buffer_load_b128(rO, (rd < 31 ? lane * 16 : 0x80000000u) + (k & 3) * 1024, so + 4 * 20480 + wave * 5120 + (k >> 2) * 4096, 16);
        asm volatile("s_waitcnt lgkmcnt(0)\n s_barrier" ::: "memory");
        bf16x8 of[20];
        const u8* oq = smem + 102400 + (rd & 1) * 20480 + g * 256 + n16 * 8;
#pragma unroll
        for (int k = 0; k < 20; ++k) of[k] = CAT4(*(const bf16x4*)(oq + k * 1024), *(const bf16x4*)(oq + k * 1024 + 128));
        SB();
#pragma unroll
        for (int k = 0; k < 20; ++k) { c0 = MFMA32(wo[k][0], of[k], c0); c1 = MFMA32(wo[k][1], of[k], c1); }
        __builtin_amdgcn_raw_buffer_store_b128(canon4(__builtin_bit_cast(u32x4, CAT4(cvt4(c0), cvt4(c1)))), rR, cb * 1024 + lane * 16, so, 16);
        if (layer * 32 + rd < 56) __builtin_amdgcn_raw_buffer_store_b128(ARM4, rR, cb * 1024 + lane * 16, so + 32 * 20480, 16);
        if (rd == 1 || (rd % 7 == 0 && rd > 0 && rd < 22)) batch(rd == 1 ? 4 : rd + 4);
      }
      TL(5 + layer * 8);
      return true;
    }
#pragma unroll 1
    for (int rd = 0; rd < 32; ++rd) {
      if (rd == 0) TL(2 + layer * 8);
      if (rd == 1) TL(3 + layer * 8);
      if (rd == 8) TL(4 + layer * 8);
      if (rd == (id < 24 ? 2 : 0)) batch(4);
      if (rd == 4 || rd == 10 || rd == 16) batch(rd == 4 ? 11 : rd == 10 ? 18 : 25);
      if ((rd % 6) == ((id - 20) >> 1)) {
        const unsigned sg = layer * 64 + rd * 2 + (id & 1), tok = (wave & 1) * 8 + (lane & 7), part = lane >> 3;
        if (layer == 0) {
#pragma unroll
          for (int i = 0; i < 10; ++i) __builtin_amdgcn_raw_buffer_store_b128(ARM4, rX2, tid * 16, sg * SEGB + i * 4096, 16);
        }
        u32x4 x[10], one[1];
        if (!poll_frags<1, 0>(rR, part * 2560 + tok * 16, sg * SEGB + (wave >> 1) * 20480, one, ctl)) return false;
        if (rd == 0 && layer == 1) TL(2);
        if (!poll_frags<10, 256>(rR, part * 2560 + tok * 16, sg * SEGB + (wave >> 1) * 20480, x, ctl)) return false;
        if (rd == 0 && layer == 1) TL(3);
        float s = 0.f, q = 0.f;
        bf16x2 one2; one2[0] = one2[1] = (__bf16)1.f;
#pragma unroll
        for (int i = 0; i < 10; ++i)
#pragma unroll
          for (int d = 0; d < 4; ++d) {
            const unsigned xd = x[i][d];
            const bf16x2 x2 = __builtin_bit_cast(bf16x2, xd);
            s = __builtin_amdgcn_fdot2_f32_bf16(x2, one2, s, false); q = __builtin_amdgcn_fdot2_f32_bf16(x2, x2, q, false);
          }
        s += __shfl_xor(s, 8); q += __shfl_xor(q, 8); s += __shfl_xor(s, 16); q += __shfl_xor(q, 16); s += __shfl_xor(s, 32); q += __shfl_xor(q, 32);
        OPQ(s);
        float mu = s * (1.f / 640.f);
        OPQ(mu);
        float rs = __builtin_amdgcn_rsqf(fmaxf(q * (1.f / 640.f) - mu * mu, 0.f) + 1e-5f), mr = -mu * rs;
        OPQ(rs); OPQ(mr);
#pragma unroll
        for (int i = 0; i < 5; ++i) {
          unsigned y[8];
#pragma unroll
          for (int d = 0; d < 8; ++d) { const unsigned xd = x[2 * i + (d >> 2)][d & 3]; y[d] = pk2(__builtin_fmaf(lo16(xd), rs, mr), __builtin_fmaf(hi16(xd), rs, mr)); }
          const unsigned gr = part * 5 + i, t = gr >> 2, u = (gr >> 1) & 1;
          const unsigned vo = ((2 * u + (wave >> 1)) * 10 + t) * 1024 + (gr & 1) * 512 + tok * 16;
          __builtin_amdgcn_raw_buffer_store_b128((u32x4){y[0], y[4], y[1], y[5]}, rN, vo, sg * SEGB, 16);
          __builtin_amdgcn_raw_buffer_store_b128((u32x4){y[2], y[6], y[3], y[7]}, rN, vo + 256, sg * SEGB, 16);
        }
        if (rd == 0 && layer == 1) TL(4);
        asm volatile("s_waitcnt vmcnt(0)" ::: "memory");
        __syncthreads();
        if (tid == 0) __hip_atomic_store((GAS unsigned*)(a.ws + FLB + sg * 128), 1u, __ATOMIC_RELAXED, __HIP_MEMORY_SCOPE_AGENT);
      }
    }
    TL(5 + layer * 8);
  return true;
}
DEV bool lin_second(const Args& a, u8* smem, const int id, f32x4& ks, bf16x8 (&pq)[4]) {
  int tid = threadIdx.x;
  OPQ(tid);
  const int wave = __builtin_amdgcn_readfirstlane(tid >> 6), lane = tid & 63;
  const int hd = (id & 7) * 4 + (id >> 3);
  unsigned* ctl = (unsigned*)(a.ws + OFF_CTL);
  u8* KS = smem + 81920;
  const __amdgpu_buffer_rsrc_t rq = rsrc(a.ws + OFF_PQ, 4u << 20), rX2 = rsrc(a.ws + OFF_X2, SZ_X);
  auto ldq = [&](int rd) { return __builtin_bit_cast(bf16x8, __builtin_amdgcn_raw_buffer_load_b128(rq, lane * 16, ((rd * 4 + wave) * 32 + hd) * 1024, 16)); };
    LinS w; lin_sums(smem + lane * 16, w);
    f32x4 kv[4] = {Z4, Z4, Z4, Z4};
    u32x4 x[20];
    unsigned dbg[3] = {0, 0, 0};
    bool have = false;
#pragma unroll 1
    for (int tile = wave; tile < 128;) {
      if (tile >= 124) { pq[0] = ldq(0); pq[1] = ldq(1); pq[2] = ldq(2); pq[3] = ldq(3); }
      const unsigned probe = __builtin_amdgcn_raw_buffer_load_b32(rX2, tile < 122 ? id * 512 + lane * 4 : 0x80000000u, (tile + 6) * 20480, 16);
      if (!have) {
        u32x4 one[1];
        if (!poll_frags<1, 0>(rX2, (id & 15) * 1024 + lane * 16, tile * 20480, one, ctl)) return false;
        if (tile >= 124) TL(6);
        if (!poll_frags<20, 1024>(rX2, lane * 16, tile * 20480, x, ctl)) return false;
      }
      const bool ahead = tile < 122 && __all(probe != 0xFFFFFFFFu);
      if (lin_tile<true>(x, rX2, tile, smem + lane * 16, w, kv, rq, hd, lane, have, ahead ? lane * 16 : 0x80000000u, dbg)) { have = ahead; tile += 4; }
      else have = false;
    }
    DBGOUT(12);
#pragma unroll
    for (int t = 0; t < 4; ++t) *(f32x4*)(KS + wave * 4096 + t * 1024 + lane * 16) = kv[t];
    TL(7);
    asm volatile("s_waitcnt lgkmcnt(0)\n s_barrier" ::: "memory");
    ks = (*(const f32x4*)(KS + tid * 16) + *(const f32x4*)(KS + 4096 + tid * 16)) + (*(const f32x4*)(KS + 8192 + tid * 16) + *(const f32x4*)(KS + 12288 + tid * 16));
  return true;
}
DEV void lin_role(const Args& a, u8* smem, const int id) {
  int tid = threadIdx.x;
  OPQ(tid);
  const int wave = __builtin_amdgcn_readfirstlane(tid >> 6), lane = tid & 63;
  const int hd = (id & 7) * 4 + (id >> 3);
  unsigned* ctl = (unsigned*)(a.ws + OFF_CTL);
  u8* KS = smem + 81920;
  const __amdgpu_buffer_rsrc_t rq = rsrc(a.ws + OFF_PQ, 4u << 20), rX2 = rsrc(a.ws + OFF_X2, SZ_X);
  auto ldq = [&](int rd) { return __builtin_bit_cast(bf16x8, __builtin_amdgcn_raw_buffer_load_b128(rq, lane * 16, ((rd * 4 + wave) * 32 + hd) * 1024, 16)); };
  TL(0);
  bf16x8 pq[4] = {ldq(0), ldq(1), ldq(2), ldq(3)};
  bf16x8 wo[20][2];
#pragma unroll
  for (int k = 0; k < 40; ++k) wo[k >> 1][k & 1] = __builtin_bit_cast(bf16x8, __builtin_amdgcn_raw_buffer_load_b128(rsrc(a.ws + OFF_WOF, 819200), id < 20 ? lane * 16 + (k & 3) * 1024 : 0x80000000u, ((id % 5) * 4 + wave) * 40960 + (k >> 2) * 4096, 0));
  f32x4 ks;
  {
    u32x4 p[NKV];
    if (!poll_frags<NKV, 4096>(rsrc(a.ws + OFF_KVP, 1u << 20), tid * 16, hd * NKV * 4096, p, ctl)) return;
    ks = __builtin_bit_cast(f32x4, p[0]);
#pragma unroll
    for (int i = 1; i < NKV; ++i) ks += __builtin_bit_cast(f32x4, p[i]);
  }
#pragma unroll 1
  for (int layer = 0; layer < 2; ++layer)
    if (!lin_out(a, smem, id, layer, ks, pq, wo) || layer == 1 || !lin_second(a, smem, id, ks, pq)) return;
}

__global__ void __launch_bounds__(256, 1) k_main(Args a) {
  extern __shared__ __attribute__((aligned(16))) u8 smem[];
  const unsigned b = blockIdx.x, wave = __builtin_amdgcn_readfirstlane(threadIdx.x >> 6), lane = threadIdx.x & 63;
  const unsigned par = (b & 7) >> 2, sub = b & 3, jj = b >> 3;
  TL(14);
  unsigned m0v = 0;
  if (jj >= 8 && jj < 28) m0v = *(const unsigned*)(a.mem0 + (lane & 7) * 640 + ((jj - 8) * 4 + sub) * 8 + (lane >> 4) * 2);
  if (FOLD1 && jj >= 8 && jj < 28) fold_vo(a);
  TL(8);
  if (jj < 8 ? LH != 0 : jj < 28 ? LC != 0 : true) lin_first(a);
  if (!FOLD1 && jj >= 8 && jj < 28) fold_vo(a);
  if (jj >= 28) { lin_role(a, smem, b - 224); return; }
  const unsigned g = lane >> 4, n16 = lane & 15, L16 = lane * 16, OOB = 0x80000000u;
  const bool isH = jj < 8;
  const unsigned h = sub * 8 + jj, c = (jj - 8) * 4 + sub;
  u32x16 sa, sb; u32x8 sc;
  SRD(sa, 0, a.ws, SZ_RING); SRD(sa, 4, a.ws + OFF_X2, SZ_X); SRD(sa, 8, a.ws + OFF_XNF, 2 * SZ_X + 4 * SEGB);
  const unsigned wcol = h * 384;
  if (isH) { SRD(sa, 12, (const u8*)a.Wq + wcol, 640 * 12288 - wcol); SRD(sb, 0, (const u8*)a.Wk + wcol, 640 * 12288 - wcol); }
  else { SRD(sa, 12, a.ws + OFF_CF + (size_t)(c * 4 + wave) * 81920, 81920); SRD(sb, 0, a.ws + OFF_XR, 2 * SZ_X); }
  SRD(sb, 4, a.out, SZ_X); SRD(sb, 8, a.ws + OFF_CTL, 2u << 20);
  sb[12] = wave; sb[13] = par; sb[14] = 0; sb[15] = __builtin_amdgcn_readfirstlane(((const unsigned*)(a.ws + OFF_CTL))[1]);
  sc[0] = isH ? (wave < 2 ? OOB : 0u) : (unsigned)a.dp; sc[1] = isH ? PXB + (wave == 1 ? 20480 - 512 + h * 128 : wave * 20480 + h * 640) : (unsigned)a.dm;
  sc[2] = isH ? sub * 65536 + 3 * 16384 + 7168 : (unsigned)a.skew; sc[3] = isH ? (wave >> 1) * 20480 + (wave & 1) * 5120 : (unsigned)a.delta; sc[4] = a.lat; sc[5] = isH ? PXB + (wave == 1 ? 8 : wave * 20480) + h * 640 : (unsigned)a.marg | (c == 0 ? OOB : 0u); sc[6] = jj;
  sc[7] = isH ? (h >> 3) * 16384 + (h & 7) * 640 : (unsigned)a.pubw;
  const unsigned sto = (isH ? wave != 3 : wave > 1) ? OOB : FLB + (136 + par * 128 + (isH ? h : 32 + c)) * 128;
  const unsigned dbgoff = 256 + (b * 4 + wave) * 32;
  const unsigned mo = sub * 65536 + wave * 16384 + 5120 + L16, mo2 = sub * 65536 + (wave & 1) * 16384 + 7168 + (wave >> 1) * 512 + lane * 8;
  const unsigned m2w = (8 + (wave & 1)) * 1024 + (wave >> 1) * 512 + lane * 8, idx = (lane & 8) ? 0xEEEEu : 0x4444u, sch = sub * 65536 + 3 * 16384 + 7168;
  u32x16 va; u32x8 vb, vc = {0, 0, 0, 0, 0, 0, 0, 0};
  if (isH) {
    const unsigned r8 = lane >> 3, q8 = lane & 7;
    va[0] = L16; va[1] = L16 + (wave ^ 1) * 1024; va[2] = L16 + (wave ^ 2) * 1024; va[3] = L16 + (wave ^ 3) * 1024; va[4] = L16 + wave * 1024;
    va[5] = 65536 + wave * 24576 + L16; va[6] = mo; va[7] = mo2; va[8] = 10240 + m2w; va[9] = idx;
    const unsigned sw = n16 * 128 + (((2 * g) ^ n16) & 7) * 16, r7 = n16 & 7, a8 = n16 >> 3, rd = 4 * (wave & 1) + g, cp = (2 * (wave >> 1) + a8) * 65536;
    va[10] = 30720 + wave * 1024 + r7 * 128 + ((2 * g + a8) ^ r7) * 16; va[11] = (q8 & 1) ? OOB : 512 + r8 * 16 + q8 * 2;
    va[12] = g >= 2 ? 34816 + wave * 256 + r7 * 32 + (g - 2) * 16 + a8 * 8 : 35840 + lane * 8;
    va[13] = 30720 + a8 * 2048 + rd * 128 + (q8 ^ rd) * 16; va[14] = 34816 + a8 * 512 + rd * 32 + q8 * 4;
    va[15] = cp + (q8 >> 1) * 128 + rd * 16 + (q8 & 1) * 8;
    vb[0] = (q8 & 1) ? OOB : cp + 512 + rd * 16 + q8 * 2; vb[1] = 40960 + wave * 4096 + sw;
    vb[2] = 57344 + wave * 1024 + (g >> 1) * 512 + n16 * 32 + (g & 1) * 16; vb[3] = (q8 & 1) * 16 + (q8 >> 1) * 4 - (wave * 8 + r8);
#pragma unroll
    for (int j = 0; j < 3; ++j) { const unsigned cc = 64 * j + lane; vc[j] = (cc / 24) * 12288 + (cc % 24) * 16; }
#pragma unroll
    for (int j = 0; j < 2; ++j) { const unsigned rw = drow(0, g, 4 * j + (n16 >> 2)); vc[3 + j] = rw * 384 + (rw >> 3) * 64 + wave * 96 + (lane & 3) * 8; }
#pragma unroll
    for (int j = 0; j < 4; ++j) {
      const unsigned base = (j < 2 ? 0 : 8) + 4 * (g & 1) + 2 * (j & 1);
      vb[4 + j] = (n16 == base ? 0x3F80u : 0u) | (n16 == base + 1 ? 0x3F800000u : 0u);
    }
  } else {
    const unsigned e1 = (lane >> 3) & 1, r7 = lane & 7, t = c >> 3;
    va[0] = L16; va[1] = 81920 + wave * 1024 + L16; va[2] = mo; va[3] = mo2; va[4] = 81920 + m2w; va[5] = idx;
    va[6] = sub * 65536 + e1 * 640 + g * 128 + r7 * 16; va[7] = sub * 65536 + (2 * g + e1) * 640 + 512 + r7 * 16;
    va[8] = PXB + e1 * 640 + g * 128 + r7 * 16; va[9] = PXB + (2 * g + e1) * 640 + 512 + r7 * 16;
    va[10] = sch;
    va[11] = (t & 3) * 16384 + 5120 + (t >> 2) * 1024 + (c & 7) * 128 + r7 * 16 + g * 4 + e1 * 65536;
    const unsigned tok = wave == 1 ? e1 * 8 + r7 : wave * 8 + r7;
    const bool off = e1 && wave != 1;
    va[12] = off ? OOB : tok * 1280 + c * 16 + g * 4;
    va[13] = wave == 1 ? PXB + 20480 + (2 * g + e1) * 128 + r7 * 16 : OOB;
    va[14] = off ? OOB : (tok >> 4) * 20480 + c * 256 + (tok & 15) * 16 + g * 4; va[15] = 0;
    vb[0] = (c == 0 && wave == 0 && lane < 4) ? lane * 65536 + 3 * 16384 + 7168 : OOB;
    vb[1] = vb[2] = vb[3] = vb[4] = vb[5] = vb[6] = 0;
    vc[0] = lane < 40 ? CFB + (wave * 40 + lane) * 128 : OOB; vc[1] = ((n16 >> 1) & 1) * 512 + g * 128 + ((n16 >> 2) * 2 + (n16 & 1)) * 16;
    vb[7] = min(m0v, 0xFFFFFFFEu);
  }
  asm volatile(
  "s_waitcnt vmcnt(0) lgkmcnt(0)\n s_nop 7\n s_cmp_ge_u32 s54, 8\n s_cbranch_scc1 C_ENTRY_%=\n"
  "s_mov_b32 s76, 0x3dd53b95\n"
  "s_mov_b32 s77, 0xceabfb8f\n"
  "s_mov_b32 s78, 0xfffffffe\n"
  "s_mov_b32 s46, 0\n"
  "v_cmp_ge_i32 s[8:9], 0, v19\n"
  "v_cmp_ge_i32 s[10:11], -1, v19\n"
  "v_cmp_ge_i32 s[12:13], -2, v19\n"
  "v_cmp_ge_i32 s[14:15], -3, v19\n"
  "s_mul_i32 s69, s44, 12544\n"
  "s_mul_i32 s68, s44, 786432\n"
  "s_add_u32 s68, s68, 0\n"
  "s_add_u32 m0, s69, 0\n"
  "s_nop 0\n"
  "buffer_load_dwordx4 v24, s[28:31], s68 offen nt lds\n"
  "s_add_u32 m0, s69, 1024\n"
  "s_nop 0\n"
  "buffer_load_dwordx4 v25, s[28:31], s68 offen nt lds\n"
  "s_add_u32 m0, s69, 2048\n"
  "s_nop 0\n"
  "buffer_load_dwordx4 v26, s[28:31], s68 offen nt lds\n"
  "s_add_u32 s68, s68, 98304\n"
  "s_add_u32 m0, s69, 3136\n"
  "s_nop 0\n"
  "buffer_load_dwordx4 v24, s[28:31], s68 offen nt lds\n"
  "s_add_u32 m0, s69, 4160\n"
  "s_nop 0\n"
  "buffer_load_dwordx4 v25, s[28:31], s68 offen nt lds\n"
  "s_add_u32 m0, s69, 5184\n"
  "s_nop 0\n"
  "buffer_load_dwordx4 v26, s[28:31], s68 offen nt lds\n"
  "s_add_u32 s68, s68, 98304\n"
  "s_add_u32 m0, s69, 6272\n"
  "s_nop 0\n"
  "buffer_load_dwordx4 v24, s[28:31], s68 offen nt lds\n"
  "s_add_u32 m0, s69, 7296\n"
  "s_nop 0\n"
  "buffer_load_dwordx4 v25, s[28:31], s68 offen nt lds\n"
  "s_add_u32 m0, s69, 8320\n"
  "s_nop 0\n"
  "buffer_load_dwordx4 v26, s[28:31], s68 offen nt lds\n"
  "s_add_u32 s68, s68, 98304\n"
  "s_add_u32 m0, s69, 9408\n"
  "s_nop 0\n"
  "buffer_load_dwordx4 v24, s[28:31], s68 offen nt lds\n"
  "s_add_u32 m0, s69, 10432\n"
  "s_nop 0\n"
  "buffer_load_dwordx4 v25, s[28:31], s68 offen nt lds\n"
  "s_add_u32 m0, s69, 11456\n"
  "s_nop 0\n"
  "buffer_load_dwordx4 v26, s[28:31], s68 offen nt lds\n"
  "s_mul_i32 s68, s44, 786432\n"
  "s_add_u32 s68, s68, 393216\n"
  "s_add_u32 m0, s69, 50176\n"
  "s_nop 0\n"
  "buffer_load_dwordx4 v24, s[28:31], s68 offen nt lds\n"
  "s_add_u32 m0, s69, 51200\n"
  "s_nop 0\n"
  "buffer_load_dwordx4 v25, s[28:31], s68 offen nt lds\n"
  "s_add_u32 m0, s69, 52224\n"
  "s_nop 0\n"
  "buffer_load_dwordx4 v26, s[28:31], s68 offen nt lds\n"
  "s_add_u32 s68, s68, 98304\n"
  "s_add_u32 m0, s69, 53312\n"
  "s_nop 0\n"
  "buffer_load_dwordx4 v24, s[28:31], s68 offen nt lds\n"
  "s_add_u32 m0, s69, 54336\n"
  "s_nop 0\n"
  "buffer_load_dwordx4 v25, s[28:31], s68 offen nt lds\n"
  "s_add_u32 m0, s69, 55360\n"
  "s_nop 0\n"
  "buffer_load_dwordx4 v26, s[28:31], s68 offen nt lds\n"
  "s_add_u32 s68, s68, 98304\n"
  "s_add_u32 m0, s69, 56448\n"
  "s_nop 0\n"
  "buffer_load_dwordx4 v24, s[28:31], s68 offen nt lds\n"
  "s_add_u32 m0, s69, 57472\n"
  "s_nop 0\n"
  "buffer_load_dwordx4 v25, s[28:31], s68 offen nt lds\n"
  "s_add_u32 m0, s69, 58496\n"
  "s_nop 0\n"
  "buffer_load_dwordx4 v26, s[28:31], s68 offen nt lds\n"
  "s_add_u32 s68, s68, 98304\n"
  "s_add_u32 m0, s69, 59584\n"
  "s_nop 0\n"
  "buffer_load_dwordx4 v24, s[28:31], s68 offen nt lds\n"
  "s_add_u32 m0, s69, 60608\n"
  "s_nop 0\n"
  "buffer_load_dwordx4 v25, s[28:31], s68 offen nt lds\n"
  "s_add_u32 m0, s69, 61632\n"
  "s_nop 0\n"
  "buffer_load_dwordx4 v26, s[28:31], s68 offen nt lds\n"
  "s_mul_i32 s68, s44, 786432\n"
  "s_add_u32 s68, s68, 0\n"
  "s_add_u32 m0, s69, 100352\n"
  "s_nop 0\n"
  "buffer_load_dwordx4 v24, s[32:35], s68 offen nt lds\n"
  "s_add_u32 m0, s69, 101376\n"
  "s_nop 0\n"
  "buffer_load_dwordx4 v25, s[32:35], s68 offen nt lds\n"
  "s_add_u32 m0, s69, 102400\n"
  "s_nop 0\n"
  "buffer_load_dwordx4 v26, s[32:35], s68 offen nt lds\n"
  "s_add_u32 s68, s68, 98304\n"
  "s_add_u32 m0, s69, 103488\n"
  "s_nop 0\n"
  "buffer_load_dwordx4 v24, s[32:35], s68 offen nt lds\n"
  "s_add_u32 m0, s69, 104512\n"
  "s_nop 0\n"
  "buffer_load_dwordx4 v25, s[32:35], s68 offen nt lds\n"
  "s_add_u32 m0, s69, 105536\n"
  "s_nop 0\n"
  "buffer_load_dwordx4 v26, s[32:35], s68 offen nt lds\n"
  "s_add_u32 s68, s68, 98304\n"
  "s_add_u32 m0, s69, 106624\n"
  "s_nop 0\n"
  "buffer_load_dwordx4 v24, s[32:35], s68 offen nt lds\n"
  "s_add_u32 m0, s69, 107648\n"
  "s_nop 0\n"
  "buffer_load_dwordx4 v25, s[32:35], s68 offen nt lds\n"
  "s_add_u32 m0, s69, 108672\n"
  "s_nop 0\n"
  "buffer_load_dwordx4 v26, s[32:35], s68 offen nt lds\n"
  "s_add_u32 s68, s68, 98304\n"
  "s_add_u32 m0, s69, 109760\n"
  "s_nop 0\n"
  "buffer_load_dwordx4 v24, s[32:35], s68 offen nt lds\n"
  "s_add_u32 m0, s69, 110784\n"
  "s_nop 0\n"
  "buffer_load_dwordx4 v25, s[32:35], s68 offen nt lds\n"
  "s_add_u32 m0, s69, 111808\n"
  "s_nop 0\n"
  "buffer_load_dwordx4 v26, s[32:35], s68 offen nt lds\n"
  "s_waitcnt vmcnt(24)\n"
  "s_barrier\n"
  "s_xor_b32 s70, s44, 0\n"
  "s_mul_i32 s70, s70, 12544\n"
  "s_add_u32 s70, s70, 0\n"
  "v_add_u32 v29, s70, v27\n"
  "v_add_u32 v30, s70, v28\n"
  "ds_read_b64_tr_b16 a[0:1], v29 offset:0\n"
  "ds_read_b64_tr_b16 a[2:3], v30 offset:0\n"
  "ds_read_b64_tr_b16 a[8:9], v29 offset:32\n"
  "ds_read_b64_tr_b16 a[10:11], v30 offset:32\n"
  "ds_read_b64_tr_b16 a[16:17], v29 offset:64\n"
  "ds_read_b64_tr_b16 a[18:19], v30 offset:64\n"
  "s_xor_b32 s70, s44, 1\n"
  "s_mul_i32 s70, s70, 12544\n"
  "s_add_u32 s70, s70, 0\n"
  "v_add_u32 v29, s70, v27\n"
  "v_add_u32 v30, s70, v28\n"
  "ds_read_b64_tr_b16 a[80:81], v29 offset:0\n"
  "ds_read_b64_tr_b16 a[82:83], v30 offset:0\n"
  "ds_read_b64_tr_b16 a[88:89], v29 offset:32\n"
  "ds_read_b64_tr_b16 a[90:91], v30 offset:32\n"
  "ds_read_b64_tr_b16 a[96:97], v29 offset:64\n"
  "ds_read_b64_tr_b16 a[98:99], v30 offset:64\n"
  "s_xor_b32 s70, s44, 2\n"
  "s_mul_i32 s70, s70, 12544\n"
  "s_add_u32 s70, s70, 0\n"
  "v_add_u32 v29, s70, v27\n"
  "v_add_u32 v30, s70, v28\n"
  "ds_read_b64_tr_b16 a[112:113], v29 offset:0\n"
  "ds_read_b64_tr_b16 a[114:115], v30 offset:0\n"
  "s_waitcnt lgkmcnt(10)\n"
  "ds_read_b64_tr_b16 a[120:121], v29 offset:32\n"
  "ds_read_b64_tr_b16 a[122:123], v30 offset:32\n"
  "ds_read_b64_tr_b16 a[128:129], v29 offset:64\n"
  "ds_read_b64_tr_b16 a[130:131], v30 offset:64\n"
  "s_xor_b32 s70, s44, 3\n"
  "s_mul_i32 s70, s70, 12544\n"
  "s_add_u32 s70, s70, 0\n"
  "v_add_u32 v29, s70, v27\n"
  "v_add_u32 v30, s70, v28\n"
  "s_waitcnt lgkmcnt(10)\n"
  "ds_read_b64_tr_b16 a[152:153], v29 offset:0\n"
  "ds_read_b64_tr_b16 a[154:155], v30 offset:0\n"
  "ds_read_b64_tr_b16 a[160:161], v29 offset:32\n"
  "ds_read_b64_tr_b16 a[162:163], v30 offset:32\n"
  "s_waitcnt lgkmcnt(10)\n"
  "ds_read_b64_tr_b16 a[168:169], v29 offset:64\n"
  "ds_read_b64_tr_b16 a[170:171], v30 offset:64\n"
  "s_waitcnt lgkmcnt(0)\n"
  "s_barrier\n"
  "s_mul_i32 s68, s44, 786432\n"
  "s_add_u32 s68, s68, 393216\n"
  "s_add_u32 m0, s69, 0\n"
  "s_nop 0\n"
  "buffer_load_dwordx4 v24, s[32:35], s68 offen nt lds\n"
  "s_add_u32 m0, s69, 1024\n"
  "s_nop 0\n"
  "buffer_load_dwordx4 v25, s[32:35], s68 offen nt lds\n"
  "s_add_u32 m0, s69, 2048\n"
  "s_nop 0\n"
  "buffer_load_dwordx4 v26, s[32:35], s68 offen nt lds\n"
  "s_add_u32 s68, s68, 98304\n"
  "s_add_u32 m0, s69, 3136\n"
  "s_nop 0\n"
  "buffer_load_dwordx4 v24, s[32:35], s68 offen nt lds\n"
  "s_add_u32 m0, s69, 4160\n"
  "s_nop 0\n"
  "buffer_load_dwordx4 v25, s[32:35], s68 offen nt lds\n"
  "s_add_u32 m0, s69, 5184\n"
  "s_nop 0\n"
  "buffer_load_dwordx4 v26, s[32:35], s68 offen nt lds\n"
  "s_add_u32 s68, s68, 98304\n"
  "s_add_u32 m0, s69, 6272\n"
  "s_nop 0\n"
  "buffer_load_dwordx4 v24, s[32:35], s68 offen nt lds\n"
  "s_add_u32 m0, s69, 7296\n"
  "s_nop 0\n"
  "buffer_load_dwordx4 v25, s[32:35], s68 offen nt lds\n"
  "s_add_u32 m0, s69, 8320\n"
  "s_nop 0\n"
  "buffer_load_dwordx4 v26, s[32:35], s68 offen nt lds\n"
  "s_add_u32 s68, s68, 98304\n"
  "s_add_u32 m0, s69, 9408\n"
  "s_nop 0\n"
  "buffer_load_dwordx4 v24, s[32:35], s68 offen nt lds\n"
  "s_add_u32 m0, s69, 10432\n"
  "s_nop 0\n"
  "buffer_load_dwordx4 v25, s[32:35], s68 offen nt lds\n"
  "s_add_u32 m0, s69, 11456\n"
  "s_nop 0\n"
  "buffer_load_dwordx4 v26, s[32:35], s68 offen nt lds\n"
  "s_waitcnt vmcnt(24)\n"
  "s_barrier\n"
  "s_xor_b32 s70, s44, 0\n"
  "s_mul_i32 s70, s70, 12544\n"
  "s_add_u32 s70, s70, 50176\n"
  "v_add_u32 v29, s70, v27\n"
  "v_add_u32 v30, s70, v28\n"
  "ds_read_b64_tr_b16 a[4:5], v29 offset:0\n"
  "ds_read_b64_tr_b16 a[6:7], v30 offset:0\n"
  "ds_read_b64_tr_b16 a[12:13], v29 offset:32\n"
  "ds_read_b64_tr_b16 a[14:15], v30 offset:32\n"
  "ds_read_b64_tr_b16 a[20:21], v29 offset:64\n"
  "ds_read_b64_tr_b16 a[22:23], v30 offset:64\n"
  "s_xor_b32 s70, s44, 1\n"
  "s_mul_i32 s70, s70, 12544\n"
  "s_add_u32 s70, s70, 50176\n"
  "v_add_u32 v29, s70, v27\n"
  "v_add_u32 v30, s70, v28\n"
  "ds_read_b64_tr_b16 a[84:85], v29 offset:0\n"
  "ds_read_b64_tr_b16 a[86:87], v30 offset:0\n"
  "ds_read_b64_tr_b16 a[92:93], v29 offset:32\n"
  "ds_read_b64_tr_b16 a[94:95], v30 offset:32\n"
  "ds_read_b64_tr_b16 a[100:101], v29 offset:64\n"
  "ds_read_b64_tr_b16 a[102:103], v30 offset:64\n"
  "s_xor_b32 s70, s44, 2\n"
  "s_mul_i32 s70, s70, 12544\n"
  "s_add_u32 s70, s70, 50176\n"
  "v_add_u32 v29, s70, v27\n"
  "v_add_u32 v30, s70, v28\n"
  "ds_read_b64_tr_b16 a[116:117], v29 offset:0\n"
  "ds_read_b64_tr_b16 a[118:119], v30 offset:0\n"
  "s_waitcnt lgkmcnt(10)\n"
  "ds_read_b64_tr_b16 a[124:125], v29 offset:32\n"
  "ds_read_b64_tr_b16 a[126:127], v30 offset:32\n"
  "ds_read_b64_tr_b16 a[132:133], v29 offset:64\n"
  "ds_read_b64_tr_b16 a[134:135], v30 offset:64\n"
  "s_xor_b32 s70, s44, 3\n"
  "s_mul_i32 s70, s70, 12544\n"
  "s_add_u32 s70, s70, 50176\n"
  "v_add_u32 v29, s70, v27\n"
  "v_add_u32 v30, s70, v28\n"
  "s_waitcnt lgkmcnt(10)\n"
  "ds_read_b64_tr_b16 a[156:157], v29 offset:0\n"
  "ds_read_b64_tr_b16 a[158:159], v30 offset:0\n"
  "ds_read_b64_tr_b16 a[164:165], v29 offset:32\n"
  "ds_read_b64_tr_b16 a[166:167], v30 offset:32\n"
  "s_waitcnt lgkmcnt(10)\n"
  "ds_read_b64_tr_b16 a[172:173], v29 offset:64\n"
  "ds_read_b64_tr_b16 a[174:175], v30 offset:64\n"
  "s_waitcnt lgkmcnt(0)\n"
  "s_barrier\n"
  "s_mul_i32 s68, s44, 786432\n"
  "s_add_u32 s68, s68, 3145728\n"
  "s_add_u32 m0, s69, 50176\n"
  "s_nop 0\n"
  "buffer_load_dwordx4 v24, s[28:31], s68 offen nt lds\n"
  "s_add_u32 m0, s69, 51200\n"
  "s_nop 0\n"
  "buffer_load_dwordx4 v25, s[28:31], s68 offen nt lds\n"
  "s_add_u32 m0, s69, 52224\n"
  "s_nop 0\n"
  "buffer_load_dwordx4 v26, s[28:31], s68 offen nt lds\n"
  "s_add_u32 s68, s68, 98304\n"
  "s_add_u32 m0, s69, 53312\n"
  "s_nop 0\n"
  "buffer_load_dwordx4 v24, s[28:31], s68 offen nt lds\n"
  "s_add_u32 m0, s69, 54336\n"
  "s_nop 0\n"
  "buffer_load_dwordx4 v25, s[28:31], s68 offen nt lds\n"
  "s_add_u32 m0, s69, 55360\n"
  "s_nop 0\n"
  "buffer_load_dwordx4 v26, s[28:31], s68 offen nt lds\n"
  "s_add_u32 s68, s68, 98304\n"
  "s_add_u32 m0, s69, 56448\n"
  "s_nop 0\n"
  "buffer_load_dwordx4 v24, s[28:31], s68 offen nt lds\n"
  "s_add_u32 m0, s69, 57472\n"
  "s_nop 0\n"
  "buffer_load_dwordx4 v25, s[28:31], s68 offen nt lds\n"
  "s_add_u32 m0, s69, 58496\n"
  "s_nop 0\n"
  "buffer_load_dwordx4 v26, s[28:31], s68 offen nt lds\n"
  "s_add_u32 s68, s68, 98304\n"
  "s_add_u32 m0, s69, 59584\n"
  "s_nop 0\n"
  "buffer_load_dwordx4 v24, s[28:31], s68 offen nt lds\n"
  "s_add_u32 m0, s69, 60608\n"
  "s_nop 0\n"
  "buffer_load_dwordx4 v25, s[28:31], s68 offen nt lds\n"
  "s_add_u32 m0, s69, 61632\n"
  "s_nop 0\n"
  "buffer_load_dwordx4 v26, s[28:31], s68 offen nt lds\n"
  "s_waitcnt vmcnt(24)\n"
  "s_barrier\n"
  "s_xor_b32 s70, s44, 0\n"
  "s_mul_i32 s70, s70, 12544\n"
  "s_add_u32 s70, s70, 100352\n"
  "v_add_u32 v29, s70, v27\n"
  "v_add_u32 v30, s70, v28\n"
  "ds_read_b64_tr_b16 a[24:25], v29 offset:0\n"
  "ds_read_b64_tr_b16 a[26:27], v30 offset:0\n"
  "ds_read_b64_tr_b16 a[32:33], v29 offset:32\n"
  "ds_read_b64_tr_b16 a[34:35], v30 offset:32\n"
  "ds_read_b64_tr_b16 v[32:33], v29 offset:64\n"
  "ds_read_b64_tr_b16 v[34:35], v30 offset:64\n"
  "s_xor_b32 s70, s44, 1\n"
  "s_mul_i32 s70, s70, 12544\n"
  "s_add_u32 s70, s70, 100352\n"
  "v_add_u32 v29, s70, v27\n"
  "v_add_u32 v30, s70, v28\n"
  "ds_read_b64_tr_b16 a[104:105], v29 offset:0\n"
  "ds_read_b64_tr_b16 a[106:107], v30 offset:0\n"
  "ds_read_b64_tr_b16 v[48:49], v29 offset:32\n"
  "ds_read_b64_tr_b16 v[50:51], v30 offset:32\n"
  "ds_read_b64_tr_b16 v[56:57], v29 offset:64\n"
  "ds_read_b64_tr_b16 v[58:59], v30 offset:64\n"
  "s_xor_b32 s70, s44, 2\n"
  "s_mul_i32 s70, s70, 12544\n"
  "s_add_u32 s70, s70, 100352\n"
  "v_add_u32 v29, s70, v27\n"
  "v_add_u32 v30, s70, v28\n"
  "ds_read_b64_tr_b16 a[136:137], v29 offset:0\n"
  "ds_read_b64_tr_b16 a[138:139], v30 offset:0\n"
  "s_waitcnt lgkmcnt(10)\n"
  "ds_read_b64_tr_b16 a[144:145], v29 offset:32\n"
  "ds_read_b64_tr_b16 a[146:147], v30 offset:32\n"
  "ds_read_b64_tr_b16 v[64:65], v29 offset:64\n"
  "ds_read_b64_tr_b16 v[66:67], v30 offset:64\n"
  "s_xor_b32 s70, s44, 3\n"
  "s_mul_i32 s70, s70, 12544\n"
  "s_add_u32 s70, s70, 100352\n"
  "v_add_u32 v29, s70, v27\n"
  "v_add_u32 v30, s70, v28\n"
  "s_waitcnt lgkmcnt(10)\n"
  "ds_read_b64_tr_b16 a[176:177], v29 offset:0\n"
  "ds_read_b64_tr_b16 a[178:179], v30 offset:0\n"
  "ds_read_b64_tr_b16 v[72:73], v29 offset:32\n"
  "ds_read_b64_tr_b16 v[74:75], v30 offset:32\n"
  "s_waitcnt lgkmcnt(10)\n"
  "ds_read_b64_tr_b16 v[80:81], v29 offset:64\n"
  "ds_read_b64_tr_b16 v[82:83], v30 offset:64\n"
  "s_waitcnt lgkmcnt(0)\n"
  "s_barrier\n"
  "s_mul_i32 s68, s44, 786432\n"
  "s_add_u32 s68, s68, 3538944\n"
  "s_add_u32 m0, s69, 100352\n"
  "s_nop 0\n"
  "buffer_load_dwordx4 v24, s[28:31], s68 offen nt lds\n"
  "s_add_u32 m0, s69, 101376\n"
  "s_nop 0\n"
  "buffer_load_dwordx4 v25, s[28:31], s68 offen nt lds\n"
  "s_add_u32 m0, s69, 102400\n"
  "s_nop 0\n"
  "buffer_load_dwordx4 v26, s[28:31], s68 offen nt lds\n"
  "s_add_u32 s68, s68, 98304\n"
  "s_add_u32 m0, s69, 103488\n"
  "s_nop 0\n"
  "buffer_load_dwordx4 v24, s[28:31], s68 offen nt lds\n"
  "s_add_u32 m0, s69, 104512\n"
  "s_nop 0\n"
  "buffer_load_dwordx4 v25, s[28:31], s68 offen nt lds\n"
  "s_add_u32 m0, s69, 105536\n"
  "s_nop 0\n"
  "buffer_load_dwordx4 v26, s[28:31], s68 offen nt lds\n"
  "s_add_u32 s68, s68, 98304\n"
  "s_add_u32 m0, s69, 106624\n"
  "s_nop 0\n"
  "buffer_load_dwordx4 v24, s[28:31], s68 offen nt lds\n"
  "s_add_u32 m0, s69, 107648\n"
  "s_nop 0\n"
  "buffer_load_dwordx4 v25, s[28:31], s68 offen nt lds\n"
  "s_add_u32 m0, s69, 108672\n"
  "s_nop 0\n"
  "buffer_load_dwordx4 v26, s[28:31], s68 offen nt lds\n"
  "s_add_u32 s68, s68, 98304\n"
  "s_add_u32 m0, s69, 109760\n"
  "s_nop 0\n"
  "buffer_load_dwordx4 v24, s[28:31], s68 offen nt lds\n"
  "s_add_u32 m0, s69, 110784\n"
  "s_nop 0\n"
  "buffer_load_dwordx4 v25, s[28:31], s68 offen nt lds\n"
  "s_add_u32 m0, s69, 111808\n"
  "s_nop 0\n"
  "buffer_load_dwordx4 v26, s[28:31], s68 offen nt lds\n"
  "s_waitcnt vmcnt(24)\n"
  "s_barrier\n"
  "s_xor_b32 s70, s44, 0\n"
  "s_mul_i32 s70, s70, 12544\n"
  "s_add_u32 s70, s70, 0\n"
  "v_add_u32 v29, s70, v27\n"
  "v_add_u32 v30, s70, v28\n"
  "ds_read_b64_tr_b16 a[28:29], v29 offset:0\n"
  "ds_read_b64_tr_b16 a[30:31], v30 offset:0\n"
  "ds_read_b64_tr_b16 a[36:37], v29 offset:32\n"
  "ds_read_b64_tr_b16 a[38:39], v30 offset:32\n"
  "ds_read_b64_tr_b16 v[36:37], v29 offset:64\n"
  "ds_read_b64_tr_b16 v[38:39], v30 offset:64\n"
  "s_xor_b32 s70, s44, 1\n"
  "s_mul_i32 s70, s70, 12544\n"
  "s_add_u32 s70, s70, 0\n"
  "v_add_u32 v29, s70, v27\n"
  "v_add_u32 v30, s70, v28\n"
  "ds_read_b64_tr_b16 a[108:109], v29 offset:0\n"
  "ds_read_b64_tr_b16 a[110:111], v30 offset:0\n"
  "ds_read_b64_tr_b16 v[52:53], v29 offset:32\n"
  "ds_read_b64_tr_b16 v[54:55], v30 offset:32\n"
  "ds_read_b64_tr_b16 v[60:61], v29 offset:64\n"
  "ds_read_b64_tr_b16 v[62:63], v30 offset:64\n"
  "s_xor_b32 s70, s44, 2\n"
  "s_mul_i32 s70, s70, 12544\n"
  "s_add_u32 s70, s70, 0\n"
  "v_add_u32 v29, s70, v27\n"
  "v_add_u32 v30, s70, v28\n"
  "ds_read_b64_tr_b16 a[140:141], v29 offset:0\n"
  "ds_read_b64_tr_b16 a[142:143], v30 offset:0\n"
  "s_waitcnt lgkmcnt(10)\n"
  "ds_read_b64_tr_b16 a[148:149], v29 offset:32\n"
  "ds_read_b64_tr_b16 a[150:151], v30 offset:32\n"
  "ds_read_b64_tr_b16 v[68:69], v29 offset:64\n"
  "ds_read_b64_tr_b16 v[70:71], v30 offset:64\n"
  "s_xor_b32 s70, s44, 3\n"
  "s_mul_i32 s70, s70, 12544\n"
  "s_add_u32 s70, s70, 0\n"
  "v_add_u32 v29, s70, v27\n"
  "v_add_u32 v30, s70, v28\n"
  "s_waitcnt lgkmcnt(10)\n"
  "ds_read_b64_tr_b16 a[180:181], v29 offset:0\n"
  "ds_read_b64_tr_b16 a[182:183], v30 offset:0\n"
  "ds_read_b64_tr_b16 v[76:77], v29 offset:32\n"
  "ds_read_b64_tr_b16 v[78:79], v30 offset:32\n"
  "s_waitcnt lgkmcnt(10)\n"
  "ds_read_b64_tr_b16 v[84:85], v29 offset:64\n"
  "ds_read_b64_tr_b16 v[86:87], v30 offset:64\n"
  "s_waitcnt lgkmcnt(0)\n"
  "s_barrier\n"
  "s_mul_i32 s68, s44, 786432\n"
  "s_add_u32 s68, s68, 3145728\n"
  "s_add_u32 m0, s69, 0\n"
  "s_nop 0\n"
  "buffer_load_dwordx4 v24, s[32:35], s68 offen nt lds\n"
  "s_add_u32 m0, s69, 1024\n"
  "s_nop 0\n"
  "buffer_load_dwordx4 v25, s[32:35], s68 offen nt lds\n"
  "s_add_u32 m0, s69, 2048\n"
  "s_nop 0\n"
  "buffer_load_dwordx4 v26, s[32:35], s68 offen nt lds\n"
  "s_add_u32 s68, s68, 98304\n"
  "s_add_u32 m0, s69, 3136\n"
  "s_nop 0\n"
  "buffer_load_dwordx4 v24, s[32:35], s68 offen nt lds\n"
  "s_add_u32 m0, s69, 4160\n"
  "s_nop 0\n"
  "buffer_load_dwordx4 v25, s[32:35], s68 offen nt lds\n"
  "s_add_u32 m0, s69, 5184\n"
  "s_nop 0\n"
  "buffer_load_dwordx4 v26, s[32:35], s68 offen nt lds\n"
  "s_add_u32 s68, s68, 98304\n"
  "s_add_u32 m0, s69, 6272\n"
  "s_nop 0\n"
  "buffer_load_dwordx4 v24, s[32:35], s68 offen nt lds\n"
  "s_add_u32 m0, s69, 7296\n"
  "s_nop 0\n"
  "buffer_load_dwordx4 v25, s[32:35], s68 offen nt lds\n"
  "s_add_u32 m0, s69, 8320\n"
  "s_nop 0\n"
  "buffer_load_dwordx4 v26, s[32:35], s68 offen nt lds\n"
  "s_add_u32 s68, s68, 98304\n"
  "s_add_u32 m0, s69, 9408\n"
  "s_nop 0\n"
  "buffer_load_dwordx4 v24, s[32:35], s68 offen nt lds\n"
  "s_add_u32 m0, s69, 10432\n"
  "s_nop 0\n"
  "buffer_load_dwordx4 v25, s[32:35], s68 offen nt lds\n"
  "s_add_u32 m0, s69, 11456\n"
  "s_nop 0\n"
  "buffer_load_dwordx4 v26, s[32:35], s68 offen nt lds\n"
  "s_waitcnt vmcnt(24)\n"
  "s_barrier\n"
  "s_xor_b32 s70, s44, 0\n"
  "s_mul_i32 s70, s70, 12544\n"
  "s_add_u32 s70, s70, 50176\n"
  "v_add_u32 v29, s70, v27\n"
  "v_add_u32 v30, s70, v28\n"
  "ds_read_b64_tr_b16 a[40:41], v29 offset:0\n"
  "ds_read_b64_tr_b16 a[42:43], v30 offset:0\n"
  "ds_read_b64_tr_b16 a[48:49], v29 offset:32\n"
  "ds_read_b64_tr_b16 a[50:51], v30 offset:32\n"
  "ds_read_b64_tr_b16 a[56:57], v29 offset:64\n"
  "ds_read_b64_tr_b16 a[58:59], v30 offset:64\n"
  "s_xor_b32 s70, s44, 1\n"
  "s_mul_i32 s70, s70, 12544\n"
  "s_add_u32 s70, s70, 50176\n"
  "v_add_u32 v29, s70, v27\n"
  "v_add_u32 v30, s70, v28\n"
  "ds_read_b64_tr_b16 a[184:185], v29 offset:0\n"
  "ds_read_b64_tr_b16 a[186:187], v30 offset:0\n"
  "ds_read_b64_tr_b16 a[192:193], v29 offset:32\n"
  "ds_read_b64_tr_b16 a[194:195], v30 offset:32\n"
  "ds_read_b64_tr_b16 a[200:201], v29 offset:64\n"
  "ds_read_b64_tr_b16 a[202:203], v30 offset:64\n"
  "s_xor_b32 s70, s44, 2\n"
  "s_mul_i32 s70, s70, 12544\n"
  "s_add_u32 s70, s70, 50176\n"
  "v_add_u32 v29, s70, v27\n"
  "v_add_u32 v30, s70, v28\n"
  "ds_read_b64_tr_b16 a[224:225], v29 offset:0\n"
  "ds_read_b64_tr_b16 a[226:227], v30 offset:0\n"
  "s_waitcnt lgkmcnt(10)\n"
  "ds_read_b64_tr_b16 a[232:233], v29 offset:32\n"
  "ds_read_b64_tr_b16 a[234:235], v30 offset:32\n"
  "ds_read_b64_tr_b16 a[240:241], v29 offset:64\n"
  "ds_read_b64_tr_b16 a[242:243], v30 offset:64\n"
  "s_xor_b32 s70, s44, 3\n"
  "s_mul_i32 s70, s70, 12544\n"
  "s_add_u32 s70, s70, 50176\n"
  "v_add_u32 v29, s70, v27\n"
  "v_add_u32 v30, s70, v28\n"
  "s_waitcnt lgkmcnt(10)\n"
  "ds_read_b64_tr_b16 v[136:137], v29 offset:0\n"
  "ds_read_b64_tr_b16 v[138:139], v30 offset:0\n"
  "ds_read_b64_tr_b16 v[144:145], v29 offset:32\n"
  "ds_read_b64_tr_b16 v[146:147], v30 offset:32\n"
  "s_waitcnt lgkmcnt(10)\n"
  "ds_read_b64_tr_b16 v[152:153], v29 offset:64\n"
  "ds_read_b64_tr_b16 v[154:155], v30 offset:64\n"
  "s_waitcnt lgkmcnt(0)\n"
  "s_barrier\n"
  "s_mul_i32 s68, s44, 786432\n"
  "s_add_u32 s68, s68, 3538944\n"
  "s_add_u32 m0, s69, 50176\n"
  "s_nop 0\n"
  "buffer_load_dwordx4 v24, s[32:35], s68 offen nt lds\n"
  "s_add_u32 m0, s69, 51200\n"
  "s_nop 0\n"
  "buffer_load_dwordx4 v25, s[32:35], s68 offen nt lds\n"
  "s_add_u32 m0, s69, 52224\n"
  "s_nop 0\n"
  "buffer_load_dwordx4 v26, s[32:35], s68 offen nt lds\n"
  "s_add_u32 s68, s68, 98304\n"
  "s_add_u32 m0, s69, 53312\n"
  "s_nop 0\n"
  "buffer_load_dwordx4 v24, s[32:35], s68 offen nt lds\n"
  "s_add_u32 m0, s69, 54336\n"
  "s_nop 0\n"
  "buffer_load_dwordx4 v25, s[32:35], s68 offen nt lds\n"
  "s_add_u32 m0, s69, 55360\n"
  "s_nop 0\n"
  "buffer_load_dwordx4 v26, s[32:35], s68 offen nt lds\n"
  "s_add_u32 s68, s68, 98304\n"
  "s_add_u32 m0, s69, 56448\n"
  "s_nop 0\n"
  "buffer_load_dwordx4 v24, s[32:35], s68 offen nt lds\n"
  "s_add_u32 m0, s69, 57472\n"
  "s_nop 0\n"
  "buffer_load_dwordx4 v25, s[32:35], s68 offen nt lds\n"
  "s_add_u32 m0, s69, 58496\n"
  "s_nop 0\n"
  "buffer_load_dwordx4 v26, s[32:35], s68 offen nt lds\n"
  "s_add_u32 s68, s68, 98304\n"
  "s_add_u32 m0, s69, 59584\n"
  "s_nop 0\n"
  "buffer_load_dwordx4 v24, s[32:35], s68 offen nt lds\n"
  "s_add_u32 m0, s69, 60608\n"
  "s_nop 0\n"
  "buffer_load_dwordx4 v25, s[32:35], s68 offen nt lds\n"
  "s_add_u32 m0, s69, 61632\n"
  "s_nop 0\n"
  "buffer_load_dwordx4 v26, s[32:35], s68 offen nt lds\n"
  "s_waitcnt vmcnt(24)\n"
  "s_barrier\n"
  "s_xor_b32 s70, s44, 0\n"
  "s_mul_i32 s70, s70, 12544\n"
  "s_add_u32 s70, s70, 100352\n"
  "v_add_u32 v29, s70, v27\n"
  "v_add_u32 v30, s70, v28\n"
  "ds_read_b64_tr_b16 a[44:45], v29 offset:0\n"
  "ds_read_b64_tr_b16 a[46:47], v30 offset:0\n"
  "ds_read_b64_tr_b16 a[52:53], v29 offset:32\n"
  "ds_read_b64_tr_b16 a[54:55], v30 offset:32\n"
  "ds_read_b64_tr_b16 a[60:61], v29 offset:64\n"
  "ds_read_b64_tr_b16 a[62:63], v30 offset:64\n"
  "s_xor_b32 s70, s44, 1\n"
  "s_mul_i32 s70, s70, 12544\n"
  "s_add_u32 s70, s70, 100352\n"
  "v_add_u32 v29, s70, v27\n"
  "v_add_u32 v30, s70, v28\n"
  "ds_read_b64_tr_b16 a[188:189], v29 offset:0\n"
  "ds_read_b64_tr_b16 a[190:191], v30 offset:0\n"
  "ds_read_b64_tr_b16 a[196:197], v29 offset:32\n"
  "ds_read_b64_tr_b16 a[198:199], v30 offset:32\n"
  "ds_read_b64_tr_b16 a[204:205], v29 offset:64\n"
  "ds_read_b64_tr_b16 a[206:207], v30 offset:64\n"
  "s_xor_b32 s70, s44, 2\n"
  "s_mul_i32 s70, s70, 12544\n"
  "s_add_u32 s70, s70, 100352\n"
  "v_add_u32 v29, s70, v27\n"
  "v_add_u32 v30, s70, v28\n"
  "ds_read_b64_tr_b16 a[228:229], v29 offset:0\n"
  "ds_read_b64_tr_b16 a[230:231], v30 offset:0\n"
  "s_waitcnt lgkmcnt(10)\n"
  "ds_read_b64_tr_b16 a[236:237], v29 offset:32\n"
  "ds_read_b64_tr_b16 a[238:239], v30 offset:32\n"
  "ds_read_b64_tr_b16 a[244:245], v29 offset:64\n"
  "ds_read_b64_tr_b16 a[246:247], v30 offset:64\n"
  "s_xor_b32 s70, s44, 3\n"
  "s_mul_i32 s70, s70, 12544\n"
  "s_add_u32 s70, s70, 100352\n"
  "v_add_u32 v29, s70, v27\n"
  "v_add_u32 v30, s70, v28\n"
  "s_waitcnt lgkmcnt(10)\n"
  "ds_read_b64_tr_b16 v[140:141], v29 offset:0\n"
  "ds_read_b64_tr_b16 v[142:143], v30 offset:0\n"
  "ds_read_b64_tr_b16 v[148:149], v29 offset:32\n"
  "ds_read_b64_tr_b16 v[150:151], v30 offset:32\n"
  "s_waitcnt lgkmcnt(10)\n"
  "ds_read_b64_tr_b16 v[156:157], v29 offset:64\n"
  "ds_read_b64_tr_b16 v[158:159], v30 offset:64\n"
  "s_waitcnt lgkmcnt(0)\n"
  "s_barrier\n"
  "s_mul_i32 s68, s44, 393216\n"
  "s_add_u32 s68, s68, 6291456\n"
  "s_add_u32 m0, s69, 100352\n"
  "s_nop 0\n"
  "buffer_load_dwordx4 v24, s[28:31], s68 offen nt lds\n"
  "s_add_u32 m0, s69, 101376\n"
  "s_nop 0\n"
  "buffer_load_dwordx4 v25, s[28:31], s68 offen nt lds\n"
  "s_add_u32 m0, s69, 102400\n"
  "s_nop 0\n"
  "buffer_load_dwordx4 v26, s[28:31], s68 offen nt lds\n"
  "s_add_u32 s68, s68, 98304\n"
  "s_add_u32 m0, s69, 103488\n"
  "s_nop 0\n"
  "buffer_load_dwordx4 v24, s[28:31], s68 offen nt lds\n"
  "s_add_u32 m0, s69, 104512\n"
  "s_nop 0\n"
  "buffer_load_dwordx4 v25, s[28:31], s68 offen nt lds\n"
  "s_add_u32 m0, s69, 105536\n"
  "s_nop 0\n"
  "buffer_load_dwordx4 v26, s[28:31], s68 offen nt lds\n"
  "s_add_u32 s68, s68, 98304\n"
  "s_add_u32 m0, s69, 106624\n"
  "s_nop 0\n"
  "buffer_load_dwordx4 v24, s[28:31], s68 offen nt lds\n"
  "s_add_u32 m0, s69, 107648\n"
  "s_nop 0\n"
  "buffer_load_dwordx4 v25, s[28:31], s68 offen nt lds\n"
  "s_add_u32 m0, s69, 108672\n"
  "s_nop 0\n"
  "buffer_load_dwordx4 v26, s[28:31], s68 offen nt lds\n"
  "s_add_u32 s68, s68, 98304\n"
  "s_add_u32 m0, s69, 109760\n"
  "s_nop 0\n"
  "buffer_load_dwordx4 v24, s[28:31], s68 offen nt lds\n"
  "s_add_u32 m0, s69, 110784\n"
  "s_nop 0\n"
  "buffer_load_dwordx4 v25, s[28:31], s68 offen nt lds\n"
  "s_add_u32 m0, s69, 111808\n"
  "s_nop 0\n"
  "buffer_load_dwordx4 v26, s[28:31], s68 offen nt lds\n"
  "s_waitcnt vmcnt(24)\n"
  "s_barrier\n"
  "s_xor_b32 s70, s44, 0\n"
  "s_mul_i32 s70, s70, 12544\n"
  "s_add_u32 s70, s70, 0\n"
  "v_add_u32 v29, s70, v27\n"
  "v_add_u32 v30, s70, v28\n"
  "ds_read_b64_tr_b16 a[64:65], v29 offset:0\n"
  "ds_read_b64_tr_b16 a[66:67], v30 offset:0\n"
  "ds_read_b64_tr_b16 a[72:73], v29 offset:32\n"
  "ds_read_b64_tr_b16 a[74:75], v30 offset:32\n"
  "ds_read_b64_tr_b16 v[40:41], v29 offset:64\n"
  "ds_read_b64_tr_b16 v[42:43], v30 offset:64\n"
  "s_xor_b32 s70, s44, 1\n"
  "s_mul_i32 s70, s70, 12544\n"
  "s_add_u32 s70, s70, 0\n"
  "v_add_u32 v29, s70, v27\n"
  "v_add_u32 v30, s70, v28\n"
  "ds_read_b64_tr_b16 v[88:89], v29 offset:0\n"
  "ds_read_b64_tr_b16 v[90:91], v30 offset:0\n"
  "ds_read_b64_tr_b16 a[208:209], v29 offset:32\n"
  "ds_read_b64_tr_b16 a[210:211], v30 offset:32\n"
  "ds_read_b64_tr_b16 a[216:217], v29 offset:64\n"
  "ds_read_b64_tr_b16 a[218:219], v30 offset:64\n"
  "s_xor_b32 s70, s44, 2\n"
  "s_mul_i32 s70, s70, 12544\n"
  "s_add_u32 s70, s70, 0\n"
  "v_add_u32 v29, s70, v27\n"
  "v_add_u32 v30, s70, v28\n"
  "ds_read_b64_tr_b16 a[248:249], v29 offset:0\n"
  "ds_read_b64_tr_b16 a[250:251], v30 offset:0\n"
  "s_waitcnt lgkmcnt(10)\n"
  "ds_read_b64_tr_b16 v[96:97], v29 offset:32\n"
  "ds_read_b64_tr_b16 v[98:99], v30 offset:32\n"
  "ds_read_b64_tr_b16 v[128:129], v29 offset:64\n"
  "ds_read_b64_tr_b16 v[130:131], v30 offset:64\n"
  "s_xor_b32 s70, s44, 3\n"
  "s_mul_i32 s70, s70, 12544\n"
  "s_add_u32 s70, s70, 0\n"
  "v_add_u32 v29, s70, v27\n"
  "v_add_u32 v30, s70, v28\n"
  "s_waitcnt lgkmcnt(10)\n"
  "ds_read_b64_tr_b16 v[104:105], v29 offset:0\n"
  "ds_read_b64_tr_b16 v[106:107], v30 offset:0\n"
  "ds_read_b64_tr_b16 v[160:161], v29 offset:32\n"
  "ds_read_b64_tr_b16 v[162:163], v30 offset:32\n"
  "s_waitcnt lgkmcnt(10)\n"
  "ds_read_b64_tr_b16 v[168:169], v29 offset:64\n"
  "ds_read_b64_tr_b16 v[170:171], v30 offset:64\n"
  "s_waitcnt lgkmcnt(0)\n"
  "s_barrier\n"
  "s_mul_i32 s68, s44, 393216\n"
  "s_add_u32 s68, s68, 6291456\n"
  "s_add_u32 m0, s69, 0\n"
  "s_nop 0\n"
  "buffer_load_dwordx4 v24, s[32:35], s68 offen nt lds\n"
  "s_add_u32 m0, s69, 1024\n"
  "s_nop 0\n"
  "buffer_load_dwordx4 v25, s[32:35], s68 offen nt lds\n"
  "s_add_u32 m0, s69, 2048\n"
  "s_nop 0\n"
  "buffer_load_dwordx4 v26, s[32:35], s68 offen nt lds\n"
  "s_add_u32 s68, s68, 98304\n"
  "s_add_u32 m0, s69, 3136\n"
  "s_nop 0\n"
  "buffer_load_dwordx4 v24, s[32:35], s68 offen nt lds\n"
  "s_add_u32 m0, s69, 4160\n"
  "s_nop 0\n"
  "buffer_load_dwordx4 v25, s[32:35], s68 offen nt lds\n"
  "s_add_u32 m0, s69, 5184\n"
  "s_nop 0\n"
  "buffer_load_dwordx4 v26, s[32:35], s68 offen nt lds\n"
  "s_add_u32 s68, s68, 98304\n"
  "s_add_u32 m0, s69, 6272\n"
  "s_nop 0\n"
  "buffer_load_dwordx4 v24, s[32:35], s68 offen nt lds\n"
  "s_add_u32 m0, s69, 7296\n"
  "s_nop 0\n"
  "buffer_load_dwordx4 v25, s[32:35], s68 offen nt lds\n"
  "s_add_u32 m0, s69, 8320\n"
  "s_nop 0\n"
  "buffer_load_dwordx4 v26, s[32:35], s68 offen nt lds\n"
  "s_add_u32 s68, s68, 98304\n"
  "s_add_u32 m0, s69, 9408\n"
  "s_nop 0\n"
  "buffer_load_dwordx4 v24, s[32:35], s68 offen nt lds\n"
  "s_add_u32 m0, s69, 10432\n"
  "s_nop 0\n"
  "buffer_load_dwordx4 v25, s[32:35], s68 offen nt lds\n"
  "s_add_u32 m0, s69, 11456\n"
  "s_nop 0\n"
  "buffer_load_dwordx4 v26, s[32:35], s68 offen nt lds\n"
  "s_waitcnt vmcnt(24)\n"
  "s_barrier\n"
  "s_xor_b32 s70, s44, 0\n"
  "s_mul_i32 s70, s70, 12544\n"
  "s_add_u32 s70, s70, 50176\n"
  "v_add_u32 v29, s70, v27\n"
  "v_add_u32 v30, s70, v28\n"
  "ds_read_b64_tr_b16 a[68:69], v29 offset:0\n"
  "ds_read_b64_tr_b16 a[70:71], v30 offset:0\n"
  "ds_read_b64_tr_b16 a[76:77], v29 offset:32\n"
  "ds_read_b64_tr_b16 a[78:79], v30 offset:32\n"
  "ds_read_b64_tr_b16 v[44:45], v29 offset:64\n"
  "ds_read_b64_tr_b16 v[46:47], v30 offset:64\n"
  "s_xor_b32 s70, s44, 1\n"
  "s_mul_i32 s70, s70, 12544\n"
  "s_add_u32 s70, s70, 50176\n"
  "v_add_u32 v29, s70, v27\n"
  "v_add_u32 v30, s70, v28\n"
  "ds_read_b64_tr_b16 v[92:93], v29 offset:0\n"
  "ds_read_b64_tr_b16 v[94:95], v30 offset:0\n"
  "ds_read_b64_tr_b16 a[212:213], v29 offset:32\n"
  "ds_read_b64_tr_b16 a[214:215], v30 offset:32\n"
  "ds_read_b64_tr_b16 a[220:221], v29 offset:64\n"
  "ds_read_b64_tr_b16 a[222:223], v30 offset:64\n"
  "s_xor_b32 s70, s44, 2\n"
  "s_mul_i32 s70, s70, 12544\n"
  "s_add_u32 s70, s70, 50176\n"
  "v_add_u32 v29, s70, v27\n"
  "v_add_u32 v30, s70, v28\n"
  "ds_read_b64_tr_b16 a[252:253], v29 offset:0\n"
  "ds_read_b64_tr_b16 a[254:255], v30 offset:0\n"
  "s_waitcnt lgkmcnt(10)\n"
  "ds_read_b64_tr_b16 v[100:101], v29 offset:32\n"
  "ds_read_b64_tr_b16 v[102:103], v30 offset:32\n"
  "ds_read_b64_tr_b16 v[132:133], v29 offset:64\n"
  "ds_read_b64_tr_b16 v[134:135], v30 offset:64\n"
  "s_xor_b32 s70, s44, 3\n"
  "s_mul_i32 s70, s70, 12544\n"
  "s_add_u32 s70, s70, 50176\n"
  "v_add_u32 v29, s70, v27\n"
  "v_add_u32 v30, s70, v28\n"
  "s_waitcnt lgkmcnt(10)\n"
  "ds_read_b64_tr_b16 v[108:109], v29 offset:0\n"
  "ds_read_b64_tr_b16 v[110:111], v30 offset:0\n"
  "ds_read_b64_tr_b16 v[164:165], v29 offset:32\n"
  "ds_read_b64_tr_b16 v[166:167], v30 offset:32\n"
  "s_waitcnt lgkmcnt(10)\n"
  "ds_read_b64_tr_b16 v[172:173], v29 offset:64\n"
  "ds_read_b64_tr_b16 v[174:175], v30 offset:64\n"
  "s_waitcnt lgkmcnt(0)\n"
  "s_barrier\n"
  "s_waitcnt vmcnt(12)\n"
  "s_barrier\n"
  "s_mov_b32 s70, 100352\n"
  "v_add_u32 v29, s70, v27\n"
  "v_add_u32 v30, s70, v28\n"
  "ds_read_b64_tr_b16 v[176:177], v29 offset:0\n"
  "ds_read_b64_tr_b16 v[178:179], v30 offset:0\n"
  "ds_read_b64_tr_b16 v[184:185], v29 offset:32\n"
  "ds_read_b64_tr_b16 v[186:187], v30 offset:32\n"
  "ds_read_b64_tr_b16 v[192:193], v29 offset:64\n"
  "ds_read_b64_tr_b16 v[194:195], v30 offset:64\n"
  "s_mov_b32 s70, 112896\n"
  "v_add_u32 v29, s70, v27\n"
  "v_add_u32 v30, s70, v28\n"
  "ds_read_b64_tr_b16 v[180:181], v29 offset:0\n"
  "ds_read_b64_tr_b16 v[182:183], v30 offset:0\n"
  "ds_read_b64_tr_b16 v[188:189], v29 offset:32\n"
  "ds_read_b64_tr_b16 v[190:191], v30 offset:32\n"
  "ds_read_b64_tr_b16 v[196:197], v29 offset:64\n"
  "ds_read_b64_tr_b16 v[198:199], v30 offset:64\n"
  "s_mov_b32 s70, 125440\n"
  "v_add_u32 v29, s70, v27\n"
  "v_add_u32 v30, s70, v28\n"
  "ds_read_b64_tr_b16 v[216:217], v29 offset:0\n"
  "ds_read_b64_tr_b16 v[218:219], v30 offset:0\n"
  "s_waitcnt lgkmcnt(10)\n"
  "ds_read_b64_tr_b16 v[224:225], v29 offset:32\n"
  "ds_read_b64_tr_b16 v[226:227], v30 offset:32\n"
  "ds_read_b64_tr_b16 v[232:233], v29 offset:64\n"
  "ds_read_b64_tr_b16 v[234:235], v30 offset:64\n"
  "s_mov_b32 s70, 137984\n"
  "v_add_u32 v29, s70, v27\n"
  "v_add_u32 v30, s70, v28\n"
  "s_waitcnt lgkmcnt(10)\n"
  "ds_read_b64_tr_b16 v[220:221], v29 offset:0\n"
  "ds_read_b64_tr_b16 v[222:223], v30 offset:0\n"
  "ds_read_b64_tr_b16 v[228:229], v29 offset:32\n"
  "ds_read_b64_tr_b16 v[230:231], v30 offset:32\n"
  "s_waitcnt lgkmcnt(10)\n"
  "ds_read_b64_tr_b16 v[236:237], v29 offset:64\n"
  "ds_read_b64_tr_b16 v[238:239], v30 offset:64\n"
  "s_waitcnt lgkmcnt(0)\n"
  "s_barrier\n"
  "s_waitcnt vmcnt(0)\n"
  "s_barrier\n"
  "s_mov_b32 s70, 0\n"
  "v_add_u32 v29, s70, v27\n"
  "v_add_u32 v30, s70, v28\n"
  "ds_read_b64_tr_b16 v[200:201], v29 offset:0\n"
  "ds_read_b64_tr_b16 v[202:203], v30 offset:0\n"
  "ds_read_b64_tr_b16 v[112:113], v29 offset:32\n"
  "ds_read_b64_tr_b16 v[114:115], v30 offset:32\n"
  "ds_read_b64_tr_b16 v[208:209], v29 offset:64\n"
  "ds_read_b64_tr_b16 v[210:211], v30 offset:64\n"
  "s_mov_b32 s70, 12544\n"
  "v_add_u32 v29, s70, v27\n"
  "v_add_u32 v30, s70, v28\n"
  "ds_read_b64_tr_b16 v[204:205], v29 offset:0\n"
  "ds_read_b64_tr_b16 v[206:207], v30 offset:0\n"
  "ds_read_b64_tr_b16 v[116:117], v29 offset:32\n"
  "ds_read_b64_tr_b16 v[118:119], v30 offset:32\n"
  "ds_read_b64_tr_b16 v[212:213], v29 offset:64\n"
  "ds_read_b64_tr_b16 v[214:215], v30 offset:64\n"
  "s_mov_b32 s70, 25088\n"
  "v_add_u32 v29, s70, v27\n"
  "v_add_u32 v30, s70, v28\n"
  "ds_read_b64_tr_b16 v[240:241], v29 offset:0\n"
  "ds_read_b64_tr_b16 v[242:243], v30 offset:0\n"
  "s_waitcnt lgkmcnt(10)\n"
  "ds_read_b64_tr_b16 v[248:249], v29 offset:32\n"
  "ds_read_b64_tr_b16 v[250:251], v30 offset:32\n"
  "ds_read_b64_tr_b16 v[120:121], v29 offset:64\n"
  "ds_read_b64_tr_b16 v[122:123], v30 offset:64\n"
  "s_mov_b32 s70, 37632\n"
  "v_add_u32 v29, s70, v27\n"
  "v_add_u32 v30, s70, v28\n"
  "s_waitcnt lgkmcnt(10)\n"
  "ds_read_b64_tr_b16 v[244:245], v29 offset:0\n"
  "ds_read_b64_tr_b16 v[246:247], v30 offset:0\n"
  "ds_read_b64_tr_b16 v[252:253], v29 offset:32\n"
  "ds_read_b64_tr_b16 v[254:255], v30 offset:32\n"
  "s_waitcnt lgkmcnt(10)\n"
  "ds_read_b64_tr_b16 v[124:125], v29 offset:64\n"
  "ds_read_b64_tr_b16 v[126:127], v30 offset:64\n"
  "s_waitcnt lgkmcnt(0)\n"
  "s_barrier\n"
  "ds_write_b128 v5, v[32:35] offset:0\n"
  "ds_write_b128 v5, v[36:39] offset:1024\n"
  "ds_write_b128 v5, v[40:43] offset:2048\n"
  "ds_write_b128 v5, v[44:47] offset:3072\n"
  "ds_write_b128 v5, v[48:51] offset:4096\n"
  "ds_write_b128 v5, v[52:55] offset:5120\n"
  "ds_write_b128 v5, v[56:59] offset:6144\n"
  "ds_write_b128 v5, v[60:63] offset:7168\n"
  "ds_write_b128 v5, v[64:67] offset:8192\n"
  "ds_write_b128 v5, v[68:71] offset:9216\n"
  "ds_write_b128 v5, v[72:75] offset:10240\n"
  "ds_write_b128 v5, v[76:79] offset:11264\n"
  "ds_write_b128 v5, v[80:83] offset:12288\n"
  "ds_write_b128 v5, v[84:87] offset:13312\n"
  "s_waitcnt lgkmcnt(10)\n"
  "ds_write_b128 v5, v[88:91] offset:14336\n"
  "ds_write_b128 v5, v[92:95] offset:15360\n"
  "ds_write_b128 v5, v[96:99] offset:16384\n"
  "ds_write_b128 v5, v[100:103] offset:17408\n"
  "s_waitcnt lgkmcnt(10)\n"
  "ds_write_b128 v5, v[104:107] offset:18432\n"
  "ds_write_b128 v5, v[108:111] offset:19456\n"
  "ds_write_b128 v5, v[112:115] offset:20480\n"
  "ds_write_b128 v5, v[116:119] offset:21504\n"
  "s_waitcnt lgkmcnt(10)\n"
  "ds_write_b128 v5, v[120:123] offset:22528\n"
  "ds_write_b128 v5, v[124:127] offset:23552\n"
  "s_waitcnt lgkmcnt(0)\n"
  "s_memtime s[64:65]\n"
  "s_memrealtime s[66:67]\n"
  "s_waitcnt lgkmcnt(0)\n"
  "s_mov_b32 s96, s64\n"
  "s_mov_b32 s97, s66\n"
  "s_mov_b32 s98, 352\n"
  "H_LAYER_%=:\n"
  "s_nop 7\n"
  "s_mov_b32 s56, s45\n"
  "s_add_u32 s99, s46, s45\n"
  "s_add_u32 s74, s99, 0\n"
  "s_lshl_b32 s74, s74, 7\n"
  "s_add_u32 s74, s74, 1835008\n"
  "s_mov_b32 s62, 0\n"
  "s_cmp_ge_u32 s56, 64\n"
  "s_cbranch_scc1 H_F0K_%=\n"
  "s_waitcnt vmcnt(0) lgkmcnt(0)\n"
  "H_F0P_%=:\n"
  "buffer_load_dword v19, off, s[16:19], s74 sc1\n"
  "s_waitcnt vmcnt(0)\n"
  "v_readfirstlane_b32 s60, v19\n"
  "s_cmp_lg_u32 s60, -1\n"
  "s_cbranch_scc1 H_F0K_%=\n"
  "s_sleep 4\n"
  "s_add_u32 s62, s62, 1\n"
  "s_cmp_gt_u32 s62, 300000\n"
  "s_cbranch_scc1 H_FAIL_%=\n"
  "s_and_b32 s60, s62, 255\n"
  "s_cmp_lg_u32 s60, 0\n"
  "s_cbranch_scc1 H_F0P_%=\n"
  "v_mov_b32 v19, 0\n"
  "buffer_load_dword v19, v19, s[40:43], 0 offen sc1\n"
  "s_waitcnt vmcnt(0)\n"
  "v_readfirstlane_b32 s60, v19\n"
  "s_cmp_lg_u32 s60, 0\n"
  "s_cbranch_scc1 H_EXIT_%=\n"
  "s_branch H_F0P_%=\n"
  "H_F0K_%=:\n"
  "s_barrier\n"
  "s_add_u32 s72, s99, 0\n"
  "s_mul_i32 s72, s72, 40960\n"
  "s_add_u32 s73, s51, 0\n"
  "s_add_u32 s72, s72, s73\n"
  "s_mov_b32 m0, s73\n"
  "s_nop 0\n"
  "buffer_load_dwordx4 v0, s[24:27], s72 offen offset:0 sc1 lds\n"
  "buffer_load_dwordx4 v0, s[24:27], s72 offen offset:1024 sc1 lds\n"
  "buffer_load_dwordx4 v0, s[24:27], s72 offen offset:2048 sc1 lds\n"
  "buffer_load_dwordx4 v0, s[24:27], s72 offen offset:3072 sc1 lds\n"
  "s_add_u32 s72, s72, 4096\n"
  "s_add_u32 m0, s73, 4096\n"
  "s_nop 0\n"
  "buffer_load_dwordx4 v0, s[24:27], s72 offen offset:0 sc1 lds\n"
  "s_waitcnt vmcnt(0)\n"
  "s_barrier\n"
  "ds_read_b128 v[40:43], v4 offset:0\n"
  "ds_read_b128 v[44:47], v4 offset:20480\n"
  "ds_read_b128 v[80:83], v5 offset:0\n"
  "ds_read_b128 v[84:87], v5 offset:1024\n"
  "s_waitcnt lgkmcnt(3)\n"
  "v_mfma_f32_16x16x32_bf16 v[56:59], a[0:3], v[40:43], 0\n"
  "ds_read_b128 v[48:51], v4 offset:4096\n"
  "s_add_u32 s72, s99, 0\n"
  "v_mfma_f32_16x16x32_bf16 v[60:63], a[8:11], v[40:43], 0\n"
  "ds_read_b128 v[52:55], v4 offset:24576\n"
  "s_mul_i32 s72, s72, 40960\n"
  "v_mfma_f32_16x16x32_bf16 v[64:67], a[16:19], v[40:43], 0\n"
  "ds_read_b128 v[88:91], v5 offset:2048\n"
  "s_add_u32 s73, s51, 10240\n"
  "v_mfma_f32_16x16x32_bf16 v[68:71], a[24:27], v[40:43], 0\n"
  "ds_read_b128 v[92:95], v5 offset:3072\n"
  "s_add_u32 s72, s72, s73\n"
  "v_mfma_f32_16x16x32_bf16 v[72:75], a[32:35], v[40:43], 0\n"
  "s_mov_b32 m0, s73\n"
  "s_nop 0\n"
  "buffer_load_dwordx4 v0, s[24:27], s72 offen offset:0 sc1 lds\n"
  "s_waitcnt lgkmcnt(5)\n"
  "v_mfma_f32_16x16x32_bf16 v[76:79], v[80:83], v[40:43], 0\n"
  "buffer_load_dwordx4 v0, s[24:27], s72 offen offset:1024 sc1 lds\n"
  "v_mfma_f32_16x16x32_bf16 v[56:59], a[4:7], v[44:47], v[56:59]\n"
  "buffer_load_dwordx4 v0, s[24:27], s72 offen offset:2048 sc1 lds\n"
  "v_mfma_f32_16x16x32_bf16 v[60:63], a[12:15], v[44:47], v[60:63]\n"
  "buffer_load_dwordx4 v0, s[24:27], s72 offen offset:3072 sc1 lds\n"
  "v_mfma_f32_16x16x32_bf16 v[64:67], a[20:23], v[44:47], v[64:67]\n"
  "s_add_u32 s72, s72, 4096\n"
  "s_add_u32 m0, s73, 4096\n"
  "v_mfma_f32_16x16x32_bf16 v[68:71], a[28:31], v[44:47], v[68:71]\n"
  "buffer_load_dwordx4 v0, s[24:27], s72 offen offset:0 sc1 lds\n"
  "v_mfma_f32_16x16x32_bf16 v[72:75], a[36:39], v[44:47], v[72:75]\n"
  "s_add_u32 s70, s99, 2\n"
  "s_lshl_b32 s70, s70, 7\n"
  "s_waitcnt lgkmcnt(4)\n"
  "v_mfma_f32_16x16x32_bf16 v[76:79], v[84:87], v[44:47], v[76:79]\n"
  "s_add_u32 s70, s70, 1835008\n"
  "buffer_load_dword v19, off, s[16:19], s70 sc1\n"
  "s_waitcnt lgkmcnt(3)\n"
  "v_mfma_f32_16x16x32_bf16 v[56:59], a[40:43], v[48:51], v[56:59]\n"
  "ds_read_b128 v[40:43], v1 offset:0\n"
  "v_mfma_f32_16x16x32_bf16 v[60:63], a[48:51], v[48:51], v[60:63]\n"
  "ds_read_b128 v[44:47], v1 offset:20480\n"
  "v_mfma_f32_16x16x32_bf16 v[64:67], a[56:59], v[48:51], v[64:67]\n"
  "ds_read_b128 v[96:99], v5 offset:4096\n"
  "v_mfma_f32_16x16x32_bf16 v[68:71], a[64:67], v[48:51], v[68:71]\n"
  "ds_read_b128 v[100:103], v5 offset:5120\n"
  "v_mfma_f32_16x16x32_bf16 v[72:75], a[72:75], v[48:51], v[72:75]\n"
  "ds_read_b128 v[80:83], v5 offset:6144\n"
  "s_waitcnt lgkmcnt(6)\n"
  "v_mfma_f32_16x16x32_bf16 v[76:79], v[88:91], v[48:51], v[76:79]\n"
  "ds_read_b128 v[84:87], v5 offset:7168\n"
  "v_mfma_f32_16x16x32_bf16 v[56:59], a[44:47], v[52:55], v[56:59]\n"
  "v_mfma_f32_16x16x32_bf16 v[60:63], a[52:55], v[52:55], v[60:63]\n"
  "v_mfma_f32_16x16x32_bf16 v[64:67], a[60:63], v[52:55], v[64:67]\n"
  "v_mfma_f32_16x16x32_bf16 v[68:71], a[68:71], v[52:55], v[68:71]\n"
  "v_mfma_f32_16x16x32_bf16 v[72:75], a[76:79], v[52:55], v[72:75]\n"
  "s_waitcnt lgkmcnt(6)\n"
  "v_mfma_f32_16x16x32_bf16 v[76:79], v[92:95], v[52:55], v[76:79]\n"
  "s_waitcnt lgkmcnt(5)\n"
  "v_mfma_f32_16x16x32_bf16 v[56:59], a[80:83], v[40:43], v[56:59]\n"
  "ds_read_b128 v[48:51], v2 offset:0\n"
  "v_mfma_f32_16x16x32_bf16 v[60:63], a[88:91], v[40:43], v[60:63]\n"
  "ds_read_b128 v[52:55], v2 offset:20480\n"
  "v_mfma_f32_16x16x32_bf16 v[64:67], a[96:99], v[40:43], v[64:67]\n"
  "ds_read_b128 v[88:91], v5 offset:8192\n"
  "v_mfma_f32_16x16x32_bf16 v[68:71], a[104:107], v[40:43], v[68:71]\n"
  "ds_read_b128 v[92:95], v5 offset:9216\n"
  "s_waitcnt lgkmcnt(7)\n"
  "v_mfma_f32_16x16x32_bf16 v[72:75], v[96:99], v[40:43], v[72:75]\n"
  "s_waitcnt lgkmcnt(5)\n"
  "v_mfma_f32_16x16x32_bf16 v[76:79], v[80:83], v[40:43], v[76:79]\n"
  "v_mfma_f32_16x16x32_bf16 v[56:59], a[84:87], v[44:47], v[56:59]\n"
  "v_mfma_f32_16x16x32_bf16 v[60:63], a[92:95], v[44:47], v[60:63]\n"
  "v_mfma_f32_16x16x32_bf16 v[64:67], a[100:103], v[44:47], v[64:67]\n"
  "v_mfma_f32_16x16x32_bf16 v[68:71], a[108:111], v[44:47], v[68:71]\n"
  "v_mfma_f32_16x16x32_bf16 v[72:75], v[100:103], v[44:47], v[72:75]\n"
  "s_waitcnt lgkmcnt(4)\n"
  "v_mfma_f32_16x16x32_bf16 v[76:79], v[84:87], v[44:47], v[76:79]\n"
  "s_waitcnt lgkmcnt(3)\n"
  "v_mfma_f32_16x16x32_bf16 v[56:59], a[112:115], v[48:51], v[56:59]\n"
  "ds_read_b128 v[40:43], v3 offset:0\n"
  "v_mfma_f32_16x16x32_bf16 v[60:63], a[120:123], v[48:51], v[60:63]\n"
  "ds_read_b128 v[44:47], v3 offset:20480\n"
  "v_mfma_f32_16x16x32_bf16 v[64:67], a[128:131], v[48:51], v[64:67]\n"
  "ds_read_b128 v[96:99], v5 offset:10240\n"
  "v_mfma_f32_16x16x32_bf16 v[68:71], a[136:139], v[48:51], v[68:71]\n"
  "ds_read_b128 v[100:103], v5 offset:11264\n"
  "v_mfma_f32_16x16x32_bf16 v[72:75], a[144:147], v[48:51], v[72:75]\n"
  "ds_read_b128 v[80:83], v5 offset:12288\n"
  "s_waitcnt lgkmcnt(6)\n"
  "v_mfma_f32_16x16x32_bf16 v[76:79], v[88:91], v[48:51], v[76:79]\n"
  "ds_read_b128 v[84:87], v5 offset:13312\n"
  "v_mfma_f32_16x16x32_bf16 v[56:59], a[116:119], v[52:55], v[56:59]\n"
  "v_mfma_f32_16x16x32_bf16 v[60:63], a[124:127], v[52:55], v[60:63]\n"
  "v_mfma_f32_16x16x32_bf16 v[64:67], a[132:135], v[52:55], v[64:67]\n"
  "v_mfma_f32_16x16x32_bf16 v[68:71], a[140:143], v[52:55], v[68:71]\n"
  "v_mfma_f32_16x16x32_bf16 v[72:75], a[148:151], v[52:55], v[72:75]\n"
  "s_waitcnt lgkmcnt(6)\n"
  "v_mfma_f32_16x16x32_bf16 v[76:79], v[92:95], v[52:55], v[76:79]\n"
  "s_waitcnt lgkmcnt(5)\n"
  "v_mfma_f32_16x16x32_bf16 v[56:59], a[152:155], v[40:43], v[56:59]\n"
  "ds_read_b128 v[48:51], v1 offset:4096\n"
  "v_mfma_f32_16x16x32_bf16 v[60:63], a[160:163], v[40:43], v[60:63]\n"
  "ds_read_b128 v[52:55], v1 offset:24576\n"
  "v_mfma_f32_16x16x32_bf16 v[64:67], a[168:171], v[40:43], v[64:67]\n"
  "ds_read_b128 v[88:91], v5 offset:14336\n"
  "v_mfma_f32_16x16x32_bf16 v[68:71], a[176:179], v[40:43], v[68:71]\n"
  "ds_read_b128 v[92:95], v5 offset:15360\n"
  "s_waitcnt lgkmcnt(7)\n"
  "v_mfma_f32_16x16x32_bf16 v[72:75], v[96:99], v[40:43], v[72:75]\n"
  "s_waitcnt lgkmcnt(5)\n"
  "v_mfma_f32_16x16x32_bf16 v[76:79], v[80:83], v[40:43], v[76:79]\n"
  "v_mfma_f32_16x16x32_bf16 v[56:59], a[156:159], v[44:47], v[56:59]\n"
  "v_mfma_f32_16x16x32_bf16 v[60:63], a[164:167], v[44:47], v[60:63]\n"
  "v_mfma_f32_16x16x32_bf16 v[64:67], a[172:175], v[44:47], v[64:67]\n"
  "v_mfma_f32_16x16x32_bf16 v[68:71], a[180:183], v[44:47], v[68:71]\n"
  "v_mfma_f32_16x16x32_bf16 v[72:75], v[100:103], v[44:47], v[72:75]\n"
  "s_waitcnt lgkmcnt(4)\n"
  "v_mfma_f32_16x16x32_bf16 v[76:79], v[84:87], v[44:47], v[76:79]\n"
  "s_waitcnt lgkmcnt(3)\n"
  "v_mfma_f32_16x16x32_bf16 v[56:59], a[184:187], v[48:51], v[56:59]\n"
  "ds_read_b128 v[40:43], v2 offset:4096\n"
  "v_mfma_f32_16x16x32_bf16 v[60:63], a[192:195], v[48:51], v[60:63]\n"
  "ds_read_b128 v[44:47], v2 offset:24576\n"
  "v_mfma_f32_16x16x32_bf16 v[64:67], a[200:203], v[48:51], v[64:67]\n"
  "ds_read_b128 v[96:99], v5 offset:16384\n"
  "s_waitcnt lgkmcnt(4)\n"
  "v_mfma_f32_16x16x32_bf16 v[68:71], v[88:91], v[48:51], v[68:71]\n"
  "ds_read_b128 v[100:103], v5 offset:17408\n"
  "v_mfma_f32_16x16x32_bf16 v[72:75], a[208:211], v[48:51], v[72:75]\n"
  "v_mfma_f32_16x16x32_bf16 v[76:79], a[216:219], v[48:51], v[76:79]\n"
  "v_mfma_f32_16x16x32_bf16 v[56:59], a[188:191], v[52:55], v[56:59]\n"
  "v_mfma_f32_16x16x32_bf16 v[60:63], a[196:199], v[52:55], v[60:63]\n"
  "v_mfma_f32_16x16x32_bf16 v[64:67], a[204:207], v[52:55], v[64:67]\n"
  "s_waitcnt lgkmcnt(4)\n"
  "v_mfma_f32_16x16x32_bf16 v[68:71], v[92:95], v[52:55], v[68:71]\n"
  "v_mfma_f32_16x16x32_bf16 v[72:75], a[212:215], v[52:55], v[72:75]\n"
  "v_mfma_f32_16x16x32_bf16 v[76:79], a[220:223], v[52:55], v[76:79]\n"
  "s_waitcnt lgkmcnt(3)\n"
  "v_mfma_f32_16x16x32_bf16 v[56:59], a[224:227], v[40:43], v[56:59]\n"
  "ds_read_b128 v[48:51], v3 offset:4096\n"
  "v_mfma_f32_16x16x32_bf16 v[60:63], a[232:235], v[40:43], v[60:63]\n"
  "ds_read_b128 v[52:55], v3 offset:24576\n"
  "v_mfma_f32_16x16x32_bf16 v[64:67], a[240:243], v[40:43], v[64:67]\n"
  "ds_read_b128 v[80:83], v5 offset:18432\n"
  "v_mfma_f32_16x16x32_bf16 v[68:71], a[248:251], v[40:43], v[68:71]\n"
  "ds_read_b128 v[84:87], v5 offset:19456\n"
  "s_waitcnt lgkmcnt(5)\n"
  "v_mfma_f32_16x16x32_bf16 v[72:75], v[96:99], v[40:43], v[72:75]\n"
  "v_mfma_f32_16x16x32_bf16 v[76:79], v[128:131], v[40:43], v[76:79]\n"
  "v_mfma_f32_16x16x32_bf16 v[56:59], a[228:231], v[44:47], v[56:59]\n"
  "v_mfma_f32_16x16x32_bf16 v[60:63], a[236:239], v[44:47], v[60:63]\n"
  "v_mfma_f32_16x16x32_bf16 v[64:67], a[244:247], v[44:47], v[64:67]\n"
  "v_mfma_f32_16x16x32_bf16 v[68:71], a[252:255], v[44:47], v[68:71]\n"
  "s_waitcnt lgkmcnt(4)\n"
  "v_mfma_f32_16x16x32_bf16 v[72:75], v[100:103], v[44:47], v[72:75]\n"
  "v_mfma_f32_16x16x32_bf16 v[76:79], v[132:135], v[44:47], v[76:79]\n"
  "s_waitcnt lgkmcnt(3)\n"
  "v_mfma_f32_16x16x32_bf16 v[56:59], v[136:139], v[48:51], v[56:59]\n"
  "ds_read_b128 v[40:43], v0 offset:8192\n"
  "v_mfma_f32_16x16x32_bf16 v[60:63], v[144:147], v[48:51], v[60:63]\n"
  "ds_read_b128 v[44:47], v0 offset:28672\n"
  "v_mfma_f32_16x16x32_bf16 v[64:67], v[152:155], v[48:51], v[64:67]\n"
  "ds_read_b128 v[88:91], v5 offset:20480\n"
  "s_waitcnt lgkmcnt(4)\n"
  "v_mfma_f32_16x16x32_bf16 v[68:71], v[80:83], v[48:51], v[68:71]\n"
  "ds_read_b128 v[92:95], v5 offset:21504\n"
  "v_mfma_f32_16x16x32_bf16 v[72:75], v[160:163], v[48:51], v[72:75]\n"
  "v_mfma_f32_16x16x32_bf16 v[76:79], v[168:171], v[48:51], v[76:79]\n"
  "v_mfma_f32_16x16x32_bf16 v[56:59], v[140:143], v[52:55], v[56:59]\n"
  "v_mfma_f32_16x16x32_bf16 v[60:63], v[148:151], v[52:55], v[60:63]\n"
  "v_mfma_f32_16x16x32_bf16 v[64:67], v[156:159], v[52:55], v[64:67]\n"
  "s_waitcnt lgkmcnt(4)\n"
  "v_mfma_f32_16x16x32_bf16 v[68:71], v[84:87], v[52:55], v[68:71]\n"
  "v_mfma_f32_16x16x32_bf16 v[72:75], v[164:167], v[52:55], v[72:75]\n"
  "v_mfma_f32_16x16x32_bf16 v[76:79], v[172:175], v[52:55], v[76:79]\n"
  "s_waitcnt vmcnt(0) lgkmcnt(0)\n"
  "H_LOOP_%=:\n"
  "v_mfma_f32_16x16x32_bf16 v[56:59], v[176:179], v[40:43], v[56:59]\n"
  "ds_read_b128 v[48:51], v0 offset:9216\n"
  "s_add_u32 s60, s56, 0\n"
  "v_mfma_f32_16x16x32_bf16 v[60:63], v[184:187], v[40:43], v[60:63]\n"
  "ds_read_b128 v[52:55], v0 offset:29696\n"
  "s_bfe_u32 s57, s60, 131073\n"
  "v_mfma_f32_16x16x32_bf16 v[64:67], v[192:195], v[40:43], v[64:67]\n"
  "ds_read_b128 v[96:99], v5 offset:22528\n"
  "s_lshl_b32 s57, s57, 18\n"
  "v_mfma_f32_16x16x32_bf16 v[68:71], v[200:203], v[40:43], v[68:71]\n"
  "ds_read_b128 v[100:103], v5 offset:23552\n"
  "s_xor_b32 s60, s60, s47\n"
  "v_mfma_f32_16x16x32_bf16 v[72:75], v[88:91], v[40:43], v[72:75]\n"
  "s_and_b32 s60, s60, 1\n"
  "s_lshl_b32 s60, s60, 13\n"
  "v_mfma_f32_16x16x32_bf16 v[76:79], v[208:211], v[40:43], v[76:79]\n"
  "s_add_u32 s57, s57, s60\n"
  "s_add_u32 s63, s57, s55\n"
  "v_mfma_f32_16x16x32_bf16 v[56:59], v[180:183], v[44:47], v[56:59]\n"
  "s_add_u32 s75, s57, s50\n"
  "v_mfma_f32_16x16x32_bf16 v[60:63], v[188:191], v[44:47], v[60:63]\n"
  "v_mfma_f32_16x16x32_bf16 v[64:67], v[196:199], v[44:47], v[64:67]\n"
  "v_mfma_f32_16x16x32_bf16 v[68:71], v[204:207], v[44:47], v[68:71]\n"
  "v_mfma_f32_16x16x32_bf16 v[72:75], v[92:95], v[44:47], v[72:75]\n"
  "v_mfma_f32_16x16x32_bf16 v[76:79], v[212:215], v[44:47], v[76:79]\n"
  "s_waitcnt lgkmcnt(3)\n"
  "v_mfma_f32_16x16x32_bf16 v[56:59], v[216:219], v[48:51], v[56:59]\n"
  "v_mfma_f32_16x16x32_bf16 v[60:63], v[224:227], v[48:51], v[60:63]\n"
  "v_mfma_f32_16x16x32_bf16 v[64:67], v[232:235], v[48:51], v[64:67]\n"
  "v_mfma_f32_16x16x32_bf16 v[68:71], v[240:243], v[48:51], v[68:71]\n"
  "v_mfma_f32_16x16x32_bf16 v[72:75], v[248:251], v[48:51], v[72:75]\n"
  "s_waitcnt vmcnt(5)\n"
  "s_waitcnt lgkmcnt(0)\n"
  "s_barrier\n"
  "s_mov_b32 s62, 0\n"
  "v_readfirstlane_b32 s60, v19\n"
  "s_cmp_lg_u32 s60, -1\n"
  "s_cbranch_scc1 H_F2K_%=\n"
  "s_cmp_ge_u32 s56, 62\n"
  "s_cbranch_scc1 H_F2K_%=\n"
  "s_waitcnt vmcnt(0) lgkmcnt(0)\n"
  "H_F2P_%=:\n"
  "buffer_load_dword v19, off, s[16:19], s70 sc1\n"
  "s_waitcnt vmcnt(0)\n"
  "v_readfirstlane_b32 s60, v19\n"
  "s_cmp_lg_u32 s60, -1\n"
  "s_cbranch_scc1 H_F2K_%=\n"
  "s_sleep 4\n"
  "s_add_u32 s62, s62, 1\n"
  "s_cmp_gt_u32 s62, 300000\n"
  "s_cbranch_scc1 H_FAIL_%=\n"
  "s_and_b32 s60, s62, 255\n"
  "s_cmp_lg_u32 s60, 0\n"
  "s_cbranch_scc1 H_F2P_%=\n"
  "v_mov_b32 v19, 0\n"
  "buffer_load_dword v19, v19, s[40:43], 0 offen sc1\n"
  "s_waitcnt vmcnt(0)\n"
  "v_readfirstlane_b32 s60, v19\n"
  "s_cmp_lg_u32 s60, 0\n"
  "s_cbranch_scc1 H_EXIT_%=\n"
  "s_branch H_F2P_%=\n"
  "H_F2K_%=:\n"
  "v_mfma_f32_16x16x32_bf16 v[76:79], v[96:99], v[48:51], v[76:79]\n"
  "ds_read_b128 v[40:43], v4 offset:10240\n"
  "v_mfma_f32_16x16x32_bf16 v[56:59], v[220:223], v[52:55], v[56:59]\n"
  "ds_read_b128 v[44:47], v4 offset:30720\n"
  "v_mfma_f32_16x16x32_bf16 v[60:63], v[228:231], v[52:55], v[60:63]\n"
  "ds_read_b128 v[80:83], v5 offset:0\n"
  "v_mfma_f32_16x16x32_bf16 v[64:67], v[236:239], v[52:55], v[64:67]\n"
  "ds_read_b128 v[84:87], v5 offset:1024\n"
  "v_mfma_f32_16x16x32_bf16 v[68:71], v[244:247], v[52:55], v[68:71]\n"
  "v_mfma_f32_16x16x32_bf16 v[72:75], v[252:255], v[52:55], v[72:75]\n"
  "v_mfma_f32_16x16x32_bf16 v[76:79], v[100:103], v[52:55], v[76:79]\n"
  "s_waitcnt lgkmcnt(3)\n"
  "v_mfma_f32_16x16x32_bf16 v[104:107], a[0:3], v[40:43], 0\n"
  "ds_read_b128 v[48:51], v4 offset:14336\n"
  "s_add_u32 s72, s99, 2\n"
  "v_mfma_f32_16x16x32_bf16 v[108:111], a[8:11], v[40:43], 0\n"
  "ds_read_b128 v[52:55], v4 offset:34816\n"
  "s_mul_i32 s72, s72, 40960\n"
  "v_mfma_f32_16x16x32_bf16 v[112:115], a[16:19], v[40:43], 0\n"
  "ds_read_b128 v[88:91], v5 offset:2048\n"
  "s_add_u32 s73, s51, 0\n"
  "v_mfma_f32_16x16x32_bf16 v[116:119], a[24:27], v[40:43], 0\n"
  "ds_read_b128 v[92:95], v5 offset:3072\n"
  "s_add_u32 s72, s72, s73\n"
  "v_mfma_f32_16x16x32_bf16 v[120:123], a[32:35], v[40:43], 0\n"
  "s_mov_b32 m0, s73\n"
  "s_nop 0\n"
  "buffer_load_dwordx4 v0, s[24:27], s72 offen offset:0 sc1 lds\n"
  "s_waitcnt lgkmcnt(5)\n"
  "v_mfma_f32_16x16x32_bf16 v[124:127], v[80:83], v[40:43], 0\n"
  "v_mfma_f32_16x16x32_bf16 v[104:107], a[4:7], v[44:47], v[104:107]\n"
  "v_mfma_f32_16x16x32_bf16 v[108:111], a[12:15], v[44:47], v[108:111]\n"
  "v_mfma_f32_16x16x32_bf16 v[112:115], a[20:23], v[44:47], v[112:115]\n"
  "buffer_load_dwordx4 v0, s[24:27], s72 offen offset:1024 sc1 lds\n"
  "v_mfma_f32_16x16x32_bf16 v[116:119], a[28:31], v[44:47], v[116:119]\n"
  "v_mfma_f32_16x16x32_bf16 v[120:123], a[36:39], v[44:47], v[120:123]\n"
  "s_waitcnt lgkmcnt(4)\n"
  "v_mfma_f32_16x16x32_bf16 v[124:127], v[84:87], v[44:47], v[124:127]\n"
  "s_waitcnt lgkmcnt(3)\n"
  "v_mfma_f32_16x16x32_bf16 v[104:107], a[40:43], v[48:51], v[104:107]\n"
  "ds_read_b128 v[40:43], v1 offset:10240\n"
  "buffer_load_dwordx4 v0, s[24:27], s72 offen offset:2048 sc1 lds\n"
  "v_mfma_f32_16x16x32_bf16 v[108:111], a[48:51], v[48:51], v[108:111]\n"
  "ds_read_b128 v[44:47], v1 offset:30720\n"
  "v_mfma_f32_16x16x32_bf16 v[112:115], a[56:59], v[48:51], v[112:115]\n"
  "ds_read_b128 v[96:99], v5 offset:4096\n"
  "v_mfma_f32_16x16x32_bf16 v[116:119], a[64:67], v[48:51], v[116:119]\n"
  "ds_read_b128 v[100:103], v5 offset:5120\n"
  "v_mfma_f32_16x16x32_bf16 v[120:123], a[72:75], v[48:51], v[120:123]\n"
  "ds_read_b128 v[80:83], v5 offset:6144\n"
  "s_waitcnt lgkmcnt(6)\n"
  "v_mfma_f32_16x16x32_bf16 v[124:127], v[88:91], v[48:51], v[124:127]\n"
  "ds_read_b128 v[84:87], v5 offset:7168\n"
  "v_mfma_f32_16x16x32_bf16 v[104:107], a[44:47], v[52:55], v[104:107]\n"
  "buffer_load_dwordx4 v0, s[24:27], s72 offen offset:3072 sc1 lds\n"
  "v_mfma_f32_16x16x32_bf16 v[108:111], a[52:55], v[52:55], v[108:111]\n"
  "v_mfma_f32_16x16x32_bf16 v[112:115], a[60:63], v[52:55], v[112:115]\n"
  "v_mfma_f32_16x16x32_bf16 v[116:119], a[68:71], v[52:55], v[116:119]\n"
  "v_mfma_f32_16x16x32_bf16 v[120:123], a[76:79], v[52:55], v[120:123]\n"
  "s_add_u32 s72, s72, 4096\n"
  "s_add_u32 m0, s73, 4096\n"
  "s_waitcnt lgkmcnt(6)\n"
  "v_mfma_f32_16x16x32_bf16 v[124:127], v[92:95], v[52:55], v[124:127]\n"
  "buffer_load_dwordx4 v0, s[24:27], s72 offen offset:0 sc1 lds\n"
  "s_waitcnt lgkmcnt(5)\n"
  "v_mfma_f32_16x16x32_bf16 v[104:107], a[80:83], v[40:43], v[104:107]\n"
  "ds_read_b128 v[48:51], v2 offset:10240\n"
  "v_mfma_f32_16x16x32_bf16 v[108:111], a[88:91], v[40:43], v[108:111]\n"
  "ds_read_b128 v[52:55], v2 offset:30720\n"
  "v_mfma_f32_16x16x32_bf16 v[112:115], a[96:99], v[40:43], v[112:115]\n"
  "ds_read_b128 v[88:91], v5 offset:8192\n"
  "v_mfma_f32_16x16x32_bf16 v[116:119], a[104:107], v[40:43], v[116:119]\n"
  "ds_read_b128 v[92:95], v5 offset:9216\n"
  "s_waitcnt lgkmcnt(7)\n"
  "v_mfma_f32_16x16x32_bf16 v[120:123], v[96:99], v[40:43], v[120:123]\n"
  "s_waitcnt lgkmcnt(5)\n"
  "v_mfma_f32_16x16x32_bf16 v[124:127], v[80:83], v[40:43], v[124:127]\n"
  "v_cvt_pk_bf16_f32 v24, v68, v69\n"
  "v_cvt_pk_bf16_f32 v25, v70, v71\n"
  "v_mfma_f32_16x16x32_bf16 v[104:107], a[84:87], v[44:47], v[104:107]\n"
  "v_cvt_pk_bf16_f32 v26, v72, v73\n"
  "v_cvt_pk_bf16_f32 v27, v74, v75\n"
  "v_mfma_f32_16x16x32_bf16 v[108:111], a[92:95], v[44:47], v[108:111]\n"
  "v_cvt_pk_bf16_f32 v28, v56, v57\n"
  "v_cvt_pk_bf16_f32 v29, v58, v59\n"
  "v_mfma_f32_16x16x32_bf16 v[112:115], a[100:103], v[44:47], v[112:115]\n"
  "v_cvt_pk_bf16_f32 v30, v60, v61\n"
  "v_cvt_pk_bf16_f32 v31, v62, v63\n"
  "v_mfma_f32_16x16x32_bf16 v[116:119], a[108:111], v[44:47], v[116:119]\n"
  "v_cvt_pk_bf16_f32 v34, v64, v65\n"
  "v_cvt_pk_bf16_f32 v35, v66, v67\n"
  "v_mfma_f32_16x16x32_bf16 v[120:123], v[100:103], v[44:47], v[120:123]\n"
  "v_cvt_pk_bf16_f32 v32, v76, v77\n"
  "v_cvt_pk_bf16_f32 v33, v78, v79\n"
  "s_waitcnt lgkmcnt(4)\n"
  "v_mfma_f32_16x16x32_bf16 v[124:127], v[84:87], v[44:47], v[124:127]\n"
  "v_mov_b64 v[56:57], 0\n"
  "s_waitcnt lgkmcnt(3)\n"
  "v_mfma_f32_16x16x32_bf16 v[104:107], a[112:115], v[48:51], v[104:107]\n"
  "ds_read_b128 v[40:43], v3 offset:10240\n"
  "v_mov_b64 v[58:59], 0\n"
  "v_mfma_f32_16x16x32_bf16 v[108:111], a[120:123], v[48:51], v[108:111]\n"
  "ds_read_b128 v[44:47], v3 offset:30720\n"
  "v_mov_b64 v[60:61], 0\n"
  "v_mfma_f32_16x16x32_bf16 v[112:115], a[128:131], v[48:51], v[112:115]\n"
  "ds_read_b128 v[96:99], v5 offset:10240\n"
  "v_mov_b64 v[62:63], 0\n"
  "v_mfma_f32_16x16x32_bf16 v[116:119], a[136:139], v[48:51], v[116:119]\n"
  "ds_read_b128 v[100:103], v5 offset:11264\n"
  "v_mov_b64 v[64:65], 0\n"
  "v_mfma_f32_16x16x32_bf16 v[120:123], a[144:147], v[48:51], v[120:123]\n"
  "ds_read_b128 v[80:83], v5 offset:12288\n"
  "v_mov_b64 v[66:67], 0\n"
  "s_waitcnt lgkmcnt(6)\n"
  "v_mfma_f32_16x16x32_bf16 v[124:127], v[88:91], v[48:51], v[124:127]\n"
  "ds_read_b128 v[84:87], v5 offset:13312\n"
  "v_mov_b64 v[68:69], 0\n"
  "v_mfma_f32_16x16x32_bf16 v[104:107], a[116:119], v[52:55], v[104:107]\n"
  "v_mov_b64 v[70:71], 0\n"
  "v_mfma_f32_16x16x32_bf16 v[108:111], a[124:127], v[52:55], v[108:111]\n"
  "v_mov_b64 v[72:73], 0\n"
  "v_mfma_f32_16x16x32_bf16 v[112:115], a[132:135], v[52:55], v[112:115]\n"
  "v_mov_b64 v[74:75], 0\n"
  "v_mfma_f32_16x16x32_bf16 v[116:119], a[140:143], v[52:55], v[116:119]\n"
  "v_mov_b64 v[76:77], 0\n"
  "v_mfma_f32_16x16x32_bf16 v[120:123], a[148:151], v[52:55], v[120:123]\n"
  "v_mov_b64 v[78:79], 0\n"
  "s_waitcnt lgkmcnt(6)\n"
  "v_mfma_f32_16x16x32_bf16 v[124:127], v[92:95], v[52:55], v[124:127]\n"
  "s_waitcnt lgkmcnt(5)\n"
  "v_mfma_f32_16x16x32_bf16 v[104:107], a[152:155], v[40:43], v[104:107]\n"
  "ds_read_b128 v[48:51], v1 offset:14336\n"
  "v_mfma_f32_16x16x32_bf16 v[108:111], a[160:163], v[40:43], v[108:111]\n"
  "ds_read_b128 v[52:55], v1 offset:34816\n"
  "v_mfma_f32_16x16x32_bf16 v[112:115], a[168:171], v[40:43], v[112:115]\n"
  "ds_read_b128 v[88:91], v5 offset:14336\n"
  "v_mfma_f32_16x16x32_bf16 v[116:119], a[176:179], v[40:43], v[116:119]\n"
  "ds_read_b128 v[92:95], v5 offset:15360\n"
  "s_waitcnt lgkmcnt(7)\n"
  "v_mfma_f32_16x16x32_bf16 v[120:123], v[96:99], v[40:43], v[120:123]\n"
  "s_waitcnt lgkmcnt(5)\n"
  "v_mfma_f32_16x16x32_bf16 v[124:127], v[80:83], v[40:43], v[124:127]\n"
  "v_mfma_f32_16x16x32_bf16 v[104:107], a[156:159], v[44:47], v[104:107]\n"
  "v_mfma_f32_16x16x32_bf16 v[108:111], a[164:167], v[44:47], v[108:111]\n"
  "v_mfma_f32_16x16x32_bf16 v[112:115], a[172:175], v[44:47], v[112:115]\n"
  "v_mfma_f32_16x16x32_bf16 v[116:119], a[180:183], v[44:47], v[116:119]\n"
  "v_mfma_f32_16x16x32_bf16 v[120:123], v[100:103], v[44:47], v[120:123]\n"
  "s_waitcnt lgkmcnt(4)\n"
  "v_mfma_f32_16x16x32_bf16 v[124:127], v[84:87], v[44:47], v[124:127]\n"
  "s_waitcnt lgkmcnt(3)\n"
  "v_mfma_f32_16x16x32_bf16 v[104:107], a[184:187], v[48:51], v[104:107]\n"
  "ds_read_b128 v[40:43], v2 offset:14336\n"
  "v_mfma_f32_16x16x32_bf16 v[108:111], a[192:195], v[48:51], v[108:111]\n"
  "ds_read_b128 v[44:47], v2 offset:34816\n"
  "v_mfma_f32_16x16x32_bf16 v[112:115], a[200:203], v[48:51], v[112:115]\n"
  "ds_read_b128 v[96:99], v5 offset:16384\n"
  "s_waitcnt lgkmcnt(4)\n"
  "v_mfma_f32_16x16x32_bf16 v[116:119], v[88:91], v[48:51], v[116:119]\n"
  "ds_read_b128 v[100:103], v5 offset:17408\n"
  "v_mfma_f32_16x16x32_bf16 v[120:123], a[208:211], v[48:51], v[120:123]\n"
  "v_mfma_f32_16x16x32_bf16 v[124:127], a[216:219], v[48:51], v[124:127]\n"
  "v_mfma_f32_16x16x32_bf16 v[104:107], a[188:191], v[52:55], v[104:107]\n"
  "v_mfma_f32_16x16x32_bf16 v[108:111], a[196:199], v[52:55], v[108:111]\n"
  "v_mfma_f32_16x16x32_bf16 v[112:115], a[204:207], v[52:55], v[112:115]\n"
  "s_waitcnt lgkmcnt(4)\n"
  "v_mfma_f32_16x16x32_bf16 v[116:119], v[92:95], v[52:55], v[116:119]\n"
  "v_mfma_f32_16x16x32_bf16 v[120:123], a[212:215], v[52:55], v[120:123]\n"
  "v_mfma_f32_16x16x32_bf16 v[124:127], a[220:223], v[52:55], v[124:127]\n"
  "s_waitcnt lgkmcnt(3)\n"
  "v_mfma_f32_16x16x32_bf16 v[104:107], a[224:227], v[40:43], v[104:107]\n"
  "ds_read_b128 v[48:51], v3 offset:14336\n"
  "buffer_load_dword v19, off, s[16:19], s75 sc1\n"
  "v_mfma_f32_16x16x32_bf16 v[108:111], a[232:235], v[40:43], v[108:111]\n"
  "ds_read_b128 v[52:55], v3 offset:34816\n"
  "s_mov_b64 s[66:67], 0\n"
  "v_mfma_f32_16x16x32_bf16 v[112:115], a[240:243], v[40:43], v[112:115]\n"
  "ds_read_b128 v[80:83], v5 offset:18432\n"
  "s_mov_b32 s52, 0\n"
  "v_mfma_f32_16x16x32_bf16 v[116:119], a[248:251], v[40:43], v[116:119]\n"
  "ds_read_b128 v[84:87], v5 offset:19456\n"
  "s_waitcnt lgkmcnt(5)\n"
  "v_mfma_f32_16x16x32_bf16 v[120:123], v[96:99], v[40:43], v[120:123]\n"
  "v_mfma_f32_16x16x32_bf16 v[124:127], v[128:131], v[40:43], v[124:127]\n"
  "v_mfma_f32_16x16x32_bf16 v[104:107], a[228:231], v[44:47], v[104:107]\n"
  "v_mfma_f32_16x16x32_bf16 v[108:111], a[236:239], v[44:47], v[108:111]\n"
  "v_mfma_f32_16x16x32_bf16 v[112:115], a[244:247], v[44:47], v[112:115]\n"
  "v_mfma_f32_16x16x32_bf16 v[116:119], a[252:255], v[44:47], v[116:119]\n"
  "s_waitcnt lgkmcnt(4)\n"
  "v_mfma_f32_16x16x32_bf16 v[120:123], v[100:103], v[44:47], v[120:123]\n"
  "v_mfma_f32_16x16x32_bf16 v[124:127], v[132:135], v[44:47], v[124:127]\n"
  "s_waitcnt lgkmcnt(3)\n"
  "v_mfma_f32_16x16x32_bf16 v[104:107], v[136:139], v[48:51], v[104:107]\n"
  "ds_read_b128 v[40:43], v0 offset:18432\n"
  "v_mfma_f32_16x16x32_bf16 v[108:111], v[144:147], v[48:51], v[108:111]\n"
  "ds_read_b128 v[44:47], v0 offset:38912\n"
  "v_mfma_f32_16x16x32_bf16 v[112:115], v[152:155], v[48:51], v[112:115]\n"
  "ds_read_b128 v[88:91], v5 offset:20480\n"
  "s_waitcnt lgkmcnt(4)\n"
  "v_mfma_f32_16x16x32_bf16 v[116:119], v[80:83], v[48:51], v[116:119]\n"
  "ds_read_b128 v[92:95], v5 offset:21504\n"
  "v_mfma_f32_16x16x32_bf16 v[120:123], v[160:163], v[48:51], v[120:123]\n"
  "v_mfma_f32_16x16x32_bf16 v[124:127], v[168:171], v[48:51], v[124:127]\n"
  "v_mfma_f32_16x16x32_bf16 v[104:107], v[140:143], v[52:55], v[104:107]\n"
  "v_mfma_f32_16x16x32_bf16 v[108:111], v[148:151], v[52:55], v[108:111]\n"
  "v_mfma_f32_16x16x32_bf16 v[112:115], v[156:159], v[52:55], v[112:115]\n"
  "s_waitcnt lgkmcnt(4)\n"
  "v_mfma_f32_16x16x32_bf16 v[116:119], v[84:87], v[52:55], v[116:119]\n"
  "v_mfma_f32_16x16x32_bf16 v[120:123], v[164:167], v[52:55], v[120:123]\n"
  "v_mfma_f32_16x16x32_bf16 v[124:127], v[172:175], v[52:55], v[124:127]\n"
  "s_waitcnt lgkmcnt(3)\n"
  "v_mfma_f32_16x16x32_bf16 v[104:107], v[176:179], v[40:43], v[104:107]\n"
  "ds_read_b128 v[48:51], v0 offset:19456\n"
  "v_mfma_f32_16x16x32_bf16 v[108:111], v[184:187], v[40:43], v[108:111]\n"
  "ds_read_b128 v[52:55], v0 offset:39936\n"
  "v_mfma_f32_16x16x32_bf16 v[112:115], v[192:195], v[40:43], v[112:115]\n"
  "ds_read_b128 v[96:99], v5 offset:22528\n"
  "v_mfma_f32_16x16x32_bf16 v[116:119], v[200:203], v[40:43], v[116:119]\n"
  "ds_read_b128 v[100:103], v5 offset:23552\n"
  "s_waitcnt lgkmcnt(5)\n"
  "v_mfma_f32_16x16x32_bf16 v[120:123], v[88:91], v[40:43], v[120:123]\n"
  "s_waitcnt vmcnt(0)\n"
  "v_readfirstlane_b32 s59, v19\n"
  "s_cmp_eq_u32 s59, -1\n"
  "s_cbranch_scc1 H_SCHP_%=\n"
  "H_SCHOK_%=:\n"
  "s_memrealtime s[64:65]\n"
  "v_mfma_f32_16x16x32_bf16 v[124:127], v[208:211], v[40:43], v[124:127]\n"
  "v_mfma_f32_16x16x32_bf16 v[104:107], v[180:183], v[44:47], v[104:107]\n"
  "v_mfma_f32_16x16x32_bf16 v[108:111], v[188:191], v[44:47], v[108:111]\n"
  "s_waitcnt lgkmcnt(0)\n"
  "s_sub_i32 s60, s59, s64\n"
  "s_cmp_gt_i32 s60, 2000\n"
  "v_mfma_f32_16x16x32_bf16 v[112:115], v[196:199], v[44:47], v[112:115]\n"
  "s_cselect_b32 s59, s64, s59\n"
  "s_cmp_lt_i32 s60, -100000\n"
  "v_mfma_f32_16x16x32_bf16 v[116:119], v[204:207], v[44:47], v[116:119]\n"
  "s_cselect_b32 s59, s64, s59\n"
  "s_sub_u32 s0, s59, 23\n"
  "v_mfma_f32_16x16x32_bf16 v[120:123], v[92:95], v[44:47], v[120:123]\n"
  "s_sub_i32 s60, s64, s0\n"
  "s_cmp_gt_i32 s60, 0\n"
  "v_mfma_f32_16x16x32_bf16 v[124:127], v[212:215], v[44:47], v[124:127]\n"
  "s_cselect_b32 s0, s64, s0\n"
  "s_add_i32 s60, s60, 100\n"
  "s_max_i32 s60, s60, 0\n"
  "s_min_i32 s3, s60, 200\n"
  "s_cmp_lt_u32 s56, 4\n"
  "s_cselect_b32 s3, 0, s3\n"
  "s_add_u32 s59, s0, 23\n"
  "s_barrier\n"
  "H_WT_%=:\n"
  "s_memrealtime s[64:65]\n"
  "s_waitcnt lgkmcnt(0)\n"
  "s_sub_i32 s60, s64, s0\n"
  "s_cmp_lt_i32 s60, 0\n"
  "s_cbranch_scc1 H_WT_%=\n"
  "v_mfma_f32_16x16x32_bf16 v[116:119], v[240:243], v[48:51], v[116:119]\n"
  "s_memtime s[20:21]\n"
  "s_memrealtime s[22:23]\n"
  "v_mfma_f32_16x16x32_bf16 v[116:119], v[244:247], v[52:55], v[116:119]\n"
  "s_memrealtime s[28:29]\n"
  "v_mfma_f32_16x16x32_bf16 v[120:123], v[248:251], v[48:51], v[120:123]\n"
  "s_memrealtime s[30:31]\n"
  "v_mfma_f32_16x16x32_bf16 v[120:123], v[252:255], v[52:55], v[120:123]\n"
  "s_memrealtime s[32:33]\n"
  "v_mfma_f32_16x16x32_bf16 v[104:107], v[216:219], v[48:51], v[104:107]\n"
  "s_memrealtime s[34:35]\n"
  "v_mfma_f32_16x16x32_bf16 v[104:107], v[220:223], v[52:55], v[104:107]\n"
  "s_memrealtime s[36:37]\n"
  "v_mfma_f32_16x16x32_bf16 v[108:111], v[224:227], v[48:51], v[108:111]\n"
  "s_memrealtime s[38:39]\n"
  "v_mfma_f32_16x16x32_bf16 v[108:111], v[228:231], v[52:55], v[108:111]\n"
  "s_mov_b32 s61, 0\n"
  "v_mfma_f32_16x16x32_bf16 v[112:115], v[232:235], v[48:51], v[112:115]\n"
  "v_cvt_pk_bf16_f32 v36, v116, v117\n"
  "v_cvt_pk_bf16_f32 v37, v118, v119\n"
  "v_mfma_f32_16x16x32_bf16 v[112:115], v[236:239], v[52:55], v[112:115]\n"
  "v_cvt_pk_bf16_f32 v38, v120, v121\n"
  "v_cvt_pk_bf16_f32 v39, v122, v123\n"
  "v_mfma_f32_16x16x32_bf16 v[124:127], v[96:99], v[48:51], v[124:127]\n"
  "s_waitcnt lgkmcnt(0)\n"
  "s_cmp_eq_u32 s28, s22\n"
  "s_addc_u32 s61, s61, 0\n"
  "v_mfma_f32_16x16x32_bf16 v[124:127], v[100:103], v[52:55], v[124:127]\n"
  "v_cvt_pk_bf16_f32 v40, v104, v105\n"
  "s_cmp_eq_u32 s30, s22\n"
  "s_addc_u32 s61, s61, 0\n"
  "v_cvt_pk_bf16_f32 v41, v106, v107\n"
  "s_cmp_eq_u32 s32, s22\n"
  "s_addc_u32 s61, s61, 0\n"
  "v_cvt_pk_bf16_f32 v42, v108, v109\n"
  "s_cmp_eq_u32 s34, s22\n"
  "s_addc_u32 s61, s61, 0\n"
  "v_cvt_pk_bf16_f32 v43, v110, v111\n"
  "s_cmp_eq_u32 s36, s22\n"
  "s_addc_u32 s61, s61, 0\n"
  "v_cvt_pk_bf16_f32 v46, v112, v113\n"
  "s_cmp_eq_u32 s38, s22\n"
  "s_addc_u32 s61, s61, 0\n"
  "v_cvt_pk_bf16_f32 v47, v114, v115\n"
  "s_sub_i32 s60, s59, s22\n"
  "s_sub_i32 s60, s60, 4\n"
  "v_cvt_pk_bf16_f32 v44, v124, v125\n"
  "s_mul_i32 s60, s60, s98\n"
  "s_ashr_i32 s60, s60, 4\n"
  "v_cvt_pk_bf16_f32 v45, v126, v127\n"
  "s_lshl4_add_u32 s60, s61, s60\n"
  "s_max_i32 s60, s60, 344\n"
  "v_xor_b32 v19, 16, v17\n"
  "s_min_i32 s60, s60, 596\n"
  "s_add_u32 s0, s20, s60\n"
  "s_and_b32 s60, s60, -4\n"
  "s_getpc_b64 s[64:65]\n"
  "s_sub_u32 s60, 620, s60\n"
  "s_add_u32 s64, s64, s60\n"
  "s_addc_u32 s65, s65, 0\n"
  "s_setpc_b64 s[64:65]\n"
  "s_nop 0\n"
  "s_nop 0\n"
  "s_nop 0\n"
  "s_nop 0\n"
  "s_nop 0\n"
  "s_nop 0\n"
  "s_nop 0\n"
  "s_nop 0\n"
  "s_nop 0\n"
  "s_nop 0\n"
  "s_nop 0\n"
  "s_nop 0\n"
  "s_nop 0\n"
  "s_nop 0\n"
  "s_nop 0\n"
  "s_nop 0\n"
  "s_nop 0\n"
  "s_nop 0\n"
  "s_nop 0\n"
  "s_nop 0\n"
  "s_nop 0\n"
  "s_nop 0\n"
  "s_nop 0\n"
  "s_nop 0\n"
  "s_nop 0\n"
  "s_nop 0\n"
  "s_nop 0\n"
  "s_nop 0\n"
  "s_nop 0\n"
  "s_nop 0\n"
  "s_nop 0\n"
  "s_nop 0\n"
  "s_nop 0\n"
  "s_nop 0\n"
  "s_nop 0\n"
  "s_nop 0\n"
  "s_nop 0\n"
  "s_nop 0\n"
  "s_nop 0\n"
  "s_nop 0\n"
  "s_nop 0\n"
  "s_nop 0\n"
  "s_nop 0\n"
  "s_nop 0\n"
  "s_nop 0\n"
  "s_nop 0\n"
  "s_nop 0\n"
  "s_nop 0\n"
  "s_nop 0\n"
  "s_nop 0\n"
  "s_nop 0\n"
  "s_nop 0\n"
  "s_nop 0\n"
  "s_nop 0\n"
  "s_nop 0\n"
  "s_nop 0\n"
  "s_nop 0\n"
  "s_nop 0\n"
  "s_nop 0\n"
  "s_nop 0\n"
  "s_nop 0\n"
  "s_nop 0\n"
  "s_nop 0\n"
  "s_nop 0\n"
  "buffer_load_dwordx4 v[104:107], v6, s[16:19], s57 offen sc1\n"
  "v_mfma_f32_16x16x32_bf16 v[112:115], v[24:27], v[28:31], 0\n"
  "s_nop 1\n"
  "v_mfma_f32_16x16x32_bf16 v[116:119], v[24:27], v[40:43], 0\n"
  "s_nop 1\n"
  "v_mfma_f32_16x16x32_bf16 v[120:123], v[36:39], v[40:43], 0\n"
  "s_nop 1\n"
  "buffer_load_dwordx4 v[108:111], v6, s[16:19], s57 offen offset:1024 sc1\n"
  "v_mfma_f32_16x16x16_bf16 v[112:115], v[32:33], v[34:35], v[112:115]\n"
  "s_nop 1\n"
  "v_mfma_f32_16x16x16_bf16 v[116:119], v[32:33], v[46:47], v[116:119]\n"
  "s_nop 1\n"
  "v_mfma_f32_16x16x16_bf16 v[120:123], v[44:45], v[46:47], v[120:123]\n"
  "s_nop 1\n"
  "buffer_load_dwordx2 v[54:55], v7, s[16:19], s57 offen sc1\n"
  "ds_read_b128 v[80:83], v5 offset:0\n"
  "ds_read_b128 v[84:87], v5 offset:1024\n"
  "ds_read_b128 v[88:91], v5 offset:2048\n"
  "ds_read_b128 v[92:95], v5 offset:3072\n"
  "ds_read_b128 v[96:99], v5 offset:4096\n"
  "ds_read_b128 v[100:103], v5 offset:5120\n"
  "ds_write_b128 v17, v[112:115]\n"
  "ds_write_b128 v17, v[116:119] offset:2048\n"
  "ds_write_b128 v19, v[120:123] offset:2048\n"
  "s_waitcnt lgkmcnt(0)\n"
  "H_D0_%=:\n"
  "s_waitcnt vmcnt(2)\n"
  "v_smfmac_f32_16x16x64_bf16 v[56:59], v[104:107], a[0:7], v9\n"
  "ds_write_b128 v4, v[104:107] offset:10240\n"
  "v_smfmac_f32_16x16x64_bf16 v[60:63], v[104:107], a[8:15], v9\n"
  "v_cmp_eq_u32 vcc, -1, v104\n"
  "v_smfmac_f32_16x16x64_bf16 v[64:67], v[104:107], a[16:23], v9\n"
  "v_smfmac_f32_16x16x64_bf16 v[68:71], v[104:107], a[24:31], v9\n"
  "v_smfmac_f32_16x16x64_bf16 v[72:75], v[104:107], a[32:39], v9\n"
  "s_cbranch_vccnz H_FIX0_%=\n"
  "v_smfmac_f32_16x16x64_bf16 v[76:79], v[104:107], v[80:87], v9\n"
  "s_waitcnt lgkmcnt(0)\n"
  "s_barrier\n"
  "ds_read_b128 v[112:115], v1 offset:10240\n"
  "s_waitcnt vmcnt(1)\n"
  "v_smfmac_f32_16x16x64_bf16 v[56:59], v[108:111], a[40:47], v9\n"
  "ds_read_b128 v[116:119], v2 offset:10240\n"
  "v_smfmac_f32_16x16x64_bf16 v[60:63], v[108:111], a[48:55], v9\n"
  "ds_write_b128 v4, v[108:111] offset:14336\n"
  "v_smfmac_f32_16x16x64_bf16 v[64:67], v[108:111], a[56:63], v9\n"
  "v_smfmac_f32_16x16x64_bf16 v[68:71], v[108:111], a[64:71], v9\n"
  "s_waitcnt vmcnt(0)\n"
  "ds_write_b64 v8, v[54:55]\n"
  "v_smfmac_f32_16x16x64_bf16 v[72:75], v[108:111], a[72:79], v9\n"
  "ds_read_b128 v[80:83], v5 offset:6144\n"
  "v_smfmac_f32_16x16x64_bf16 v[76:79], v[108:111], v[88:95], v9\n"
  "ds_read_b128 v[84:87], v5 offset:7168\n"
  "s_waitcnt lgkmcnt(5)\n"
  "v_smfmac_f32_16x16x64_bf16 v[56:59], v[112:115], a[80:87], v9\n"
  "ds_read_b128 v[120:123], v3 offset:10240\n"
  "v_smfmac_f32_16x16x64_bf16 v[60:63], v[112:115], a[88:95], v9\n"
  "ds_read_b128 v[88:91], v5 offset:8192\n"
  "v_smfmac_f32_16x16x64_bf16 v[64:67], v[112:115], a[96:103], v9\n"
  "ds_read_b128 v[92:95], v5 offset:9216\n"
  "v_smfmac_f32_16x16x64_bf16 v[68:71], v[112:115], a[104:111], v9\n"
  "s_waitcnt lgkmcnt(5)\n"
  "s_barrier\n"
  "v_smfmac_f32_16x16x64_bf16 v[72:75], v[112:115], v[96:103], v9\n"
  "ds_read_b128 v[124:127], v1 offset:14336\n"
  "s_waitcnt lgkmcnt(4)\n"
  "v_smfmac_f32_16x16x64_bf16 v[76:79], v[112:115], v[80:87], v9\n"
  "ds_read_b128 v[104:107], v2 offset:14336\n"
  "v_smfmac_f32_16x16x64_bf16 v[56:59], v[116:119], a[112:119], v9\n"
  "ds_read_b128 v[96:99], v5 offset:10240\n"
  "v_smfmac_f32_16x16x64_bf16 v[60:63], v[116:119], a[120:127], v9\n"
  "ds_read_b128 v[100:103], v5 offset:11264\n"
  "v_smfmac_f32_16x16x64_bf16 v[64:67], v[116:119], a[128:135], v9\n"
  "ds_read_b128 v[80:83], v5 offset:12288\n"
  "v_smfmac_f32_16x16x64_bf16 v[68:71], v[116:119], a[136:143], v9\n"
  "ds_read_b128 v[84:87], v5 offset:13312\n"
  "v_smfmac_f32_16x16x64_bf16 v[72:75], v[116:119], a[144:151], v9\n"
  "ds_read_b128 v[108:111], v3 offset:14336\n"
  "s_waitcnt lgkmcnt(7)\n"
  "v_smfmac_f32_16x16x64_bf16 v[76:79], v[116:119], v[88:95], v9\n"
  "ds_read_b128 v[112:115], v0 offset:18432\n"
  "v_smfmac_f32_16x16x64_bf16 v[56:59], v[120:123], a[152:159], v9\n"
  "ds_read_b128 v[88:91], v5 offset:14336\n"
  "v_smfmac_f32_16x16x64_bf16 v[60:63], v[120:123], a[160:167], v9\n"
  "ds_read_b128 v[92:95], v5 offset:15360\n"
  "v_smfmac_f32_16x16x64_bf16 v[64:67], v[120:123], a[168:175], v9\n"
  "ds_read_b128 v[116:119], v0 offset:19456\n"
  "v_smfmac_f32_16x16x64_bf16 v[68:71], v[120:123], a[176:183], v9\n"
  "s_waitcnt lgkmcnt(7)\n"
  "v_smfmac_f32_16x16x64_bf16 v[72:75], v[120:123], v[96:103], v9\n"
  "s_waitcnt lgkmcnt(5)\n"
  "v_smfmac_f32_16x16x64_bf16 v[76:79], v[120:123], v[80:87], v9\n"
  "v_smfmac_f32_16x16x64_bf16 v[56:59], v[124:127], a[184:191], v9\n"
  "ds_read_b128 v[96:99], v5 offset:18432\n"
  "s_waitcnt lgkmcnt(2)\n"
  "v_smfmac_f32_16x16x64_bf16 v[68:71], v[124:127], v[88:95], v9\n"
  "ds_read_b128 v[100:103], v5 offset:19456\n"
  "v_smfmac_f32_16x16x64_bf16 v[56:59], v[104:107], a[224:231], v9\n"
  "ds_read_b128 v[80:83], v5 offset:16384\n"
  "v_smfmac_f32_16x16x64_bf16 v[68:71], v[104:107], a[248:255], v9\n"
  "ds_read_b128 v[84:87], v5 offset:17408\n"
  "v_smfmac_f32_16x16x64_bf16 v[56:59], v[108:111], v[136:143], v9\n"
  "ds_read_b128 v[88:91], v5 offset:20480\n"
  "s_waitcnt lgkmcnt(3)\n"
  "v_smfmac_f32_16x16x64_bf16 v[68:71], v[108:111], v[96:103], v9\n"
  "ds_read_b128 v[92:95], v5 offset:21504\n"
  "v_smfmac_f32_16x16x64_bf16 v[56:59], v[112:115], v[176:183], v9\n"
  "ds_read_b128 v[96:99], v5 offset:22528\n"
  "v_smfmac_f32_16x16x64_bf16 v[68:71], v[112:115], v[200:207], v9\n"
  "ds_read_b128 v[100:103], v5 offset:23552\n"
  "v_smfmac_f32_16x16x64_bf16 v[56:59], v[116:119], v[216:223], v9\n"
  "v_smfmac_f32_16x16x64_bf16 v[68:71], v[116:119], v[240:247], v9\n"
  "v_smfmac_f32_16x16x64_bf16 v[60:63], v[124:127], a[192:199], v9\n"
  "s_nop 1\n"
  "v_smfmac_f32_16x16x64_bf16 v[72:75], v[124:127], a[208:215], v9\n"
  "s_nop 1\n"
  "v_smfmac_f32_16x16x64_bf16 v[60:63], v[104:107], a[232:239], v9\n"
  "s_nop 1\n"
  "s_waitcnt lgkmcnt(4)\n"
  "v_smfmac_f32_16x16x64_bf16 v[72:75], v[104:107], v[80:87], v9\n"
  "v_cvt_pk_bf16_f32 v56, v56, v57\n"
  "v_cvt_pk_bf16_f32 v57, v58, v59\n"
  "v_smfmac_f32_16x16x64_bf16 v[60:63], v[108:111], v[144:151], v9\n"
  "v_cvt_pk_bf16_f32 v58, v68, v69\n"
  "v_cvt_pk_bf16_f32 v59, v70, v71\n"
  "v_smfmac_f32_16x16x64_bf16 v[72:75], v[108:111], v[160:167], v9\n"
  "s_nop 1\n"
  "v_mfma_f32_16x16x32_bf16 v[68:71], v[56:59], v[20:23], 0\n"
  "v_smfmac_f32_16x16x64_bf16 v[60:63], v[112:115], v[184:191], v9\n"
  "s_nop 1\n"
  "s_waitcnt lgkmcnt(2)\n"
  "v_smfmac_f32_16x16x64_bf16 v[72:75], v[112:115], v[88:95], v9\n"
  "s_nop 1\n"
  "v_smfmac_f32_16x16x64_bf16 v[60:63], v[116:119], v[224:231], v9\n"
  "s_nop 1\n"
  "v_smfmac_f32_16x16x64_bf16 v[72:75], v[116:119], v[248:255], v9\n"
  "v_cvt_pk_bf16_f32 v48, v68, v69\n"
  "v_cvt_pk_bf16_f32 v49, v70, v71\n"
  "v_smfmac_f32_16x16x64_bf16 v[64:67], v[124:127], a[200:207], v9\n"
  "v_cmp_u_f32 vcc, v68, v68\n"
  "s_nop 0\n"
  "v_smfmac_f32_16x16x64_bf16 v[76:79], v[124:127], a[216:223], v9\n"
  "s_andn2_b64 vcc, vcc, s[66:67]\n"
  "s_cbranch_vccnz H_REDO_%=\n"
  "v_smfmac_f32_16x16x64_bf16 v[64:67], v[104:107], a[240:247], v9\n"
  "v_cvt_pk_bf16_f32 v60, v60, v61\n"
  "v_cvt_pk_bf16_f32 v61, v62, v63\n"
  "v_smfmac_f32_16x16x64_bf16 v[76:79], v[104:107], v[128:135], v9\n"
  "v_cvt_pk_bf16_f32 v62, v72, v73\n"
  "v_cvt_pk_bf16_f32 v63, v74, v75\n"
  "v_smfmac_f32_16x16x64_bf16 v[64:67], v[108:111], v[152:159], v9\n"
  "s_nop 1\n"
  "v_mfma_f32_16x16x32_bf16 v[72:75], v[60:63], v[20:23], 0\n"
  "v_smfmac_f32_16x16x64_bf16 v[76:79], v[108:111], v[168:175], v9\n"
  "s_nop 1\n"
  "v_smfmac_f32_16x16x64_bf16 v[64:67], v[112:115], v[192:199], v9\n"
  "s_nop 1\n"
  "v_smfmac_f32_16x16x64_bf16 v[76:79], v[112:115], v[208:215], v9\n"
  "s_nop 1\n"
  "v_smfmac_f32_16x16x64_bf16 v[64:67], v[116:119], v[232:239], v9\n"
  "v_cvt_pk_bf16_f32 v50, v72, v73\n"
  "v_cvt_pk_bf16_f32 v51, v74, v75\n"
  "s_waitcnt lgkmcnt(0)\n"
  "v_smfmac_f32_16x16x64_bf16 v[76:79], v[116:119], v[96:103], v9\n"
  "v_mfma_f32_16x16x32_bf16 v[120:123], v[24:27], v[48:51], 0\n"
  "s_nop 1\n"
  "v_mfma_f32_16x16x32_bf16 v[56:59], v[36:39], v[48:51], 0\n"
  "s_nop 1\n"
  "v_cvt_pk_bf16_f32 v64, v64, v65\n"
  "v_cvt_pk_bf16_f32 v65, v66, v67\n"
  "v_cvt_pk_bf16_f32 v66, v76, v77\n"
  "v_cvt_pk_bf16_f32 v67, v78, v79\n"
  "s_nop 1\n"
  "v_mfma_f32_16x16x32_bf16 v[76:79], v[64:67], v[20:23], 0\n"
  "v_mov_b32 v0, v0\n"
  "v_mov_b32 v0, v0\n"
  "v_mfma_f32_16x16x32_bf16 v[68:71], v[48:51], v[48:51], 0\n"
  "v_mov_b32 v0, v0\n"
  "v_mov_b32 v0, v0\n"
  "v_mov_b32 v0, v0\n"
  "v_mov_b32 v0, v0\n"
  "v_mov_b32 v0, v0\n"
  "v_cvt_pk_bf16_f32 v52, v76, v77\n"
  "v_cvt_pk_bf16_f32 v53, v78, v79\n"
  "s_nop 1\n"
  "v_mfma_f32_16x16x16_bf16 v[56:59], v[44:45], v[52:53], v[56:59]\n"
  "s_nop 1\n"
  "v_mfma_f32_16x16x16_bf16 v[120:123], v[32:33], v[52:53], v[120:123]\n"
  "s_nop 1\n"
  "v_mfma_f32_16x16x16_bf16 v[68:71], v[52:53], v[52:53], v[68:71]\n"
  "v_mov_b32 v0, v0\n"
  "v_mov_b32 v0, v0\n"
  "v_mov_b32 v0, v0\n"
  "v_mov_b32 v0, v0\n"
  "v_mov_b32 v0, v0\n"
  "v_mov_b32_dpp v120, v56 row_ror:8 row_mask:0xf bank_mask:0xc\n"
  "v_mov_b32_dpp v121, v57 row_ror:8 row_mask:0xf bank_mask:0xc\n"
  "v_mov_b32_dpp v122, v58 row_ror:8 row_mask:0xf bank_mask:0xc\n"
  "v_mov_b32_dpp v123, v59 row_ror:8 row_mask:0xf bank_mask:0xc\n"
  "ds_write_b128 v10, v[120:123]\n"
  "v_mov_b32_dpp v68, v70 row_ror:8 row_mask:0xf bank_mask:0xc\n"
  "v_mov_b32_dpp v69, v71 row_ror:8 row_mask:0xf bank_mask:0xc\n"
  "ds_write_b64 v12, v[68:69]\n"
  "v_mfma_f32_16x16x32_bf16 v[104:107], v[48:51], v[28:31], 0\n"
  "v_mfma_f32_16x16x32_bf16 v[108:111], v[48:51], v[40:43], 0\n"
  "v_lshrrev_b32 v124, 3, v0\n"
  "v_lshrrev_b32 v126, 2, v4\n"
  "s_waitcnt lgkmcnt(0)\n"
  "s_barrier\n"
  "ds_read_b128 v[56:59], v13\n"
  "ds_read_b128 v[60:63], v13 offset:1024\n"
  "v_and_b32 v124, 112, v124\n"
  "ds_read2st64_b32 v[64:65], v14 offset0:0 offset1:1\n"
  "v_mfma_f32_16x16x16_bf16 v[104:107], v[52:53], v[34:35], v[104:107]\n"
  "v_mfma_f32_16x16x16_bf16 v[108:111], v[52:53], v[46:47], v[108:111]\n"
  "v_add_u32 v126, 57344, v126\n"
  "s_waitcnt lgkmcnt(1)\n"
  "v_pk_add_f32 v[56:57], v[56:57], v[60:61]\n"
  "v_pk_add_f32 v[58:59], v[58:59], v[62:63]\n"
  "s_waitcnt lgkmcnt(0)\n"
  "v_add_f32 v64, v64, v65\n"
  "v_add_f32_dpp v56, v56, v56 row_ror:8 row_mask:0xf bank_mask:0xf\n"
  "v_add_f32_dpp v57, v57, v57 row_ror:8 row_mask:0xf bank_mask:0xf\n"
  "v_add_f32_dpp v58, v58, v58 row_ror:8 row_mask:0xf bank_mask:0xf\n"
  "v_add_f32_dpp v59, v59, v59 row_ror:8 row_mask:0xf bank_mask:0xf\n"
  "v_add_f32_dpp v64, v64, v64 row_ror:8 row_mask:0xf bank_mask:0xf\n"
  "v_max3_f32 v68, v56, v57, v64\n"
  "v_max3_f32 v68, v58, v59, v68\n"
  "v_mul_f32 v70, s76, v56\n"
  "v_mul_f32 v71, s76, v57\n"
  "v_max_f32_dpp v68, v68, v68 quad_perm:[1,0,3,2] row_mask:0xf bank_mask:0xf\n"
  "v_mul_f32 v72, s76, v58\n"
  "v_mul_f32 v73, s76, v59\n"
  "v_max_f32_dpp v68, v68, v68 quad_perm:[2,3,0,1] row_mask:0xf bank_mask:0xf\n"
  "v_mul_f32 v74, s76, v64\n"
  "v_xor_b32 v125, v124, v4\n"
  "v_max_f32_dpp v68, v68, v68 row_half_mirror row_mask:0xf bank_mask:0xf\n"
  "v_fma_f32 v70, -v68, s76, v70\n"
  "v_fma_f32 v71, -v68, s76, v71\n"
  "v_fma_f32 v72, -v68, s76, v72\n"
  "v_fma_f32 v73, -v68, s76, v73\n"
  "v_fma_f32 v74, -v68, s76, v74\n"
  "v_exp_f32 v74, v74\n"
  "v_exp_f32 v70, v70\n"
  "v_exp_f32 v71, v71\n"
  "v_exp_f32 v72, v72\n"
  "v_exp_f32 v73, v73\n"
  "v_add_f32 v66, v74, v70\n"
  "v_add_f32 v67, v71, v72\n"
  "v_add_f32 v66, v66, v73\n"
  "v_add_f32 v68, v66, v67\n"
  "v_mov_b32_dpp v75, v74 quad_perm:[1,0,3,2] row_mask:0xf bank_mask:0xf\n"
  "ds_read_b128 v[40:43], v4 offset:0\n"
  "v_add_f32_dpp v68, v68, v68 quad_perm:[1,0,3,2] row_mask:0xf bank_mask:0xf\n"
  "ds_read_b128 v[44:47], v4 offset:20480\n"
  "ds_read_b128 v[80:83], v5 offset:0\n"
  "v_add_f32_dpp v68, v68, v68 quad_perm:[2,3,0,1] row_mask:0xf bank_mask:0xf\n"
  "ds_read_b128 v[84:87], v5 offset:1024\n"
  "v_add_u32 v125, 40960, v125\n"
  "v_add_f32_dpp v68, v68, v68 row_half_mirror row_mask:0xf bank_mask:0xf\n"
  "v_rcp_f32 v68, v68\n"
  "v_permlane32_swap_b32 v104, v108\n"
  "v_pk_mul_f32 v[70:71], v[70:71], v[68:69] op_sel_hi:[1,0]\n"
  "v_pk_mul_f32 v[72:73], v[72:73], v[68:69] op_sel_hi:[1,0]\n"
  "v_pk_mul_f32 v[74:75], v[74:75], v[68:69] op_sel_hi:[1,0]\n"
  "v_cvt_pk_bf16_f32 v66, |v70|, |v71|\n"
  "v_cvt_pk_bf16_f32 v67, |v72|, |v73|\n"
  "v_cvt_pk_bf16_f32 v68, |v74|, |v75|\n"
  "buffer_store_dwordx2 v[66:67], v15, s[16:19], s63 offen sc1\n"
  "buffer_store_dword v68, v16, s[16:19], s63 offen sc1\n"
  "s_memtime s[64:65]\n"
  "s_memrealtime s[20:21]\n"
  "s_waitcnt lgkmcnt(0)\n"
  "v_mfma_f32_16x16x32_bf16 v[56:59], a[0:3], v[40:43], 0\n"
  "ds_read_b128 v[48:51], v4 offset:4096\n"
  "s_and_b32 s71, s56, 7\n"
  "v_mfma_f32_16x16x32_bf16 v[60:63], a[8:11], v[40:43], 0\n"
  "ds_read_b128 v[52:55], v4 offset:24576\n"
  "s_xor_b32 s75, s71, 4\n"
  "v_mfma_f32_16x16x32_bf16 v[64:67], a[16:19], v[40:43], 0\n"
  "ds_read_b128 v[88:91], v5 offset:2048\n"
  "s_mul_i32 s71, s71, 81920\n"
  "v_mfma_f32_16x16x32_bf16 v[68:71], a[24:27], v[40:43], 0\n"
  "ds_read_b128 v[92:95], v5 offset:3072\n"
  "s_mul_i32 s75, s75, 81920\n"
  "v_mfma_f32_16x16x32_bf16 v[72:75], a[32:35], v[40:43], 0\n"
  "s_add_u32 s68, s71, s49\n"
  "s_add_u32 s71, s71, s53\n"
  "v_mfma_f32_16x16x32_bf16 v[76:79], v[80:83], v[40:43], 0\n"
  "s_add_u32 s74, s75, s49\n"
  "s_add_u32 s75, s75, s53\n"
  "v_mfma_f32_16x16x32_bf16 v[56:59], a[4:7], v[44:47], v[56:59]\n"
  "s_xor_b32 s63, s63, 524288\n"
  "s_or_b32 s66, s66, s52\n"
  "v_mfma_f32_16x16x32_bf16 v[60:63], a[12:15], v[44:47], v[60:63]\n"
  "s_cmp_lt_u32 s56, 2\n"
  "s_cselect_b64 s[66:67], -1, s[66:67]\n"
  "v_mfma_f32_16x16x32_bf16 v[64:67], a[20:23], v[44:47], v[64:67]\n"
  "s_cmp_eq_u64 s[66:67], 0\n"
  "s_cselect_b32 s69, s2, 0x80000000\n"
  "v_mfma_f32_16x16x32_bf16 v[68:71], a[28:31], v[44:47], v[68:71]\n"
  "v_permlane32_swap_b32 v105, v109\n"
  "v_mfma_f32_16x16x32_bf16 v[72:75], a[36:39], v[44:47], v[72:75]\n"
  "v_permlane32_swap_b32 v106, v110\n"
  "v_mfma_f32_16x16x32_bf16 v[76:79], v[84:87], v[44:47], v[76:79]\n"
  "v_permlane32_swap_b32 v107, v111\n"
  "s_waitcnt lgkmcnt(3)\n"
  "v_mfma_f32_16x16x32_bf16 v[56:59], a[40:43], v[48:51], v[56:59]\n"
  "ds_read_b128 v[40:43], v1 offset:0\n"
  "ds_write_b128 v18, v[108:111]\n"
  "v_mfma_f32_16x16x32_bf16 v[60:63], a[48:51], v[48:51], v[60:63]\n"
  "ds_read_b128 v[44:47], v1 offset:20480\n"
  "v_mfma_f32_16x16x32_bf16 v[64:67], a[56:59], v[48:51], v[64:67]\n"
  "ds_read_b128 v[96:99], v5 offset:4096\n"
  "v_mfma_f32_16x16x32_bf16 v[68:71], a[64:67], v[48:51], v[68:71]\n"
  "ds_read_b128 v[100:103], v5 offset:5120\n"
  "v_mfma_f32_16x16x32_bf16 v[72:75], a[72:75], v[48:51], v[72:75]\n"
  "ds_read_b128 v[80:83], v5 offset:6144\n"
  "s_waitcnt lgkmcnt(7)\n"
  "v_mfma_f32_16x16x32_bf16 v[76:79], v[88:91], v[48:51], v[76:79]\n"
  "ds_read_b128 v[84:87], v5 offset:7168\n"
  "v_mfma_f32_16x16x32_bf16 v[56:59], a[44:47], v[52:55], v[56:59]\n"
  "s_waitcnt lgkmcnt(0)\n"
  "s_barrier\n"
  "v_mfma_f32_16x16x32_bf16 v[60:63], a[52:55], v[52:55], v[60:63]\n"
  "ds_read_b128 v[112:115], v125 offset:8192\n"
  "ds_read_b128 v[116:119], v125 offset:12288\n"
  "v_mfma_f32_16x16x32_bf16 v[64:67], a[60:63], v[52:55], v[64:67]\n"
  "ds_read_b128 v[108:111], v125 offset:4096\n"
  "ds_read_b128 v[104:107], v125\n"
  "v_mfma_f32_16x16x32_bf16 v[68:71], a[68:71], v[52:55], v[68:71]\n"
  "ds_read2st64_b32 v[120:121], v126 offset0:0 offset1:4\n"
  "ds_read2st64_b32 v[122:123], v126 offset0:8 offset1:12\n"
  "v_mfma_f32_16x16x32_bf16 v[72:75], a[76:79], v[52:55], v[72:75]\n"
  "v_mfma_f32_16x16x32_bf16 v[76:79], v[92:95], v[52:55], v[76:79]\n"
  "v_mfma_f32_16x16x32_bf16 v[56:59], a[80:83], v[40:43], v[56:59]\n"
  "ds_read_b128 v[48:51], v2 offset:0\n"
  "v_mfma_f32_16x16x32_bf16 v[60:63], a[88:91], v[40:43], v[60:63]\n"
  "ds_read_b128 v[52:55], v2 offset:20480\n"
  "v_and_b32 v127, 96, v0\n"
  "v_mfma_f32_16x16x32_bf16 v[64:67], a[96:99], v[40:43], v[64:67]\n"
  "ds_read_b128 v[88:91], v5 offset:8192\n"
  "v_lshl_add_u32 v124, v127, 2, v124\n"
  "v_mfma_f32_16x16x32_bf16 v[68:71], a[104:107], v[40:43], v[68:71]\n"
  "ds_read_b128 v[92:95], v5 offset:9216\n"
  "v_bfe_u32 v127, v0, 4, 1\n"
  "v_mfma_f32_16x16x32_bf16 v[72:75], v[96:99], v[40:43], v[72:75]\n"
  "v_mfma_f32_16x16x32_bf16 v[76:79], v[80:83], v[40:43], v[76:79]\n"
  "s_waitcnt lgkmcnt(8)\n"
  "v_add_f32 v112, v112, v116\n"
  "v_add_f32 v113, v113, v117\n"
  "v_add_f32 v114, v114, v118\n"
  "v_mfma_f32_16x16x32_bf16 v[56:59], a[84:87], v[44:47], v[56:59]\n"
  "v_add_f32 v115, v115, v119\n"
  "s_waitcnt lgkmcnt(6)\n"
  "v_add_f32 v104, v104, v108\n"
  "v_add_f32 v105, v105, v109\n"
  "v_mfma_f32_16x16x32_bf16 v[60:63], a[92:95], v[44:47], v[60:63]\n"
  "v_add_f32 v106, v106, v110\n"
  "v_add_f32 v107, v107, v111\n"
  "s_waitcnt lgkmcnt(4)\n"
  "v_add_f32 v120, v120, v122\n"
  "v_mfma_f32_16x16x32_bf16 v[64:67], a[100:103], v[44:47], v[64:67]\n"
  "v_add_f32 v121, v121, v123\n"
  "v_add_f32 v104, v104, v112\n"
  "v_add_f32 v105, v105, v113\n"
  "v_mfma_f32_16x16x32_bf16 v[68:71], a[108:111], v[44:47], v[68:71]\n"
  "v_add_f32 v106, v106, v114\n"
  "v_add_f32 v107, v107, v115\n"
  "v_add_f32 v120, v120, v121\n"
  "v_mfma_f32_16x16x32_bf16 v[72:75], v[100:103], v[44:47], v[72:75]\n"
  "v_mul_f32 v112, s76, v104\n"
  "v_mul_f32 v113, s76, v105\n"
  "v_mul_f32 v114, s76, v106\n"
  "v_mfma_f32_16x16x32_bf16 v[76:79], v[84:87], v[44:47], v[76:79]\n"
  "v_mul_f32 v115, s76, v107\n"
  "v_mul_f32 v116, s76, v120\n"
  "v_mov_b32 v108, s77\n"
  "s_waitcnt lgkmcnt(3)\n"
  "v_mfma_f32_16x16x32_bf16 v[56:59], a[112:115], v[48:51], v[56:59]\n"
  "ds_read_b128 v[40:43], v3 offset:0\n"
  "v_cndmask_b32 v112, v108, v112, s[8:9]\n"
  "v_cndmask_b32 v113, v108, v113, s[10:11]\n"
  "v_mfma_f32_16x16x32_bf16 v[60:63], a[120:123], v[48:51], v[60:63]\n"
  "ds_read_b128 v[44:47], v3 offset:20480\n"
  "v_cndmask_b32 v114, v108, v114, s[12:13]\n"
  "v_cndmask_b32 v115, v108, v115, s[14:15]\n"
  "v_mfma_f32_16x16x32_bf16 v[64:67], a[128:131], v[48:51], v[64:67]\n"
  "ds_read_b128 v[96:99], v5 offset:10240\n"
  "v_max3_f32 v110, v112, v113, v116\n"
  "v_max3_f32 v110, v114, v115, v110\n"
  "v_mfma_f32_16x16x32_bf16 v[68:71], a[136:139], v[48:51], v[68:71]\n"
  "ds_read_b128 v[100:103], v5 offset:11264\n"
  "v_lshl_add_u32 v124, v127, 3, v124\n"
  "v_max_f32_dpp v110, v110, v110 quad_perm:[1,0,3,2] row_mask:0xf bank_mask:0xf\n"
  "v_mfma_f32_16x16x32_bf16 v[72:75], a[144:147], v[48:51], v[72:75]\n"
  "ds_read_b128 v[80:83], v5 offset:12288\n"
  "v_and_or_b32 v124, v11, s48, v124\n"
  "v_max_f32_dpp v110, v110, v110 quad_perm:[2,3,0,1] row_mask:0xf bank_mask:0xf\n"
  "s_waitcnt lgkmcnt(6)\n"
  "v_mfma_f32_16x16x32_bf16 v[76:79], v[88:91], v[48:51], v[76:79]\n"
  "ds_read_b128 v[84:87], v5 offset:13312\n"
  "v_mov_b64 v[122:123], -1\n"
  "v_mfma_f32_16x16x32_bf16 v[56:59], a[116:119], v[52:55], v[56:59]\n"
  "v_max_f32_dpp v110, v110, v110 row_half_mirror row_mask:0xf bank_mask:0xf\n"
  "v_sub_f32 v112, v112, v110\n"
  "v_sub_f32 v113, v113, v110\n"
  "v_mfma_f32_16x16x32_bf16 v[60:63], a[124:127], v[52:55], v[60:63]\n"
  "v_sub_f32 v114, v114, v110\n"
  "v_sub_f32 v115, v115, v110\n"
  "v_sub_f32 v116, v116, v110\n"
  "v_mfma_f32_16x16x32_bf16 v[64:67], a[132:135], v[52:55], v[64:67]\n"
  "v_exp_f32 v116, v116\n"
  "v_exp_f32 v112, v112\n"
  "v_mfma_f32_16x16x32_bf16 v[68:71], a[140:143], v[52:55], v[68:71]\n"
  "v_exp_f32 v113, v113\n"
  "v_exp_f32 v114, v114\n"
  "v_mfma_f32_16x16x32_bf16 v[72:75], a[148:151], v[52:55], v[72:75]\n"
  "v_exp_f32 v115, v115\n"
  "v_add_f32 v108, v116, v112\n"
  "s_waitcnt lgkmcnt(6)\n"
  "v_mfma_f32_16x16x32_bf16 v[76:79], v[92:95], v[52:55], v[76:79]\n"
  "v_add_f32 v109, v113, v114\n"
  "v_add_f32 v108, v108, v115\n"
  "v_add_f32 v110, v108, v109\n"
  "s_waitcnt lgkmcnt(5)\n"
  "v_mfma_f32_16x16x32_bf16 v[56:59], a[152:155], v[40:43], v[56:59]\n"
  "ds_read_b128 v[48:51], v1 offset:4096\n"
  "v_mov_b32_dpp v117, v116 quad_perm:[1,0,3,2] row_mask:0xf bank_mask:0xf\n"
  "v_add_f32_dpp v110, v110, v110 quad_perm:[1,0,3,2] row_mask:0xf bank_mask:0xf\n"
  "v_mfma_f32_16x16x32_bf16 v[60:63], a[160:163], v[40:43], v[60:63]\n"
  "ds_read_b128 v[52:55], v1 offset:24576\n"
  "s_sub_u32 s60, s0, s96\n"
  "v_add_f32_dpp v110, v110, v110 quad_perm:[2,3,0,1] row_mask:0xf bank_mask:0xf\n"
  "v_mfma_f32_16x16x32_bf16 v[64:67], a[168:171], v[40:43], v[64:67]\n"
  "ds_read_b128 v[88:91], v5 offset:14336\n"
  "s_sub_u32 s61, s59, s97\n"
  "v_add_f32_dpp v110, v110, v110 row_half_mirror row_mask:0xf bank_mask:0xf\n"
  "v_mfma_f32_16x16x32_bf16 v[68:71], a[176:179], v[40:43], v[68:71]\n"
  "ds_read_b128 v[92:95], v5 offset:15360\n"
  "v_rcp_f32 v110, v110\n"
  "s_waitcnt lgkmcnt(7)\n"
  "v_mfma_f32_16x16x32_bf16 v[72:75], v[96:99], v[40:43], v[72:75]\n"
  "v_mul_f32 v112, v112, v110\n"
  "v_mul_f32 v113, v113, v110\n"
  "v_mul_f32 v114, v114, v110\n"
  "s_waitcnt lgkmcnt(5)\n"
  "v_mfma_f32_16x16x32_bf16 v[76:79], v[80:83], v[40:43], v[76:79]\n"
  "v_mul_f32 v115, v115, v110\n"
  "v_mul_f32 v116, v116, v110\n"
  "v_mul_f32 v117, v117, v110\n"
  "v_mfma_f32_16x16x32_bf16 v[56:59], a[156:159], v[44:47], v[56:59]\n"
  "v_cvt_pk_bf16_f32 v108, |v112|, |v113|\n"
  "v_cvt_pk_bf16_f32 v109, |v114|, |v115|\n"
  "v_cvt_pk_bf16_f32 v110, |v116|, |v117|\n"
  "v_mfma_f32_16x16x32_bf16 v[60:63], a[164:167], v[44:47], v[60:63]\n"
  "buffer_store_dwordx2 v[108:109], v124, s[16:19], s71 offen sc1\n"
  "buffer_store_dword v110, v11, s[16:19], s68 offen sc1\n"
  "v_mfma_f32_16x16x32_bf16 v[64:67], a[172:175], v[44:47], v[64:67]\n"
  "s_add_u32 s72, s99, 2\n"
  "v_mfma_f32_16x16x32_bf16 v[68:71], a[180:183], v[44:47], v[68:71]\n"
  "s_mul_i32 s72, s72, 40960\n"
  "s_add_u32 s73, s51, 10240\n"
  "v_mfma_f32_16x16x32_bf16 v[72:75], v[100:103], v[44:47], v[72:75]\n"
  "s_add_u32 s72, s72, s73\n"
  "s_mov_b32 m0, s73\n"
  "s_waitcnt lgkmcnt(4)\n"
  "v_mfma_f32_16x16x32_bf16 v[76:79], v[84:87], v[44:47], v[76:79]\n"
  "buffer_load_dwordx4 v0, s[24:27], s72 offen offset:0 sc1 lds\n"
  "s_waitcnt lgkmcnt(3)\n"
  "v_mfma_f32_16x16x32_bf16 v[56:59], a[184:187], v[48:51], v[56:59]\n"
  "ds_read_b128 v[40:43], v2 offset:4096\n"
  "buffer_load_dwordx4 v0, s[24:27], s72 offen offset:1024 sc1 lds\n"
  "v_mfma_f32_16x16x32_bf16 v[60:63], a[192:195], v[48:51], v[60:63]\n"
  "ds_read_b128 v[44:47], v2 offset:24576\n"
  "buffer_load_dwordx4 v0, s[24:27], s72 offen offset:2048 sc1 lds\n"
  "v_mfma_f32_16x16x32_bf16 v[64:67], a[200:203], v[48:51], v[64:67]\n"
  "ds_read_b128 v[96:99], v5 offset:16384\n"
  "buffer_load_dwordx4 v0, s[24:27], s72 offen offset:3072 sc1 lds\n"
  "s_waitcnt lgkmcnt(4)\n"
  "v_mfma_f32_16x16x32_bf16 v[68:71], v[88:91], v[48:51], v[68:71]\n"
  "ds_read_b128 v[100:103], v5 offset:17408\n"
  "s_add_u32 s72, s72, 4096\n"
  "v_mfma_f32_16x16x32_bf16 v[72:75], a[208:211], v[48:51], v[72:75]\n"
  "s_add_u32 m0, s73, 4096\n"
  "s_nop 0\n"
  "buffer_load_dwordx4 v0, s[24:27], s72 offen offset:0 sc1 lds\n"
  "v_mfma_f32_16x16x32_bf16 v[76:79], a[216:219], v[48:51], v[76:79]\n"
  "s_add_u32 s70, s99, 4\n"
  "s_lshl_b32 s70, s70, 7\n"
  "v_mfma_f32_16x16x32_bf16 v[56:59], a[188:191], v[52:55], v[56:59]\n"
  "s_add_u32 s70, s70, 1835008\n"
  "buffer_load_dword v19, off, s[16:19], s70 sc1\n"
  "v_mfma_f32_16x16x32_bf16 v[60:63], a[196:199], v[52:55], v[60:63]\n"
  "v_cvt_f32_u32 v118, s61\n"
  "v_cvt_f32_u32 v119, s60\n"
  "v_mfma_f32_16x16x32_bf16 v[64:67], a[204:207], v[52:55], v[64:67]\n"
  "v_rcp_f32 v118, v118\n"
  "s_waitcnt lgkmcnt(4)\n"
  "v_mfma_f32_16x16x32_bf16 v[68:71], v[92:95], v[52:55], v[68:71]\n"
  "s_mov_b32 s96, s0\n"
  "s_mov_b32 s97, s59\n"
  "v_mfma_f32_16x16x32_bf16 v[72:75], a[212:215], v[52:55], v[72:75]\n"
  "v_mul_f32 v118, v118, v119\n"
  "v_min_f32 v118, 0x41c80000, v118\n"
  "v_mfma_f32_16x16x32_bf16 v[76:79], a[220:223], v[52:55], v[76:79]\n"
  "v_max_f32 v118, 0x41600000, v118\n"
  "v_mul_f32 v119, 0x41800000, v118\n"
  "s_waitcnt lgkmcnt(3)\n"
  "v_mfma_f32_16x16x32_bf16 v[56:59], a[224:227], v[40:43], v[56:59]\n"
  "ds_read_b128 v[48:51], v3 offset:4096\n"
  "v_rcp_f32 v118, v118\n"
  "v_mfma_f32_16x16x32_bf16 v[60:63], a[232:235], v[40:43], v[60:63]\n"
  "ds_read_b128 v[52:55], v3 offset:24576\n"
  "v_cvt_u32_f32 v119, v119\n"
  "v_mfma_f32_16x16x32_bf16 v[64:67], a[240:243], v[40:43], v[64:67]\n"
  "ds_read_b128 v[80:83], v5 offset:18432\n"
  "s_sub_i32 s60, s64, s0\n"
  "v_mfma_f32_16x16x32_bf16 v[68:71], a[248:251], v[40:43], v[68:71]\n"
  "ds_read_b128 v[84:87], v5 offset:19456\n"
  "s_lshl_b32 s60, s60, 2\n"
  "s_waitcnt lgkmcnt(5)\n"
  "v_mfma_f32_16x16x32_bf16 v[72:75], v[96:99], v[40:43], v[72:75]\n"
  "v_readfirstlane_b32 s98, v119\n"
  "v_cvt_f32_i32 v119, s60\n"
  "v_mfma_f32_16x16x32_bf16 v[76:79], v[128:131], v[40:43], v[76:79]\n"
  "s_sub_i32 s60, s20, s59\n"
  "v_mul_f32 v118, v118, v119\n"
  "v_mfma_f32_16x16x32_bf16 v[56:59], a[228:231], v[44:47], v[56:59]\n"
  "s_lshl_b32 s60, s60, 2\n"
  "v_cvt_i32_f32 v118, v118\n"
  "v_mfma_f32_16x16x32_bf16 v[60:63], a[236:239], v[44:47], v[60:63]\n"
  "s_add_u32 s61, s60, 16\n"
  "v_max_i32 v118, s60, v118\n"
  "v_mfma_f32_16x16x32_bf16 v[64:67], a[244:247], v[44:47], v[64:67]\n"
  "v_min_i32 v118, s61, v118\n"
  "v_mov_b32 v119, s3\n"
  "v_mfma_f32_16x16x32_bf16 v[68:71], a[252:255], v[44:47], v[68:71]\n"
  "v_mov_b32 v127, s69\n"
  "s_mov_b64 exec, 1\n"
  "buffer_store_dwordx2 v[118:119], v127, s[16:19], 0 offen sc1\n"
  "s_mov_b64 exec, -1\n"
  "s_waitcnt lgkmcnt(4)\n"
  "v_mfma_f32_16x16x32_bf16 v[72:75], v[100:103], v[44:47], v[72:75]\n"
  "buffer_store_dwordx2 v[122:123], v124, s[16:19], s75 offen sc1\n"
  "v_mfma_f32_16x16x32_bf16 v[76:79], v[132:135], v[44:47], v[76:79]\n"
  "buffer_store_dword v122, v11, s[16:19], s74 offen sc1\n"
  "s_waitcnt lgkmcnt(3)\n"
  "v_mfma_f32_16x16x32_bf16 v[56:59], v[136:139], v[48:51], v[56:59]\n"
  "ds_read_b128 v[40:43], v0 offset:8192\n"
  "buffer_store_dwordx2 v[122:123], v15, s[16:19], s63 offen sc1\n"
  "v_mfma_f32_16x16x32_bf16 v[60:63], v[144:147], v[48:51], v[60:63]\n"
  "ds_read_b128 v[44:47], v0 offset:28672\n"
  "buffer_store_dword v122, v16, s[16:19], s63 offen sc1\n"
  "v_mfma_f32_16x16x32_bf16 v[64:67], v[152:155], v[48:51], v[64:67]\n"
  "ds_read_b128 v[88:91], v5 offset:20480\n"
  "s_waitcnt lgkmcnt(4)\n"
  "v_mfma_f32_16x16x32_bf16 v[68:71], v[80:83], v[48:51], v[68:71]\n"
  "ds_read_b128 v[92:95], v5 offset:21504\n"
  "v_mfma_f32_16x16x32_bf16 v[72:75], v[160:163], v[48:51], v[72:75]\n"
  "v_mfma_f32_16x16x32_bf16 v[76:79], v[168:171], v[48:51], v[76:79]\n"
  "v_mfma_f32_16x16x32_bf16 v[56:59], v[140:143], v[52:55], v[56:59]\n"
  "v_mfma_f32_16x16x32_bf16 v[60:63], v[148:151], v[52:55], v[60:63]\n"
  "v_mfma_f32_16x16x32_bf16 v[64:67], v[156:159], v[52:55], v[64:67]\n"
  "s_waitcnt lgkmcnt(4)\n"
  "v_mfma_f32_16x16x32_bf16 v[68:71], v[84:87], v[52:55], v[68:71]\n"
  "v_mfma_f32_16x16x32_bf16 v[72:75], v[164:167], v[52:55], v[72:75]\n"
  "v_mfma_f32_16x16x32_bf16 v[76:79], v[172:175], v[52:55], v[76:79]\n"
  "s_waitcnt lgkmcnt(0)\n"
  "s_add_u32 s56, s56, 2\n"
  "s_add_u32 s99, s99, 2\n"
  "s_cmp_lt_u32 s56, 64\n"
  "s_cbranch_scc1 H_LOOP_%=\n"
  "s_waitcnt vmcnt(0)\n"
  "s_add_u32 s46, s46, 64\n"
  "s_cmp_lt_u32 s46, 128\n"
  "s_cbranch_scc1 H_LAYER_%=\n"
  "s_branch H_EXIT_%=\n"
  "H_SCHP_%=:\n"
  "s_nop 7\n"
  "s_waitcnt vmcnt(0) lgkmcnt(0)\n"
  "s_mov_b32 s62, 0\n"
  "H_SCHQ_%=:\n"
  "buffer_load_dword v19, off, s[16:19], s75 sc1\n"
  "s_waitcnt vmcnt(0)\n"
  "v_readfirstlane_b32 s59, v19\n"
  "s_cmp_lg_u32 s59, -1\n"
  "s_cbranch_scc1 H_SCHOK_%=\n"
  "s_sleep 2\n"
  "s_add_u32 s62, s62, 1\n"
  "s_cmp_gt_u32 s62, 300000\n"
  "s_cbranch_scc1 H_FAIL_%=\n"
  "s_and_b32 s60, s62, 255\n"
  "s_cmp_lg_u32 s60, 0\n"
  "s_cbranch_scc1 H_SCHQ_%=\n"
  "v_mov_b32 v19, 0\n"
  "buffer_load_dword v19, v19, s[40:43], 0 offen sc1\n"
  "s_waitcnt vmcnt(0)\n"
  "v_readfirstlane_b32 s60, v19\n"
  "s_cmp_lg_u32 s60, 0\n"
  "s_cbranch_scc1 H_EXIT_%=\n"
  "s_branch H_SCHQ_%=\n"
  "H_REDO_%=:\n"
  "s_nop 7\n"
  "s_waitcnt vmcnt(0) lgkmcnt(0)\n"
  "s_barrier\n"
  "s_mov_b32 s62, 0\n"
  "s_mov_b64 s[66:67], -1\n"
  "v_mov_b64 v[56:57], 0\n"
  "v_mov_b64 v[58:59], 0\n"
  "v_mov_b64 v[60:61], 0\n"
  "v_mov_b64 v[62:63], 0\n"
  "v_mov_b64 v[64:65], 0\n"
  "v_mov_b64 v[66:67], 0\n"
  "v_mov_b64 v[68:69], 0\n"
  "v_mov_b64 v[70:71], 0\n"
  "v_mov_b64 v[72:73], 0\n"
  "v_mov_b64 v[74:75], 0\n"
  "v_mov_b64 v[76:77], 0\n"
  "v_mov_b64 v[78:79], 0\n"
  "ds_read_b128 v[80:83], v5 offset:0\n"
  "ds_read_b128 v[84:87], v5 offset:1024\n"
  "ds_read_b128 v[88:91], v5 offset:2048\n"
  "ds_read_b128 v[92:95], v5 offset:3072\n"
  "ds_read_b128 v[96:99], v5 offset:4096\n"
  "ds_read_b128 v[100:103], v5 offset:5120\n"
  "s_waitcnt lgkmcnt(0)\n"
  "H_RP_%=:\n"
  "buffer_load_dwordx4 v[104:107], v6, s[16:19], s57 offen sc1\n"
  "buffer_load_dwordx4 v[108:111], v6, s[16:19], s57 offen offset:1024 sc1\n"
  "buffer_load_dwordx2 v[54:55], v7, s[16:19], s57 offen sc1\n"
  "s_waitcnt vmcnt(2)\n"
  "v_max3_u32 v19, v104, v105, v106\n"
  "s_waitcnt vmcnt(1)\n"
  "v_max3_u32 v19, v107, v108, v19\n"
  "v_max3_u32 v19, v109, v110, v19\n"
  "s_waitcnt vmcnt(0)\n"
  "v_max3_u32 v19, v111, v54, v19\n"
  "v_max_u32 v19, v55, v19\n"
  "v_cmp_eq_u32 vcc, -1, v19\n"
  "s_nop 1\n"
  "s_cbranch_vccz H_D0_%=\n"
  "s_add_u32 s62, s62, 1\n"
  "s_cmp_gt_u32 s62, 300000\n"
  "s_cbranch_scc1 H_FAIL_%=\n"
  "s_and_b32 s60, s62, 255\n"
  "s_cmp_lg_u32 s60, 0\n"
  "s_cbranch_scc1 H_RP_%=\n"
  "v_mov_b32 v19, 0\n"
  "buffer_load_dword v19, v19, s[40:43], 0 offen sc1\n"
  "s_waitcnt vmcnt(0)\n"
  "v_readfirstlane_b32 s60, v19\n"
  "s_cmp_lg_u32 s60, 0\n"
  "s_cbranch_scc1 H_EXIT_%=\n"
  "s_branch H_RP_%=\n"
  "H_FIX0_%=:\n"
  "s_nop 7\n"
  "s_waitcnt vmcnt(0) lgkmcnt(0)\n"
  "s_mov_b32 s62, 0\n"
  "s_mov_b32 s52, 1\n"
  "v_mov_b64 v[56:57], 0\n"
  "v_mov_b64 v[58:59], 0\n"
  "v_mov_b64 v[60:61], 0\n"
  "v_mov_b64 v[62:63], 0\n"
  "v_mov_b64 v[64:65], 0\n"
  "v_mov_b64 v[66:67], 0\n"
  "v_mov_b64 v[68:69], 0\n"
  "v_mov_b64 v[70:71], 0\n"
  "v_mov_b64 v[72:73], 0\n"
  "v_mov_b64 v[74:75], 0\n"
  "v_mov_b64 v[76:77], 0\n"
  "v_mov_b64 v[78:79], 0\n"
  "s_branch H_RP_%=\n"
  "H_FAIL_%=:\n"
  "s_nop 7\n"
  "v_mov_b32 v56, 0\n"
  "v_mov_b32 v57, 1\n"
  "buffer_store_dword v57, v56, s[40:43], 0 offen sc1\n"
  "H_EXIT_%=:\n"
  "s_nop 7\n"
  "s_waitcnt vmcnt(0)\n"
  "s_endpgm\n"
  "C_ENTRY_%=:\n"
  "s_mov_b32 s76, 0x3dd53b95\n"
  "s_mov_b32 s77, 0xceabfb8f\n"
  "s_mov_b32 s78, 0xfffffffe\n"
  "s_mov_b32 s46, 0\n"
  "s_mov_b32 s62, 0\n"
  "C_CFP_%=:\n"
  "s_nop 7\n"
  "buffer_load_dword v15, v24, s[16:19], 0 offen sc1\n"
  "s_waitcnt vmcnt(0)\n"
  "v_cmp_eq_u32 vcc, -1, v15\n"
  "s_nop 1\n"
  "s_cbranch_vccz C_CFK_%=\n"
  "s_sleep 4\n"
  "s_add_u32 s62, s62, 1\n"
  "s_cmp_gt_u32 s62, 300000\n"
  "s_cbranch_scc1 C_FAIL_%=\n"
  "s_and_b32 s60, s62, 255\n"
  "s_cmp_lg_u32 s60, 0\n"
  "s_cbranch_scc1 C_CFP_%=\n"
  "v_mov_b32 v15, 0\n"
  "buffer_load_dword v15, v15, s[40:43], 0 offen sc1\n"
  "s_waitcnt vmcnt(0)\n"
  "v_readfirstlane_b32 s60, v15\n"
  "s_cmp_lg_u32 s60, 0\n"
  "s_cbranch_scc1 C_EXIT_%=\n"
  "s_branch C_CFP_%=\n"
  "C_CFK_%=:\n"
  "s_nop 7\n"
  "s_mov_b32 s68, 0\n"
  "buffer_load_dwordx4 a[0:3], v25, s[28:31], s68 offen offset:0 sc1\n"
  "buffer_load_dwordx4 a[4:7], v25, s[28:31], s68 offen offset:1024 sc1\n"
  "s_mov_b32 s68, 2048\n"
  "buffer_load_dwordx4 a[8:11], v25, s[28:31], s68 offen offset:0 sc1\n"
  "buffer_load_dwordx4 a[12:15], v25, s[28:31], s68 offen offset:1024 sc1\n"
  "s_mov_b32 s68, 4096\n"
  "buffer_load_dwordx4 a[16:19], v25, s[28:31], s68 offen offset:0 sc1\n"
  "buffer_load_dwordx4 a[20:23], v25, s[28:31], s68 offen offset:1024 sc1\n"
  "s_mov_b32 s68, 6144\n"
  "buffer_load_dwordx4 a[24:27], v25, s[28:31], s68 offen offset:0 sc1\n"
  "buffer_load_dwordx4 a[28:31], v25, s[28:31], s68 offen offset:1024 sc1\n"
  "s_mov_b32 s68, 8192\n"
  "buffer_load_dwordx4 a[32:35], v25, s[28:31], s68 offen offset:0 sc1\n"
  "buffer_load_dwordx4 a[36:39], v25, s[28:31], s68 offen offset:1024 sc1\n"
  "s_mov_b32 s68, 10240\n"
  "buffer_load_dwordx4 a[40:43], v25, s[28:31], s68 offen offset:0 sc1\n"
  "buffer_load_dwordx4 a[44:47], v25, s[28:31], s68 offen offset:1024 sc1\n"
  "s_mov_b32 s68, 12288\n"
  "buffer_load_dwordx4 a[48:51], v25, s[28:31], s68 offen offset:0 sc1\n"
  "buffer_load_dwordx4 a[52:55], v25, s[28:31], s68 offen offset:1024 sc1\n"
  "s_mov_b32 s68, 14336\n"
  "buffer_load_dwordx4 a[56:59], v25, s[28:31], s68 offen offset:0 sc1\n"
  "buffer_load_dwordx4 a[60:63], v25, s[28:31], s68 offen offset:1024 sc1\n"
  "s_mov_b32 s68, 16384\n"
  "buffer_load_dwordx4 a[64:67], v25, s[28:31], s68 offen offset:0 sc1\n"
  "buffer_load_dwordx4 a[68:71], v25, s[28:31], s68 offen offset:1024 sc1\n"
  "s_mov_b32 s68, 18432\n"
  "buffer_load_dwordx4 a[72:75], v25, s[28:31], s68 offen offset:0 sc1\n"
  "buffer_load_dwordx4 a[76:79], v25, s[28:31], s68 offen offset:1024 sc1\n"
  "s_mov_b32 s68, 20480\n"
  "buffer_load_dwordx4 a[80:83], v25, s[28:31], s68 offen offset:0 sc1\n"
  "buffer_load_dwordx4 a[84:87], v25, s[28:31], s68 offen offset:1024 sc1\n"
  "s_mov_b32 s68, 22528\n"
  "buffer_load_dwordx4 a[88:91], v25, s[28:31], s68 offen offset:0 sc1\n"
  "buffer_load_dwordx4 a[92:95], v25, s[28:31], s68 offen offset:1024 sc1\n"
  "s_mov_b32 s68, 24576\n"
  "buffer_load_dwordx4 a[96:99], v25, s[28:31], s68 offen offset:0 sc1\n"
  "buffer_load_dwordx4 a[100:103], v25, s[28:31], s68 offen offset:1024 sc1\n"
  "s_mov_b32 s68, 26624\n"
  "buffer_load_dwordx4 a[104:107], v25, s[28:31], s68 offen offset:0 sc1\n"
  "buffer_load_dwordx4 a[108:111], v25, s[28:31], s68 offen offset:1024 sc1\n"
  "s_mov_b32 s68, 28672\n"
  "buffer_load_dwordx4 a[112:115], v25, s[28:31], s68 offen offset:0 sc1\n"
  "buffer_load_dwordx4 a[116:119], v25, s[28:31], s68 offen offset:1024 sc1\n"
  "s_mov_b32 s68, 30720\n"
  "buffer_load_dwordx4 a[120:123], v25, s[28:31], s68 offen offset:0 sc1\n"
  "buffer_load_dwordx4 a[124:127], v25, s[28:31], s68 offen offset:1024 sc1\n"
  "s_mov_b32 s68, 32768\n"
  "buffer_load_dwordx4 a[128:131], v25, s[28:31], s68 offen offset:0 sc1\n"
  "buffer_load_dwordx4 a[132:135], v25, s[28:31], s68 offen offset:1024 sc1\n"
  "s_mov_b32 s68, 34816\n"
  "buffer_load_dwordx4 a[136:139], v25, s[28:31], s68 offen offset:0 sc1\n"
  "buffer_load_dwordx4 a[140:143], v25, s[28:31], s68 offen offset:1024 sc1\n"
  "s_mov_b32 s68, 36864\n"
  "buffer_load_dwordx4 a[144:147], v25, s[28:31], s68 offen offset:0 sc1\n"
  "buffer_load_dwordx4 a[148:151], v25, s[28:31], s68 offen offset:1024 sc1\n"
  "s_mov_b32 s68, 38912\n"
  "buffer_load_dwordx4 a[152:155], v25, s[28:31], s68 offen offset:0 sc1\n"
  "buffer_load_dwordx4 a[156:159], v25, s[28:31], s68 offen offset:1024 sc1\n"
  "s_mov_b32 s68, 40960\n"
  "buffer_load_dwordx4 a[160:163], v25, s[28:31], s68 offen offset:0 sc1\n"
  "buffer_load_dwordx4 a[164:167], v25, s[28:31], s68 offen offset:1024 sc1\n"
  "s_mov_b32 s68, 43008\n"
  "buffer_load_dwordx4 a[168:171], v25, s[28:31], s68 offen offset:0 sc1\n"
  "buffer_load_dwordx4 a[172:175], v25, s[28:31], s68 offen offset:1024 sc1\n"
  "s_mov_b32 s68, 45056\n"
  "buffer_load_dwordx4 a[176:179], v25, s[28:31], s68 offen offset:0 sc1\n"
  "buffer_load_dwordx4 a[180:183], v25, s[28:31], s68 offen offset:1024 sc1\n"
  "s_mov_b32 s68, 47104\n"
  "buffer_load_dwordx4 a[184:187], v25, s[28:31], s68 offen offset:0 sc1\n"
  "buffer_load_dwordx4 a[188:191], v25, s[28:31], s68 offen offset:1024 sc1\n"
  "s_mov_b32 s68, 49152\n"
  "buffer_load_dwordx4 a[192:195], v25, s[28:31], s68 offen offset:0 sc1\n"
  "buffer_load_dwordx4 a[196:199], v25, s[28:31], s68 offen offset:1024 sc1\n"
  "s_mov_b32 s68, 51200\n"
  "buffer_load_dwordx4 a[200:203], v25, s[28:31], s68 offen offset:0 sc1\n"
  "buffer_load_dwordx4 a[204:207], v25, s[28:31], s68 offen offset:1024 sc1\n"
  "s_mov_b32 s68, 53248\n"
  "buffer_load_dwordx4 a[208:211], v25, s[28:31], s68 offen offset:0 sc1\n"
  "buffer_load_dwordx4 a[212:215], v25, s[28:31], s68 offen offset:1024 sc1\n"
  "s_mov_b32 s68, 55296\n"
  "buffer_load_dwordx4 a[216:219], v25, s[28:31], s68 offen offset:0 sc1\n"
  "buffer_load_dwordx4 a[220:223], v25, s[28:31], s68 offen offset:1024 sc1\n"
  "s_mov_b32 s68, 57344\n"
  "buffer_load_dwordx4 a[224:227], v25, s[28:31], s68 offen offset:0 sc1\n"
  "buffer_load_dwordx4 a[228:231], v25, s[28:31], s68 offen offset:1024 sc1\n"
  "s_mov_b32 s68, 59392\n"
  "buffer_load_dwordx4 a[232:235], v25, s[28:31], s68 offen offset:0 sc1\n"
  "buffer_load_dwordx4 a[236:239], v25, s[28:31], s68 offen offset:1024 sc1\n"
  "s_mov_b32 s68, 61440\n"
  "s_waitcnt vmcnt(50)\n"
  "buffer_load_dwordx4 a[240:243], v25, s[28:31], s68 offen offset:0 sc1\n"
  "buffer_load_dwordx4 a[244:247], v25, s[28:31], s68 offen offset:1024 sc1\n"
  "s_mov_b32 s68, 63488\n"
  "buffer_load_dwordx4 a[248:251], v25, s[28:31], s68 offen offset:0 sc1\n"
  "buffer_load_dwordx4 a[252:255], v25, s[28:31], s68 offen offset:1024 sc1\n"
  "s_mov_b32 s68, 65536\n"
  "buffer_load_dwordx4 v[192:195], v25, s[28:31], s68 offen offset:0 sc1\n"
  "buffer_load_dwordx4 v[196:199], v25, s[28:31], s68 offen offset:1024 sc1\n"
  "s_mov_b32 s68, 67584\n"
  "buffer_load_dwordx4 v[200:203], v25, s[28:31], s68 offen offset:0 sc1\n"
  "buffer_load_dwordx4 v[204:207], v25, s[28:31], s68 offen offset:1024 sc1\n"
  "s_mov_b32 s68, 69632\n"
  "buffer_load_dwordx4 v[208:211], v25, s[28:31], s68 offen offset:0 sc1\n"
  "buffer_load_dwordx4 v[212:215], v25, s[28:31], s68 offen offset:1024 sc1\n"
  "s_mov_b32 s68, 71680\n"
  "s_waitcnt vmcnt(50)\n"
  "buffer_load_dwordx4 v[216:219], v25, s[28:31], s68 offen offset:0 sc1\n"
  "buffer_load_dwordx4 v[220:223], v25, s[28:31], s68 offen offset:1024 sc1\n"
  "s_mov_b32 s68, 73728\n"
  "buffer_load_dwordx4 v[224:227], v25, s[28:31], s68 offen offset:0 sc1\n"
  "buffer_load_dwordx4 v[228:231], v25, s[28:31], s68 offen offset:1024 sc1\n"
  "s_mov_b32 s68, 75776\n"
  "buffer_load_dwordx4 v[232:235], v25, s[28:31], s68 offen offset:0 sc1\n"
  "buffer_load_dwordx4 v[236:239], v25, s[28:31], s68 offen offset:1024 sc1\n"
  "s_mov_b32 s68, 77824\n"
  "buffer_load_dwordx4 v[240:243], v25, s[28:31], s68 offen offset:0 sc1\n"
  "buffer_load_dwordx4 v[244:247], v25, s[28:31], s68 offen offset:1024 sc1\n"
  "s_mov_b32 s68, 79872\n"
  "buffer_load_dwordx4 v[248:251], v25, s[28:31], s68 offen offset:0 sc1\n"
  "buffer_load_dwordx4 v[252:255], v25, s[28:31], s68 offen offset:1024 sc1\n"
  "s_mov_b32 s38, 0\n"
  "v_add_u32 v20, 81920, v0\n"
  "s_waitcnt vmcnt(0)\n"
  "s_cmp_eq_u32 s45, 0\n"
  "s_cbranch_scc1 C_NOM0_%=\n"
  "s_mov_b32 s56, 0\n"
  "s_add_u32 s60, s56, 0\n"
  "s_bfe_u32 s58, s60, 131073\n"
  "s_lshl_b32 s58, s58, 18\n"
  "s_xor_b32 s60, s60, s47\n"
  "s_and_b32 s60, s60, 1\n"
  "s_lshl_b32 s60, s60, 13\n"
  "s_add_u32 s58, s58, s60\n"
  "v_mov_b32 v15, 0\n"
  "buffer_store_dword v15, v16, s[16:19], s58 offen sc1\n"
  "s_cmp_lg_u32 s44, 0\n"
  "s_cbranch_scc1 C_NOM0_%=\n"
  "s_add_u32 s72, s58, 131072\n"
  "buffer_store_dword v23, v11, s[16:19], s58 offen sc1\n"
  "buffer_store_dword v23, v11, s[16:19], s72 offen sc1\n"
  "C_NOM0_%=:\n"
  "s_nop 7\n"
  "s_waitcnt lgkmcnt(0)\n"
  "s_memtime s[64:65]\n"
  "s_memrealtime s[66:67]\n"
  "s_waitcnt lgkmcnt(0)\n"
  "s_mov_b32 s4, s64\n"
  "s_mov_b32 s5, s66\n"
  "s_mov_b32 s6, 352\n"
  "s_mov_b32 s8, 0\n"
  "s_mov_b32 s9, 0\n"
  "s_mov_b32 s10, 0\n"
  "C_LAYER_%=:\n"
  "s_nop 7\n"
  "s_mov_b32 s56, s45\n"
  "s_add_u32 s99, s46, s45\n"
  "s_add_u32 s74, s99, 0\n"
  "s_lshl_b32 s74, s74, 7\n"
  "s_add_u32 s74, s74, 1835008\n"
  "s_mov_b32 s62, 0\n"
  "s_cmp_ge_u32 s56, 64\n"
  "s_cbranch_scc1 C_F0K_%=\n"
  "s_waitcnt vmcnt(0) lgkmcnt(0)\n"
  "C_F0P_%=:\n"
  "buffer_load_dword v15, off, s[16:19], s74 sc1\n"
  "s_waitcnt vmcnt(0)\n"
  "v_readfirstlane_b32 s60, v15\n"
  "s_cmp_lg_u32 s60, -1\n"
  "s_cbranch_scc1 C_F0K_%=\n"
  "s_sleep 4\n"
  "s_add_u32 s62, s62, 1\n"
  "s_cmp_gt_u32 s62, 300000\n"
  "s_cbranch_scc1 C_FAIL_%=\n"
  "s_and_b32 s60, s62, 255\n"
  "s_cmp_lg_u32 s60, 0\n"
  "s_cbranch_scc1 C_F0P_%=\n"
  "v_mov_b32 v15, 0\n"
  "buffer_load_dword v15, v15, s[40:43], 0 offen sc1\n"
  "s_waitcnt vmcnt(0)\n"
  "v_readfirstlane_b32 s60, v15\n"
  "s_cmp_lg_u32 s60, 0\n"
  "s_cbranch_scc1 C_EXIT_%=\n"
  "s_branch C_F0P_%=\n"
  "C_F0K_%=:\n"
  "s_memrealtime s[64:65]\n"
  "s_waitcnt lgkmcnt(0)\n"
  "s_bfe_u32 s60, s53, 458776\n"
  "s_mul_i32 s60, s60, s48\n"
  "s_lshr_b32 s60, s60, 4\n"
  "s_add_u32 s63, s64, s60\n"
  "s_barrier\n"
  "s_mov_b32 s96, 0\n"
  "s_mov_b32 s97, 108544\n"
  "s_add_u32 s60, s99, 0\n"
  "s_mul_i32 s72, s60, 40960\n"
  "s_mul_i32 s73, s44, 10240\n"
  "s_add_u32 s72, s72, s73\n"
  "s_add_u32 s73, s73, s96\n"
  "s_mov_b32 m0, s73\n"
  "s_nop 0\n"
  "buffer_load_dwordx4 v0, s[24:27], s72 offen offset:0 sc1 lds\n"
  "buffer_load_dwordx4 v0, s[24:27], s72 offen offset:1024 sc1 lds\n"
  "buffer_load_dwordx4 v0, s[24:27], s72 offen offset:2048 sc1 lds\n"
  "buffer_load_dwordx4 v0, s[24:27], s72 offen offset:3072 sc1 lds\n"
  "s_add_u32 s72, s72, 4096\n"
  "s_add_u32 s73, s73, 4096\n"
  "s_mov_b32 m0, s73\n"
  "s_nop 0\n"
  "buffer_load_dwordx4 v0, s[24:27], s72 offen offset:0 sc1 lds\n"
  "buffer_load_dwordx4 v0, s[24:27], s72 offen offset:1024 sc1 lds\n"
  "buffer_load_dwordx4 v0, s[24:27], s72 offen offset:2048 sc1 lds\n"
  "buffer_load_dwordx4 v0, s[24:27], s72 offen offset:3072 sc1 lds\n"
  "s_add_u32 s72, s72, 4096\n"
  "s_add_u32 s73, s73, 4096\n"
  "s_mov_b32 m0, s73\n"
  "s_nop 0\n"
  "buffer_load_dwordx4 v0, s[24:27], s72 offen offset:0 sc1 lds\n"
  "buffer_load_dwordx4 v0, s[24:27], s72 offen offset:1024 sc1 lds\n"
  "s_waitcnt vmcnt(0)\n"
  "s_barrier\n"
  "v_mov_b32 v17, v0\n"
  "s_mul_i32 s60, s44, 5120\n"
  "s_add_u32 s60, s60, s97\n"
  "v_add_u32 v19, s60, v0\n"
  "ds_read_b128 v[64:67], v17 offset:0\n"
  "ds_read_b128 v[68:71], v17 offset:10240\n"
  "ds_read_b128 v[72:75], v17 offset:20480\n"
  "ds_read_b128 v[76:79], v17 offset:30720\n"
  "s_waitcnt lgkmcnt(3)\n"
  "v_mfma_f32_16x16x32_bf16 v[32:35], v[64:67], a[0:3], 0\n"
  "s_waitcnt lgkmcnt(2)\n"
  "v_mfma_f32_16x16x32_bf16 v[36:39], v[68:71], a[0:3], 0\n"
  "ds_read_b128 v[80:83], v17 offset:1024\n"
  "v_mfma_f32_16x16x32_bf16 v[40:43], v[64:67], a[8:11], 0\n"
  "v_mfma_f32_16x16x32_bf16 v[44:47], v[68:71], a[8:11], 0\n"
  "ds_read_b128 v[84:87], v17 offset:11264\n"
  "v_mfma_f32_16x16x32_bf16 v[48:51], v[64:67], a[16:19], 0\n"
  "v_mfma_f32_16x16x32_bf16 v[52:55], v[68:71], a[16:19], 0\n"
  "ds_read_b128 v[88:91], v17 offset:21504\n"
  "v_mfma_f32_16x16x32_bf16 v[56:59], v[64:67], a[24:27], 0\n"
  "v_mfma_f32_16x16x32_bf16 v[60:63], v[68:71], a[24:27], 0\n"
  "ds_read_b128 v[92:95], v17 offset:31744\n"
  "s_waitcnt lgkmcnt(5)\n"
  "v_mfma_f32_16x16x32_bf16 v[32:35], v[72:75], a[4:7], v[32:35]\n"
  "s_waitcnt lgkmcnt(4)\n"
  "v_mfma_f32_16x16x32_bf16 v[36:39], v[76:79], a[4:7], v[36:39]\n"
  "v_mfma_f32_16x16x32_bf16 v[40:43], v[72:75], a[12:15], v[40:43]\n"
  "v_mfma_f32_16x16x32_bf16 v[44:47], v[76:79], a[12:15], v[44:47]\n"
  "v_mfma_f32_16x16x32_bf16 v[48:51], v[72:75], a[20:23], v[48:51]\n"
  "v_mfma_f32_16x16x32_bf16 v[52:55], v[76:79], a[20:23], v[52:55]\n"
  "v_mfma_f32_16x16x32_bf16 v[56:59], v[72:75], a[28:31], v[56:59]\n"
  "v_mfma_f32_16x16x32_bf16 v[60:63], v[76:79], a[28:31], v[60:63]\n"
  "s_waitcnt lgkmcnt(3)\n"
  "v_mfma_f32_16x16x32_bf16 v[32:35], v[80:83], a[32:35], v[32:35]\n"
  "s_waitcnt lgkmcnt(2)\n"
  "v_mfma_f32_16x16x32_bf16 v[36:39], v[84:87], a[32:35], v[36:39]\n"
  "ds_read_b128 v[64:67], v17 offset:2048\n"
  "v_mfma_f32_16x16x32_bf16 v[40:43], v[80:83], a[40:43], v[40:43]\n"
  "v_mfma_f32_16x16x32_bf16 v[44:47], v[84:87], a[40:43], v[44:47]\n"
  "ds_read_b128 v[68:71], v17 offset:12288\n"
  "v_mfma_f32_16x16x32_bf16 v[48:51], v[80:83], a[48:51], v[48:51]\n"
  "v_mfma_f32_16x16x32_bf16 v[52:55], v[84:87], a[48:51], v[52:55]\n"
  "ds_read_b128 v[72:75], v17 offset:22528\n"
  "v_mfma_f32_16x16x32_bf16 v[56:59], v[80:83], a[56:59], v[56:59]\n"
  "v_mfma_f32_16x16x32_bf16 v[60:63], v[84:87], a[56:59], v[60:63]\n"
  "ds_read_b128 v[76:79], v17 offset:32768\n"
  "s_waitcnt lgkmcnt(5)\n"
  "v_mfma_f32_16x16x32_bf16 v[32:35], v[88:91], a[36:39], v[32:35]\n"
  "s_waitcnt lgkmcnt(4)\n"
  "v_mfma_f32_16x16x32_bf16 v[36:39], v[92:95], a[36:39], v[36:39]\n"
  "v_mfma_f32_16x16x32_bf16 v[40:43], v[88:91], a[44:47], v[40:43]\n"
  "v_mfma_f32_16x16x32_bf16 v[44:47], v[92:95], a[44:47], v[44:47]\n"
  "v_mfma_f32_16x16x32_bf16 v[48:51], v[88:91], a[52:55], v[48:51]\n"
  "v_mfma_f32_16x16x32_bf16 v[52:55], v[92:95], a[52:55], v[52:55]\n"
  "v_mfma_f32_16x16x32_bf16 v[56:59], v[88:91], a[60:63], v[56:59]\n"
  "v_mfma_f32_16x16x32_bf16 v[60:63], v[92:95], a[60:63], v[60:63]\n"
  "s_waitcnt lgkmcnt(3)\n"
  "v_mfma_f32_16x16x32_bf16 v[32:35], v[64:67], a[64:67], v[32:35]\n"
  "s_waitcnt lgkmcnt(2)\n"
  "v_mfma_f32_16x16x32_bf16 v[36:39], v[68:71], a[64:67], v[36:39]\n"
  "ds_read_b128 v[80:83], v17 offset:3072\n"
  "v_mfma_f32_16x16x32_bf16 v[40:43], v[64:67], a[72:75], v[40:43]\n"
  "v_mfma_f32_16x16x32_bf16 v[44:47], v[68:71], a[72:75], v[44:47]\n"
  "ds_read_b128 v[84:87], v17 offset:13312\n"
  "v_mfma_f32_16x16x32_bf16 v[48:51], v[64:67], a[80:83], v[48:51]\n"
  "v_mfma_f32_16x16x32_bf16 v[52:55], v[68:71], a[80:83], v[52:55]\n"
  "ds_read_b128 v[88:91], v17 offset:23552\n"
  "v_mfma_f32_16x16x32_bf16 v[56:59], v[64:67], a[88:91], v[56:59]\n"
  "v_mfma_f32_16x16x32_bf16 v[60:63], v[68:71], a[88:91], v[60:63]\n"
  "ds_read_b128 v[92:95], v17 offset:33792\n"
  "s_waitcnt lgkmcnt(5)\n"
  "v_mfma_f32_16x16x32_bf16 v[32:35], v[72:75], a[68:71], v[32:35]\n"
  "s_waitcnt lgkmcnt(4)\n"
  "v_mfma_f32_16x16x32_bf16 v[36:39], v[76:79], a[68:71], v[36:39]\n"
  "v_mfma_f32_16x16x32_bf16 v[40:43], v[72:75], a[76:79], v[40:43]\n"
  "v_mfma_f32_16x16x32_bf16 v[44:47], v[76:79], a[76:79], v[44:47]\n"
  "v_mfma_f32_16x16x32_bf16 v[48:51], v[72:75], a[84:87], v[48:51]\n"
  "v_mfma_f32_16x16x32_bf16 v[52:55], v[76:79], a[84:87], v[52:55]\n"
  "v_mfma_f32_16x16x32_bf16 v[56:59], v[72:75], a[92:95], v[56:59]\n"
  "v_mfma_f32_16x16x32_bf16 v[60:63], v[76:79], a[92:95], v[60:63]\n"
  "s_waitcnt lgkmcnt(3)\n"
  "v_mfma_f32_16x16x32_bf16 v[32:35], v[80:83], a[96:99], v[32:35]\n"
  "s_waitcnt lgkmcnt(2)\n"
  "v_mfma_f32_16x16x32_bf16 v[36:39], v[84:87], a[96:99], v[36:39]\n"
  "ds_read_b128 v[64:67], v17 offset:4096\n"
  "v_mfma_f32_16x16x32_bf16 v[40:43], v[80:83], a[104:107], v[40:43]\n"
  "v_mfma_f32_16x16x32_bf16 v[44:47], v[84:87], a[104:107], v[44:47]\n"
  "ds_read_b128 v[68:71], v17 offset:14336\n"
  "v_mfma_f32_16x16x32_bf16 v[48:51], v[80:83], a[112:115], v[48:51]\n"
  "v_mfma_f32_16x16x32_bf16 v[52:55], v[84:87], a[112:115], v[52:55]\n"
  "ds_read_b128 v[72:75], v17 offset:24576\n"
  "v_mfma_f32_16x16x32_bf16 v[56:59], v[80:83], a[120:123], v[56:59]\n"
  "v_mfma_f32_16x16x32_bf16 v[60:63], v[84:87], a[120:123], v[60:63]\n"
  "ds_read_b128 v[76:79], v17 offset:34816\n"
  "s_waitcnt lgkmcnt(5)\n"
  "v_mfma_f32_16x16x32_bf16 v[32:35], v[88:91], a[100:103], v[32:35]\n"
  "s_waitcnt lgkmcnt(4)\n"
  "v_mfma_f32_16x16x32_bf16 v[36:39], v[92:95], a[100:103], v[36:39]\n"
  "v_mfma_f32_16x16x32_bf16 v[40:43], v[88:91], a[108:111], v[40:43]\n"
  "v_mfma_f32_16x16x32_bf16 v[44:47], v[92:95], a[108:111], v[44:47]\n"
  "v_mfma_f32_16x16x32_bf16 v[48:51], v[88:91], a[116:119], v[48:51]\n"
  "v_mfma_f32_16x16x32_bf16 v[52:55], v[92:95], a[116:119], v[52:55]\n"
  "v_mfma_f32_16x16x32_bf16 v[56:59], v[88:91], a[124:127], v[56:59]\n"
  "v_mfma_f32_16x16x32_bf16 v[60:63], v[92:95], a[124:127], v[60:63]\n"
  "s_waitcnt lgkmcnt(3)\n"
  "v_mfma_f32_16x16x32_bf16 v[32:35], v[64:67], a[128:131], v[32:35]\n"
  "s_waitcnt lgkmcnt(2)\n"
  "v_mfma_f32_16x16x32_bf16 v[36:39], v[68:71], a[128:131], v[36:39]\n"
  "ds_read_b128 v[80:83], v17 offset:5120\n"
  "v_mfma_f32_16x16x32_bf16 v[40:43], v[64:67], a[136:139], v[40:43]\n"
  "v_mfma_f32_16x16x32_bf16 v[44:47], v[68:71], a[136:139], v[44:47]\n"
  "ds_read_b128 v[84:87], v17 offset:15360\n"
  "v_mfma_f32_16x16x32_bf16 v[48:51], v[64:67], a[144:147], v[48:51]\n"
  "v_mfma_f32_16x16x32_bf16 v[52:55], v[68:71], a[144:147], v[52:55]\n"
  "ds_read_b128 v[88:91], v17 offset:25600\n"
  "v_mfma_f32_16x16x32_bf16 v[56:59], v[64:67], a[152:155], v[56:59]\n"
  "v_mfma_f32_16x16x32_bf16 v[60:63], v[68:71], a[152:155], v[60:63]\n"
  "ds_read_b128 v[92:95], v17 offset:35840\n"
  "s_waitcnt lgkmcnt(5)\n"
  "v_mfma_f32_16x16x32_bf16 v[32:35], v[72:75], a[132:135], v[32:35]\n"
  "s_waitcnt lgkmcnt(4)\n"
  "v_mfma_f32_16x16x32_bf16 v[36:39], v[76:79], a[132:135], v[36:39]\n"
  "v_mfma_f32_16x16x32_bf16 v[40:43], v[72:75], a[140:143], v[40:43]\n"
  "v_mfma_f32_16x16x32_bf16 v[44:47], v[76:79], a[140:143], v[44:47]\n"
  "v_mfma_f32_16x16x32_bf16 v[48:51], v[72:75], a[148:151], v[48:51]\n"
  "v_mfma_f32_16x16x32_bf16 v[52:55], v[76:79], a[148:151], v[52:55]\n"
  "v_mfma_f32_16x16x32_bf16 v[56:59], v[72:75], a[156:159], v[56:59]\n"
  "v_mfma_f32_16x16x32_bf16 v[60:63], v[76:79], a[156:159], v[60:63]\n"
  "s_waitcnt lgkmcnt(3)\n"
  "v_mfma_f32_16x16x32_bf16 v[32:35], v[80:83], a[160:163], v[32:35]\n"
  "s_waitcnt lgkmcnt(2)\n"
  "v_mfma_f32_16x16x32_bf16 v[36:39], v[84:87], a[160:163], v[36:39]\n"
  "ds_read_b128 v[64:67], v17 offset:6144\n"
  "v_mfma_f32_16x16x32_bf16 v[40:43], v[80:83], a[168:171], v[40:43]\n"
  "v_mfma_f32_16x16x32_bf16 v[44:47], v[84:87], a[168:171], v[44:47]\n"
  "ds_read_b128 v[68:71], v17 offset:16384\n"
  "v_mfma_f32_16x16x32_bf16 v[48:51], v[80:83], a[176:179], v[48:51]\n"
  "v_mfma_f32_16x16x32_bf16 v[52:55], v[84:87], a[176:179], v[52:55]\n"
  "ds_read_b128 v[72:75], v17 offset:26624\n"
  "v_mfma_f32_16x16x32_bf16 v[56:59], v[80:83], a[184:187], v[56:59]\n"
  "v_mfma_f32_16x16x32_bf16 v[60:63], v[84:87], a[184:187], v[60:63]\n"
  "ds_read_b128 v[76:79], v17 offset:36864\n"
  "s_waitcnt lgkmcnt(5)\n"
  "v_mfma_f32_16x16x32_bf16 v[32:35], v[88:91], a[164:167], v[32:35]\n"
  "s_waitcnt lgkmcnt(4)\n"
  "v_mfma_f32_16x16x32_bf16 v[36:39], v[92:95], a[164:167], v[36:39]\n"
  "v_mfma_f32_16x16x32_bf16 v[40:43], v[88:91], a[172:175], v[40:43]\n"
  "v_mfma_f32_16x16x32_bf16 v[44:47], v[92:95], a[172:175], v[44:47]\n"
  "v_mfma_f32_16x16x32_bf16 v[48:51], v[88:91], a[180:183], v[48:51]\n"
  "v_mfma_f32_16x16x32_bf16 v[52:55], v[92:95], a[180:183], v[52:55]\n"
  "v_mfma_f32_16x16x32_bf16 v[56:59], v[88:91], a[188:191], v[56:59]\n"
  "v_mfma_f32_16x16x32_bf16 v[60:63], v[92:95], a[188:191], v[60:63]\n"
  "s_waitcnt lgkmcnt(3)\n"
  "v_mfma_f32_16x16x32_bf16 v[32:35], v[64:67], a[192:195], v[32:35]\n"
  "s_waitcnt lgkmcnt(2)\n"
  "v_mfma_f32_16x16x32_bf16 v[36:39], v[68:71], a[192:195], v[36:39]\n"
  "ds_read_b128 v[80:83], v17 offset:7168\n"
  "v_mfma_f32_16x16x32_bf16 v[40:43], v[64:67], a[200:203], v[40:43]\n"
  "v_mfma_f32_16x16x32_bf16 v[44:47], v[68:71], a[200:203], v[44:47]\n"
  "ds_read_b128 v[84:87], v17 offset:17408\n"
  "v_mfma_f32_16x16x32_bf16 v[48:51], v[64:67], a[208:211], v[48:51]\n"
  "v_mfma_f32_16x16x32_bf16 v[52:55], v[68:71], a[208:211], v[52:55]\n"
  "ds_read_b128 v[88:91], v17 offset:27648\n"
  "v_mfma_f32_16x16x32_bf16 v[56:59], v[64:67], a[216:219], v[56:59]\n"
  "v_mfma_f32_16x16x32_bf16 v[60:63], v[68:71], a[216:219], v[60:63]\n"
  "ds_read_b128 v[92:95], v17 offset:37888\n"
  "s_waitcnt lgkmcnt(5)\n"
  "v_mfma_f32_16x16x32_bf16 v[32:35], v[72:75], a[196:199], v[32:35]\n"
  "s_waitcnt lgkmcnt(4)\n"
  "v_mfma_f32_16x16x32_bf16 v[36:39], v[76:79], a[196:199], v[36:39]\n"
  "v_mfma_f32_16x16x32_bf16 v[40:43], v[72:75], a[204:207], v[40:43]\n"
  "v_mfma_f32_16x16x32_bf16 v[44:47], v[76:79], a[204:207], v[44:47]\n"
  "v_mfma_f32_16x16x32_bf16 v[48:51], v[72:75], a[212:215], v[48:51]\n"
  "v_mfma_f32_16x16x32_bf16 v[52:55], v[76:79], a[212:215], v[52:55]\n"
  "v_mfma_f32_16x16x32_bf16 v[56:59], v[72:75], a[220:223], v[56:59]\n"
  "v_mfma_f32_16x16x32_bf16 v[60:63], v[76:79], a[220:223], v[60:63]\n"
  "s_waitcnt lgkmcnt(3)\n"
  "v_mfma_f32_16x16x32_bf16 v[32:35], v[80:83], a[224:227], v[32:35]\n"
  "s_waitcnt lgkmcnt(2)\n"
  "v_mfma_f32_16x16x32_bf16 v[36:39], v[84:87], a[224:227], v[36:39]\n"
  "ds_read_b128 v[64:67], v17 offset:8192\n"
  "v_mfma_f32_16x16x32_bf16 v[40:43], v[80:83], a[232:235], v[40:43]\n"
  "v_mfma_f32_16x16x32_bf16 v[44:47], v[84:87], a[232:235], v[44:47]\n"
  "ds_read_b128 v[68:71], v17 offset:18432\n"
  "v_mfma_f32_16x16x32_bf16 v[48:51], v[80:83], a[240:243], v[48:51]\n"
  "v_mfma_f32_16x16x32_bf16 v[52:55], v[84:87], a[240:243], v[52:55]\n"
  "ds_read_b128 v[72:75], v17 offset:28672\n"
  "v_mfma_f32_16x16x32_bf16 v[56:59], v[80:83], a[248:251], v[56:59]\n"
  "v_mfma_f32_16x16x32_bf16 v[60:63], v[84:87], a[248:251], v[60:63]\n"
  "ds_read_b128 v[76:79], v17 offset:38912\n"
  "s_waitcnt lgkmcnt(5)\n"
  "v_mfma_f32_16x16x32_bf16 v[32:35], v[88:91], a[228:231], v[32:35]\n"
  "s_waitcnt lgkmcnt(4)\n"
  "v_mfma_f32_16x16x32_bf16 v[36:39], v[92:95], a[228:231], v[36:39]\n"
  "v_mfma_f32_16x16x32_bf16 v[40:43], v[88:91], a[236:239], v[40:43]\n"
  "v_mfma_f32_16x16x32_bf16 v[44:47], v[92:95], a[236:239], v[44:47]\n"
  "v_mfma_f32_16x16x32_bf16 v[48:51], v[88:91], a[244:247], v[48:51]\n"
  "v_mfma_f32_16x16x32_bf16 v[52:55], v[92:95], a[244:247], v[52:55]\n"
  "v_mfma_f32_16x16x32_bf16 v[56:59], v[88:91], a[252:255], v[56:59]\n"
  "v_mfma_f32_16x16x32_bf16 v[60:63], v[92:95], a[252:255], v[60:63]\n"
  "s_waitcnt lgkmcnt(3)\n"
  "v_mfma_f32_16x16x32_bf16 v[32:35], v[64:67], v[192:195], v[32:35]\n"
  "s_waitcnt lgkmcnt(2)\n"
  "v_mfma_f32_16x16x32_bf16 v[36:39], v[68:71], v[192:195], v[36:39]\n"
  "ds_read_b128 v[80:83], v17 offset:9216\n"
  "v_mfma_f32_16x16x32_bf16 v[40:43], v[64:67], v[200:203], v[40:43]\n"
  "v_mfma_f32_16x16x32_bf16 v[44:47], v[68:71], v[200:203], v[44:47]\n"
  "ds_read_b128 v[84:87], v17 offset:19456\n"
  "v_mfma_f32_16x16x32_bf16 v[48:51], v[64:67], v[208:211], v[48:51]\n"
  "v_mfma_f32_16x16x32_bf16 v[52:55], v[68:71], v[208:211], v[52:55]\n"
  "ds_read_b128 v[88:91], v17 offset:29696\n"
  "v_mfma_f32_16x16x32_bf16 v[56:59], v[64:67], v[216:219], v[56:59]\n"
  "v_mfma_f32_16x16x32_bf16 v[60:63], v[68:71], v[216:219], v[60:63]\n"
  "ds_read_b128 v[92:95], v17 offset:39936\n"
  "s_waitcnt lgkmcnt(5)\n"
  "v_mfma_f32_16x16x32_bf16 v[32:35], v[72:75], v[196:199], v[32:35]\n"
  "s_waitcnt lgkmcnt(4)\n"
  "v_mfma_f32_16x16x32_bf16 v[36:39], v[76:79], v[196:199], v[36:39]\n"
  "v_mfma_f32_16x16x32_bf16 v[40:43], v[72:75], v[204:207], v[40:43]\n"
  "v_mfma_f32_16x16x32_bf16 v[44:47], v[76:79], v[204:207], v[44:47]\n"
  "v_mfma_f32_16x16x32_bf16 v[48:51], v[72:75], v[212:215], v[48:51]\n"
  "v_mfma_f32_16x16x32_bf16 v[52:55], v[76:79], v[212:215], v[52:55]\n"
  "v_mfma_f32_16x16x32_bf16 v[56:59], v[72:75], v[220:223], v[56:59]\n"
  "v_mfma_f32_16x16x32_bf16 v[60:63], v[76:79], v[220:223], v[60:63]\n"
  "s_waitcnt lgkmcnt(3)\n"
  "v_mfma_f32_16x16x32_bf16 v[32:35], v[80:83], v[224:227], v[32:35]\n"
  "s_waitcnt lgkmcnt(2)\n"
  "v_mfma_f32_16x16x32_bf16 v[36:39], v[84:87], v[224:227], v[36:39]\n"
  "v_mfma_f32_16x16x32_bf16 v[40:43], v[80:83], v[232:235], v[40:43]\n"
  "v_mfma_f32_16x16x32_bf16 v[44:47], v[84:87], v[232:235], v[44:47]\n"
  "v_mfma_f32_16x16x32_bf16 v[48:51], v[80:83], v[240:243], v[48:51]\n"
  "v_mfma_f32_16x16x32_bf16 v[52:55], v[84:87], v[240:243], v[52:55]\n"
  "v_mfma_f32_16x16x32_bf16 v[56:59], v[80:83], v[248:251], v[56:59]\n"
  "v_mfma_f32_16x16x32_bf16 v[60:63], v[84:87], v[248:251], v[60:63]\n"
  "s_waitcnt lgkmcnt(1)\n"
  "v_mfma_f32_16x16x32_bf16 v[32:35], v[88:91], v[228:231], v[32:35]\n"
  "s_waitcnt lgkmcnt(0)\n"
  "v_mfma_f32_16x16x32_bf16 v[36:39], v[92:95], v[228:231], v[36:39]\n"
  "v_mfma_f32_16x16x32_bf16 v[40:43], v[88:91], v[236:239], v[40:43]\n"
  "v_mfma_f32_16x16x32_bf16 v[44:47], v[92:95], v[236:239], v[44:47]\n"
  "v_mfma_f32_16x16x32_bf16 v[48:51], v[88:91], v[244:247], v[48:51]\n"
  "v_mfma_f32_16x16x32_bf16 v[52:55], v[92:95], v[244:247], v[52:55]\n"
  "v_mfma_f32_16x16x32_bf16 v[56:59], v[88:91], v[252:255], v[56:59]\n"
  "v_mfma_f32_16x16x32_bf16 v[60:63], v[92:95], v[252:255], v[60:63]\n"
  "v_cvt_pk_bf16_f32 v32, v32, v33\n"
  "v_cvt_pk_bf16_f32 v33, v34, v35\n"
  "v_cvt_pk_bf16_f32 v34, v36, v37\n"
  "v_cvt_pk_bf16_f32 v35, v38, v39\n"
  "ds_write_b128 v19, v[32:35] offset:0\n"
  "v_cvt_pk_bf16_f32 v40, v40, v41\n"
  "v_cvt_pk_bf16_f32 v41, v42, v43\n"
  "v_cvt_pk_bf16_f32 v42, v44, v45\n"
  "v_cvt_pk_bf16_f32 v43, v46, v47\n"
  "ds_write_b128 v19, v[40:43] offset:1024\n"
  "v_cvt_pk_bf16_f32 v48, v48, v49\n"
  "v_cvt_pk_bf16_f32 v49, v50, v51\n"
  "v_cvt_pk_bf16_f32 v50, v52, v53\n"
  "v_cvt_pk_bf16_f32 v51, v54, v55\n"
  "ds_write_b128 v19, v[48:51] offset:2048\n"
  "v_cvt_pk_bf16_f32 v56, v56, v57\n"
  "v_cvt_pk_bf16_f32 v57, v58, v59\n"
  "v_cvt_pk_bf16_f32 v58, v60, v61\n"
  "v_cvt_pk_bf16_f32 v59, v62, v63\n"
  "ds_write_b128 v19, v[56:59] offset:3072\n"
  "s_add_u32 s74, s99, 2\n"
  "s_lshl_b32 s74, s74, 7\n"
  "s_add_u32 s74, s74, 1835008\n"
  "s_mov_b32 s62, 0\n"
  "s_cmp_ge_u32 s56, 62\n"
  "s_cbranch_scc1 C_F2K_%=\n"
  "s_waitcnt vmcnt(0) lgkmcnt(0)\n"
  "C_F2P_%=:\n"
  "buffer_load_dword v15, off, s[16:19], s74 sc1\n"
  "s_waitcnt vmcnt(0)\n"
  "v_readfirstlane_b32 s60, v15\n"
  "s_cmp_lg_u32 s60, -1\n"
  "s_cbranch_scc1 C_F2K_%=\n"
  "s_sleep 4\n"
  "s_add_u32 s62, s62, 1\n"
  "s_cmp_gt_u32 s62, 300000\n"
  "s_cbranch_scc1 C_FAIL_%=\n"
  "s_and_b32 s60, s62, 255\n"
  "s_cmp_lg_u32 s60, 0\n"
  "s_cbranch_scc1 C_F2P_%=\n"
  "v_mov_b32 v15, 0\n"
  "buffer_load_dword v15, v15, s[40:43], 0 offen sc1\n"
  "s_waitcnt vmcnt(0)\n"
  "v_readfirstlane_b32 s60, v15\n"
  "s_cmp_lg_u32 s60, 0\n"
  "s_cbranch_scc1 C_EXIT_%=\n"
  "s_branch C_F2P_%=\n"
  "C_F2K_%=:\n"
  "s_mov_b32 s75, 40960\n"
  "s_add_u32 s60, s99, 2\n"
  "s_mul_i32 s72, s60, 40960\n"
  "s_mul_i32 s73, s44, 10240\n"
  "s_add_u32 s72, s72, s73\n"
  "s_add_u32 s73, s73, s75\n"
  "s_mov_b32 m0, s73\n"
  "s_nop 0\n"
  "buffer_load_dwordx4 v0, s[24:27], s72 offen offset:0 sc1 lds\n"
  "buffer_load_dwordx4 v0, s[24:27], s72 offen offset:1024 sc1 lds\n"
  "buffer_load_dwordx4 v0, s[24:27], s72 offen offset:2048 sc1 lds\n"
  "buffer_load_dwordx4 v0, s[24:27], s72 offen offset:3072 sc1 lds\n"
  "s_add_u32 s72, s72, 4096\n"
  "s_add_u32 s73, s73, 4096\n"
  "s_mov_b32 m0, s73\n"
  "s_nop 0\n"
  "buffer_load_dwordx4 v0, s[24:27], s72 offen offset:0 sc1 lds\n"
  "buffer_load_dwordx4 v0, s[24:27], s72 offen offset:1024 sc1 lds\n"
  "buffer_load_dwordx4 v0, s[24:27], s72 offen offset:2048 sc1 lds\n"
  "buffer_load_dwordx4 v0, s[24:27], s72 offen offset:3072 sc1 lds\n"
  "s_add_u32 s72, s72, 4096\n"
  "s_add_u32 s73, s73, 4096\n"
  "s_mov_b32 m0, s73\n"
  "s_nop 0\n"
  "buffer_load_dwordx4 v0, s[24:27], s72 offen offset:0 sc1 lds\n"
  "buffer_load_dwordx4 v0, s[24:27], s72 offen offset:1024 sc1 lds\n"
  "s_add_u32 s60, s56, 0\n"
  "s_bfe_u32 s57, s60, 131073\n"
  "s_lshl_b32 s57, s57, 18\n"
  "s_xor_b32 s60, s60, s47\n"
  "s_and_b32 s60, s60, 1\n"
  "s_lshl_b32 s60, s60, 13\n"
  "s_add_u32 s57, s57, s60\n"
  "v_mov_b32 v30, -1\n"
  "v_mov_b32 v31, -1\n"
  "C_LOOP_%=:\n"
  "s_nop 7\n"
  "s_add_u32 s60, s56, 1\n"
  "s_bfe_u32 s58, s60, 131073\n"
  "s_lshl_b32 s58, s58, 18\n"
  "s_xor_b32 s60, s60, s47\n"
  "s_and_b32 s60, s60, 1\n"
  "s_lshl_b32 s60, s60, 13\n"
  "s_add_u32 s58, s58, s60\n"
  "v_readfirstlane_b32 s61, v31\n"
  "s_cmp_lg_u32 s61, -1\n"
  "s_cselect_b32 s48, s61, s48\n"
  "s_mov_b32 s62, 0\n"
  "v_readfirstlane_b32 s59, v30\n"
  "s_cmp_lg_u32 s59, -1\n"
  "s_cbranch_scc1 C_SCHOK_%=\n"
  "C_SCHP_%=:\n"
  "s_nop 7\n"
  "buffer_load_dword v30, v10, s[16:19], s57 offen sc1\n"
  "s_waitcnt vmcnt(0)\n"
  "v_readfirstlane_b32 s59, v30\n"
  "s_cmp_lg_u32 s59, -1\n"
  "s_cbranch_scc1 C_SCHOK_%=\n"
  "s_sleep 2\n"
  "s_add_u32 s62, s62, 1\n"
  "s_cmp_gt_u32 s62, 300000\n"
  "s_cbranch_scc1 C_FAIL_%=\n"
  "s_and_b32 s60, s62, 255\n"
  "s_cmp_lg_u32 s60, 0\n"
  "s_cbranch_scc1 C_SCHP_%=\n"
  "v_mov_b32 v30, 0\n"
  "buffer_load_dword v30, v30, s[40:43], 0 offen sc1\n"
  "s_waitcnt vmcnt(0)\n"
  "v_readfirstlane_b32 s60, v30\n"
  "s_cmp_lg_u32 s60, 0\n"
  "s_cbranch_scc1 C_EXIT_%=\n"
  "s_branch C_SCHP_%=\n"
  "C_SCHOK_%=:\n"
  "s_nop 7\n"
  "s_waitcnt lgkmcnt(0)\n"
  "s_memrealtime s[64:65]\n"
  "s_waitcnt lgkmcnt(0)\n"
  "s_sub_i32 s60, s59, s64\n"
  "s_cmp_gt_i32 s60, 2000\n"
  "s_cselect_b32 s59, s64, s59\n"
  "s_cmp_lt_i32 s60, -100000\n"
  "s_cselect_b32 s59, s64, s59\n"
  "s_add_u32 s68, s59, s51\n"
  "s_mov_b32 s69, s68\n"
  "s_sub_i32 s60, s64, s69\n"
  "s_cmp_gt_i32 s60, 0\n"
  "s_cselect_b32 s69, s64, s69\n"
  "s_add_i32 s60, s60, 100\n"
  "s_max_i32 s60, s60, 0\n"
  "s_min_i32 s3, s60, 200\n"
  "s_cmp_lt_u32 s56, 4\n"
  "s_cselect_b32 s3, 0, s3\n"
  "v_mov_b32 v152, 0\n"
  "v_mov_b32 v153, 0\n"
  "v_mov_b32 v154, 0\n"
  "v_mov_b32 v155, 0\n"
  "v_mov_b32 v156, 0\n"
  "v_mov_b32 v157, 0\n"
  "v_mov_b32 v158, 0\n"
  "v_mov_b32 v159, 0\n"
  "v_mov_b32 v160, 0\n"
  "v_mov_b32 v161, 0\n"
  "v_mov_b32 v162, 0\n"
  "v_mov_b32 v163, 0\n"
  "v_mov_b32 v164, 0\n"
  "v_mov_b32 v165, 0\n"
  "v_mov_b32 v166, 0\n"
  "v_mov_b32 v167, 0\n"
  "v_add_u32 v18, s97, v0\n"
  "s_mov_b32 s62, 0\n"
  "C_WT_%=:\n"
  "s_memrealtime s[64:65]\n"
  "s_waitcnt lgkmcnt(0)\n"
  "s_sub_i32 s60, s64, s68\n"
  "s_cmp_lt_i32 s60, 0\n"
  "s_cbranch_scc1 C_WT_%=\n"
  "C_MP_%=:\n"
  "buffer_load_dwordx4 v[168:171], v2, s[16:19], s57 offen sc1\n"
  "buffer_load_dwordx4 v[172:175], v2, s[16:19], s57 offen offset:1024 sc1\n"
  "buffer_load_dwordx2 v[176:177], v3, s[16:19], s57 offen sc1\n"
  "s_waitcnt vmcnt(2)\n"
  "ds_write_b128 v1, v[168:171]\n"
  "v_max3_u32 v15, v168, v169, v170\n"
  "s_waitcnt vmcnt(1)\n"
  "ds_write_b128 v1, v[172:175] offset:4096\n"
  "v_max3_u32 v15, v171, v172, v15\n"
  "v_max3_u32 v15, v173, v174, v15\n"
  "s_waitcnt vmcnt(0)\n"
  "ds_write_b64 v4, v[176:177]\n"
  "v_max3_u32 v15, v175, v176, v15\n"
  "v_max_u32 v15, v177, v15\n"
  "v_cmp_eq_u32 vcc, -1, v15\n"
  "s_nop 1\n"
  "s_cbranch_vccnz C_MBAD_%=\n"
  "s_waitcnt lgkmcnt(0)\n"
  "s_barrier\n"
  "s_memrealtime s[64:65]\n"
  "ds_read_b128 v[112:115], v20 offset:0\n"
  "ds_read_b128 v[116:119], v20 offset:1024\n"
  "ds_read_b128 v[120:123], v20 offset:2048\n"
  "ds_read_b128 v[124:127], v20 offset:3072\n"
  "ds_read_b128 v[128:131], v20 offset:4096\n"
  "ds_read_b128 v[132:135], v20 offset:5120\n"
  "ds_read_b128 v[136:139], v20 offset:6144\n"
  "ds_read_b128 v[140:143], v20 offset:7168\n"
  "ds_read_b128 v[144:147], v20 offset:8192\n"
  "ds_read_b128 v[148:151], v20 offset:9216\n"
  "s_waitcnt lgkmcnt(0)\n"
  "v_smfmac_f32_16x16x64_bf16 v[152:155], v[112:115], a[0:7], v5\n"
  "s_sub_i32 s60, s64, s68\n"
  "s_add_u32 s61, s64, s50\n"
  "s_cmp_gt_i32 s60, s52\n"
  "s_cselect_b32 s59, s61, s59\n"
  "v_smfmac_f32_16x16x64_bf16 v[156:159], v[112:115], a[8:15], v5\n"
  "v_smfmac_f32_16x16x64_bf16 v[160:163], v[112:115], a[16:23], v5\n"
  "s_sub_i32 s60, s63, s59\n"
  "s_max_i32 s60, s60, 0\n"
  "s_cmp_eq_u32 s56, 0\n"
  "s_cselect_b32 s60, s60, 0\n"
  "s_add_u32 s59, s59, s60\n"
  "s_add_u32 s59, s59, s48\n"
  "s_add_u32 s60, s59, s49\n"
  "s_min_u32 s60, s60, s78\n"
  "v_mov_b32 v15, s60\n"
  "v_smfmac_f32_16x16x64_bf16 v[164:167], v[112:115], a[24:31], v5\n"
  "v_smfmac_f32_16x16x64_bf16 v[152:155], v[116:119], a[32:39], v5\n"
  "buffer_store_dword v15, v16, s[16:19], s58 offen sc1\n"
  "s_xor_b32 s73, s58, 524288\n"
  "v_mov_b32 v21, -1\n"
  "v_smfmac_f32_16x16x64_bf16 v[156:159], v[116:119], a[40:47], v5\n"
  "v_smfmac_f32_16x16x64_bf16 v[160:163], v[116:119], a[48:55], v5\n"
  "buffer_store_dword v21, v16, s[16:19], s73 offen sc1\n"
  "v_smfmac_f32_16x16x64_bf16 v[164:167], v[116:119], a[56:63], v5\n"
  "v_smfmac_f32_16x16x64_bf16 v[152:155], v[120:123], a[64:71], v5\n"
  "ds_read_b128 v[32:35], v18 offset:0\n"
  "v_smfmac_f32_16x16x64_bf16 v[156:159], v[120:123], a[72:79], v5\n"
  "v_smfmac_f32_16x16x64_bf16 v[160:163], v[120:123], a[80:87], v5\n"
  "ds_read_b128 v[36:39], v18 offset:1024\n"
  "v_smfmac_f32_16x16x64_bf16 v[164:167], v[120:123], a[88:95], v5\n"
  "v_smfmac_f32_16x16x64_bf16 v[152:155], v[124:127], a[96:103], v5\n"
  "ds_read_b128 v[40:43], v18 offset:2048\n"
  "v_smfmac_f32_16x16x64_bf16 v[156:159], v[124:127], a[104:111], v5\n"
  "v_smfmac_f32_16x16x64_bf16 v[160:163], v[124:127], a[112:119], v5\n"
  "ds_read_b128 v[44:47], v18 offset:3072\n"
  "v_smfmac_f32_16x16x64_bf16 v[164:167], v[124:127], a[120:127], v5\n"
  "v_smfmac_f32_16x16x64_bf16 v[152:155], v[128:131], a[128:135], v5\n"
  "ds_read_b128 v[52:55], v18 offset:5120\n"
  "v_smfmac_f32_16x16x64_bf16 v[156:159], v[128:131], a[136:143], v5\n"
  "v_smfmac_f32_16x16x64_bf16 v[160:163], v[128:131], a[144:151], v5\n"
  "ds_read_b128 v[56:59], v18 offset:6144\n"
  "v_smfmac_f32_16x16x64_bf16 v[164:167], v[128:131], a[152:159], v5\n"
  "v_smfmac_f32_16x16x64_bf16 v[152:155], v[132:135], a[160:167], v5\n"
  "ds_read_b128 v[60:63], v18 offset:7168\n"
  "v_smfmac_f32_16x16x64_bf16 v[156:159], v[132:135], a[168:175], v5\n"
  "v_smfmac_f32_16x16x64_bf16 v[160:163], v[132:135], a[176:183], v5\n"
  "ds_read_b128 v[64:67], v18 offset:8192\n"
  "v_smfmac_f32_16x16x64_bf16 v[164:167], v[132:135], a[184:191], v5\n"
  "v_smfmac_f32_16x16x64_bf16 v[152:155], v[136:139], a[192:199], v5\n"
  "ds_read_b128 v[72:75], v18 offset:10240\n"
  "v_smfmac_f32_16x16x64_bf16 v[156:159], v[136:139], a[200:207], v5\n"
  "v_smfmac_f32_16x16x64_bf16 v[160:163], v[136:139], a[208:215], v5\n"
  "ds_read_b128 v[76:79], v18 offset:11264\n"
  "v_smfmac_f32_16x16x64_bf16 v[164:167], v[136:139], a[216:223], v5\n"
  "v_smfmac_f32_16x16x64_bf16 v[152:155], v[140:143], a[224:231], v5\n"
  "ds_read_b128 v[80:83], v18 offset:12288\n"
  "v_smfmac_f32_16x16x64_bf16 v[156:159], v[140:143], a[232:239], v5\n"
  "v_smfmac_f32_16x16x64_bf16 v[160:163], v[140:143], a[240:247], v5\n"
  "ds_read_b128 v[84:87], v18 offset:13312\n"
  "v_smfmac_f32_16x16x64_bf16 v[164:167], v[140:143], a[248:255], v5\n"
  "v_smfmac_f32_16x16x64_bf16 v[152:155], v[144:147], v[192:199], v5\n"
  "ds_read_b128 v[92:95], v18 offset:15360\n"
  "v_smfmac_f32_16x16x64_bf16 v[156:159], v[144:147], v[200:207], v5\n"
  "v_smfmac_f32_16x16x64_bf16 v[160:163], v[144:147], v[208:215], v5\n"
  "ds_read_b128 v[96:99], v18 offset:16384\n"
  "v_smfmac_f32_16x16x64_bf16 v[164:167], v[144:147], v[216:223], v5\n"
  "v_smfmac_f32_16x16x64_bf16 v[152:155], v[148:151], v[224:231], v5\n"
  "s_waitcnt lgkmcnt(10)\n"
  "ds_read_b128 v[100:103], v18 offset:17408\n"
  "v_smfmac_f32_16x16x64_bf16 v[156:159], v[148:151], v[232:239], v5\n"
  "v_smfmac_f32_16x16x64_bf16 v[160:163], v[148:151], v[240:247], v5\n"
  "ds_read_b128 v[104:107], v18 offset:18432\n"
  "v_smfmac_f32_16x16x64_bf16 v[164:167], v[148:151], v[248:255], v5\n"
  "v_mov_b32 v0, v0\n"
  "v_mov_b32 v0, v0\n"
  "v_mov_b32 v0, v0\n"
  "v_mov_b32 v0, v0\n"
  "v_mov_b32 v0, v0\n"
  "v_mov_b32 v0, v0\n"
  "v_permlane32_swap_b32 v152, v160\n"
  "v_permlane32_swap_b32 v153, v161\n"
  "v_permlane32_swap_b32 v154, v162\n"
  "v_permlane32_swap_b32 v155, v163\n"
  "v_permlane32_swap_b32 v156, v164\n"
  "v_permlane32_swap_b32 v157, v165\n"
  "v_permlane32_swap_b32 v158, v166\n"
  "v_permlane32_swap_b32 v159, v167\n"
  "v_pk_add_f32 v[152:153], v[152:153], v[160:161]\n"
  "v_pk_add_f32 v[154:155], v[154:155], v[162:163]\n"
  "v_pk_add_f32 v[156:157], v[156:157], v[164:165]\n"
  "v_pk_add_f32 v[158:159], v[158:159], v[166:167]\n"
  "v_cvt_pk_bf16_f32 v152, v152, v153\n"
  "v_cvt_pk_bf16_f32 v153, v154, v155\n"
  "v_cvt_pk_bf16_f32 v154, v156, v157\n"
  "v_cvt_pk_bf16_f32 v155, v158, v159\n"
  "s_nop 0\n"
  "v_permlane16_swap_b32 v152, v154\n"
  "v_permlane16_swap_b32 v153, v155\n"
  "ds_write_b128 v19, v[152:155] offset:4096\n"
  "s_waitcnt lgkmcnt(0)\n"
  "s_barrier\n"
  "s_cmp_lg_u32 s44, 0\n"
  "s_cbranch_scc1 C_OTHER_%=\n"
  "s_cmp_eq_u32 s56, 63\n"
  "s_cbranch_scc1 C_LAST_%=\n"
  "ds_read_b128 v[48:51], v18 offset:4096\n"
  "ds_read_b128 v[68:71], v18 offset:9216\n"
  "ds_read_b128 v[88:91], v18 offset:14336\n"
  "ds_read_b128 v[108:111], v18 offset:19456\n"
  "s_add_u32 s68, s57, 0\n"
  "s_add_u32 s69, s57, 16384\n"
  "s_add_u32 s70, s57, 32768\n"
  "s_add_u32 s71, s57, 49152\n"
  "s_add_u32 s72, s58, 131072\n"
  "s_mov_b64 s[66:67], 0\n"
  "s_sub_u32 s11, s59, 19\n"
  "s_waitcnt lgkmcnt(0)\n"
  "C_WT2_%=:\n"
  "s_memrealtime s[64:65]\n"
  "s_waitcnt lgkmcnt(0)\n"
  "s_sub_i32 s60, s64, s11\n"
  "s_cmp_lt_i32 s60, 0\n"
  "s_cbranch_scc1 C_WT2_%=\n"
  "s_cmp_gt_i32 s60, 10\n"
  "s_cbranch_scc1 C_WT2C_%=\n"
  "s_memtime vcc\n"
  "s_memrealtime s[28:29]\n"
  "s_nop 2\n"
  "s_memrealtime s[30:31]\n"
  "s_nop 2\n"
  "s_memrealtime s[12:13]\n"
  "s_nop 2\n"
  "s_memrealtime s[14:15]\n"
  "s_nop 2\n"
  "s_memrealtime s[0:1]\n"
  "s_nop 2\n"
  "s_memrealtime s[64:65]\n"
  "s_nop 2\n"
  "s_memrealtime s[74:75]\n"
  "s_mov_b32 s61, 0\n"
  "s_waitcnt lgkmcnt(0)\n"
  "s_cmp_eq_u32 s30, s28\n"
  "s_addc_u32 s61, s61, 0\n"
  "s_cmp_eq_u32 s12, s28\n"
  "s_addc_u32 s61, s61, 0\n"
  "s_cmp_eq_u32 s14, s28\n"
  "s_addc_u32 s61, s61, 0\n"
  "s_cmp_eq_u32 s0, s28\n"
  "s_addc_u32 s61, s61, 0\n"
  "s_cmp_eq_u32 s64, s28\n"
  "s_addc_u32 s61, s61, 0\n"
  "s_cmp_eq_u32 s74, s28\n"
  "s_addc_u32 s61, s61, 0\n"
  "s_sub_i32 s60, s59, s28\n"
  "s_sub_i32 s60, s60, 4\n"
  "s_mul_i32 s60, s60, s6\n"
  "s_ashr_i32 s60, s60, 4\n"
  "s_lshl4_add_u32 s60, s61, s60\n"
  "s_max_i32 s60, s60, 256\n"
  "s_min_i32 s60, s60, 508\n"
  "s_add_u32 s11, vcc_lo, s60\n"
  "s_and_b32 s60, s60, -4\n"
  "s_getpc_b64 s[64:65]\n"
  "s_sub_u32 s60, 532, s60\n"
  "s_add_u32 s64, s64, s60\n"
  "s_addc_u32 s65, s65, 0\n"
  "s_setpc_b64 s[64:65]\n"
  "s_nop 0\n"
  "s_nop 0\n"
  "s_nop 0\n"
  "s_nop 0\n"
  "s_nop 0\n"
  "s_nop 0\n"
  "s_nop 0\n"
  "s_nop 0\n"
  "s_nop 0\n"
  "s_nop 0\n"
  "s_nop 0\n"
  "s_nop 0\n"
  "s_nop 0\n"
  "s_nop 0\n"
  "s_nop 0\n"
  "s_nop 0\n"
  "s_nop 0\n"
  "s_nop 0\n"
  "s_nop 0\n"
  "s_nop 0\n"
  "s_nop 0\n"
  "s_nop 0\n"
  "s_nop 0\n"
  "s_nop 0\n"
  "s_nop 0\n"
  "s_nop 0\n"
  "s_nop 0\n"
  "s_nop 0\n"
  "s_nop 0\n"
  "s_nop 0\n"
  "s_nop 0\n"
  "s_nop 0\n"
  "s_nop 0\n"
  "s_nop 0\n"
  "s_nop 0\n"
  "s_nop 0\n"
  "s_nop 0\n"
  "s_nop 0\n"
  "s_nop 0\n"
  "s_nop 0\n"
  "s_nop 0\n"
  "s_nop 0\n"
  "s_nop 0\n"
  "s_nop 0\n"
  "s_nop 0\n"
  "s_nop 0\n"
  "s_nop 0\n"
  "s_nop 0\n"
  "s_nop 0\n"
  "s_nop 0\n"
  "s_nop 0\n"
  "s_nop 0\n"
  "s_nop 0\n"
  "s_nop 0\n"
  "s_nop 0\n"
  "s_nop 0\n"
  "s_nop 0\n"
  "s_nop 0\n"
  "s_nop 0\n"
  "s_nop 0\n"
  "s_nop 0\n"
  "s_nop 0\n"
  "s_nop 0\n"
  "s_nop 0\n"
  "C_WT2K_%=:\n"
  "buffer_load_dwordx4 v[112:115], v6, s[16:19], s68 offen offset:0 sc1\n"
  "buffer_load_dwordx4 v[116:119], v6, s[16:19], s68 offen offset:1280 sc1\n"
  "buffer_load_dwordx4 v[120:123], v6, s[16:19], s68 offen offset:2560 sc1\n"
  "buffer_load_dwordx4 v[124:127], v6, s[16:19], s68 offen offset:3840 sc1\n"
  "buffer_load_dwordx4 v[128:131], v7, s[16:19], s68 offen sc1\n"
  "buffer_load_dwordx4 v[132:135], v6, s[16:19], s69 offen offset:0 sc1\n"
  "buffer_load_dwordx4 v[136:139], v6, s[16:19], s69 offen offset:1280 sc1\n"
  "buffer_load_dwordx4 v[140:143], v6, s[16:19], s69 offen offset:2560 sc1\n"
  "buffer_load_dwordx4 v[144:147], v6, s[16:19], s69 offen offset:3840 sc1\n"
  "buffer_load_dwordx4 v[148:151], v7, s[16:19], s69 offen sc1\n"
  "buffer_load_dwordx4 v[152:155], v6, s[16:19], s70 offen offset:0 sc1\n"
  "buffer_load_dwordx4 v[156:159], v6, s[16:19], s70 offen offset:1280 sc1\n"
  "buffer_load_dwordx4 v[160:163], v6, s[16:19], s70 offen offset:2560 sc1\n"
  "buffer_load_dwordx4 v[164:167], v6, s[16:19], s70 offen offset:3840 sc1\n"
  "buffer_load_dwordx4 v[168:171], v7, s[16:19], s70 offen sc1\n"
  "buffer_load_dwordx4 v[172:175], v6, s[16:19], s71 offen offset:0 sc1\n"
  "buffer_load_dwordx4 v[176:179], v6, s[16:19], s71 offen offset:1280 sc1\n"
  "buffer_load_dwordx4 v[180:183], v6, s[16:19], s71 offen offset:2560 sc1\n"
  "buffer_load_dwordx4 v[184:187], v6, s[16:19], s71 offen offset:3840 sc1\n"
  "buffer_load_dwordx4 v[188:191], v7, s[16:19], s71 offen sc1\n"
  "C_P2_%=:\n"
  "s_waitcnt vmcnt(19)\n"
  "v_mfma_f32_16x16x32_bf16 v[24:27], v[32:35], v[112:115], 0\n"
  "s_waitcnt vmcnt(18)\n"
  "v_mfma_f32_16x16x32_bf16 v[24:27], v[36:39], v[116:119], v[24:27]\n"
  "s_waitcnt vmcnt(17)\n"
  "v_mfma_f32_16x16x32_bf16 v[24:27], v[40:43], v[120:123], v[24:27]\n"
  "s_waitcnt vmcnt(16)\n"
  "v_mfma_f32_16x16x32_bf16 v[24:27], v[44:47], v[124:127], v[24:27]\n"
  "s_waitcnt vmcnt(15)\n"
  "v_mfma_f32_16x16x32_bf16 v[24:27], v[48:51], v[128:131], v[24:27]\n"
  "s_waitcnt vmcnt(14)\n"
  "v_mfma_f32_16x16x32_bf16 v[24:27], v[52:55], v[132:135], v[24:27]\n"
  "s_waitcnt vmcnt(13)\n"
  "v_mfma_f32_16x16x32_bf16 v[24:27], v[56:59], v[136:139], v[24:27]\n"
  "s_waitcnt vmcnt(12)\n"
  "v_mfma_f32_16x16x32_bf16 v[24:27], v[60:63], v[140:143], v[24:27]\n"
  "s_waitcnt vmcnt(11)\n"
  "v_mfma_f32_16x16x32_bf16 v[24:27], v[64:67], v[144:147], v[24:27]\n"
  "v_mov_b32 v0, v0\n"
  "s_waitcnt vmcnt(10)\n"
  "v_mfma_f32_16x16x32_bf16 v[24:27], v[68:71], v[148:151], v[24:27]\n"
  "v_mov_b32 v0, v0\n"
  "s_waitcnt vmcnt(9)\n"
  "v_mfma_f32_16x16x32_bf16 v[24:27], v[72:75], v[152:155], v[24:27]\n"
  "v_mov_b32 v0, v0\n"
  "s_waitcnt vmcnt(8)\n"
  "v_mfma_f32_16x16x32_bf16 v[24:27], v[76:79], v[156:159], v[24:27]\n"
  "v_mov_b32 v0, v0\n"
  "s_waitcnt vmcnt(7)\n"
  "v_mfma_f32_16x16x32_bf16 v[24:27], v[80:83], v[160:163], v[24:27]\n"
  "v_mov_b32 v0, v0\n"
  "s_waitcnt vmcnt(6)\n"
  "v_mfma_f32_16x16x32_bf16 v[24:27], v[84:87], v[164:167], v[24:27]\n"
  "v_mov_b32 v0, v0\n"
  "s_waitcnt vmcnt(5)\n"
  "v_mfma_f32_16x16x32_bf16 v[24:27], v[88:91], v[168:171], v[24:27]\n"
  "v_mov_b32 v0, v0\n"
  "s_waitcnt vmcnt(4)\n"
  "v_mfma_f32_16x16x32_bf16 v[24:27], v[92:95], v[172:175], v[24:27]\n"
  "v_mov_b32 v0, v0\n"
  "s_waitcnt vmcnt(3)\n"
  "v_mfma_f32_16x16x32_bf16 v[24:27], v[96:99], v[176:179], v[24:27]\n"
  "v_mov_b32 v0, v0\n"
  "s_waitcnt vmcnt(2)\n"
  "v_mfma_f32_16x16x32_bf16 v[24:27], v[100:103], v[180:183], v[24:27]\n"
  "v_mov_b32 v0, v0\n"
  "s_waitcnt vmcnt(1)\n"
  "v_mfma_f32_16x16x32_bf16 v[24:27], v[104:107], v[184:187], v[24:27]\n"
  "v_mov_b32 v0, v0\n"
  "s_waitcnt vmcnt(0)\n"
  "v_mfma_f32_16x16x32_bf16 v[24:27], v[108:111], v[188:191], v[24:27]\n"
  "v_mov_b32 v0, v0\n"
  "v_mov_b32 v0, v0\n"
  "v_mov_b32 v0, v0\n"
  "v_mov_b32 v0, v0\n"
  "v_mov_b32 v0, v0\n"
  "v_mov_b32 v0, v0\n"
  "v_mov_b32 v0, v0\n"
  "v_mov_b32 v0, v0\n"
  "v_add_f32_dpp v24, v26, v24 row_ror:8 row_mask:0xf bank_mask:0xf\n"
  "v_add_f32_dpp v25, v27, v25 row_ror:8 row_mask:0xf bank_mask:0xf\n"
  "v_cmp_u_f32 vcc, v24, v24\n"
  "v_cvt_pk_bf16_f32 v24, v24, v25\n"
  "s_andn2_b64 vcc, vcc, s[66:67]\n"
  "v_min_u32 v24, s78, v24\n"
  "s_cbranch_vccnz C_REDO_%=\n"
  "s_nop 0\n"
  "v_mov_b32_dpp v24, v24 row_ror:8 row_mask:0xf bank_mask:0xc\n"
  "buffer_store_dword v24, v11, s[16:19], s58 offen sc1\n"
  "buffer_store_dword v24, v11, s[16:19], s72 offen sc1\n"
  "s_memtime s[64:65]\n"
  "s_memrealtime s[28:29]\n"
  "s_waitcnt lgkmcnt(0)\n"
  "C_SKIP2_%=:\n"
  "s_add_u32 s60, s56, 2\n"
  "s_bfe_u32 s57, s60, 131073\n"
  "s_lshl_b32 s57, s57, 18\n"
  "s_xor_b32 s60, s60, s47\n"
  "s_and_b32 s60, s60, 1\n"
  "s_lshl_b32 s60, s60, 13\n"
  "s_add_u32 s57, s57, s60\n"
  "s_xor_b32 s75, s96, 40960\n"
  "v_add_u32 v17, s75, v0\n"
  "s_sub_u32 s97, 237568, s97\n"
  "s_mul_i32 s60, s44, 5120\n"
  "s_add_u32 s60, s60, s97\n"
  "v_add_u32 v19, s60, v0\n"
  "s_cmp_ge_u32 s56, 62\n"
  "s_cbranch_scc0 C_DOU0_%=\n"
  "s_add_u32 s72, s73, 131072\n"
  "buffer_store_dword v21, v11, s[16:19], s73 offen sc1\n"
  "buffer_store_dword v21, v11, s[16:19], s72 offen sc1\n"
  "s_branch C_JOIN_%=\n"
  "C_DOU0_%=:\n"
  "ds_read_b128 v[64:67], v17 offset:0\n"
  "ds_read_b128 v[68:71], v17 offset:10240\n"
  "ds_read_b128 v[72:75], v17 offset:20480\n"
  "ds_read_b128 v[76:79], v17 offset:30720\n"
  "s_waitcnt lgkmcnt(3)\n"
  "v_mfma_f32_16x16x32_bf16 v[32:35], v[64:67], a[0:3], 0\n"
  "s_waitcnt lgkmcnt(2)\n"
  "v_mfma_f32_16x16x32_bf16 v[36:39], v[68:71], a[0:3], 0\n"
  "ds_read_b128 v[80:83], v17 offset:1024\n"
  "v_mfma_f32_16x16x32_bf16 v[40:43], v[64:67], a[8:11], 0\n"
  "v_mfma_f32_16x16x32_bf16 v[44:47], v[68:71], a[8:11], 0\n"
  "ds_read_b128 v[84:87], v17 offset:11264\n"
  "v_mfma_f32_16x16x32_bf16 v[48:51], v[64:67], a[16:19], 0\n"
  "v_mfma_f32_16x16x32_bf16 v[52:55], v[68:71], a[16:19], 0\n"
  "ds_read_b128 v[88:91], v17 offset:21504\n"
  "v_mfma_f32_16x16x32_bf16 v[56:59], v[64:67], a[24:27], 0\n"
  "v_mfma_f32_16x16x32_bf16 v[60:63], v[68:71], a[24:27], 0\n"
  "ds_read_b128 v[92:95], v17 offset:31744\n"
  "s_waitcnt lgkmcnt(5)\n"
  "v_mfma_f32_16x16x32_bf16 v[32:35], v[72:75], a[4:7], v[32:35]\n"
  "s_waitcnt lgkmcnt(4)\n"
  "v_mfma_f32_16x16x32_bf16 v[36:39], v[76:79], a[4:7], v[36:39]\n"
  "s_add_u32 s72, s73, 131072\n"
  "buffer_store_dword v21, v11, s[16:19], s73 offen sc1\n"
  "v_mfma_f32_16x16x32_bf16 v[40:43], v[72:75], a[12:15], v[40:43]\n"
  "buffer_store_dword v21, v11, s[16:19], s72 offen sc1\n"
  "v_mfma_f32_16x16x32_bf16 v[44:47], v[76:79], a[12:15], v[44:47]\n"
  "v_mfma_f32_16x16x32_bf16 v[48:51], v[72:75], a[20:23], v[48:51]\n"
  "v_mfma_f32_16x16x32_bf16 v[52:55], v[76:79], a[20:23], v[52:55]\n"
  "v_mfma_f32_16x16x32_bf16 v[56:59], v[72:75], a[28:31], v[56:59]\n"
  "v_mfma_f32_16x16x32_bf16 v[60:63], v[76:79], a[28:31], v[60:63]\n"
  "s_waitcnt lgkmcnt(3)\n"
  "v_mfma_f32_16x16x32_bf16 v[32:35], v[80:83], a[32:35], v[32:35]\n"
  "s_waitcnt lgkmcnt(2)\n"
  "v_mfma_f32_16x16x32_bf16 v[36:39], v[84:87], a[32:35], v[36:39]\n"
  "ds_read_b128 v[64:67], v17 offset:2048\n"
  "s_sub_u32 s60, s11, s4\n"
  "v_mfma_f32_16x16x32_bf16 v[40:43], v[80:83], a[40:43], v[40:43]\n"
  "s_sub_u32 s61, s59, s5\n"
  "v_cvt_f32_u32 v28, s61\n"
  "v_mfma_f32_16x16x32_bf16 v[44:47], v[84:87], a[40:43], v[44:47]\n"
  "ds_read_b128 v[68:71], v17 offset:12288\n"
  "v_cvt_f32_u32 v29, s60\n"
  "v_mfma_f32_16x16x32_bf16 v[48:51], v[80:83], a[48:51], v[48:51]\n"
  "v_rcp_f32 v28, v28\n"
  "v_mfma_f32_16x16x32_bf16 v[52:55], v[84:87], a[48:51], v[52:55]\n"
  "ds_read_b128 v[72:75], v17 offset:22528\n"
  "s_mov_b32 s4, s11\n"
  "v_mfma_f32_16x16x32_bf16 v[56:59], v[80:83], a[56:59], v[56:59]\n"
  "s_mov_b32 s5, s59\n"
  "v_mul_f32 v28, v28, v29\n"
  "v_mfma_f32_16x16x32_bf16 v[60:63], v[84:87], a[56:59], v[60:63]\n"
  "ds_read_b128 v[76:79], v17 offset:32768\n"
  "v_min_f32 v28, 0x41c80000, v28\n"
  "s_waitcnt lgkmcnt(5)\n"
  "v_mfma_f32_16x16x32_bf16 v[32:35], v[88:91], a[36:39], v[32:35]\n"
  "v_max_f32 v28, 0x41600000, v28\n"
  "v_mul_f32 v29, 0x41800000, v28\n"
  "s_waitcnt lgkmcnt(4)\n"
  "v_mfma_f32_16x16x32_bf16 v[36:39], v[92:95], a[36:39], v[36:39]\n"
  "v_rcp_f32 v28, v28\n"
  "v_mfma_f32_16x16x32_bf16 v[40:43], v[88:91], a[44:47], v[40:43]\n"
  "v_cvt_u32_f32 v29, v29\n"
  "s_sub_i32 s60, s64, s11\n"
  "v_mfma_f32_16x16x32_bf16 v[44:47], v[92:95], a[44:47], v[44:47]\n"
  "s_lshl_b32 s60, s60, 2\n"
  "v_readfirstlane_b32 s6, v29\n"
  "v_mfma_f32_16x16x32_bf16 v[48:51], v[88:91], a[52:55], v[48:51]\n"
  "v_cvt_f32_i32 v29, s60\n"
  "s_sub_i32 s60, s28, s59\n"
  "v_mfma_f32_16x16x32_bf16 v[52:55], v[92:95], a[52:55], v[52:55]\n"
  "v_mul_f32 v28, v28, v29\n"
  "s_lshl_b32 s60, s60, 2\n"
  "v_mfma_f32_16x16x32_bf16 v[56:59], v[88:91], a[60:63], v[56:59]\n"
  "v_cvt_i32_f32 v28, v28\n"
  "s_add_u32 s61, s60, 16\n"
  "v_mfma_f32_16x16x32_bf16 v[60:63], v[92:95], a[60:63], v[60:63]\n"
  "v_max_i32 v28, s60, v28\n"
  "v_min_i32 v28, s61, v28\n"
  "s_waitcnt lgkmcnt(3)\n"
  "v_mfma_f32_16x16x32_bf16 v[32:35], v[64:67], a[64:67], v[32:35]\n"
  "s_cmp_lt_u32 s56, 2\n"
  "s_cselect_b64 s[66:67], -1, s[66:67]\n"
  "s_waitcnt lgkmcnt(2)\n"
  "v_mfma_f32_16x16x32_bf16 v[36:39], v[68:71], a[64:67], v[36:39]\n"
  "ds_read_b128 v[80:83], v17 offset:3072\n"
  "s_cmp_eq_u64 s[66:67], 0\n"
  "v_mfma_f32_16x16x32_bf16 v[40:43], v[64:67], a[72:75], v[40:43]\n"
  "s_cselect_b32 s60, s2, 0x80000000\n"
  "v_mov_b32 v29, s60\n"
  "v_mfma_f32_16x16x32_bf16 v[44:47], v[68:71], a[72:75], v[44:47]\n"
  "ds_read_b128 v[84:87], v17 offset:13312\n"
  "s_lshl_b32 s68, s45, 14\n"
  "v_mfma_f32_16x16x32_bf16 v[48:51], v[64:67], a[80:83], v[48:51]\n"
  "s_add_u32 s68, s68, 1852416\n"
  "s_add_u32 s69, s68, 8192\n"
  "v_mfma_f32_16x16x32_bf16 v[52:55], v[68:71], a[80:83], v[52:55]\n"
  "ds_read_b128 v[88:91], v17 offset:23552\n"
  "s_andn2_b32 s60, 0x80000000, s53\n"
  "v_mfma_f32_16x16x32_bf16 v[56:59], v[64:67], a[88:91], v[56:59]\n"
  "v_lshlrev_b32 v96, 3, v0\n"
  "v_or_b32 v96, s60, v96\n"
  "v_mfma_f32_16x16x32_bf16 v[60:63], v[68:71], a[88:91], v[60:63]\n"
  "ds_read_b128 v[92:95], v17 offset:33792\n"
  "s_mov_b64 exec, 1\n"
  "buffer_store_dword v28, v29, s[16:19], 0 offen sc1\n"
  "s_mov_b64 exec, -1\n"
  "s_waitcnt lgkmcnt(5)\n"
  "v_mfma_f32_16x16x32_bf16 v[32:35], v[72:75], a[68:71], v[32:35]\n"
  "s_waitcnt lgkmcnt(4)\n"
  "v_mfma_f32_16x16x32_bf16 v[36:39], v[76:79], a[68:71], v[36:39]\n"
  "v_mfma_f32_16x16x32_bf16 v[40:43], v[72:75], a[76:79], v[40:43]\n"
  "v_mfma_f32_16x16x32_bf16 v[44:47], v[76:79], a[76:79], v[44:47]\n"
  "v_mfma_f32_16x16x32_bf16 v[48:51], v[72:75], a[84:87], v[48:51]\n"
  "v_mfma_f32_16x16x32_bf16 v[52:55], v[76:79], a[84:87], v[52:55]\n"
  "v_mfma_f32_16x16x32_bf16 v[56:59], v[72:75], a[92:95], v[56:59]\n"
  "v_mfma_f32_16x16x32_bf16 v[60:63], v[76:79], a[92:95], v[60:63]\n"
  "s_waitcnt lgkmcnt(3)\n"
  "v_mfma_f32_16x16x32_bf16 v[32:35], v[80:83], a[96:99], v[32:35]\n"
  "s_waitcnt lgkmcnt(2)\n"
  "v_mfma_f32_16x16x32_bf16 v[36:39], v[84:87], a[96:99], v[36:39]\n"
  "ds_read_b128 v[64:67], v17 offset:4096\n"
  "v_mfma_f32_16x16x32_bf16 v[40:43], v[80:83], a[104:107], v[40:43]\n"
  "v_mfma_f32_16x16x32_bf16 v[44:47], v[84:87], a[104:107], v[44:47]\n"
  "ds_read_b128 v[68:71], v17 offset:14336\n"
  "v_mfma_f32_16x16x32_bf16 v[48:51], v[80:83], a[112:115], v[48:51]\n"
  "v_mfma_f32_16x16x32_bf16 v[52:55], v[84:87], a[112:115], v[52:55]\n"
  "ds_read_b128 v[72:75], v17 offset:24576\n"
  "v_mfma_f32_16x16x32_bf16 v[56:59], v[80:83], a[120:123], v[56:59]\n"
  "v_mfma_f32_16x16x32_bf16 v[60:63], v[84:87], a[120:123], v[60:63]\n"
  "ds_read_b128 v[76:79], v17 offset:34816\n"
  "s_waitcnt lgkmcnt(5)\n"
  "v_mfma_f32_16x16x32_bf16 v[32:35], v[88:91], a[100:103], v[32:35]\n"
  "s_waitcnt lgkmcnt(4)\n"
  "v_mfma_f32_16x16x32_bf16 v[36:39], v[92:95], a[100:103], v[36:39]\n"
  "v_mfma_f32_16x16x32_bf16 v[40:43], v[88:91], a[108:111], v[40:43]\n"
  "v_mfma_f32_16x16x32_bf16 v[44:47], v[92:95], a[108:111], v[44:47]\n"
  "v_mfma_f32_16x16x32_bf16 v[48:51], v[88:91], a[116:119], v[48:51]\n"
  "v_mfma_f32_16x16x32_bf16 v[52:55], v[92:95], a[116:119], v[52:55]\n"
  "v_mfma_f32_16x16x32_bf16 v[56:59], v[88:91], a[124:127], v[56:59]\n"
  "v_mfma_f32_16x16x32_bf16 v[60:63], v[92:95], a[124:127], v[60:63]\n"
  "s_waitcnt lgkmcnt(3)\n"
  "v_mfma_f32_16x16x32_bf16 v[32:35], v[64:67], a[128:131], v[32:35]\n"
  "s_waitcnt lgkmcnt(2)\n"
  "v_mfma_f32_16x16x32_bf16 v[36:39], v[68:71], a[128:131], v[36:39]\n"
  "ds_read_b128 v[80:83], v17 offset:5120\n"
  "v_mfma_f32_16x16x32_bf16 v[40:43], v[64:67], a[136:139], v[40:43]\n"
  "v_mfma_f32_16x16x32_bf16 v[44:47], v[68:71], a[136:139], v[44:47]\n"
  "ds_read_b128 v[84:87], v17 offset:15360\n"
  "v_mfma_f32_16x16x32_bf16 v[48:51], v[64:67], a[144:147], v[48:51]\n"
  "v_mfma_f32_16x16x32_bf16 v[52:55], v[68:71], a[144:147], v[52:55]\n"
  "ds_read_b128 v[88:91], v17 offset:25600\n"
  "v_mfma_f32_16x16x32_bf16 v[56:59], v[64:67], a[152:155], v[56:59]\n"
  "v_mfma_f32_16x16x32_bf16 v[60:63], v[68:71], a[152:155], v[60:63]\n"
  "ds_read_b128 v[92:95], v17 offset:35840\n"
  "s_waitcnt lgkmcnt(5)\n"
  "v_mfma_f32_16x16x32_bf16 v[32:35], v[72:75], a[132:135], v[32:35]\n"
  "s_waitcnt lgkmcnt(4)\n"
  "v_mfma_f32_16x16x32_bf16 v[36:39], v[76:79], a[132:135], v[36:39]\n"
  "v_mfma_f32_16x16x32_bf16 v[40:43], v[72:75], a[140:143], v[40:43]\n"
  "buffer_load_dwordx2 v[98:99], v96, s[16:19], s68 offen sc1\n"
  "v_mfma_f32_16x16x32_bf16 v[44:47], v[76:79], a[140:143], v[44:47]\n"
  "buffer_load_dwordx2 v[100:101], v96, s[16:19], s69 offen sc1\n"
  "v_mfma_f32_16x16x32_bf16 v[48:51], v[72:75], a[148:151], v[48:51]\n"
  "v_mfma_f32_16x16x32_bf16 v[52:55], v[76:79], a[148:151], v[52:55]\n"
  "v_mfma_f32_16x16x32_bf16 v[56:59], v[72:75], a[156:159], v[56:59]\n"
  "v_mfma_f32_16x16x32_bf16 v[60:63], v[76:79], a[156:159], v[60:63]\n"
  "s_waitcnt lgkmcnt(3)\n"
  "v_mfma_f32_16x16x32_bf16 v[32:35], v[80:83], a[160:163], v[32:35]\n"
  "s_waitcnt lgkmcnt(2)\n"
  "v_mfma_f32_16x16x32_bf16 v[36:39], v[84:87], a[160:163], v[36:39]\n"
  "ds_read_b128 v[64:67], v17 offset:6144\n"
  "v_mfma_f32_16x16x32_bf16 v[40:43], v[80:83], a[168:171], v[40:43]\n"
  "v_mfma_f32_16x16x32_bf16 v[44:47], v[84:87], a[168:171], v[44:47]\n"
  "ds_read_b128 v[68:71], v17 offset:16384\n"
  "v_mfma_f32_16x16x32_bf16 v[48:51], v[80:83], a[176:179], v[48:51]\n"
  "v_mfma_f32_16x16x32_bf16 v[52:55], v[84:87], a[176:179], v[52:55]\n"
  "ds_read_b128 v[72:75], v17 offset:26624\n"
  "v_mfma_f32_16x16x32_bf16 v[56:59], v[80:83], a[184:187], v[56:59]\n"
  "v_mfma_f32_16x16x32_bf16 v[60:63], v[84:87], a[184:187], v[60:63]\n"
  "ds_read_b128 v[76:79], v17 offset:36864\n"
  "s_waitcnt lgkmcnt(5)\n"
  "v_mfma_f32_16x16x32_bf16 v[32:35], v[88:91], a[164:167], v[32:35]\n"
  "s_waitcnt lgkmcnt(4)\n"
  "v_mfma_f32_16x16x32_bf16 v[36:39], v[92:95], a[164:167], v[36:39]\n"
  "v_mfma_f32_16x16x32_bf16 v[40:43], v[88:91], a[172:175], v[40:43]\n"
  "v_mfma_f32_16x16x32_bf16 v[44:47], v[92:95], a[172:175], v[44:47]\n"
  "v_mfma_f32_16x16x32_bf16 v[48:51], v[88:91], a[180:183], v[48:51]\n"
  "v_mfma_f32_16x16x32_bf16 v[52:55], v[92:95], a[180:183], v[52:55]\n"
  "v_mfma_f32_16x16x32_bf16 v[56:59], v[88:91], a[188:191], v[56:59]\n"
  "v_mfma_f32_16x16x32_bf16 v[60:63], v[92:95], a[188:191], v[60:63]\n"
  "s_waitcnt lgkmcnt(3)\n"
  "v_mfma_f32_16x16x32_bf16 v[32:35], v[64:67], a[192:195], v[32:35]\n"
  "s_waitcnt lgkmcnt(2)\n"
  "v_mfma_f32_16x16x32_bf16 v[36:39], v[68:71], a[192:195], v[36:39]\n"
  "ds_read_b128 v[80:83], v17 offset:7168\n"
  "v_mfma_f32_16x16x32_bf16 v[40:43], v[64:67], a[200:203], v[40:43]\n"
  "v_mfma_f32_16x16x32_bf16 v[44:47], v[68:71], a[200:203], v[44:47]\n"
  "ds_read_b128 v[84:87], v17 offset:17408\n"
  "v_mfma_f32_16x16x32_bf16 v[48:51], v[64:67], a[208:211], v[48:51]\n"
  "v_mfma_f32_16x16x32_bf16 v[52:55], v[68:71], a[208:211], v[52:55]\n"
  "ds_read_b128 v[88:91], v17 offset:27648\n"
  "v_mfma_f32_16x16x32_bf16 v[56:59], v[64:67], a[216:219], v[56:59]\n"
  "v_mfma_f32_16x16x32_bf16 v[60:63], v[68:71], a[216:219], v[60:63]\n"
  "ds_read_b128 v[92:95], v17 offset:37888\n"
  "s_waitcnt lgkmcnt(5)\n"
  "v_mfma_f32_16x16x32_bf16 v[32:35], v[72:75], a[196:199], v[32:35]\n"
  "s_waitcnt lgkmcnt(4)\n"
  "v_mfma_f32_16x16x32_bf16 v[36:39], v[76:79], a[196:199], v[36:39]\n"
  "v_mfma_f32_16x16x32_bf16 v[40:43], v[72:75], a[204:207], v[40:43]\n"
  "v_mfma_f32_16x16x32_bf16 v[44:47], v[76:79], a[204:207], v[44:47]\n"
  "v_mfma_f32_16x16x32_bf16 v[48:51], v[72:75], a[212:215], v[48:51]\n"
  "s_waitcnt vmcnt(0)\n"
  "v_max_i32 v99, v99, v101\n"
  "v_max_i32_dpp v98, v98, v98 row_ror:1 row_mask:0xf bank_mask:0xf\n"
  "v_mfma_f32_16x16x32_bf16 v[52:55], v[76:79], a[212:215], v[52:55]\n"
  "v_max_i32_dpp v99, v99, v99 row_ror:1 row_mask:0xf bank_mask:0xf\n"
  "v_max_i32_dpp v100, v100, v100 row_ror:1 row_mask:0xf bank_mask:0xf\n"
  "v_mfma_f32_16x16x32_bf16 v[56:59], v[72:75], a[220:223], v[56:59]\n"
  "v_max_i32_dpp v98, v98, v98 row_ror:2 row_mask:0xf bank_mask:0xf\n"
  "v_max_i32_dpp v99, v99, v99 row_ror:2 row_mask:0xf bank_mask:0xf\n"
  "v_mfma_f32_16x16x32_bf16 v[60:63], v[76:79], a[220:223], v[60:63]\n"
  "v_max_i32_dpp v100, v100, v100 row_ror:2 row_mask:0xf bank_mask:0xf\n"
  "v_max_i32_dpp v98, v98, v98 row_ror:4 row_mask:0xf bank_mask:0xf\n"
  "s_waitcnt lgkmcnt(3)\n"
  "v_mfma_f32_16x16x32_bf16 v[32:35], v[80:83], a[224:227], v[32:35]\n"
  "v_max_i32_dpp v99, v99, v99 row_ror:4 row_mask:0xf bank_mask:0xf\n"
  "v_max_i32_dpp v100, v100, v100 row_ror:4 row_mask:0xf bank_mask:0xf\n"
  "s_waitcnt lgkmcnt(2)\n"
  "v_mfma_f32_16x16x32_bf16 v[36:39], v[84:87], a[224:227], v[36:39]\n"
  "ds_read_b128 v[64:67], v17 offset:8192\n"
  "v_max_i32_dpp v98, v98, v98 row_ror:8 row_mask:0xf bank_mask:0xf\n"
  "v_mfma_f32_16x16x32_bf16 v[40:43], v[80:83], a[232:235], v[40:43]\n"
  "v_max_i32_dpp v99, v99, v99 row_ror:8 row_mask:0xf bank_mask:0xf\n"
  "v_max_i32_dpp v100, v100, v100 row_ror:8 row_mask:0xf bank_mask:0xf\n"
  "v_mfma_f32_16x16x32_bf16 v[44:47], v[84:87], a[232:235], v[44:47]\n"
  "ds_read_b128 v[68:71], v17 offset:18432\n"
  "v_readlane_b32 s68, v98, 0\n"
  "v_mfma_f32_16x16x32_bf16 v[48:51], v[80:83], a[240:243], v[48:51]\n"
  "v_readlane_b32 s71, v98, 16\n"
  "s_max_i32 s68, s68, s71\n"
  "v_mfma_f32_16x16x32_bf16 v[52:55], v[84:87], a[240:243], v[52:55]\n"
  "ds_read_b128 v[72:75], v17 offset:28672\n"
  "v_readlane_b32 s69, v98, 32\n"
  "v_mfma_f32_16x16x32_bf16 v[56:59], v[80:83], a[248:251], v[56:59]\n"
  "v_readlane_b32 s71, v98, 48\n"
  "s_max_i32 s69, s69, s71\n"
  "v_mfma_f32_16x16x32_bf16 v[60:63], v[84:87], a[248:251], v[60:63]\n"
  "ds_read_b128 v[76:79], v17 offset:38912\n"
  "v_readlane_b32 s71, v100, 0\n"
  "s_waitcnt lgkmcnt(5)\n"
  "v_mfma_f32_16x16x32_bf16 v[32:35], v[88:91], a[228:231], v[32:35]\n"
  "s_max_i32 s69, s69, s71\n"
  "v_readlane_b32 s71, v100, 16\n"
  "s_waitcnt lgkmcnt(4)\n"
  "v_mfma_f32_16x16x32_bf16 v[36:39], v[92:95], a[228:231], v[36:39]\n"
  "s_max_i32 s69, s69, s71\n"
  "v_readlane_b32 s71, v100, 32\n"
  "v_mfma_f32_16x16x32_bf16 v[40:43], v[88:91], a[236:239], v[40:43]\n"
  "s_max_i32 s69, s69, s71\n"
  "v_readlane_b32 s70, v99, 0\n"
  "v_mfma_f32_16x16x32_bf16 v[44:47], v[92:95], a[236:239], v[44:47]\n"
  "v_readlane_b32 s71, v99, 16\n"
  "s_max_i32 s70, s70, s71\n"
  "v_mfma_f32_16x16x32_bf16 v[48:51], v[88:91], a[244:247], v[48:51]\n"
  "v_readlane_b32 s71, v99, 32\n"
  "s_max_i32 s70, s70, s71\n"
  "v_mfma_f32_16x16x32_bf16 v[52:55], v[92:95], a[244:247], v[52:55]\n"
  "v_readlane_b32 s71, v99, 48\n"
  "s_max_i32 s70, s70, s71\n"
  "v_mfma_f32_16x16x32_bf16 v[56:59], v[88:91], a[252:255], v[56:59]\n"
  "s_sub_i32 s71, s68, s9\n"
  "s_ashr_i32 s60, s71, 3\n"
  "v_mfma_f32_16x16x32_bf16 v[60:63], v[92:95], a[252:255], v[60:63]\n"
  "s_min_i32 s60, s60, -1\n"
  "s_min_i32 s71, s71, 6\n"
  "s_waitcnt lgkmcnt(3)\n"
  "v_mfma_f32_16x16x32_bf16 v[32:35], v[64:67], v[192:195], v[32:35]\n"
  "s_max_i32 s71, s71, s60\n"
  "s_add_i32 s71, s71, s9\n"
  "s_waitcnt lgkmcnt(2)\n"
  "v_mfma_f32_16x16x32_bf16 v[36:39], v[68:71], v[192:195], v[36:39]\n"
  "ds_read_b128 v[80:83], v17 offset:9216\n"
  "s_cmp_gt_i32 s9, 0\n"
  "v_mfma_f32_16x16x32_bf16 v[40:43], v[64:67], v[200:203], v[40:43]\n"
  "s_cselect_b32 s9, s71, s68\n"
  "s_sub_i32 s71, s69, s10\n"
  "buffer_load_dwordx2 v[30:31], v10, s[16:19], s57 offen sc1\n"
  "v_mfma_f32_16x16x32_bf16 v[44:47], v[68:71], v[200:203], v[44:47]\n"
  "ds_read_b128 v[84:87], v17 offset:19456\n"
  "s_ashr_i32 s60, s71, 3\n"
  "v_mfma_f32_16x16x32_bf16 v[48:51], v[64:67], v[208:211], v[48:51]\n"
  "s_min_i32 s60, s60, -1\n"
  "s_min_i32 s71, s71, 6\n"
  "v_mfma_f32_16x16x32_bf16 v[52:55], v[68:71], v[208:211], v[52:55]\n"
  "ds_read_b128 v[88:91], v17 offset:29696\n"
  "s_max_i32 s71, s71, s60\n"
  "v_mfma_f32_16x16x32_bf16 v[56:59], v[64:67], v[216:219], v[56:59]\n"
  "s_add_i32 s71, s71, s10\n"
  "s_cmp_gt_i32 s10, 0\n"
  "v_mfma_f32_16x16x32_bf16 v[60:63], v[68:71], v[216:219], v[60:63]\n"
  "ds_read_b128 v[92:95], v17 offset:39936\n"
  "s_cselect_b32 s10, s71, s69\n"
  "s_waitcnt lgkmcnt(5)\n"
  "v_mfma_f32_16x16x32_bf16 v[32:35], v[72:75], v[196:199], v[32:35]\n"
  "s_bfe_u32 s71, s53, 524304\n"
  "s_add_i32 s70, s70, s71\n"
  "s_waitcnt lgkmcnt(4)\n"
  "v_mfma_f32_16x16x32_bf16 v[36:39], v[76:79], v[196:199], v[36:39]\n"
  "s_sub_i32 s70, s70, 99\n"
  "s_ashr_i32 s70, s70, 1\n"
  "v_mfma_f32_16x16x32_bf16 v[40:43], v[72:75], v[204:207], v[40:43]\n"
  "s_min_i32 s70, s70, 4\n"
  "s_max_i32 s70, s70, -1\n"
  "v_mfma_f32_16x16x32_bf16 v[44:47], v[76:79], v[204:207], v[44:47]\n"
  "s_add_i32 s8, s8, s70\n"
  "s_max_i32 s8, s8, 0\n"
  "v_mfma_f32_16x16x32_bf16 v[48:51], v[72:75], v[212:215], v[48:51]\n"
  "s_min_i32 s8, s8, 60\n"
  "s_bfe_u32 s71, s53, 524296\n"
  "v_mfma_f32_16x16x32_bf16 v[52:55], v[76:79], v[212:215], v[52:55]\n"
  "s_add_i32 s71, s71, s10\n"
  "s_lshr_b32 s71, s71, 2\n"
  "v_mfma_f32_16x16x32_bf16 v[56:59], v[72:75], v[220:223], v[56:59]\n"
  "s_add_i32 s71, s71, s8\n"
  "s_cmp_gt_i32 s10, 0\n"
  "v_mfma_f32_16x16x32_bf16 v[60:63], v[76:79], v[220:223], v[60:63]\n"
  "s_cselect_b32 s49, s71, s49\n"
  "s_and_b32 s71, s53, 255\n"
  "s_waitcnt lgkmcnt(3)\n"
  "v_mfma_f32_16x16x32_bf16 v[32:35], v[80:83], v[224:227], v[32:35]\n"
  "s_add_i32 s71, s71, s9\n"
  "s_lshr_b32 s71, s71, 2\n"
  "s_waitcnt lgkmcnt(2)\n"
  "v_mfma_f32_16x16x32_bf16 v[36:39], v[84:87], v[224:227], v[36:39]\n"
  "s_cmp_gt_i32 s9, 0\n"
  "s_cselect_b32 s71, s71, -1\n"
  "v_mfma_f32_16x16x32_bf16 v[40:43], v[80:83], v[232:235], v[40:43]\n"
  "v_mov_b32 v102, s71\n"
  "s_add_u32 s60, s56, 4\n"
  "v_mfma_f32_16x16x32_bf16 v[44:47], v[84:87], v[232:235], v[44:47]\n"
  "s_bfe_u32 s68, s60, 131073\n"
  "s_lshl_b32 s68, s68, 18\n"
  "v_mfma_f32_16x16x32_bf16 v[48:51], v[80:83], v[240:243], v[48:51]\n"
  "s_xor_b32 s60, s60, s47\n"
  "s_and_b32 s60, s60, 1\n"
  "v_mfma_f32_16x16x32_bf16 v[52:55], v[84:87], v[240:243], v[52:55]\n"
  "s_lshl_b32 s60, s60, 13\n"
  "s_add_u32 s68, s68, s60\n"
  "v_mfma_f32_16x16x32_bf16 v[56:59], v[80:83], v[248:251], v[56:59]\n"
  "buffer_store_dword v102, v16, s[16:19], s68 offen offset:4 sc1\n"
  "v_mfma_f32_16x16x32_bf16 v[60:63], v[84:87], v[248:251], v[60:63]\n"
  "s_waitcnt lgkmcnt(1)\n"
  "v_mfma_f32_16x16x32_bf16 v[32:35], v[88:91], v[228:231], v[32:35]\n"
  "s_waitcnt lgkmcnt(0)\n"
  "v_mfma_f32_16x16x32_bf16 v[36:39], v[92:95], v[228:231], v[36:39]\n"
  "v_mfma_f32_16x16x32_bf16 v[40:43], v[88:91], v[236:239], v[40:43]\n"
  "v_mfma_f32_16x16x32_bf16 v[44:47], v[92:95], v[236:239], v[44:47]\n"
  "v_mfma_f32_16x16x32_bf16 v[48:51], v[88:91], v[244:247], v[48:51]\n"
  "v_mfma_f32_16x16x32_bf16 v[52:55], v[92:95], v[244:247], v[52:55]\n"
  "v_mfma_f32_16x16x32_bf16 v[56:59], v[88:91], v[252:255], v[56:59]\n"
  "v_mfma_f32_16x16x32_bf16 v[60:63], v[92:95], v[252:255], v[60:63]\n"
  "v_cvt_pk_bf16_f32 v32, v32, v33\n"
  "v_cvt_pk_bf16_f32 v33, v34, v35\n"
  "v_cvt_pk_bf16_f32 v34, v36, v37\n"
  "v_cvt_pk_bf16_f32 v35, v38, v39\n"
  "ds_write_b128 v19, v[32:35] offset:0\n"
  "v_cvt_pk_bf16_f32 v40, v40, v41\n"
  "v_cvt_pk_bf16_f32 v41, v42, v43\n"
  "v_cvt_pk_bf16_f32 v42, v44, v45\n"
  "v_cvt_pk_bf16_f32 v43, v46, v47\n"
  "ds_write_b128 v19, v[40:43] offset:1024\n"
  "v_cvt_pk_bf16_f32 v48, v48, v49\n"
  "v_cvt_pk_bf16_f32 v49, v50, v51\n"
  "v_cvt_pk_bf16_f32 v50, v52, v53\n"
  "v_cvt_pk_bf16_f32 v51, v54, v55\n"
  "ds_write_b128 v19, v[48:51] offset:2048\n"
  "v_cvt_pk_bf16_f32 v56, v56, v57\n"
  "v_cvt_pk_bf16_f32 v57, v58, v59\n"
  "v_cvt_pk_bf16_f32 v58, v60, v61\n"
  "v_cvt_pk_bf16_f32 v59, v62, v63\n"
  "ds_write_b128 v19, v[56:59] offset:3072\n"
  "s_waitcnt vmcnt(1) lgkmcnt(0)\n"
  "s_branch C_JOIN_%=\n"
  "C_LAST_%=:\n"
  "s_nop 7\n"
  "s_add_u32 s72, s58, 131072\n"
  "buffer_store_dword v23, v11, s[16:19], s58 offen sc1\n"
  "buffer_store_dword v23, v11, s[16:19], s72 offen sc1\n"
  "s_branch C_SKIP2_%=\n"
  "C_OTHER_%=:\n"
  "s_nop 7\n"
  "s_add_u32 s60, s56, 2\n"
  "s_bfe_u32 s57, s60, 131073\n"
  "s_lshl_b32 s57, s57, 18\n"
  "s_xor_b32 s60, s60, s47\n"
  "s_and_b32 s60, s60, 1\n"
  "s_lshl_b32 s60, s60, 13\n"
  "s_add_u32 s57, s57, s60\n"
  "s_xor_b32 s75, s96, 40960\n"
  "v_add_u32 v17, s75, v0\n"
  "s_sub_u32 s97, 237568, s97\n"
  "s_mul_i32 s60, s44, 5120\n"
  "s_add_u32 s60, s60, s97\n"
  "v_add_u32 v19, s60, v0\n"
  "s_lshr_b32 s60, s48, 1\n"
  "s_add_u32 s59, s59, s60\n"
  "s_add_u32 s59, s59, s55\n"
  "s_cmp_eq_u32 s44, 1\n"
  "s_cselect_b32 s98, 0, s44\n"
  "s_and_b32 s68, s56, 7\n"
  "s_mul_i32 s68, s68, 81920\n"
  "s_mul_i32 s60, s98, 20480\n"
  "s_add_u32 s68, s68, s60\n"
  "s_add_u32 s69, s68, 5120\n"
  "s_add_u32 s70, s68, 10240\n"
  "s_add_u32 s71, s68, 15360\n"
  "s_mul_i32 s74, s99, 40960\n"
  "s_mul_i32 s63, s56, 40960\n"
  "s_add_u32 s58, s99, 4\n"
  "s_lshl_b32 s58, s58, 7\n"
  "s_add_u32 s58, s58, 1835008\n"
  "s_mov_b64 s[66:67], 0\n"
  "v_mov_b32 v28, s3\n"
  "v_mov_b32 v29, s2\n"
  "s_cmp_ge_u32 s56, 62\n"
  "s_cbranch_scc0 C_DOU_%=\n"
  "C_WT4_%=:\n"
  "s_memrealtime s[64:65]\n"
  "s_waitcnt lgkmcnt(0)\n"
  "s_sub_i32 s60, s64, s59\n"
  "s_cmp_lt_i32 s60, 0\n"
  "s_cbranch_scc1 C_WT4_%=\n"
  "buffer_load_dword v22, v14, s[32:35], s74 offen sc1\n"
  "s_mov_b64 exec, 1\n"
  "buffer_store_dword v28, v29, s[16:19], 0 offen offset:4 sc1\n"
  "s_mov_b64 exec, -1\n"
  "buffer_load_dword v15, off, s[16:19], s58 sc1\n"
  "buffer_load_dwordx4 v[112:115], v8, s[16:19], s68 offen offset:0 sc1\n"
  "buffer_load_dwordx4 v[116:119], v8, s[16:19], s68 offen offset:1280 sc1\n"
  "buffer_load_dwordx4 v[120:123], v8, s[16:19], s68 offen offset:2560 sc1\n"
  "buffer_load_dwordx4 v[124:127], v8, s[16:19], s68 offen offset:3840 sc1\n"
  "buffer_load_dwordx4 v[128:131], v9, s[16:19], s68 offen sc1\n"
  "buffer_load_dwordx4 v[132:135], v8, s[16:19], s69 offen offset:0 sc1\n"
  "buffer_load_dwordx4 v[136:139], v8, s[16:19], s69 offen offset:1280 sc1\n"
  "buffer_load_dwordx4 v[140:143], v8, s[16:19], s69 offen offset:2560 sc1\n"
  "buffer_load_dwordx4 v[144:147], v8, s[16:19], s69 offen offset:3840 sc1\n"
  "buffer_load_dwordx4 v[148:151], v9, s[16:19], s69 offen sc1\n"
  "buffer_load_dwordx4 v[152:155], v8, s[16:19], s70 offen offset:0 sc1\n"
  "buffer_load_dwordx4 v[156:159], v8, s[16:19], s70 offen offset:1280 sc1\n"
  "buffer_load_dwordx4 v[160:163], v8, s[16:19], s70 offen offset:2560 sc1\n"
  "buffer_load_dwordx4 v[164:167], v8, s[16:19], s70 offen offset:3840 sc1\n"
  "buffer_load_dwordx4 v[168:171], v9, s[16:19], s70 offen sc1\n"
  "buffer_load_dwordx4 v[172:175], v8, s[16:19], s71 offen offset:0 sc1\n"
  "buffer_load_dwordx4 v[176:179], v8, s[16:19], s71 offen offset:1280 sc1\n"
  "buffer_load_dwordx4 v[180:183], v8, s[16:19], s71 offen offset:2560 sc1\n"
  "buffer_load_dwordx4 v[184:187], v8, s[16:19], s71 offen offset:3840 sc1\n"
  "buffer_load_dwordx4 v[188:191], v9, s[16:19], s71 offen sc1\n"
  "buffer_load_dwordx4 v[96:99], v13, s[16:19], s68 offen offset:0 sc1\n"
  "buffer_load_dwordx4 v[100:103], v13, s[16:19], s68 offen offset:1024 sc1\n"
  "buffer_load_dwordx4 v[104:107], v13, s[16:19], s68 offen offset:2048 sc1\n"
  "buffer_load_dwordx4 v[108:111], v13, s[16:19], s68 offen offset:3072 sc1\n"
  "s_branch C_NOU_%=\n"
  "C_DOU_%=:\n"
  "s_nop 7\n"
  "ds_read_b128 v[64:67], v17 offset:0\n"
  "ds_read_b128 v[68:71], v17 offset:10240\n"
  "ds_read_b128 v[72:75], v17 offset:20480\n"
  "ds_read_b128 v[76:79], v17 offset:30720\n"
  "s_waitcnt lgkmcnt(3)\n"
  "v_mfma_f32_16x16x32_bf16 v[32:35], v[64:67], a[0:3], 0\n"
  "s_waitcnt lgkmcnt(2)\n"
  "v_mfma_f32_16x16x32_bf16 v[36:39], v[68:71], a[0:3], 0\n"
  "ds_read_b128 v[80:83], v17 offset:1024\n"
  "v_mfma_f32_16x16x32_bf16 v[40:43], v[64:67], a[8:11], 0\n"
  "v_mfma_f32_16x16x32_bf16 v[44:47], v[68:71], a[8:11], 0\n"
  "ds_read_b128 v[84:87], v17 offset:11264\n"
  "v_mfma_f32_16x16x32_bf16 v[48:51], v[64:67], a[16:19], 0\n"
  "v_mfma_f32_16x16x32_bf16 v[52:55], v[68:71], a[16:19], 0\n"
  "ds_read_b128 v[88:91], v17 offset:21504\n"
  "v_mfma_f32_16x16x32_bf16 v[56:59], v[64:67], a[24:27], 0\n"
  "v_mfma_f32_16x16x32_bf16 v[60:63], v[68:71], a[24:27], 0\n"
  "ds_read_b128 v[92:95], v17 offset:31744\n"
  "s_waitcnt lgkmcnt(5)\n"
  "v_mfma_f32_16x16x32_bf16 v[32:35], v[72:75], a[4:7], v[32:35]\n"
  "s_waitcnt lgkmcnt(4)\n"
  "v_mfma_f32_16x16x32_bf16 v[36:39], v[76:79], a[4:7], v[36:39]\n"
  "v_mfma_f32_16x16x32_bf16 v[40:43], v[72:75], a[12:15], v[40:43]\n"
  "v_mfma_f32_16x16x32_bf16 v[44:47], v[76:79], a[12:15], v[44:47]\n"
  "v_mfma_f32_16x16x32_bf16 v[48:51], v[72:75], a[20:23], v[48:51]\n"
  "v_mfma_f32_16x16x32_bf16 v[52:55], v[76:79], a[20:23], v[52:55]\n"
  "v_mfma_f32_16x16x32_bf16 v[56:59], v[72:75], a[28:31], v[56:59]\n"
  "v_mfma_f32_16x16x32_bf16 v[60:63], v[76:79], a[28:31], v[60:63]\n"
  "s_waitcnt lgkmcnt(3)\n"
  "v_mfma_f32_16x16x32_bf16 v[32:35], v[80:83], a[32:35], v[32:35]\n"
  "s_waitcnt lgkmcnt(2)\n"
  "v_mfma_f32_16x16x32_bf16 v[36:39], v[84:87], a[32:35], v[36:39]\n"
  "ds_read_b128 v[64:67], v17 offset:2048\n"
  "v_mfma_f32_16x16x32_bf16 v[40:43], v[80:83], a[40:43], v[40:43]\n"
  "v_mfma_f32_16x16x32_bf16 v[44:47], v[84:87], a[40:43], v[44:47]\n"
  "ds_read_b128 v[68:71], v17 offset:12288\n"
  "v_mfma_f32_16x16x32_bf16 v[48:51], v[80:83], a[48:51], v[48:51]\n"
  "v_mfma_f32_16x16x32_bf16 v[52:55], v[84:87], a[48:51], v[52:55]\n"
  "ds_read_b128 v[72:75], v17 offset:22528\n"
  "v_mfma_f32_16x16x32_bf16 v[56:59], v[80:83], a[56:59], v[56:59]\n"
  "v_mfma_f32_16x16x32_bf16 v[60:63], v[84:87], a[56:59], v[60:63]\n"
  "ds_read_b128 v[76:79], v17 offset:32768\n"
  "s_waitcnt lgkmcnt(5)\n"
  "v_mfma_f32_16x16x32_bf16 v[32:35], v[88:91], a[36:39], v[32:35]\n"
  "s_waitcnt lgkmcnt(4)\n"
  "v_mfma_f32_16x16x32_bf16 v[36:39], v[92:95], a[36:39], v[36:39]\n"
  "v_mfma_f32_16x16x32_bf16 v[40:43], v[88:91], a[44:47], v[40:43]\n"
  "v_mfma_f32_16x16x32_bf16 v[44:47], v[92:95], a[44:47], v[44:47]\n"
  "v_mfma_f32_16x16x32_bf16 v[48:51], v[88:91], a[52:55], v[48:51]\n"
  "v_mfma_f32_16x16x32_bf16 v[52:55], v[92:95], a[52:55], v[52:55]\n"
  "v_mfma_f32_16x16x32_bf16 v[56:59], v[88:91], a[60:63], v[56:59]\n"
  "v_mfma_f32_16x16x32_bf16 v[60:63], v[92:95], a[60:63], v[60:63]\n"
  "s_waitcnt lgkmcnt(3)\n"
  "v_mfma_f32_16x16x32_bf16 v[32:35], v[64:67], a[64:67], v[32:35]\n"
  "s_waitcnt lgkmcnt(2)\n"
  "v_mfma_f32_16x16x32_bf16 v[36:39], v[68:71], a[64:67], v[36:39]\n"
  "ds_read_b128 v[80:83], v17 offset:3072\n"
  "v_mfma_f32_16x16x32_bf16 v[40:43], v[64:67], a[72:75], v[40:43]\n"
  "v_mfma_f32_16x16x32_bf16 v[44:47], v[68:71], a[72:75], v[44:47]\n"
  "ds_read_b128 v[84:87], v17 offset:13312\n"
  "v_mfma_f32_16x16x32_bf16 v[48:51], v[64:67], a[80:83], v[48:51]\n"
  "v_mfma_f32_16x16x32_bf16 v[52:55], v[68:71], a[80:83], v[52:55]\n"
  "ds_read_b128 v[88:91], v17 offset:23552\n"
  "v_mfma_f32_16x16x32_bf16 v[56:59], v[64:67], a[88:91], v[56:59]\n"
  "v_mfma_f32_16x16x32_bf16 v[60:63], v[68:71], a[88:91], v[60:63]\n"
  "ds_read_b128 v[92:95], v17 offset:33792\n"
  "s_waitcnt lgkmcnt(5)\n"
  "v_mfma_f32_16x16x32_bf16 v[32:35], v[72:75], a[68:71], v[32:35]\n"
  "s_waitcnt lgkmcnt(4)\n"
  "v_mfma_f32_16x16x32_bf16 v[36:39], v[76:79], a[68:71], v[36:39]\n"
  "v_mfma_f32_16x16x32_bf16 v[40:43], v[72:75], a[76:79], v[40:43]\n"
  "v_mfma_f32_16x16x32_bf16 v[44:47], v[76:79], a[76:79], v[44:47]\n"
  "v_mfma_f32_16x16x32_bf16 v[48:51], v[72:75], a[84:87], v[48:51]\n"
  "v_mfma_f32_16x16x32_bf16 v[52:55], v[76:79], a[84:87], v[52:55]\n"
  "v_mfma_f32_16x16x32_bf16 v[56:59], v[72:75], a[92:95], v[56:59]\n"
  "v_mfma_f32_16x16x32_bf16 v[60:63], v[76:79], a[92:95], v[60:63]\n"
  "s_waitcnt lgkmcnt(3)\n"
  "v_mfma_f32_16x16x32_bf16 v[32:35], v[80:83], a[96:99], v[32:35]\n"
  "s_waitcnt lgkmcnt(2)\n"
  "v_mfma_f32_16x16x32_bf16 v[36:39], v[84:87], a[96:99], v[36:39]\n"
  "ds_read_b128 v[64:67], v17 offset:4096\n"
  "v_mfma_f32_16x16x32_bf16 v[40:43], v[80:83], a[104:107], v[40:43]\n"
  "v_mfma_f32_16x16x32_bf16 v[44:47], v[84:87], a[104:107], v[44:47]\n"
  "ds_read_b128 v[68:71], v17 offset:14336\n"
  "v_mfma_f32_16x16x32_bf16 v[48:51], v[80:83], a[112:115], v[48:51]\n"
  "v_mfma_f32_16x16x32_bf16 v[52:55], v[84:87], a[112:115], v[52:55]\n"
  "ds_read_b128 v[72:75], v17 offset:24576\n"
  "v_mfma_f32_16x16x32_bf16 v[56:59], v[80:83], a[120:123], v[56:59]\n"
  "v_mfma_f32_16x16x32_bf16 v[60:63], v[84:87], a[120:123], v[60:63]\n"
  "ds_read_b128 v[76:79], v17 offset:34816\n"
  "s_waitcnt lgkmcnt(5)\n"
  "v_mfma_f32_16x16x32_bf16 v[32:35], v[88:91], a[100:103], v[32:35]\n"
  "s_waitcnt lgkmcnt(4)\n"
  "v_mfma_f32_16x16x32_bf16 v[36:39], v[92:95], a[100:103], v[36:39]\n"
  "v_mfma_f32_16x16x32_bf16 v[40:43], v[88:91], a[108:111], v[40:43]\n"
  "v_mfma_f32_16x16x32_bf16 v[44:47], v[92:95], a[108:111], v[44:47]\n"
  "v_mfma_f32_16x16x32_bf16 v[48:51], v[88:91], a[116:119], v[48:51]\n"
  "v_mfma_f32_16x16x32_bf16 v[52:55], v[92:95], a[116:119], v[52:55]\n"
  "v_mfma_f32_16x16x32_bf16 v[56:59], v[88:91], a[124:127], v[56:59]\n"
  "v_mfma_f32_16x16x32_bf16 v[60:63], v[92:95], a[124:127], v[60:63]\n"
  "s_waitcnt lgkmcnt(3)\n"
  "v_mfma_f32_16x16x32_bf16 v[32:35], v[64:67], a[128:131], v[32:35]\n"
  "s_waitcnt lgkmcnt(2)\n"
  "v_mfma_f32_16x16x32_bf16 v[36:39], v[68:71], a[128:131], v[36:39]\n"
  "ds_read_b128 v[80:83], v17 offset:5120\n"
  "v_mfma_f32_16x16x32_bf16 v[40:43], v[64:67], a[136:139], v[40:43]\n"
  "v_mfma_f32_16x16x32_bf16 v[44:47], v[68:71], a[136:139], v[44:47]\n"
  "ds_read_b128 v[84:87], v17 offset:15360\n"
  "v_mfma_f32_16x16x32_bf16 v[48:51], v[64:67], a[144:147], v[48:51]\n"
  "v_mfma_f32_16x16x32_bf16 v[52:55], v[68:71], a[144:147], v[52:55]\n"
  "ds_read_b128 v[88:91], v17 offset:25600\n"
  "v_mfma_f32_16x16x32_bf16 v[56:59], v[64:67], a[152:155], v[56:59]\n"
  "v_mfma_f32_16x16x32_bf16 v[60:63], v[68:71], a[152:155], v[60:63]\n"
  "ds_read_b128 v[92:95], v17 offset:35840\n"
  "s_waitcnt lgkmcnt(5)\n"
  "v_mfma_f32_16x16x32_bf16 v[32:35], v[72:75], a[132:135], v[32:35]\n"
  "s_waitcnt lgkmcnt(4)\n"
  "v_mfma_f32_16x16x32_bf16 v[36:39], v[76:79], a[132:135], v[36:39]\n"
  "v_mfma_f32_16x16x32_bf16 v[40:43], v[72:75], a[140:143], v[40:43]\n"
  "v_mfma_f32_16x16x32_bf16 v[44:47], v[76:79], a[140:143], v[44:47]\n"
  "v_mfma_f32_16x16x32_bf16 v[48:51], v[72:75], a[148:151], v[48:51]\n"
  "v_mfma_f32_16x16x32_bf16 v[52:55], v[76:79], a[148:151], v[52:55]\n"
  "v_mfma_f32_16x16x32_bf16 v[56:59], v[72:75], a[156:159], v[56:59]\n"
  "v_mfma_f32_16x16x32_bf16 v[60:63], v[76:79], a[156:159], v[60:63]\n"
  "s_waitcnt lgkmcnt(0)\n"
  "C_WT3_%=:\n"
  "s_memrealtime s[64:65]\n"
  "s_waitcnt lgkmcnt(0)\n"
  "s_sub_i32 s60, s64, s59\n"
  "s_cmp_lt_i32 s60, 0\n"
  "s_cbranch_scc1 C_WT3_%=\n"
  "v_mfma_f32_16x16x32_bf16 v[32:35], v[80:83], a[160:163], v[32:35]\n"
  "buffer_load_dword v22, v14, s[32:35], s74 offen sc1\n"
  "v_mfma_f32_16x16x32_bf16 v[36:39], v[84:87], a[160:163], v[36:39]\n"
  "ds_read_b128 v[64:67], v17 offset:6144\n"
  "v_mfma_f32_16x16x32_bf16 v[40:43], v[80:83], a[168:171], v[40:43]\n"
  "v_mfma_f32_16x16x32_bf16 v[44:47], v[84:87], a[168:171], v[44:47]\n"
  "ds_read_b128 v[68:71], v17 offset:16384\n"
  "s_mov_b64 exec, 1\n"
  "buffer_store_dword v28, v29, s[16:19], 0 offen offset:4 sc1\n"
  "s_mov_b64 exec, -1\n"
  "buffer_load_dword v15, off, s[16:19], s58 sc1\n"
  "v_mfma_f32_16x16x32_bf16 v[48:51], v[80:83], a[176:179], v[48:51]\n"
  "v_mfma_f32_16x16x32_bf16 v[52:55], v[84:87], a[176:179], v[52:55]\n"
  "ds_read_b128 v[72:75], v17 offset:26624\n"
  "v_mfma_f32_16x16x32_bf16 v[56:59], v[80:83], a[184:187], v[56:59]\n"
  "buffer_load_dwordx4 v[112:115], v8, s[16:19], s68 offen offset:0 sc1\n"
  "v_mfma_f32_16x16x32_bf16 v[60:63], v[84:87], a[184:187], v[60:63]\n"
  "ds_read_b128 v[76:79], v17 offset:36864\n"
  "v_mfma_f32_16x16x32_bf16 v[32:35], v[88:91], a[164:167], v[32:35]\n"
  "v_mfma_f32_16x16x32_bf16 v[36:39], v[92:95], a[164:167], v[36:39]\n"
  "buffer_load_dwordx4 v[116:119], v8, s[16:19], s68 offen offset:1280 sc1\n"
  "v_mfma_f32_16x16x32_bf16 v[40:43], v[88:91], a[172:175], v[40:43]\n"
  "v_mfma_f32_16x16x32_bf16 v[44:47], v[92:95], a[172:175], v[44:47]\n"
  "v_mfma_f32_16x16x32_bf16 v[48:51], v[88:91], a[180:183], v[48:51]\n"
  "buffer_load_dwordx4 v[120:123], v8, s[16:19], s68 offen offset:2560 sc1\n"
  "v_mfma_f32_16x16x32_bf16 v[52:55], v[92:95], a[180:183], v[52:55]\n"
  "v_mfma_f32_16x16x32_bf16 v[56:59], v[88:91], a[188:191], v[56:59]\n"
  "v_mfma_f32_16x16x32_bf16 v[60:63], v[92:95], a[188:191], v[60:63]\n"
  "buffer_load_dwordx4 v[124:127], v8, s[16:19], s68 offen offset:3840 sc1\n"
  "s_waitcnt lgkmcnt(3)\n"
  "v_mfma_f32_16x16x32_bf16 v[32:35], v[64:67], a[192:195], v[32:35]\n"
  "s_waitcnt lgkmcnt(2)\n"
  "v_mfma_f32_16x16x32_bf16 v[36:39], v[68:71], a[192:195], v[36:39]\n"
  "ds_read_b128 v[80:83], v17 offset:7168\n"
  "v_mfma_f32_16x16x32_bf16 v[40:43], v[64:67], a[200:203], v[40:43]\n"
  "buffer_load_dwordx4 v[128:131], v9, s[16:19], s68 offen sc1\n"
  "v_mfma_f32_16x16x32_bf16 v[44:47], v[68:71], a[200:203], v[44:47]\n"
  "ds_read_b128 v[84:87], v17 offset:17408\n"
  "v_mfma_f32_16x16x32_bf16 v[48:51], v[64:67], a[208:211], v[48:51]\n"
  "v_mfma_f32_16x16x32_bf16 v[52:55], v[68:71], a[208:211], v[52:55]\n"
  "ds_read_b128 v[88:91], v17 offset:27648\n"
  "buffer_load_dwordx4 v[132:135], v8, s[16:19], s69 offen offset:0 sc1\n"
  "v_mfma_f32_16x16x32_bf16 v[56:59], v[64:67], a[216:219], v[56:59]\n"
  "v_mfma_f32_16x16x32_bf16 v[60:63], v[68:71], a[216:219], v[60:63]\n"
  "ds_read_b128 v[92:95], v17 offset:37888\n"
  "s_waitcnt lgkmcnt(5)\n"
  "v_mfma_f32_16x16x32_bf16 v[32:35], v[72:75], a[196:199], v[32:35]\n"
  "buffer_load_dwordx4 v[136:139], v8, s[16:19], s69 offen offset:1280 sc1\n"
  "s_waitcnt lgkmcnt(4)\n"
  "v_mfma_f32_16x16x32_bf16 v[36:39], v[76:79], a[196:199], v[36:39]\n"
  "v_mfma_f32_16x16x32_bf16 v[40:43], v[72:75], a[204:207], v[40:43]\n"
  "v_mfma_f32_16x16x32_bf16 v[44:47], v[76:79], a[204:207], v[44:47]\n"
  "buffer_load_dwordx4 v[140:143], v8, s[16:19], s69 offen offset:2560 sc1\n"
  "v_mfma_f32_16x16x32_bf16 v[48:51], v[72:75], a[212:215], v[48:51]\n"
  "v_mfma_f32_16x16x32_bf16 v[52:55], v[76:79], a[212:215], v[52:55]\n"
  "v_mfma_f32_16x16x32_bf16 v[56:59], v[72:75], a[220:223], v[56:59]\n"
  "buffer_load_dwordx4 v[144:147], v8, s[16:19], s69 offen offset:3840 sc1\n"
  "v_mfma_f32_16x16x32_bf16 v[60:63], v[76:79], a[220:223], v[60:63]\n"
  "s_waitcnt lgkmcnt(3)\n"
  "v_mfma_f32_16x16x32_bf16 v[32:35], v[80:83], a[224:227], v[32:35]\n"
  "s_waitcnt lgkmcnt(2)\n"
  "v_mfma_f32_16x16x32_bf16 v[36:39], v[84:87], a[224:227], v[36:39]\n"
  "ds_read_b128 v[64:67], v17 offset:8192\n"
  "buffer_load_dwordx4 v[148:151], v9, s[16:19], s69 offen sc1\n"
  "v_mfma_f32_16x16x32_bf16 v[40:43], v[80:83], a[232:235], v[40:43]\n"
  "v_mfma_f32_16x16x32_bf16 v[44:47], v[84:87], a[232:235], v[44:47]\n"
  "ds_read_b128 v[68:71], v17 offset:18432\n"
  "v_mfma_f32_16x16x32_bf16 v[48:51], v[80:83], a[240:243], v[48:51]\n"
  "buffer_load_dwordx4 v[152:155], v8, s[16:19], s70 offen offset:0 sc1\n"
  "v_mfma_f32_16x16x32_bf16 v[52:55], v[84:87], a[240:243], v[52:55]\n"
  "ds_read_b128 v[72:75], v17 offset:28672\n"
  "v_mfma_f32_16x16x32_bf16 v[56:59], v[80:83], a[248:251], v[56:59]\n"
  "v_mfma_f32_16x16x32_bf16 v[60:63], v[84:87], a[248:251], v[60:63]\n"
  "ds_read_b128 v[76:79], v17 offset:38912\n"
  "buffer_load_dwordx4 v[156:159], v8, s[16:19], s70 offen offset:1280 sc1\n"
  "s_waitcnt lgkmcnt(5)\n"
  "v_mfma_f32_16x16x32_bf16 v[32:35], v[88:91], a[228:231], v[32:35]\n"
  "s_waitcnt lgkmcnt(4)\n"
  "v_mfma_f32_16x16x32_bf16 v[36:39], v[92:95], a[228:231], v[36:39]\n"
  "v_mfma_f32_16x16x32_bf16 v[40:43], v[88:91], a[236:239], v[40:43]\n"
  "buffer_load_dwordx4 v[160:163], v8, s[16:19], s70 offen offset:2560 sc1\n"
  "v_mfma_f32_16x16x32_bf16 v[44:47], v[92:95], a[236:239], v[44:47]\n"
  "v_mfma_f32_16x16x32_bf16 v[48:51], v[88:91], a[244:247], v[48:51]\n"
  "v_mfma_f32_16x16x32_bf16 v[52:55], v[92:95], a[244:247], v[52:55]\n"
  "buffer_load_dwordx4 v[164:167], v8, s[16:19], s70 offen offset:3840 sc1\n"
  "v_mfma_f32_16x16x32_bf16 v[56:59], v[88:91], a[252:255], v[56:59]\n"
  "v_mfma_f32_16x16x32_bf16 v[60:63], v[92:95], a[252:255], v[60:63]\n"
  "s_waitcnt lgkmcnt(3)\n"
  "v_mfma_f32_16x16x32_bf16 v[32:35], v[64:67], v[192:195], v[32:35]\n"
  "buffer_load_dwordx4 v[168:171], v9, s[16:19], s70 offen sc1\n"
  "s_waitcnt lgkmcnt(2)\n"
  "v_mfma_f32_16x16x32_bf16 v[36:39], v[68:71], v[192:195], v[36:39]\n"
  "ds_read_b128 v[80:83], v17 offset:9216\n"
  "v_mfma_f32_16x16x32_bf16 v[40:43], v[64:67], v[200:203], v[40:43]\n"
  "v_mfma_f32_16x16x32_bf16 v[44:47], v[68:71], v[200:203], v[44:47]\n"
  "ds_read_b128 v[84:87], v17 offset:19456\n"
  "buffer_load_dwordx4 v[172:175], v8, s[16:19], s71 offen offset:0 sc1\n"
  "v_mfma_f32_16x16x32_bf16 v[48:51], v[64:67], v[208:211], v[48:51]\n"
  "v_mfma_f32_16x16x32_bf16 v[52:55], v[68:71], v[208:211], v[52:55]\n"
  "ds_read_b128 v[88:91], v17 offset:29696\n"
  "v_mfma_f32_16x16x32_bf16 v[56:59], v[64:67], v[216:219], v[56:59]\n"
  "buffer_load_dwordx4 v[176:179], v8, s[16:19], s71 offen offset:1280 sc1\n"
  "v_mfma_f32_16x16x32_bf16 v[60:63], v[68:71], v[216:219], v[60:63]\n"
  "ds_read_b128 v[92:95], v17 offset:39936\n"
  "s_waitcnt lgkmcnt(5)\n"
  "v_mfma_f32_16x16x32_bf16 v[32:35], v[72:75], v[196:199], v[32:35]\n"
  "s_waitcnt lgkmcnt(4)\n"
  "v_mfma_f32_16x16x32_bf16 v[36:39], v[76:79], v[196:199], v[36:39]\n"
  "buffer_load_dwordx4 v[180:183], v8, s[16:19], s71 offen offset:2560 sc1\n"
  "v_mfma_f32_16x16x32_bf16 v[40:43], v[72:75], v[204:207], v[40:43]\n"
  "v_mfma_f32_16x16x32_bf16 v[44:47], v[76:79], v[204:207], v[44:47]\n"
  "v_mfma_f32_16x16x32_bf16 v[48:51], v[72:75], v[212:215], v[48:51]\n"
  "buffer_load_dwordx4 v[184:187], v8, s[16:19], s71 offen offset:3840 sc1\n"
  "v_mfma_f32_16x16x32_bf16 v[52:55], v[76:79], v[212:215], v[52:55]\n"
  "v_mfma_f32_16x16x32_bf16 v[56:59], v[72:75], v[220:223], v[56:59]\n"
  "v_mfma_f32_16x16x32_bf16 v[60:63], v[76:79], v[220:223], v[60:63]\n"
  "buffer_load_dwordx4 v[188:191], v9, s[16:19], s71 offen sc1\n"
  "s_waitcnt lgkmcnt(3)\n"
  "v_mfma_f32_16x16x32_bf16 v[32:35], v[80:83], v[224:227], v[32:35]\n"
  "s_waitcnt lgkmcnt(2)\n"
  "v_mfma_f32_16x16x32_bf16 v[36:39], v[84:87], v[224:227], v[36:39]\n"
  "v_mfma_f32_16x16x32_bf16 v[40:43], v[80:83], v[232:235], v[40:43]\n"
  "buffer_load_dwordx4 v[96:99], v13, s[16:19], s68 offen offset:0 sc1\n"
  "v_mfma_f32_16x16x32_bf16 v[44:47], v[84:87], v[232:235], v[44:47]\n"
  "v_mfma_f32_16x16x32_bf16 v[48:51], v[80:83], v[240:243], v[48:51]\n"
  "v_mfma_f32_16x16x32_bf16 v[52:55], v[84:87], v[240:243], v[52:55]\n"
  "buffer_load_dwordx4 v[100:103], v13, s[16:19], s68 offen offset:1024 sc1\n"
  "v_mfma_f32_16x16x32_bf16 v[56:59], v[80:83], v[248:251], v[56:59]\n"
  "v_mfma_f32_16x16x32_bf16 v[60:63], v[84:87], v[248:251], v[60:63]\n"
  "s_waitcnt lgkmcnt(1)\n"
  "v_mfma_f32_16x16x32_bf16 v[32:35], v[88:91], v[228:231], v[32:35]\n"
  "buffer_load_dwordx4 v[104:107], v13, s[16:19], s68 offen offset:2048 sc1\n"
  "s_waitcnt lgkmcnt(0)\n"
  "v_mfma_f32_16x16x32_bf16 v[36:39], v[92:95], v[228:231], v[36:39]\n"
  "v_mfma_f32_16x16x32_bf16 v[40:43], v[88:91], v[236:239], v[40:43]\n"
  "v_mfma_f32_16x16x32_bf16 v[44:47], v[92:95], v[236:239], v[44:47]\n"
  "buffer_load_dwordx4 v[108:111], v13, s[16:19], s68 offen offset:3072 sc1\n"
  "v_mfma_f32_16x16x32_bf16 v[48:51], v[88:91], v[244:247], v[48:51]\n"
  "v_mfma_f32_16x16x32_bf16 v[52:55], v[92:95], v[244:247], v[52:55]\n"
  "v_mfma_f32_16x16x32_bf16 v[56:59], v[88:91], v[252:255], v[56:59]\n"
  "v_mfma_f32_16x16x32_bf16 v[60:63], v[92:95], v[252:255], v[60:63]\n"
  "v_cvt_pk_bf16_f32 v32, v32, v33\n"
  "v_cvt_pk_bf16_f32 v33, v34, v35\n"
  "v_cvt_pk_bf16_f32 v34, v36, v37\n"
  "v_cvt_pk_bf16_f32 v35, v38, v39\n"
  "ds_write_b128 v19, v[32:35] offset:0\n"
  "v_cvt_pk_bf16_f32 v40, v40, v41\n"
  "v_cvt_pk_bf16_f32 v41, v42, v43\n"
  "v_cvt_pk_bf16_f32 v42, v44, v45\n"
  "v_cvt_pk_bf16_f32 v43, v46, v47\n"
  "ds_write_b128 v19, v[40:43] offset:1024\n"
  "v_cvt_pk_bf16_f32 v48, v48, v49\n"
  "v_cvt_pk_bf16_f32 v49, v50, v51\n"
  "v_cvt_pk_bf16_f32 v50, v52, v53\n"
  "v_cvt_pk_bf16_f32 v51, v54, v55\n"
  "ds_write_b128 v19, v[48:51] offset:2048\n"
  "v_cvt_pk_bf16_f32 v56, v56, v57\n"
  "v_cvt_pk_bf16_f32 v57, v58, v59\n"
  "v_cvt_pk_bf16_f32 v58, v60, v61\n"
  "v_cvt_pk_bf16_f32 v59, v62, v63\n"
  "ds_write_b128 v19, v[56:59] offset:3072\n"
  "C_NOU_%=:\n"
  "s_mov_b32 s62, 0\n"
  "s_waitcnt vmcnt(24)\n"
  "v_readfirstlane_b32 s60, v15\n"
  "s_cmp_lg_u32 s60, -1\n"
  "s_cbranch_scc1 C_F4K_%=\n"
  "s_cmp_ge_u32 s56, 60\n"
  "s_cbranch_scc1 C_F4K_%=\n"
  "s_waitcnt vmcnt(0) lgkmcnt(0)\n"
  "C_F4P_%=:\n"
  "buffer_load_dword v15, off, s[16:19], s58 sc1\n"
  "s_waitcnt vmcnt(0)\n"
  "v_readfirstlane_b32 s60, v15\n"
  "s_cmp_lg_u32 s60, -1\n"
  "s_cbranch_scc1 C_F4K_%=\n"
  "s_sleep 4\n"
  "s_add_u32 s62, s62, 1\n"
  "s_cmp_gt_u32 s62, 300000\n"
  "s_cbranch_scc1 C_FAIL_%=\n"
  "s_and_b32 s60, s62, 255\n"
  "s_cmp_lg_u32 s60, 0\n"
  "s_cbranch_scc1 C_F4P_%=\n"
  "v_mov_b32 v15, 0\n"
  "buffer_load_dword v15, v15, s[40:43], 0 offen sc1\n"
  "s_waitcnt vmcnt(0)\n"
  "v_readfirstlane_b32 s60, v15\n"
  "s_cmp_lg_u32 s60, 0\n"
  "s_cbranch_scc1 C_EXIT_%=\n"
  "s_branch C_F4P_%=\n"
  "C_F4K_%=:\n"
  "s_sub_u32 s98, s44, 1\n"
  "s_cmp_ge_u32 s56, 60\n"
  "s_cselect_b32 s61, 0x80000000, 0\n"
  "v_or_b32 v15, s61, v0\n"
  "s_add_u32 s60, s99, 4\n"
  "s_mul_i32 s72, s60, 40960\n"
  "s_mul_i32 s73, s98, 13312\n"
  "s_add_u32 s72, s72, s73\n"
  "s_add_u32 s73, s73, s96\n"
  "s_mov_b32 m0, s73\n"
  "s_nop 0\n"
  "buffer_load_dwordx4 v15, s[24:27], s72 offen offset:0 sc1 lds\n"
  "buffer_load_dwordx4 v15, s[24:27], s72 offen offset:1024 sc1 lds\n"
  "buffer_load_dwordx4 v15, s[24:27], s72 offen offset:2048 sc1 lds\n"
  "buffer_load_dwordx4 v15, s[24:27], s72 offen offset:3072 sc1 lds\n"
  "s_add_u32 s72, s72, 4096\n"
  "s_add_u32 s73, s73, 4096\n"
  "s_mov_b32 m0, s73\n"
  "s_nop 0\n"
  "buffer_load_dwordx4 v15, s[24:27], s72 offen offset:0 sc1 lds\n"
  "buffer_load_dwordx4 v15, s[24:27], s72 offen offset:1024 sc1 lds\n"
  "buffer_load_dwordx4 v15, s[24:27], s72 offen offset:2048 sc1 lds\n"
  "buffer_load_dwordx4 v15, s[24:27], s72 offen offset:3072 sc1 lds\n"
  "s_add_u32 s72, s72, 4096\n"
  "s_add_u32 s73, s73, 4096\n"
  "s_mov_b32 m0, s73\n"
  "s_nop 0\n"
  "buffer_load_dwordx4 v15, s[24:27], s72 offen offset:0 sc1 lds\n"
  "buffer_load_dwordx4 v15, s[24:27], s72 offen offset:1024 sc1 lds\n"
  "buffer_load_dwordx4 v15, s[24:27], s72 offen offset:2048 sc1 lds\n"
  "buffer_load_dwordx4 v15, s[24:27], s72 offen offset:3072 sc1 lds\n"
  "s_add_u32 s72, s72, 4096\n"
  "s_add_u32 s73, s73, 4096\n"
  "s_mov_b32 m0, s73\n"
  "s_nop 0\n"
  "buffer_load_dwordx4 v15, s[24:27], s72 offen offset:0 sc1 lds\n"
  "buffer_load_dwordx4 v15, s[24:27], s72 offen offset:1024 sc1 lds\n"
  "buffer_load_dwordx2 v[30:31], v10, s[16:19], s57 offen sc1\n"
  "s_cmp_eq_u32 s44, 1\n"
  "s_cbranch_scc1 C_W1_%=\n"
  "ds_read_b128 v[32:35], v18 offset:0\n"
  "ds_read_b128 v[36:39], v18 offset:1024\n"
  "ds_read_b128 v[40:43], v18 offset:2048\n"
  "ds_read_b128 v[44:47], v18 offset:3072\n"
  "ds_read_b128 v[48:51], v18 offset:4096\n"
  "ds_read_b128 v[52:55], v18 offset:5120\n"
  "ds_read_b128 v[56:59], v18 offset:6144\n"
  "ds_read_b128 v[60:63], v18 offset:7168\n"
  "ds_read_b128 v[64:67], v18 offset:8192\n"
  "ds_read_b128 v[68:71], v18 offset:9216\n"
  "s_waitcnt lgkmcnt(10)\n"
  "ds_read_b128 v[72:75], v18 offset:10240\n"
  "ds_read_b128 v[76:79], v18 offset:11264\n"
  "ds_read_b128 v[80:83], v18 offset:12288\n"
  "ds_read_b128 v[84:87], v18 offset:13312\n"
  "s_waitcnt lgkmcnt(10)\n"
  "ds_read_b128 v[88:91], v18 offset:14336\n"
  "ds_read_b128 v[92:95], v18 offset:15360\n"
  "s_waitcnt vmcnt(18)\n"
  "ds_read_b128 v[96:99], v18 offset:16384\n"
  "s_waitcnt vmcnt(17)\n"
  "ds_read_b128 v[100:103], v18 offset:17408\n"
  "s_waitcnt vmcnt(16) lgkmcnt(10)\n"
  "ds_read_b128 v[104:107], v18 offset:18432\n"
  "s_waitcnt vmcnt(15)\n"
  "ds_read_b128 v[108:111], v18 offset:19456\n"
  "s_waitcnt lgkmcnt(0)\n"
  "C_X2_%=:\n"
  "v_mfma_f32_16x16x32_bf16 v[24:27], v[32:35], v[112:115], 0\n"
  "v_mfma_f32_16x16x32_bf16 v[24:27], v[36:39], v[116:119], v[24:27]\n"
  "v_mfma_f32_16x16x32_bf16 v[24:27], v[40:43], v[120:123], v[24:27]\n"
  "v_mfma_f32_16x16x32_bf16 v[24:27], v[44:47], v[124:127], v[24:27]\n"
  "v_mfma_f32_16x16x32_bf16 v[24:27], v[48:51], v[128:131], v[24:27]\n"
  "v_mfma_f32_16x16x32_bf16 v[24:27], v[52:55], v[132:135], v[24:27]\n"
  "v_mfma_f32_16x16x32_bf16 v[24:27], v[56:59], v[136:139], v[24:27]\n"
  "v_mfma_f32_16x16x32_bf16 v[24:27], v[60:63], v[140:143], v[24:27]\n"
  "v_mfma_f32_16x16x32_bf16 v[24:27], v[64:67], v[144:147], v[24:27]\n"
  "v_mfma_f32_16x16x32_bf16 v[24:27], v[68:71], v[148:151], v[24:27]\n"
  "v_mfma_f32_16x16x32_bf16 v[24:27], v[72:75], v[152:155], v[24:27]\n"
  "v_mfma_f32_16x16x32_bf16 v[24:27], v[76:79], v[156:159], v[24:27]\n"
  "v_mfma_f32_16x16x32_bf16 v[24:27], v[80:83], v[160:163], v[24:27]\n"
  "v_mfma_f32_16x16x32_bf16 v[24:27], v[84:87], v[164:167], v[24:27]\n"
  "v_mfma_f32_16x16x32_bf16 v[24:27], v[88:91], v[168:171], v[24:27]\n"
  "v_mfma_f32_16x16x32_bf16 v[24:27], v[92:95], v[172:175], v[24:27]\n"
  "v_mfma_f32_16x16x32_bf16 v[24:27], v[96:99], v[176:179], v[24:27]\n"
  "v_mfma_f32_16x16x32_bf16 v[24:27], v[100:103], v[180:183], v[24:27]\n"
  "v_mfma_f32_16x16x32_bf16 v[24:27], v[104:107], v[184:187], v[24:27]\n"
  "v_mfma_f32_16x16x32_bf16 v[24:27], v[108:111], v[188:191], v[24:27]\n"
  "v_mov_b32 v0, v0\n"
  "v_mov_b32 v0, v0\n"
  "v_mov_b32 v0, v0\n"
  "v_mov_b32 v0, v0\n"
  "v_mov_b32 v0, v0\n"
  "v_mov_b32 v0, v0\n"
  "v_mov_b32 v0, v0\n"
  "v_mov_b32 v0, v0\n"
  "v_add_f32_dpp v24, v26, v24 row_ror:8 row_mask:0xf bank_mask:0xf\n"
  "v_add_f32_dpp v25, v27, v25 row_ror:8 row_mask:0xf bank_mask:0xf\n"
  "v_cmp_u_f32 vcc, v24, v24\n"
  "v_lshlrev_b32 v28, 16, v22\n"
  "v_and_b32 v29, 0xffff0000, v22\n"
  "s_andn2_b64 vcc, vcc, s[66:67]\n"
  "v_add_f32 v24, v24, v28\n"
  "v_add_f32 v25, v25, v29\n"
  "s_cbranch_vccnz C_XREDO_%=\n"
  "v_cvt_pk_bf16_f32 v24, v24, v25\n"
  "buffer_store_dword v24, v12, s[36:39], s63 offen\n"
  "v_min_u32 v25, s78, v24\n"
  "buffer_store_dword v25, v14, s[20:23], s63 offen sc1\n"
  "s_waitcnt vmcnt(2)\n"
  "s_branch C_JOIN_%=\n"
  "C_W1_%=:\n"
  "ds_read_b64 v[32:33], v18 offset:0\n"
  "ds_read_b64 v[34:35], v18 offset:1024\n"
  "ds_read_b64 v[36:37], v18 offset:2048\n"
  "ds_read_b64 v[38:39], v18 offset:3072\n"
  "ds_read_b128 v[64:67], v18 offset:4096\n"
  "ds_read_b64 v[40:41], v18 offset:5120\n"
  "ds_read_b64 v[42:43], v18 offset:6144\n"
  "ds_read_b64 v[44:45], v18 offset:7168\n"
  "ds_read_b64 v[46:47], v18 offset:8192\n"
  "ds_read_b128 v[68:71], v18 offset:9216\n"
  "s_waitcnt lgkmcnt(10)\n"
  "ds_read_b64 v[48:49], v18 offset:10240\n"
  "ds_read_b64 v[50:51], v18 offset:11264\n"
  "ds_read_b64 v[52:53], v18 offset:12288\n"
  "ds_read_b64 v[54:55], v18 offset:13312\n"
  "s_waitcnt lgkmcnt(10)\n"
  "ds_read_b128 v[72:75], v18 offset:14336\n"
  "ds_read_b64 v[56:57], v18 offset:15360\n"
  "ds_read_b64 v[58:59], v18 offset:16384\n"
  "ds_read_b64 v[60:61], v18 offset:17408\n"
  "s_waitcnt lgkmcnt(10)\n"
  "ds_read_b64 v[62:63], v18 offset:18432\n"
  "ds_read_b128 v[76:79], v18 offset:19456\n"
  "s_waitcnt lgkmcnt(0)\n"
  "C_X1_%=:\n"
  "s_waitcnt vmcnt(38)\n"
  "v_mfma_f32_16x16x16_bf16 v[80:83], v[32:33], v[112:113], 0\n"
  "v_mfma_f32_16x16x16_bf16 v[84:87], v[32:33], v[114:115], 0\n"
  "s_waitcnt vmcnt(37)\n"
  "v_mfma_f32_16x16x16_bf16 v[80:83], v[34:35], v[116:117], v[80:83]\n"
  "v_mfma_f32_16x16x16_bf16 v[84:87], v[34:35], v[118:119], v[84:87]\n"
  "s_waitcnt vmcnt(36)\n"
  "v_mfma_f32_16x16x16_bf16 v[80:83], v[36:37], v[120:121], v[80:83]\n"
  "v_mfma_f32_16x16x16_bf16 v[84:87], v[36:37], v[122:123], v[84:87]\n"
  "s_waitcnt vmcnt(35)\n"
  "v_mfma_f32_16x16x16_bf16 v[80:83], v[38:39], v[124:125], v[80:83]\n"
  "v_mfma_f32_16x16x16_bf16 v[84:87], v[38:39], v[126:127], v[84:87]\n"
  "s_waitcnt vmcnt(33)\n"
  "v_mfma_f32_16x16x16_bf16 v[80:83], v[40:41], v[132:133], v[80:83]\n"
  "v_mfma_f32_16x16x16_bf16 v[84:87], v[40:41], v[134:135], v[84:87]\n"
  "s_waitcnt vmcnt(32)\n"
  "v_mfma_f32_16x16x16_bf16 v[80:83], v[42:43], v[136:137], v[80:83]\n"
  "v_mfma_f32_16x16x16_bf16 v[84:87], v[42:43], v[138:139], v[84:87]\n"
  "s_waitcnt vmcnt(31)\n"
  "v_mfma_f32_16x16x16_bf16 v[80:83], v[44:45], v[140:141], v[80:83]\n"
  "v_mfma_f32_16x16x16_bf16 v[84:87], v[44:45], v[142:143], v[84:87]\n"
  "s_waitcnt vmcnt(30)\n"
  "v_mfma_f32_16x16x16_bf16 v[80:83], v[46:47], v[144:145], v[80:83]\n"
  "v_mfma_f32_16x16x16_bf16 v[84:87], v[46:47], v[146:147], v[84:87]\n"
  "s_waitcnt vmcnt(28)\n"
  "v_mfma_f32_16x16x16_bf16 v[80:83], v[48:49], v[152:153], v[80:83]\n"
  "v_mfma_f32_16x16x16_bf16 v[84:87], v[48:49], v[154:155], v[84:87]\n"
  "s_waitcnt vmcnt(27)\n"
  "v_mfma_f32_16x16x16_bf16 v[80:83], v[50:51], v[156:157], v[80:83]\n"
  "v_mfma_f32_16x16x16_bf16 v[84:87], v[50:51], v[158:159], v[84:87]\n"
  "s_waitcnt vmcnt(26)\n"
  "v_mfma_f32_16x16x16_bf16 v[80:83], v[52:53], v[160:161], v[80:83]\n"
  "v_mfma_f32_16x16x16_bf16 v[84:87], v[52:53], v[162:163], v[84:87]\n"
  "s_waitcnt vmcnt(25)\n"
  "v_mfma_f32_16x16x16_bf16 v[80:83], v[54:55], v[164:165], v[80:83]\n"
  "v_mfma_f32_16x16x16_bf16 v[84:87], v[54:55], v[166:167], v[84:87]\n"
  "s_waitcnt vmcnt(23)\n"
  "v_mfma_f32_16x16x16_bf16 v[80:83], v[56:57], v[172:173], v[80:83]\n"
  "v_mfma_f32_16x16x16_bf16 v[84:87], v[56:57], v[174:175], v[84:87]\n"
  "s_waitcnt vmcnt(22)\n"
  "v_mfma_f32_16x16x16_bf16 v[80:83], v[58:59], v[176:177], v[80:83]\n"
  "v_mfma_f32_16x16x16_bf16 v[84:87], v[58:59], v[178:179], v[84:87]\n"
  "s_waitcnt vmcnt(21)\n"
  "v_mfma_f32_16x16x16_bf16 v[80:83], v[60:61], v[180:181], v[80:83]\n"
  "v_mfma_f32_16x16x16_bf16 v[84:87], v[60:61], v[182:183], v[84:87]\n"
  "s_waitcnt vmcnt(20)\n"
  "v_mfma_f32_16x16x16_bf16 v[80:83], v[62:63], v[184:185], v[80:83]\n"
  "v_mfma_f32_16x16x16_bf16 v[84:87], v[62:63], v[186:187], v[84:87]\n"
  "v_mfma_f32_16x16x32_bf16 v[88:91], v[64:67], v[128:131], 0\n"
  "s_waitcnt vmcnt(18)\n"
  "v_mfma_f32_16x16x32_bf16 v[92:95], v[64:67], v[96:99], 0\n"
  "v_mfma_f32_16x16x32_bf16 v[88:91], v[68:71], v[148:151], v[88:91]\n"
  "s_waitcnt vmcnt(17)\n"
  "v_mfma_f32_16x16x32_bf16 v[92:95], v[68:71], v[100:103], v[92:95]\n"
  "v_mfma_f32_16x16x32_bf16 v[88:91], v[72:75], v[168:171], v[88:91]\n"
  "s_waitcnt vmcnt(16)\n"
  "v_mfma_f32_16x16x32_bf16 v[92:95], v[72:75], v[104:107], v[92:95]\n"
  "v_mfma_f32_16x16x32_bf16 v[88:91], v[76:79], v[188:191], v[88:91]\n"
  "s_waitcnt vmcnt(15)\n"
  "v_mfma_f32_16x16x32_bf16 v[92:95], v[76:79], v[108:111], v[92:95]\n"
  "v_mov_b32 v0, v0\n"
  "v_mov_b32 v0, v0\n"
  "v_mov_b32 v0, v0\n"
  "v_mov_b32 v0, v0\n"
  "v_mov_b32 v0, v0\n"
  "v_mov_b32 v0, v0\n"
  "v_pk_add_f32 v[80:81], v[80:81], v[88:89]\n"
  "v_pk_add_f32 v[82:83], v[82:83], v[90:91]\n"
  "v_pk_add_f32 v[84:85], v[84:85], v[92:93]\n"
  "v_pk_add_f32 v[86:87], v[86:87], v[94:95]\n"
  "v_add_f32_dpp v24, v82, v80 row_ror:8 row_mask:0xf bank_mask:0x3\n"
  "v_add_f32_dpp v25, v83, v81 row_ror:8 row_mask:0xf bank_mask:0x3\n"
  "v_add_f32_dpp v24, v84, v86 row_ror:8 row_mask:0xf bank_mask:0xc\n"
  "v_add_f32_dpp v25, v85, v87 row_ror:8 row_mask:0xf bank_mask:0xc\n"
  "v_cmp_u_f32 vcc, v24, v24\n"
  "v_lshlrev_b32 v28, 16, v22\n"
  "v_and_b32 v29, 0xffff0000, v22\n"
  "s_andn2_b64 vcc, vcc, s[66:67]\n"
  "v_add_f32 v24, v24, v28\n"
  "v_add_f32 v25, v25, v29\n"
  "s_cbranch_vccnz C_X1REDO_%=\n"
  "v_cvt_pk_bf16_f32 v24, v24, v25\n"
  "buffer_store_dword v24, v12, s[36:39], s63 offen\n"
  "v_min_u32 v25, s78, v24\n"
  "buffer_store_dword v25, v14, s[20:23], s63 offen sc1\n"
  "s_waitcnt vmcnt(2)\n"
  "C_JOIN_%=:\n"
  "s_mov_b32 s96, s75\n"
  "s_add_u32 s56, s56, 2\n"
  "s_add_u32 s99, s99, 2\n"
  "s_cmp_lt_u32 s56, 64\n"
  "s_cbranch_scc1 C_LOOP_%=\n"
  "s_waitcnt vmcnt(0)\n"
  "s_mov_b32 s38, 2621440\n"
  "s_mov_b32 s22, 0\n"
  "s_add_u32 s46, s46, 64\n"
  "s_cmp_lt_u32 s46, 128\n"
  "s_cbranch_scc1 C_LAYER_%=\n"
  "s_branch C_EXIT_%=\n"
  "C_WT2C_%=:\n"
  "C_WT2D_%=:\n"
  "s_memrealtime s[64:65]\n"
  "s_waitcnt lgkmcnt(0)\n"
  "s_sub_i32 s60, s64, s59\n"
  "s_cmp_lt_i32 s60, 0\n"
  "s_cbranch_scc1 C_WT2D_%=\n"
  "s_mov_b32 s59, s64\n"
  "s_memtime s[64:65]\n"
  "s_waitcnt lgkmcnt(0)\n"
  "s_mov_b32 s11, s64\n"
  "s_branch C_WT2K_%=\n"
  "C_MBAD_%=:\n"
  "s_nop 7\n"
  "s_add_u32 s62, s62, 1\n"
  "s_cmp_gt_u32 s62, 300000\n"
  "s_cbranch_scc1 C_FAIL_%=\n"
  "s_and_b32 s60, s62, 255\n"
  "s_cmp_lg_u32 s60, 0\n"
  "s_cbranch_scc1 C_MP_%=\n"
  "v_mov_b32 v15, 0\n"
  "buffer_load_dword v15, v15, s[40:43], 0 offen sc1\n"
  "s_waitcnt vmcnt(0)\n"
  "v_readfirstlane_b32 s60, v15\n"
  "s_cmp_lg_u32 s60, 0\n"
  "s_cbranch_scc1 C_EXIT_%=\n"
  "s_branch C_MP_%=\n"
  "C_REDO_%=:\n"
  "s_nop 7\n"
  "s_waitcnt vmcnt(0) lgkmcnt(0)\n"
  "s_mov_b32 s62, 0\n"
  "C_REDOP_%=:\n"
  "s_nop 7\n"
  "buffer_load_dwordx4 v[112:115], v6, s[16:19], s68 offen offset:0 sc1\n"
  "buffer_load_dwordx4 v[116:119], v6, s[16:19], s68 offen offset:1280 sc1\n"
  "buffer_load_dwordx4 v[120:123], v6, s[16:19], s68 offen offset:2560 sc1\n"
  "buffer_load_dwordx4 v[124:127], v6, s[16:19], s68 offen offset:3840 sc1\n"
  "buffer_load_dwordx4 v[128:131], v7, s[16:19], s68 offen sc1\n"
  "buffer_load_dwordx4 v[132:135], v6, s[16:19], s69 offen offset:0 sc1\n"
  "buffer_load_dwordx4 v[136:139], v6, s[16:19], s69 offen offset:1280 sc1\n"
  "buffer_load_dwordx4 v[140:143], v6, s[16:19], s69 offen offset:2560 sc1\n"
  "buffer_load_dwordx4 v[144:147], v6, s[16:19], s69 offen offset:3840 sc1\n"
  "buffer_load_dwordx4 v[148:151], v7, s[16:19], s69 offen sc1\n"
  "buffer_load_dwordx4 v[152:155], v6, s[16:19], s70 offen offset:0 sc1\n"
  "buffer_load_dwordx4 v[156:159], v6, s[16:19], s70 offen offset:1280 sc1\n"
  "buffer_load_dwordx4 v[160:163], v6, s[16:19], s70 offen offset:2560 sc1\n"
  "buffer_load_dwordx4 v[164:167], v6, s[16:19], s70 offen offset:3840 sc1\n"
  "buffer_load_dwordx4 v[168:171], v7, s[16:19], s70 offen sc1\n"
  "buffer_load_dwordx4 v[172:175], v6, s[16:19], s71 offen offset:0 sc1\n"
  "buffer_load_dwordx4 v[176:179], v6, s[16:19], s71 offen offset:1280 sc1\n"
  "buffer_load_dwordx4 v[180:183], v6, s[16:19], s71 offen offset:2560 sc1\n"
  "buffer_load_dwordx4 v[184:187], v6, s[16:19], s71 offen offset:3840 sc1\n"
  "buffer_load_dwordx4 v[188:191], v7, s[16:19], s71 offen sc1\n"
  "s_waitcnt vmcnt(19)\n"
  "v_max3_u32 v15, v112, v113, v114\n"
  "s_waitcnt vmcnt(18)\n"
  "v_max3_u32 v15, v115, v116, v15\n"
  "v_max3_u32 v15, v117, v118, v15\n"
  "s_waitcnt vmcnt(17)\n"
  "v_max3_u32 v15, v119, v120, v15\n"
  "v_max3_u32 v15, v121, v122, v15\n"
  "s_waitcnt vmcnt(16)\n"
  "v_max3_u32 v15, v123, v124, v15\n"
  "v_max3_u32 v15, v125, v126, v15\n"
  "s_waitcnt vmcnt(15)\n"
  "v_max3_u32 v15, v127, v128, v15\n"
  "v_max3_u32 v15, v129, v130, v15\n"
  "s_waitcnt vmcnt(14)\n"
  "v_max3_u32 v15, v131, v132, v15\n"
  "v_max3_u32 v15, v133, v134, v15\n"
  "s_waitcnt vmcnt(13)\n"
  "v_max3_u32 v15, v135, v136, v15\n"
  "v_max3_u32 v15, v137, v138, v15\n"
  "s_waitcnt vmcnt(12)\n"
  "v_max3_u32 v15, v139, v140, v15\n"
  "v_max3_u32 v15, v141, v142, v15\n"
  "s_waitcnt vmcnt(11)\n"
  "v_max3_u32 v15, v143, v144, v15\n"
  "v_max3_u32 v15, v145, v146, v15\n"
  "s_waitcnt vmcnt(10)\n"
  "v_max3_u32 v15, v147, v148, v15\n"
  "v_max3_u32 v15, v149, v150, v15\n"
  "s_waitcnt vmcnt(9)\n"
  "v_max3_u32 v15, v151, v152, v15\n"
  "v_max3_u32 v15, v153, v154, v15\n"
  "s_waitcnt vmcnt(8)\n"
  "v_max3_u32 v15, v155, v156, v15\n"
  "v_max3_u32 v15, v157, v158, v15\n"
  "s_waitcnt vmcnt(7)\n"
  "v_max3_u32 v15, v159, v160, v15\n"
  "v_max3_u32 v15, v161, v162, v15\n"
  "s_waitcnt vmcnt(6)\n"
  "v_max3_u32 v15, v163, v164, v15\n"
  "v_max3_u32 v15, v165, v166, v15\n"
  "s_waitcnt vmcnt(5)\n"
  "v_max3_u32 v15, v167, v168, v15\n"
  "v_max3_u32 v15, v169, v170, v15\n"
  "s_waitcnt vmcnt(4)\n"
  "v_max3_u32 v15, v171, v172, v15\n"
  "v_max3_u32 v15, v173, v174, v15\n"
  "s_waitcnt vmcnt(3)\n"
  "v_max3_u32 v15, v175, v176, v15\n"
  "v_max3_u32 v15, v177, v178, v15\n"
  "s_waitcnt vmcnt(2)\n"
  "v_max3_u32 v15, v179, v180, v15\n"
  "v_max3_u32 v15, v181, v182, v15\n"
  "s_waitcnt vmcnt(1)\n"
  "v_max3_u32 v15, v183, v184, v15\n"
  "v_max3_u32 v15, v185, v186, v15\n"
  "s_waitcnt vmcnt(0)\n"
  "v_max3_u32 v15, v187, v188, v15\n"
  "v_max3_u32 v15, v189, v190, v15\n"
  "v_max_u32 v15, v191, v15\n"
  "s_waitcnt vmcnt(0) lgkmcnt(0)\n"
  "v_cmp_eq_u32 vcc, -1, v15\n"
  "s_nop 1\n"
  "s_mov_b64 s[66:67], -1\n"
  "s_cbranch_vccz C_P2_%=\n"
  "s_add_u32 s62, s62, 1\n"
  "s_cmp_gt_u32 s62, 300000\n"
  "s_cbranch_scc1 C_FAIL_%=\n"
  "s_and_b32 s60, s62, 255\n"
  "s_cmp_lg_u32 s60, 0\n"
  "s_cbranch_scc1 C_REDOP_%=\n"
  "v_mov_b32 v15, 0\n"
  "buffer_load_dword v15, v15, s[40:43], 0 offen sc1\n"
  "s_waitcnt vmcnt(0)\n"
  "v_readfirstlane_b32 s60, v15\n"
  "s_cmp_lg_u32 s60, 0\n"
  "s_cbranch_scc1 C_EXIT_%=\n"
  "s_branch C_REDOP_%=\n"
  "C_XREDO_%=:\n"
  "s_nop 7\n"
  "s_waitcnt vmcnt(0) lgkmcnt(0)\n"
  "s_mov_b32 s62, 0\n"
  "C_XREDOP_%=:\n"
  "s_nop 7\n"
  "buffer_load_dwordx4 v[112:115], v8, s[16:19], s68 offen offset:0 sc1\n"
  "buffer_load_dwordx4 v[116:119], v8, s[16:19], s68 offen offset:1280 sc1\n"
  "buffer_load_dwordx4 v[120:123], v8, s[16:19], s68 offen offset:2560 sc1\n"
  "buffer_load_dwordx4 v[124:127], v8, s[16:19], s68 offen offset:3840 sc1\n"
  "buffer_load_dwordx4 v[128:131], v9, s[16:19], s68 offen sc1\n"
  "buffer_load_dwordx4 v[132:135], v8, s[16:19], s69 offen offset:0 sc1\n"
  "buffer_load_dwordx4 v[136:139], v8, s[16:19], s69 offen offset:1280 sc1\n"
  "buffer_load_dwordx4 v[140:143], v8, s[16:19], s69 offen offset:2560 sc1\n"
  "buffer_load_dwordx4 v[144:147], v8, s[16:19], s69 offen offset:3840 sc1\n"
  "buffer_load_dwordx4 v[148:151], v9, s[16:19], s69 offen sc1\n"
  "buffer_load_dwordx4 v[152:155], v8, s[16:19], s70 offen offset:0 sc1\n"
  "buffer_load_dwordx4 v[156:159], v8, s[16:19], s70 offen offset:1280 sc1\n"
  "buffer_load_dwordx4 v[160:163], v8, s[16:19], s70 offen offset:2560 sc1\n"
  "buffer_load_dwordx4 v[164:167], v8, s[16:19], s70 offen offset:3840 sc1\n"
  "buffer_load_dwordx4 v[168:171], v9, s[16:19], s70 offen sc1\n"
  "buffer_load_dwordx4 v[172:175], v8, s[16:19], s71 offen offset:0 sc1\n"
  "buffer_load_dwordx4 v[176:179], v8, s[16:19], s71 offen offset:1280 sc1\n"
  "buffer_load_dwordx4 v[180:183], v8, s[16:19], s71 offen offset:2560 sc1\n"
  "buffer_load_dwordx4 v[184:187], v8, s[16:19], s71 offen offset:3840 sc1\n"
  "buffer_load_dwordx4 v[188:191], v9, s[16:19], s71 offen sc1\n"
  "s_waitcnt vmcnt(19)\n"
  "v_max3_u32 v15, v112, v113, v114\n"
  "s_waitcnt vmcnt(18)\n"
  "v_max3_u32 v15, v115, v116, v15\n"
  "v_max3_u32 v15, v117, v118, v15\n"
  "s_waitcnt vmcnt(17)\n"
  "v_max3_u32 v15, v119, v120, v15\n"
  "v_max3_u32 v15, v121, v122, v15\n"
  "s_waitcnt vmcnt(16)\n"
  "v_max3_u32 v15, v123, v124, v15\n"
  "v_max3_u32 v15, v125, v126, v15\n"
  "s_waitcnt vmcnt(15)\n"
  "v_max3_u32 v15, v127, v128, v15\n"
  "v_max3_u32 v15, v129, v130, v15\n"
  "s_waitcnt vmcnt(14)\n"
  "v_max3_u32 v15, v131, v132, v15\n"
  "v_max3_u32 v15, v133, v134, v15\n"
  "s_waitcnt vmcnt(13)\n"
  "v_max3_u32 v15, v135, v136, v15\n"
  "v_max3_u32 v15, v137, v138, v15\n"
  "s_waitcnt vmcnt(12)\n"
  "v_max3_u32 v15, v139, v140, v15\n"
  "v_max3_u32 v15, v141, v142, v15\n"
  "s_waitcnt vmcnt(11)\n"
  "v_max3_u32 v15, v143, v144, v15\n"
  "v_max3_u32 v15, v145, v146, v15\n"
  "s_waitcnt vmcnt(10)\n"
  "v_max3_u32 v15, v147, v148, v15\n"
  "v_max3_u32 v15, v149, v150, v15\n"
  "s_waitcnt vmcnt(9)\n"
  "v_max3_u32 v15, v151, v152, v15\n"
  "v_max3_u32 v15, v153, v154, v15\n"
  "s_waitcnt vmcnt(8)\n"
  "v_max3_u32 v15, v155, v156, v15\n"
  "v_max3_u32 v15, v157, v158, v15\n"
  "s_waitcnt vmcnt(7)\n"
  "v_max3_u32 v15, v159, v160, v15\n"
  "v_max3_u32 v15, v161, v162, v15\n"
  "s_waitcnt vmcnt(6)\n"
  "v_max3_u32 v15, v163, v164, v15\n"
  "v_max3_u32 v15, v165, v166, v15\n"
  "s_waitcnt vmcnt(5)\n"
  "v_max3_u32 v15, v167, v168, v15\n"
  "v_max3_u32 v15, v169, v170, v15\n"
  "s_waitcnt vmcnt(4)\n"
  "v_max3_u32 v15, v171, v172, v15\n"
  "v_max3_u32 v15, v173, v174, v15\n"
  "s_waitcnt vmcnt(3)\n"
  "v_max3_u32 v15, v175, v176, v15\n"
  "v_max3_u32 v15, v177, v178, v15\n"
  "s_waitcnt vmcnt(2)\n"
  "v_max3_u32 v15, v179, v180, v15\n"
  "v_max3_u32 v15, v181, v182, v15\n"
  "s_waitcnt vmcnt(1)\n"
  "v_max3_u32 v15, v183, v184, v15\n"
  "v_max3_u32 v15, v185, v186, v15\n"
  "s_waitcnt vmcnt(0)\n"
  "v_max3_u32 v15, v187, v188, v15\n"
  "v_max3_u32 v15, v189, v190, v15\n"
  "v_max_u32 v15, v191, v15\n"
  "s_waitcnt vmcnt(0) lgkmcnt(0)\n"
  "v_cmp_eq_u32 vcc, -1, v15\n"
  "s_nop 1\n"
  "s_mov_b64 s[66:67], -1\n"
  "s_cbranch_vccz C_X2_%=\n"
  "s_add_u32 s62, s62, 1\n"
  "s_cmp_gt_u32 s62, 300000\n"
  "s_cbranch_scc1 C_FAIL_%=\n"
  "s_and_b32 s60, s62, 255\n"
  "s_cmp_lg_u32 s60, 0\n"
  "s_cbranch_scc1 C_XREDOP_%=\n"
  "v_mov_b32 v15, 0\n"
  "buffer_load_dword v15, v15, s[40:43], 0 offen sc1\n"
  "s_waitcnt vmcnt(0)\n"
  "v_readfirstlane_b32 s60, v15\n"
  "s_cmp_lg_u32 s60, 0\n"
  "s_cbranch_scc1 C_EXIT_%=\n"
  "s_branch C_XREDOP_%=\n"
  "C_X1REDO_%=:\n"
  "s_nop 7\n"
  "s_waitcnt vmcnt(0) lgkmcnt(0)\n"
  "s_mov_b32 s62, 0\n"
  "C_X1REDOP_%=:\n"
  "s_nop 7\n"
  "buffer_load_dwordx4 v[112:115], v8, s[16:19], s68 offen offset:0 sc1\n"
  "buffer_load_dwordx4 v[116:119], v8, s[16:19], s68 offen offset:1280 sc1\n"
  "buffer_load_dwordx4 v[120:123], v8, s[16:19], s68 offen offset:2560 sc1\n"
  "buffer_load_dwordx4 v[124:127], v8, s[16:19], s68 offen offset:3840 sc1\n"
  "buffer_load_dwordx4 v[128:131], v9, s[16:19], s68 offen sc1\n"
  "buffer_load_dwordx4 v[132:135], v8, s[16:19], s69 offen offset:0 sc1\n"
  "buffer_load_dwordx4 v[136:139], v8, s[16:19], s69 offen offset:1280 sc1\n"
  "buffer_load_dwordx4 v[140:143], v8, s[16:19], s69 offen offset:2560 sc1\n"
  "buffer_load_dwordx4 v[144:147], v8, s[16:19], s69 offen offset:3840 sc1\n"
  "buffer_load_dwordx4 v[148:151], v9, s[16:19], s69 offen sc1\n"
  "buffer_load_dwordx4 v[152:155], v8, s[16:19], s70 offen offset:0 sc1\n"
  "buffer_load_dwordx4 v[156:159], v8, s[16:19], s70 offen offset:1280 sc1\n"
  "buffer_load_dwordx4 v[160:163], v8, s[16:19], s70 offen offset:2560 sc1\n"
  "buffer_load_dwordx4 v[164:167], v8, s[16:19], s70 offen offset:3840 sc1\n"
  "buffer_load_dwordx4 v[168:171], v9, s[16:19], s70 offen sc1\n"
  "buffer_load_dwordx4 v[172:175], v8, s[16:19], s71 offen offset:0 sc1\n"
  "buffer_load_dwordx4 v[176:179], v8, s[16:19], s71 offen offset:1280 sc1\n"
  "buffer_load_dwordx4 v[180:183], v8, s[16:19], s71 offen offset:2560 sc1\n"
  "buffer_load_dwordx4 v[184:187], v8, s[16:19], s71 offen offset:3840 sc1\n"
  "buffer_load_dwordx4 v[188:191], v9, s[16:19], s71 offen sc1\n"
  "buffer_load_dwordx4 v[96:99], v13, s[16:19], s68 offen offset:0 sc1\n"
  "buffer_load_dwordx4 v[100:103], v13, s[16:19], s68 offen offset:1024 sc1\n"
  "buffer_load_dwordx4 v[104:107], v13, s[16:19], s68 offen offset:2048 sc1\n"
  "buffer_load_dwordx4 v[108:111], v13, s[16:19], s68 offen offset:3072 sc1\n"
  "s_waitcnt vmcnt(3)\n"
  "v_max3_u32 v15, v96, v97, v98\n"
  "s_waitcnt vmcnt(2)\n"
  "v_max3_u32 v15, v99, v100, v15\n"
  "v_max3_u32 v15, v101, v102, v15\n"
  "s_waitcnt vmcnt(1)\n"
  "v_max3_u32 v15, v103, v104, v15\n"
  "v_max3_u32 v15, v105, v106, v15\n"
  "s_waitcnt vmcnt(0)\n"
  "v_max3_u32 v15, v107, v108, v15\n"
  "v_max3_u32 v15, v109, v110, v15\n"
  "v_max3_u32 v15, v111, v112, v15\n"
  "v_max3_u32 v15, v113, v114, v15\n"
  "v_max3_u32 v15, v115, v116, v15\n"
  "v_max3_u32 v15, v117, v118, v15\n"
  "v_max3_u32 v15, v119, v120, v15\n"
  "v_max3_u32 v15, v121, v122, v15\n"
  "v_max3_u32 v15, v123, v124, v15\n"
  "v_max3_u32 v15, v125, v126, v15\n"
  "v_max3_u32 v15, v127, v128, v15\n"
  "v_max3_u32 v15, v129, v130, v15\n"
  "v_max3_u32 v15, v131, v132, v15\n"
  "v_max3_u32 v15, v133, v134, v15\n"
  "v_max3_u32 v15, v135, v136, v15\n"
  "v_max3_u32 v15, v137, v138, v15\n"
  "v_max3_u32 v15, v139, v140, v15\n"
  "v_max3_u32 v15, v141, v142, v15\n"
  "v_max3_u32 v15, v143, v144, v15\n"
  "v_max3_u32 v15, v145, v146, v15\n"
  "v_max3_u32 v15, v147, v148, v15\n"
  "v_max3_u32 v15, v149, v150, v15\n"
  "v_max3_u32 v15, v151, v152, v15\n"
  "v_max3_u32 v15, v153, v154, v15\n"
  "v_max3_u32 v15, v155, v156, v15\n"
  "v_max3_u32 v15, v157, v158, v15\n"
  "v_max3_u32 v15, v159, v160, v15\n"
  "v_max3_u32 v15, v161, v162, v15\n"
  "v_max3_u32 v15, v163, v164, v15\n"
  "v_max3_u32 v15, v165, v166, v15\n"
  "v_max3_u32 v15, v167, v168, v15\n"
  "v_max3_u32 v15, v169, v170, v15\n"
  "v_max3_u32 v15, v171, v172, v15\n"
  "v_max3_u32 v15, v173, v174, v15\n"
  "v_max3_u32 v15, v175, v176, v15\n"
  "v_max3_u32 v15, v177, v178, v15\n"
  "v_max3_u32 v15, v179, v180, v15\n"
  "v_max3_u32 v15, v181, v182, v15\n"
  "v_max3_u32 v15, v183, v184, v15\n"
  "v_max3_u32 v15, v185, v186, v15\n"
  "v_max3_u32 v15, v187, v188, v15\n"
  "v_max3_u32 v15, v189, v190, v15\n"
  "v_max_u32 v15, v191, v15\n"
  "s_waitcnt vmcnt(0) lgkmcnt(0)\n"
  "v_cmp_eq_u32 vcc, -1, v15\n"
  "s_nop 1\n"
  "s_mov_b64 s[66:67], -1\n"
  "s_cbranch_vccz C_X1_%=\n"
  "s_add_u32 s62, s62, 1\n"
  "s_cmp_gt_u32 s62, 300000\n"
  "s_cbranch_scc1 C_FAIL_%=\n"
  "s_and_b32 s60, s62, 255\n"
  "s_cmp_lg_u32 s60, 0\n"
  "s_cbranch_scc1 C_X1REDOP_%=\n"
  "v_mov_b32 v15, 0\n"
  "buffer_load_dword v15, v15, s[40:43], 0 offen sc1\n"
  "s_waitcnt vmcnt(0)\n"
  "v_readfirstlane_b32 s60, v15\n"
  "s_cmp_lg_u32 s60, 0\n"
  "s_cbranch_scc1 C_EXIT_%=\n"
  "s_branch C_X1REDOP_%=\n"
  "C_FAIL_%=:\n"
  "s_nop 7\n"
  "v_mov_b32 v32, 0\n"
  "v_mov_b32 v33, 1\n"
  "buffer_store_dword v33, v32, s[40:43], 0 offen sc1\n"
  "C_EXIT_%=:\n"
  "s_nop 7\n"
  "s_waitcnt vmcnt(0)\n"
  "s_endpgm\n"
      :: "{s[16:31]}"(sa), "{s[32:47]}"(sb), "{s[48:55]}"(sc), "{s79}"(dbgoff), "{s2}"(sto), "{v[0:15]}"(va), "{v[16:23]}"(vb), "{v[24:31]}"(vc) : "v255", "a255", "s99", "vcc", "memory");
}

static int envi(const char*, int d) { return d; }
extern "C" void kernel_launch(void* const* d_in, const int* in_sizes, int n_in, void* d_out, int out_size, void* d_ws, size_t ws_size, hipStream_t stream) {
  Args a;
  a.x0 = (const u16*)d_in[0]; a.wqkv = (const u16*)d_in[1]; a.wo = (const u16*)d_in[2]; a.mem0 = (const u16*)d_in[4];
  a.Wq = (const u16*)d_in[5]; a.Wk = (const u16*)d_in[6]; a.Wv = (const u16*)d_in[7]; a.Wo = (const u16*)d_in[8];
  a.out = (u16*)d_out;
  a.ws = (u8*)(((uintptr_t)d_ws + (2u << 20) - 1) & ~(uintptr_t)((2u << 20) - 1));
  a.marg = envi("R_MP", 44) | (envi("R_MM", 84) << 8) | (envi("R_G", 6) << 16) | (envi("R_D0", 38) << 24); a.pad = 0;
  a.dp = envi("R_DP", 128); a.dm = envi("R_DM", 72); a.skew = envi("R_SKEW", 10); a.delta = envi("R_DELTA", 10); a.lat = envi("R_LAT", 50); a.pubw = envi("R_PUBW", -2);
  (void)hipFuncSetAttribute((const void*)k_main, hipFuncAttributeMaxDynamicSharedMemorySize, 163840);
  k_pre<<<envi("R_NB", 298), 256, 0, stream>>>(a);
  if (!envi("R_PREONLY", 0)) k_main<<<256, 256, 163840, stream>>>(a);
}
```
